```python
import torch

def _softmax(x, axis=-1):
    return torch.softmax(x, dim=axis)
def _gelu(x):
    return torch.nn.functional.gelu(x, approximate="tanh")
def _silu(x):
    return torch.nn.functional.silu(x)
def _sigmoid(x):
    return torch.sigmoid(x)
def _layernorm(x, axis=-1, eps=1e-5):
    m = x.mean(dim=axis, keepdim=True)
    v = x.var(dim=axis, keepdim=True, unbiased=False)
    return (x - m) / torch.sqrt(v + eps)
def _scatter_add(tgt, idx, src, axis=0):
    return tgt.clone().index_add_(axis, idx, src)
def _segment_sum(src, seg, n):
    out = torch.zeros((n,) + src.shape[1:], dtype=src.dtype)
    return out.index_add_(0, seg, src)
def _pad(x, pw):
    flat = tuple(v for pair in reversed(pw) for v in pair)
    return torch.nn.functional.pad(x, flat)

kernel_name = 'sequential_preLN__mha_block_cached__moe_arch_mod__decode'

def setup_inputs():

    _w = (lambda a: (lambda t: t.to(torch.bfloat16) if t.is_floating_point() else t)(torch.as_tensor(a)))
    def _A_setup():
        B, Sq, D = 4, 1, 640
        H, Sk, Dh = 32, 8192, 192
        return {
            "x": _w((lambda *s: torch.randn(*s, dtype=torch.bfloat16) * 0.05)(B, Sq, D)),
            "k_cache": _w((lambda *s: torch.randn(*s, dtype=torch.bfloat16) * 3.2)(B, H, Sk, Dh)),
            "v_cache": _w((lambda *s: torch.randn(*s, dtype=torch.bfloat16) * 0.4)(B, H, Sk, Dh)),
            "wq": _w((lambda *s: torch.randn(*s, dtype=torch.bfloat16) * 3.2)(D, H * Dh) * (D ** -0.5)),
            "wo": _w((lambda *s: torch.randn(*s, dtype=torch.bfloat16) * 0.4)(H * Dh, D) * ((H * Dh) ** -0.5)),
        }
    _A_in = _A_setup()

    def _B_setup():
        B, S, D, F, C = 4, 1, 640, 1536, 1
        return {
            "x": _w((lambda *s: torch.randn(*s, dtype=torch.bfloat16) * 0.05)(B, S, D)),
            "w_r": _w((lambda *s: torch.randn(*s, dtype=torch.bfloat16) * 0.05)(D)),
            "w_gate": _w((lambda *s: torch.randn(*s, dtype=torch.bfloat16) * 0.05)(D, F)),
            "w_up": _w((lambda *s: torch.randn(*s, dtype=torch.bfloat16) * 0.05)(D, F)),
            "w_down": _w((lambda *s: torch.randn(*s, dtype=torch.bfloat16) * 0.05)(F, D)),
        }
    _B_in = _B_setup()

    return {
        "A_x": _A_in["x"],
        "A_k_cache": _A_in["k_cache"],
        "A_v_cache": _A_in["v_cache"],
        "A_wq": _A_in["wq"],
        "A_wo": _A_in["wo"],
        "B_x": _B_in["x"],
        "B_w_r": _B_in["w_r"],
        "B_w_gate": _B_in["w_gate"],
        "B_w_up": _B_in["w_up"],
        "B_w_down": _B_in["w_down"],
    }

def reference(A_x, A_k_cache, A_v_cache, A_wq, A_wo, B_x, B_w_r, B_w_gate, B_w_up, B_w_down):
    def _A_ref(x, k_cache, v_cache, wq, wo):
        B, Sq, D = x.shape
        H, Dh = 32, 192
        q = (lambda x, *d: x.permute(*d))((x @ wq).reshape(B, Sq, H, Dh), 0, 2, 1, 3)
        scores = torch.einsum("bhqd,bhkd->bhqk", q, k_cache) * (Dh ** -0.5)
        p = _softmax(scores, axis=-1)
        o = torch.einsum("bhqk,bhkd->bhqd", p, v_cache)
        return (lambda x, *d: x.permute(*d))(o, 0, 2, 1, 3).reshape(B, Sq, H * Dh) @ wo

    def _B_ref(x, w_r, w_gate, w_up, w_down):
        B, S, D = x.shape
        C = 1
        r = x @ w_r
        idx = torch.argsort(-r, axis=-1)[:, :C]
        idx_e = (lambda x, n, axis: torch.repeat_interleave(x, n, dim=axis))(idx.reshape(B, C, 1), D, axis=2)
        x_sel = (lambda x, i, axis: torch.take_along_dim(x, i, dim=axis))(x, idx_e, axis=1)
        g = _silu(x_sel @ w_gate)
        u = x_sel @ w_up
        y = (g * u) @ w_down
        r_sel = _sigmoid((lambda x, i, axis: torch.take_along_dim(x, i, dim=axis))(r, idx, axis=1))
        y = y * r_sel.reshape(B, C, 1)
        b_off = (torch.arange(B) * S).reshape(B, 1)
        flat_idx = (idx + b_off).reshape(B * C)
        zero = (x * 0.0).reshape(B * S, D)
        upd = _scatter_add(zero, flat_idx, y.reshape(B * C, D), axis=0)
        return x + upd.reshape(B, S, D)

    x = A_x
    h = x + _A_ref(_layernorm(x, axis=-1), A_k_cache, A_v_cache, A_wq, A_wo)
    h2 = h + _B_ref(_layernorm(h, axis=-1), B_w_r, B_w_gate, B_w_up, B_w_down)
    return h2
```

```cpp
#define WNT 0
#define QNT 0
#include <hip/hip_runtime.h>
#include <hip/hip_bf16.h>
#include <stdint.h>

typedef __hip_bfloat16 bf16;
using u32x4 = __attribute__((ext_vector_type(4))) unsigned;
using u32x2 = __attribute__((ext_vector_type(2))) unsigned;
using f32x4 = __attribute__((ext_vector_type(4))) float;
using f32x2 = __attribute__((ext_vector_type(2))) float;

constexpr int NB = 4, DM = 640, NH = 32, SK = 8192, DH = 192, HD = NH * DH, FF = 1536;
constexpr int NSPLIT = 2, NWG = NB * NH * NSPLIT;
constexpr float LN_EPS = 1e-5f;
constexpr float QSCALE = 0.07216878364870322f * 1.4426950408889634f;

constexpr int WS_HP = 0;
constexpr int WS_HC = WS_HP + 2 * NWG;
constexpr int NREPL = 4, REPL = NWG + 832;
constexpr int WS_DONE = WS_HC + 2 * NWG;
constexpr int WS_TICKET = WS_DONE + NREPL * REPL;
constexpr int REC = DH + 4;
constexpr int WS_PO = WS_TICKET + 32;
constexpr int K3_KS = 12, K3_ROWS = HD / K3_KS;
constexpr int WS_SLAB = WS_PO + NWG * REC;
constexpr int WS_ACT = WS_SLAB + K3_KS * NB * DM;
constexpr int ZALL = 96 * 1024, ZWG = NWG;
constexpr int WS_END = WS_DONE + ZALL;
static_assert(WS_ACT + NB * FF <= WS_END && WS_DONE % 64 == 0 && WS_SLAB % 4 == 0 && WS_PO % 4 == 0 && ZALL / ZWG % 64 == 0, "zeroed region");
constexpr int OOB = 0x7ffffff0;

__device__ __forceinline__ float bf_lo(unsigned u) { return __uint_as_float(u << 16); }
__device__ __forceinline__ float bf_hi(unsigned u) { return __uint_as_float(u & 0xffff0000u); }
__device__ __forceinline__ f32x2 bf_pair(unsigned u) { return f32x2{bf_lo(u), bf_hi(u)}; }
__device__ __forceinline__ f32x2 fma2(float a, f32x2 b, f32x2 c) { return __builtin_elementwise_fma(f32x2{a, a}, b, c); }
__device__ __forceinline__ f32x2 fma2(f32x2 a, f32x2 b, f32x2 c) { return __builtin_elementwise_fma(a, b, c); }

template <int CTRL> __device__ __forceinline__ float dpp(float x) {
  return __builtin_bit_cast(float, __builtin_amdgcn_mov_dpp(__builtin_bit_cast(int, x), CTRL, 0xf, 0xf, true));
}
constexpr int DPP_XOR1 = 0xB1, DPP_XOR2 = 0x4E, DPP_HMIRROR = 0x141;

__device__ __forceinline__ float sum_x16_32(float x) {
  auto s = __builtin_amdgcn_permlane16_swap(__float_as_uint(x), __float_as_uint(x), false, false);
  x = __uint_as_float(s[0]) + __uint_as_float(s[1]);
  auto t = __builtin_amdgcn_permlane32_swap(__float_as_uint(x), __float_as_uint(x), false, false);
  return __uint_as_float(t[0]) + __uint_as_float(t[1]);
}
__device__ __forceinline__ float max_x16_32(float x) {
  auto s = __builtin_amdgcn_permlane16_swap(__float_as_uint(x), __float_as_uint(x), false, false);
  x = fmaxf(__uint_as_float(s[0]), __uint_as_float(s[1]));
  auto t = __builtin_amdgcn_permlane32_swap(__float_as_uint(x), __float_as_uint(x), false, false);
  return fmaxf(__uint_as_float(t[0]), __uint_as_float(t[1]));
}
__device__ __forceinline__ float tsum32(float a, float b) {
  auto t = __builtin_amdgcn_permlane32_swap(__float_as_uint(a), __float_as_uint(b), false, false);
  return __uint_as_float(t[0]) + __uint_as_float(t[1]);
}
__device__ __forceinline__ float tsum16(float a, float b) {
  auto t = __builtin_amdgcn_permlane16_swap(__float_as_uint(a), __float_as_uint(b), false, false);
  return __uint_as_float(t[0]) + __uint_as_float(t[1]);
}
constexpr int DPP_ROR8 = 0x128, DPP_ROR4 = 0x124;
__device__ __forceinline__ float sum_r(float v) { v += dpp<DPP_ROR8>(v); return sum_x16_32(v); }
__device__ __forceinline__ float max_r(float v) { v = fmaxf(v, dpp<DPP_ROR8>(v)); return max_x16_32(v); }
__device__ __forceinline__ float wave_sum(float v) {
  v += dpp<DPP_XOR1>(v); v += dpp<DPP_XOR2>(v); v += dpp<DPP_HMIRROR>(v);
  return sum_r(v);
}

__device__ __forceinline__ u32x4 ld_nt(const u32x4* p) { return __builtin_nontemporal_load(p); }

template <class T> __device__ __forceinline__ T ldw(const T* p) { return WNT ? __builtin_nontemporal_load(p) : *p; }
__device__ __forceinline__ u32x4 ldq(const u32x4* p) { return QNT ? __builtin_nontemporal_load(p) : *p; }
struct Slot { u32x4 a[3]; u32x4 b[3]; };

#define PIN8(a, o) asm volatile("" : "+v"((a)[(o) + 0]), "+v"((a)[(o) + 1]), "+v"((a)[(o) + 2]), "+v"((a)[(o) + 3]), \
                                       "+v"((a)[(o) + 4]), "+v"((a)[(o) + 5]), "+v"((a)[(o) + 6]), "+v"((a)[(o) + 7]))
#define PIN4(a, o) asm volatile("" : "+v"((a)[(o) + 0]), "+v"((a)[(o) + 1]), "+v"((a)[(o) + 2]), "+v"((a)[(o) + 3]))
#define MEMFENCE() do { asm volatile("" ::: "memory"); __builtin_amdgcn_sched_barrier(0); } while (0)
#define DRAIN() asm volatile("s_waitcnt vmcnt(0)" ::: "memory")

__device__ __forceinline__ void glds16_nt(const void* gsrc, unsigned lds_dst) {
  unsigned keep;
  asm volatile("s_mov_b32 %0, m0\n\ts_mov_b32 m0, %2\n\ts_nop 0\n\tglobal_load_lds_dwordx4 %1, off nt\n\ts_mov_b32 m0, %0"
               : "=&s"(keep) : "v"(gsrc), "s"(lds_dst) : "memory");
}

__device__ __forceinline__ void slot_load(Slot& t, const u32x4* p, int ta, int tb) {
#pragma unroll
  for (int i = 0; i < 3; ++i) t.a[i] = ld_nt(p + (size_t)ta * 192 + i * 8);
  MEMFENCE();
#pragma unroll
  for (int i = 0; i < 3; ++i) t.b[i] = ld_nt(p + (size_t)tb * 192 + i * 8);
  MEMFENCE();
}
__device__ __forceinline__ void k_tile(const u32x4 (&k)[3], const f32x2 (&q)[12], float& gmx, float* dst, int j) {
  f32x2 s0 = {0.f, 0.f}, s1 = s0, s2 = s0;
#pragma unroll
  for (int d = 0; d < 4; ++d) {
    s0 = fma2(bf_pair(k[0][d]), q[d], s0);
    s1 = fma2(bf_pair(k[1][d]), q[4 + d], s1);
    s2 = fma2(bf_pair(k[2][d]), q[8 + d], s2);
  }
  s0 = (s0 + s1) + s2;
  float s = s0[0] + s0[1];
  s += dpp<DPP_XOR1>(s);
  s += dpp<DPP_XOR2>(s);
  s += dpp<DPP_HMIRROR>(s);
  gmx = fmaxf(gmx, s);
  asm volatile("" : "+v"(gmx));
  if (j == 0) *dst = s;
}
__device__ __forceinline__ void v_tile(u32x4 (&v)[3], float p, f32x2 (&acc)[12]) {
#pragma unroll
  for (int i = 0; i < 3; ++i) {
    asm volatile("" : "+v"(v[i]), "+v"(p));
#pragma unroll
    for (int d = 0; d < 4; ++d) acc[i * 4 + d] = fma2(p, bf_pair(v[i][d]), acc[i * 4 + d]);
    PIN4(acc, i * 4);
  }
}

__device__ __forceinline__ void load_q(f32x2 (&q)[12], const float* s_buf, int j) {
#pragma unroll
  for (int i = 0; i < 6; ++i) {
    f32x4 a = *reinterpret_cast<const f32x4*>(&s_buf[(i >> 1) * 64 + j * 8 + (i & 1) * 4]);
#pragma unroll
    for (int w = 1; w < 4; ++w) a += *reinterpret_cast<const f32x4*>(&s_buf[w * DH + (i >> 1) * 64 + j * 8 + (i & 1) * 4]);
    a *= QSCALE;
    q[i * 2] = f32x2{a[0], a[1]}; q[i * 2 + 1] = f32x2{a[2], a[3]};
  }
}

constexpr int LOAD_W = 160, LOAD_G = 100, LOAD_D = 64;
constexpr int GATE_W = 8, GATE_G = 8;
constexpr int SLP_LONG = 64;
constexpr int AUX_SC1 = 16;
constexpr unsigned SPIN_CAP = 1u << 17;
__device__ __forceinline__ unsigned nz_bits(float v) { const unsigned u = __float_as_uint(v); return u == 0u ? 0x80000000u : u; }
__device__ __forceinline__ void lds_put(int* p, int v) { __hip_atomic_store(p, v, __ATOMIC_RELAXED, __HIP_MEMORY_SCOPE_WORKGROUP); }
__device__ __forceinline__ int lds_get(int* p) { return __hip_atomic_load(p, __ATOMIC_RELAXED, __HIP_MEMORY_SCOPE_WORKGROUP); }
using bf16x8 = __attribute__((ext_vector_type(8))) short;
__device__ __forceinline__ unsigned pk_lo(unsigned a, unsigned b) { return __builtin_amdgcn_perm(b, a, 0x05040100u); }
__device__ __forceinline__ unsigned pk_hi(unsigned a, unsigned b) { return __builtin_amdgcn_perm(b, a, 0x07060302u); }
__device__ __forceinline__ void split3(float v, unsigned& t0, unsigned& t1, unsigned& t2) {
  t0 = __float_as_uint(v) & 0xffff0000u;
  const float r1 = v - __uint_as_float(t0);
  t1 = __float_as_uint(r1) & 0xffff0000u;
  t2 = __float_as_uint(r1 - __uint_as_float(t1));
}
__device__ __forceinline__ f32x4 mfma16(u32x4 a, u32x4 b, f32x4 c) {
  return __builtin_amdgcn_mfma_f32_16x16x32_bf16(__builtin_bit_cast(bf16x8, a), __builtin_bit_cast(bf16x8, b), c, 0, 0, 0);
}
__device__ __forceinline__ void put_terms4(char* img, int rowbytes, f32x4 v) {
  unsigned t[3][4];
#pragma unroll
  for (int e = 0; e < 4; ++e) split3(v[e], t[0][e], t[1][e], t[2][e]);
#pragma unroll
  for (int k = 0; k < 3; ++k) *reinterpret_cast<u32x2*>(img + k * rowbytes) = u32x2{pk_hi(t[k][0], t[k][1]), pk_hi(t[k][2], t[k][3])};
}
__device__ __forceinline__ bool all_nz(u32x4 v) { return v[0] != 0u && v[1] != 0u && v[2] != 0u && v[3] != 0u; }

__device__ __forceinline__ float sum3(f32x4 v) { return (v[0] + v[1]) + v[2]; }
__device__ __forceinline__ void gate(__amdgpu_buffer_rsrc_t rs_ws, int* s_go, int w, int lane, int tk, int hlo, int hhi, int open, int near, int stage) {
  if (w == 7) {
    const int h0 = (lane & 15) * 2;
    const bool in0 = h0 >= hlo && h0 <= hhi, in1 = h0 + 1 >= hlo && h0 + 1 <= hhi;
    for (unsigned spin = 0; spin < SPIN_CAP; ++spin) {
      const u32x4 dn = __builtin_amdgcn_raw_buffer_load_b128(rs_ws, (WS_DONE + (tk & (NREPL - 1)) * REPL + lane * 4) * 4, 0, AUX_SC1);
      const int miss = __builtin_popcountll(__ballot(in0 && dn[0] == 0u)) + __builtin_popcountll(__ballot(in0 && dn[1] == 0u)) +
                       __builtin_popcountll(__ballot(in1 && dn[2] == 0u)) + __builtin_popcountll(__ballot(in1 && dn[3] == 0u));
      if (miss <= open) break;
      if (miss > near) __builtin_amdgcn_s_sleep(SLP_LONG); else __builtin_amdgcn_s_sleep(8);
    }
    lds_put(s_go, stage);
  } else {
    for (unsigned spin = 0; spin < (SPIN_CAP << 4); ++spin) {
      if (lds_get(s_go) >= stage) break;
      __builtin_amdgcn_s_sleep(2);
    }
  }
}

constexpr int NW = 8, NBUF = 4, NL = 10, NQW = 4, NX = 3, QWIN = 10;
constexpr int NSW = NW - NQW, NGRP = SK / NSPLIT / 64, NKT = 7;
constexpr int W_CT = DM / 128, G_TASKS = 2 * (FF / 32), W_TASKS = W_CT * K3_KS, D_TASKS = 2 * (DM / 32);
static_assert(G_TASKS + W_TASKS + D_TASKS <= NWG && NW == 8 && K3_ROWS == NW * 64 && NBUF == 4 && NGRP == 64 && NL + 2 * NX == 16 &&
              NW * NKT + 2 * NSW == NGRP && NQW == NSW, "shape");
constexpr int REGION = NL * 3 * 64;
constexpr int NOT_YET = 0x7fc0dead;

__global__ void __launch_bounds__(NW * 64) fused_kernel(const bf16* __restrict__ x, const bf16* __restrict__ kc, const bf16* __restrict__ vc,
                                                        const bf16* __restrict__ wq, const bf16* __restrict__ wo, const bf16* __restrict__ wr,
                                                        const bf16* __restrict__ wg, const bf16* __restrict__ wu, const bf16* __restrict__ wd,
                                                        float* ws, bf16* __restrict__ out) {
  constexpr int QROWS = DM / NQW;
  constexpr int QG = QROWS / 8;
  __shared__ __attribute__((aligned(16))) u32x4 s_kv[NSW * REGION];
  __shared__ __attribute__((aligned(16))) float s_buf[NW * DH];
  __shared__ __attribute__((aligned(16))) float s_sc[NGRP * 64];
  __shared__ int s_gm[NGRP];
  __shared__ float s_m[NW], s_l[NW];
  __shared__ float s_sig[2];
  __shared__ int s_go, s_ticket;
  __shared__ int s_prog[NW];
  __shared__ float s_stat[4];

  const int tid = threadIdx.x, lane = tid & 63, r = lane >> 3, j = lane & 7;
  const int wave = __builtin_amdgcn_readfirstlane(tid >> 6);
  const int bid = blockIdx.x, xcd = bid & 7, idx = bid >> 3;
  const int h = (idx >> 3) * 8 + xcd, slot = idx & 7, b = slot >> 1, split = slot & 1;
  const int bh = b * NH + h;
  const bool qwave = wave < NQW;
  const __amdgpu_buffer_rsrc_t rs_ws = __builtin_amdgcn_make_buffer_rsrc(ws, 0, WS_END * 4, 0x00020000);
  const size_t row0 = (size_t)bh * SK + (size_t)split * (SK / NSPLIT);
  const u32x4* kp = reinterpret_cast<const u32x4*>(kc + row0 * DH) + r * 24 + j;
  const u32x4* vp = reinterpret_cast<const u32x4*>(vc + row0 * DH) + r * 24 + j;
  const int ph = bh * 85 + split * 107;
  auto gt = [&](int g, int t) { return ((g >= 16 ? (g >> 3) * 64 + t * 8 + (g & 7) : g * 8 + t) + ph) & (NGRP * 8 - 1); };
  auto gload = [&](Slot& sl, const u32x4* p, int g, int u) { slot_load(sl, p, gt(g, 2 * u), gt(g, 2 * u + 1)); };
  auto seq = [&](int n) { return n == 0 ? (qwave ? 3 * NSW + wave : 3 * (wave - NQW)) : (n < NKT ? 8 + 8 * n + wave : 3 * wave + n - (NKT - 1)); };
  auto k_flush = [&](int g, float& gmx) {
    const float gm = max_r(gmx);
    __builtin_amdgcn_fence(__ATOMIC_RELEASE, "workgroup");
    if (lane == 0) lds_put(&s_gm[g], __float_as_int(gm));
    gmx = -INFINITY;
  };
  const unsigned lds_base = (unsigned)(size_t)(__attribute__((address_space(3))) char*)s_kv + (wave - NQW) * (REGION * 16);
  unsigned hs_nc = 0u, hs_ep = 0u;
  Slot buf[NBUF];
  f32x2 q[12];
  float gmx = -INFINITY;
  if (qwave) {
    if (wave == 0) {
      const u32x4 z4 = {0u, 0u, 0u, 0u};
#pragma unroll
      for (int i = 0; i < (ZALL / ZWG + 255) / 256; ++i)
        __builtin_amdgcn_raw_buffer_store_b128(z4, rs_ws, (bid < ZWG && i * 256 + lane * 4 < ZALL / ZWG) ? (WS_DONE + bid * (ZALL / ZWG) + i * 256 + lane * 4) * 4 : OOB, 0, AUX_SC1);
      if (lane == 0) s_go = 0;
      s_gm[lane] = NOT_YET;
      if (lane < NW) s_prog[lane] = 0;
    }
    const unsigned* x32 = reinterpret_cast<const unsigned*>(x + (size_t)b * DM);
    unsigned xr[5];
#pragma unroll
    for (int i = 0; i < 5; ++i) xr[i] = x32[lane + 64 * i];
    unsigned short xrow[QG];
#pragma unroll
    for (int g = 0; g < QG; ++g) xrow[g] = reinterpret_cast<const unsigned short*>(x)[(size_t)b * DM + wave * QROWS + g * 8 + r];
    __builtin_amdgcn_sched_barrier(0);
    const u32x4* wp = reinterpret_cast<const u32x4*>(wq + (size_t)(wave * QROWS + r) * HD + h * DH) + j;
    u32x4 wv[QWIN][3];
#pragma unroll
    for (int g = 0; g < QWIN; ++g)
#pragma unroll
      for (int i = 0; i < 3; ++i) wv[g][i] = ldq(wp + (size_t)g * 8 * (HD / 8) + i * 8);
    __builtin_amdgcn_sched_barrier(0);
    float xs = 0.f;
#pragma unroll
    for (int i = 0; i < 5; ++i) xs += bf_lo(xr[i]) + bf_hi(xr[i]);
    const float mean = wave_sum(xs) * (1.0f / DM);
    float xv = 0.f;
#pragma unroll
    for (int i = 0; i < 5; ++i) { const float a = bf_lo(xr[i]) - mean, c = bf_hi(xr[i]) - mean; xv += a * a + c * c; }
    const float var = wave_sum(xv) * (1.0f / DM);
    const float rstd = 1.0f / sqrtf(var + LN_EPS);
    f32x2 qa[12];
#pragma unroll
    for (int i = 0; i < 12; ++i) qa[i] = f32x2{0.f, 0.f};
    u32x2 pw = {0u, 0u};
    __builtin_amdgcn_sched_barrier(0);
#pragma unroll
    for (int g = 0; g < QG; ++g) {
      const float xl = (bf_lo(xrow[g]) - mean) * rstd;
#pragma unroll
      for (int i = 0; i < 3; ++i)
#pragma unroll
        for (int d = 0; d < 4; ++d) qa[i * 4 + d] = fma2(xl, bf_pair(wv[g % QWIN][i][d]), qa[i * 4 + d]);
      PIN4(qa, 0); PIN4(qa, 4); PIN4(qa, 8);
      MEMFENCE();
      if (g + QWIN < QG) {
#pragma unroll
        for (int i = 0; i < 3; ++i) wv[g % QWIN][i] = ldq(wp + (size_t)(g + QWIN) * 8 * (HD / 8) + i * 8);
      }
      if (g + QWIN == QG - 1) pw = __builtin_amdgcn_raw_buffer_load_b64(rs_ws, wave == 0 ? (WS_HP + bid * 2) * 4 : OOB, 0, AUX_SC1);
      MEMFENCE();
    }
    DRAIN();
    hs_ep = __builtin_amdgcn_readfirstlane(pw[0]);
    hs_nc = __builtin_amdgcn_readfirstlane(pw[1]) + 1u;
    __builtin_amdgcn_raw_buffer_store_b64(u32x2{hs_nc, hs_ep}, rs_ws, tid == 0 ? (WS_HC + bid * 2) * 4 : OOB, 0, AUX_SC1);
#pragma unroll
    for (int i = 0; i < 12; ++i) qa[i] = f32x2{sum_r(qa[i][0]), sum_r(qa[i][1])};
    if (r == 0) {
#pragma unroll
      for (int i = 0; i < 3; ++i) {
        *reinterpret_cast<f32x4*>(&s_buf[wave * DH + i * 64 + j * 8]) = f32x4{qa[i * 4][0], qa[i * 4][1], qa[i * 4 + 1][0], qa[i * 4 + 1][1]};
        *reinterpret_cast<f32x4*>(&s_buf[wave * DH + i * 64 + j * 8 + 4]) = f32x4{qa[i * 4 + 2][0], qa[i * 4 + 2][1], qa[i * 4 + 3][0], qa[i * 4 + 3][1]};
      }
    }
    __syncthreads();
#pragma unroll
    for (int u = 0; u < NBUF; ++u) gload(buf[u], kp, seq(0), u);
    MEMFENCE();
    load_q(q, s_buf, j);
  } else {
    Slot xt[NX];
    const int g0 = seq(0);
    const bool master = bid == 0 && wave == NQW;
    if (master) {
      u32x4 hp[2], hc[2];
#pragma unroll
      for (int i = 0; i < 2; ++i) {
        hp[i] = __builtin_amdgcn_raw_buffer_load_b128(rs_ws, (WS_HP + lane * 8 + i * 4) * 4, 0, AUX_SC1);
        hc[i] = __builtin_amdgcn_raw_buffer_load_b128(rs_ws, (WS_HC + lane * 8 + i * 4) * 4, 0, AUX_SC1);
      }
#pragma unroll
      for (int i = 0; i < 2; ++i) {
#pragma unroll
        for (int e = 0; e < 4; e += 2) { unsigned np = hp[i][e] + 1u; if (np == hc[i][e + 1]) np += 1u; hp[i][e] = np; }
        __builtin_amdgcn_raw_buffer_store_b128(hp[i], rs_ws, (WS_HP + lane * 8 + i * 4) * 4, 0, AUX_SC1);
      }
    }
    MEMFENCE();
#pragma unroll
    for (int u = 0; u < NBUF; ++u) gload(buf[u], kp, g0, u);
#pragma unroll
    for (int e = 0; e < NL * 3; ++e) {
      const int u = e / 3, i = e % 3;
      glds16_nt(kp + (size_t)gt(g0 + 1 + (u >> 3), u & 7) * 192 + i * 8, lds_base + e * 1024);
    }
#pragma unroll
    for (int u = 0; u < NX; ++u) gload(xt[u], kp, g0 + 2, u + 1);
    if (master) asm volatile("s_waitcnt vmcnt(48)" ::: "memory");
    __syncthreads();
    load_q(q, s_buf, j);
    __builtin_amdgcn_sched_barrier(0);
#pragma unroll
    for (int u = 0; u < NBUF; ++u) {
      k_tile(buf[u].a, q, gmx, &s_sc[g0 * 64 + r * 8 + 2 * u], j);
      k_tile(buf[u].b, q, gmx, &s_sc[g0 * 64 + r * 8 + 2 * u + 1], j);
      MEMFENCE();
      gload(buf[u], kp, seq(1), u);
    }
    k_flush(g0, gmx);
#pragma unroll
    for (int u = 0; u < NX; ++u) {
      k_tile(xt[u].a, q, gmx, &s_sc[(g0 + 2) * 64 + r * 8 + 2 + 2 * u], j);
      k_tile(xt[u].b, q, gmx, &s_sc[(g0 + 2) * 64 + r * 8 + 3 + 2 * u], j);
      __builtin_amdgcn_sched_barrier(0);
    }
    asm volatile("s_waitcnt vmcnt(24)" ::: "memory");
#pragma unroll 1
    for (int uu = 0; uu < NL; ++uu) {
      const int u = uu < 2 ? uu + 8 : uu - 2;
      u32x4 tmp[3];
#pragma unroll
      for (int i = 0; i < 3; ++i) tmp[i] = s_kv[(wave - NQW) * REGION + (u * 3 + i) * 64 + lane];
      k_tile(tmp, q, gmx, &s_sc[(g0 + 1) * 64 + (u >> 3) * 64 + r * 8 + (u & 7)], j);
      if (uu == 1) k_flush(g0 + 2, gmx);
    }
    k_flush(g0 + 1, gmx);
    MEMFENCE();
  }
  auto turn_prio = [&](int turn) {
    if (lane == 0) lds_put(&s_prog[wave], turn);
    const int pp = __builtin_amdgcn_readfirstlane(lds_get(&s_prog[wave ^ (NW / 2)]));
    if (turn > pp) __builtin_amdgcn_s_setprio(0); else __builtin_amdgcn_s_setprio(1);
  };
  auto look_at_p = [&](bool wait) {
    for (unsigned spin = 0; spin < SPIN_CAP; ++spin) {
      const u32x2 pw = __builtin_amdgcn_raw_buffer_load_b64(rs_ws, (WS_HP + bid * 2) * 4, 0, AUX_SC1);
      const unsigned np = __builtin_amdgcn_readfirstlane(pw[0]), ec = __builtin_amdgcn_readfirstlane(pw[1]);
      if (np != hs_ep) { hs_ep = np; __builtin_amdgcn_raw_buffer_store_b64(u32x2{hs_nc, hs_ep}, rs_ws, lane == 0 ? (WS_HC + bid * 2) * 4 : OOB, 0, AUX_SC1); }
      if (ec == hs_nc || !wait) break;
      __builtin_amdgcn_s_sleep(4);
    }
  };
  for (int n = qwave ? 0 : 1; n < NKT; ++n) {
    const int g = seq(n);
    turn_prio(n);
    const u32x4* np = n == NKT - 1 ? vp : kp;
    const int ng = seq(n == NKT - 1 ? 0 : n + 1);
    MEMFENCE();
#pragma unroll
    for (int u = 0; u < NBUF; ++u) {
      k_tile(buf[u].a, q, gmx, &s_sc[g * 64 + r * 8 + 2 * u], j);
      k_tile(buf[u].b, q, gmx, &s_sc[g * 64 + r * 8 + 2 * u + 1], j);
      MEMFENCE();
      gload(buf[u], np, ng, u);
    }
    k_flush(g, gmx);
    if (wave == 0 && n == 1) look_at_p(false);
    if (bid == 0 && wave == 1 && n == 4) {
      for (unsigned spin = 0; spin < SPIN_CAP; ++spin) {
        u32x4 hp[2], hc[2];
#pragma unroll
        for (int i = 0; i < 2; ++i) {
          hp[i] = __builtin_amdgcn_raw_buffer_load_b128(rs_ws, (WS_HP + lane * 8 + i * 4) * 4, 0, AUX_SC1);
          hc[i] = __builtin_amdgcn_raw_buffer_load_b128(rs_ws, (WS_HC + lane * 8 + i * 4) * 4, 0, AUX_SC1);
        }
        const bool ok = hc[0][1] == hp[0][0] && hc[0][3] == hp[0][2] && hc[1][1] == hp[1][0] && hc[1][3] == hp[1][2];
        if (__all(ok)) {
#pragma unroll
          for (int i = 0; i < 2; ++i)
            __builtin_amdgcn_raw_buffer_store_b128(u32x4{hp[i][0], hc[i][0], hp[i][2], hc[i][2]}, rs_ws, (WS_HP + lane * 8 + i * 4) * 4, 0, AUX_SC1);
          break;
        }
        __builtin_amdgcn_s_sleep(16);
      }
    }
  }
  float m = -INFINITY, l = 0.f;
  f32x2 acc[12];
#pragma unroll
  for (int i = 0; i < 12; ++i) acc[i] = f32x2{0.f, 0.f};
  const int nv = qwave ? NKT + 2 : NKT;
  float p[8];
  auto v_begin = [&](int g) {
    int gm = lds_get(&s_gm[g]);
    for (unsigned spin = 0; spin < (SPIN_CAP << 4) && gm == NOT_YET; ++spin) { __builtin_amdgcn_s_sleep(2); gm = lds_get(&s_gm[g]); }
    __builtin_amdgcn_fence(__ATOMIC_ACQUIRE, "workgroup");
    const f32x4 sa = *reinterpret_cast<const f32x4*>(&s_sc[g * 64 + r * 8]), sb = *reinterpret_cast<const f32x4*>(&s_sc[g * 64 + r * 8 + 4]);
    const float mn = fmaxf(m, __int_as_float(gm));
    const float alpha = __builtin_amdgcn_exp2f(m - mn);
    m = mn;
#pragma unroll
    for (int t = 0; t < 4; ++t) { p[t] = __builtin_amdgcn_exp2f(sa[t] - mn); p[4 + t] = __builtin_amdgcn_exp2f(sb[t] - mn); }
    l = fmaf(l, alpha, ((p[0] + p[1]) + (p[2] + p[3])) + ((p[4] + p[5]) + (p[6] + p[7])));
    f32x2 al2 = {alpha, alpha};
    asm volatile("" : "+v"(al2));
#pragma unroll
    for (int i = 0; i < 12; ++i) acc[i] *= al2;
    PIN4(acc, 0); PIN4(acc, 4); PIN4(acc, 8);
    MEMFENCE();
  };
  for (int n = 0; n < nv - 1; ++n) {
    turn_prio(NKT + n);
    v_begin(seq(n));
    const int ng = seq(n + 1);
#pragma unroll
    for (int u = 0; u < NBUF; ++u) {
      v_tile(buf[u].a, p[2 * u], acc);
      v_tile(buf[u].b, p[2 * u + 1], acc);
      MEMFENCE();
      gload(buf[u], vp, ng, u);
    }
    if (wave == 0 && n == 3) look_at_p(true);
  }
  v_begin(seq(nv - 1));
#pragma unroll
  for (int u = 0; u < NBUF; ++u) { v_tile(buf[u].a, p[2 * u], acc); v_tile(buf[u].b, p[2 * u + 1], acc); __builtin_amdgcn_sched_barrier(0); }
  __builtin_amdgcn_s_setprio(0);

  {
    const float mw = m, lw = sum_r(l);
    float t12[12], t6[6];
#pragma unroll
    for (int e = 0; e < 12; ++e) t12[e] = tsum32(acc[e >> 1][e & 1], acc[6 + (e >> 1)][e & 1]);
#pragma unroll
    for (int e = 0; e < 6; ++e) { t6[e] = tsum16(t12[e], t12[e + 6]); t6[e] += dpp<DPP_ROR8>(t6[e]); }
    if ((lane & 8) == 0) {
#pragma unroll
      for (int e = 0; e < 3; ++e) *reinterpret_cast<f32x2*>(&s_buf[wave * DH + j * 24 + (lane >> 4) * 6 + e * 2]) = f32x2{t6[2 * e], t6[2 * e + 1]};
    }
    if (lane == 0) { s_m[wave] = mw; s_l[wave] = lw; }
  }
  __syncthreads();
  if (wave == 0) {
    float M = s_m[0];
#pragma unroll
    for (int w = 1; w < NW; ++w) M = fmaxf(M, s_m[w]);
    float L = 0.f;
    f32x4 o = {0.f, 0.f, 0.f, 0.f};
    const int d4 = lane < DH / 4 ? ((lane >> 1) & 7) * 24 + (lane >> 4) * 8 + (lane & 1) * 4 : 0;
#pragma unroll
    for (int w = 0; w < NW; ++w) {
      const float e = __builtin_amdgcn_exp2f(s_m[w] - M);
      L = fmaf(e, s_l[w], L);
      o += e * *reinterpret_cast<const f32x4*>(&s_buf[w * DH + d4]);
    }
    if (lane == DH / 4) o = f32x4{M, L, 1.f, 1.f};
    __builtin_amdgcn_raw_buffer_store_b128(u32x4{nz_bits(o[0]), nz_bits(o[1]), nz_bits(o[2]), nz_bits(o[3])}, rs_ws,
                                           lane < REC / 4 ? (WS_PO + (bh * NSPLIT + split) * REC + lane * 4) * 4 : OOB, 0, AUX_SC1);
    __builtin_amdgcn_raw_buffer_store_b32(1u, rs_ws, lane < NREPL ? (WS_DONE + lane * REPL + bh * NSPLIT + split) * 4 : OOB, 0, AUX_SC1);
  }
  if (wave == 1) {
    const int ticket = __builtin_amdgcn_raw_ptr_buffer_atomic_add_i32(1, rs_ws, lane == 0 ? WS_TICKET * 4 : OOB, 0, 0);
    if (lane == 0) s_ticket = ticket;
  }
  __syncthreads();

  char* const s_A = reinterpret_cast<char*>(s_kv);
  float* const s_red = reinterpret_cast<float*>(&s_kv[2048]);
  float* const s_h = reinterpret_cast<float*>(&s_kv[3072]);
  constexpr int WA = 64 * 2 + 16, GA = DM * 2 + 16, DA = FF * 2 + 16;
  const int w = wave, mg = lane >> 4, mc = lane & 15;
  const int tk = __builtin_amdgcn_readfirstlane(s_ticket);
  if (tk < W_TASKS) {
    const int ks1 = tk / W_CT, ct1 = tk - ks1 * W_CT;
    u32x4 fr[16];
#pragma unroll
    for (int i = 0; i < 16; ++i) fr[i] = u32x4{0u, 0u, 0u, 0u};
    char* const img = s_A + w * (16 * WA);
#pragma unroll 1
    for (int pass = 0; pass < 2; ++pass) {
      int real = pass;
      asm volatile("" : "+s"(real));
      if (real) {
        gate(rs_ws, &s_go, w, lane, tk, 0, NH - 1, LOAD_W, LOAD_W + 40, 1);
        {
          const u32x4* wb = reinterpret_cast<const u32x4*>(wo + (size_t)(ks1 * K3_ROWS + w * 64 + mg * 8) * DM + ct1 * 128) + mc;
          asm volatile("" : "+v"(wb));
          u32x4 raw[16];
#pragma unroll
          for (int e = 0; e < 16; ++e) raw[e] = ldw(wb + ((e >> 3) * 32 + (e & 7)) * (DM / 8));
#pragma unroll
          for (int st = 0; st < 2; ++st)
#pragma unroll
            for (int i = 0; i < 4; ++i)
#pragma unroll
              for (int c = 0; c < 4; ++c) {
                fr[st * 8 + 2 * c][i] = pk_lo(raw[st * 8 + 2 * i][c], raw[st * 8 + 2 * i + 1][c]);
                fr[st * 8 + 2 * c + 1][i] = pk_hi(raw[st * 8 + 2 * i][c], raw[st * 8 + 2 * i + 1][c]);
              }
          PIN8(fr, 0); PIN8(fr, 8);
        }
        gate(rs_ws, &s_go, w, lane, tk, ks1 * K3_ROWS / DH, (ks1 * K3_ROWS + K3_ROWS - 1) / DH, GATE_W, GATE_W + 6, 2);
        const int row = ks1 * K3_ROWS + tid, hh = row / DH, d = row - hh * DH;
        const int off = (WS_PO + hh * NSPLIT * REC + d) * 4, offm = (WS_PO + hh * NSPLIT * REC + DH) * 4;
        unsigned p[NB * 2]; u32x2 ml[NB * 2];
#pragma unroll
        for (int e = 0; e < NB * 2; ++e) { p[e] = 0u; ml[e] = u32x2{0u, 0u}; }
        unsigned pend = (1u << (NB * 2)) - 1u;
        for (unsigned spin = 0; spin < SPIN_CAP; ++spin) {
#pragma unroll
          for (int e = 0; e < NB * 2; ++e)
            if (pend & (1u << e)) {
              p[e] = __builtin_amdgcn_raw_buffer_load_b32(rs_ws, off, ((e >> 1) * NH * NSPLIT + (e & 1)) * REC * 4, AUX_SC1);
              ml[e] = __builtin_amdgcn_raw_buffer_load_b64(rs_ws, offm, ((e >> 1) * NH * NSPLIT + (e & 1)) * REC * 4, AUX_SC1);
            }
#pragma unroll
          for (int e = 0; e < NB * 2; ++e)
            if ((pend & (1u << e)) && p[e] != 0u && ml[e][0] != 0u && ml[e][1] != 0u) pend &= ~(1u << e);
          if (!__any(pend != 0u)) break;
          __builtin_amdgcn_s_sleep(1);
        }
#pragma unroll
        for (int bb = 0; bb < NB; ++bb) {
          const float m0 = __uint_as_float(ml[2 * bb][0]), m1 = __uint_as_float(ml[2 * bb + 1][0]);
          const float M = fmaxf(m0, m1), e0 = __builtin_amdgcn_exp2f(m0 - M), e1 = __builtin_amdgcn_exp2f(m1 - M);
          const float o = (__uint_as_float(p[2 * bb]) * e0 + __uint_as_float(p[2 * bb + 1]) * e1) /
                          (__uint_as_float(ml[2 * bb][1]) * e0 + __uint_as_float(ml[2 * bb + 1][1]) * e1);
          unsigned t0, t1, t2;
          split3(o, t0, t1, t2);
          *reinterpret_cast<unsigned short*>(img + (bb * 4 + 0) * WA + lane * 2) = (unsigned short)(t0 >> 16);
          *reinterpret_cast<unsigned short*>(img + (bb * 4 + 1) * WA + lane * 2) = (unsigned short)(t1 >> 16);
          *reinterpret_cast<unsigned short*>(img + (bb * 4 + 2) * WA + lane * 2) = (unsigned short)(t2 >> 16);
        }
      }
      __builtin_amdgcn_fence(__ATOMIC_RELEASE, "wavefront"); __builtin_amdgcn_wave_barrier(); __builtin_amdgcn_fence(__ATOMIC_ACQUIRE, "wavefront");
      f32x4 ac[8];
#pragma unroll
      for (int t = 0; t < 8; ++t) ac[t] = f32x4{0.f, 0.f, 0.f, 0.f};
#pragma unroll
      for (int st = 0; st < 2; ++st) {
        const u32x4 a = *reinterpret_cast<const u32x4*>(img + mc * WA + st * 64 + mg * 16);
#pragma unroll
        for (int t = 0; t < 8; ++t) ac[t] = mfma16(a, fr[st * 8 + t], ac[t]);
      }
#pragma unroll
      for (int hf = 0; hf < 2; ++hf)
        *reinterpret_cast<f32x4*>(&s_red[w * 512 + lane * 8 + hf * 4]) = f32x4{sum3(ac[hf * 4]), sum3(ac[hf * 4 + 1]), sum3(ac[hf * 4 + 2]), sum3(ac[hf * 4 + 3])};
      __syncthreads();
      if (tid < 128) {
        f32x4 s0 = f32x4{0.f, 0.f, 0.f, 0.f}, s1 = s0;
#pragma unroll
        for (int k = 0; k < NW; k += 2) { s0 += *reinterpret_cast<const f32x4*>(&s_red[k * 512 + tid * 4]); s1 += *reinterpret_cast<const f32x4*>(&s_red[(k + 1) * 512 + tid * 4]); }
        s0 += s1;
        __builtin_amdgcn_raw_buffer_store_b128(u32x4{nz_bits(s0[0]), nz_bits(s0[1]), nz_bits(s0[2]), nz_bits(s0[3])}, rs_ws,
                                               real ? (WS_SLAB + (ks1 * NB + (tid >> 5)) * DM + ct1 * 128 + (tid & 31) * 4) * 4 : OOB, 0, AUX_SC1);
      }
      if (!real) __syncthreads();
    }
  } else if (tk < W_TASKS + G_TASKS + D_TASKS) {
    const bool isG = tk < W_TASKS + G_TASKS;
    const int tt = isG ? tk - W_TASKS : tk - W_TASKS - G_TASKS;
    const int bp = tt & 1, ct = tt >> 1;
    const __amdgpu_buffer_rsrc_t rs_out = __builtin_amdgcn_make_buffer_rsrc(out, 0, NB * DM * 2, 0x00020000);
    uint2 xx = *reinterpret_cast<const uint2*>(x + (size_t)bp * 2 * DM + (tid < 2 * DM / 4 ? tid : 0) * 4);
    unsigned wr2[5];
#pragma unroll
    for (int i = 0; i < 5; ++i) wr2[i] = reinterpret_cast<const unsigned*>(wr)[lane + 64 * i];
    u32x4 fr[12];
#pragma unroll
    for (int i = 0; i < 12; ++i) fr[i] = u32x4{0u, 0u, 0u, 0u};
    if (isG && tid < DM / 8) *reinterpret_cast<u32x4*>(s_A + 3 * GA + tid * 16) = u32x4{0x3f803f80u, 0x3f803f80u, 0x3f803f80u, 0x3f803f80u};
#pragma unroll 1
    for (int pass = 0; pass < 2; ++pass) {
      int real = pass;
      asm volatile("" : "+s"(real));
      if (real) {
        gate(rs_ws, &s_go, w, lane, tk, 0, NH - 1, isG ? LOAD_G : LOAD_D, (isG ? LOAD_G : LOAD_D) + 40, 1);
        u32x2 raw[24];
        {
          const bf16* wb = (isG ? (mc < 8 ? wg : wu) + (size_t)(w * 32 + mg * 8) * FF : wd + (size_t)(w * 192 + (mc >> 3) * 32 + mg * 8) * DM) + ct * 32 + (mc & 7) * 4;
          asm volatile("" : "+v"(wb));
          const int rstr = isG ? FF : DM, o1 = isG ? 256 * FF : 64 * DM, o2 = isG ? (w < 4 ? 512 * FF : 0) : 128 * DM;
#pragma unroll
          for (int st = 0; st < 3; ++st)
#pragma unroll
            for (int jj = 0; jj < 8; ++jj)
              raw[st * 8 + jj] = ldw(reinterpret_cast<const u32x2*>(wb + (size_t)((st == 0 ? 0 : (st == 1 ? o1 : o2)) + jj * rstr)));
        }
#pragma unroll
        for (int st = 0; st < 3; ++st)
#pragma unroll
          for (int i = 0; i < 4; ++i) {
            const u32x2 r0 = raw[st * 8 + 2 * i], r1 = raw[st * 8 + 2 * i + 1];
            fr[st * 4 + 0][i] = pk_lo(r0[0], r1[0]); fr[st * 4 + 1][i] = pk_hi(r0[0], r1[0]);
            fr[st * 4 + 2][i] = pk_lo(r0[1], r1[1]); fr[st * 4 + 3][i] = pk_hi(r0[1], r1[1]);
          }
        PIN4(fr, 0); PIN4(fr, 4); PIN4(fr, 8);
        PIN4(wr2, 0); asm volatile("" : "+v"(wr2[4]), "+v"(xx.x), "+v"(xx.y));
        gate(rs_ws, &s_go, w, lane, tk, 0, NH - 1, GATE_G, 80, 2);
      }
      if (real && tid < 2 * DM / 4) {
        const int off = (WS_SLAB + bp * 2 * DM + tid * 4) * 4;
        const int poff = (WS_SLAB + bp * 2 * DM + w * 256 + lane * (NB * DM)) * 4;
        u32x4 sl[K3_KS];
#pragma unroll
        for (int ks = 0; ks < K3_KS; ++ks) sl[ks] = u32x4{0u, 0u, 0u, 0u};
        unsigned pend = (1u << K3_KS) - 1u;
        for (unsigned spin = 0; spin < SPIN_CAP; ++spin) {
          unsigned go = pend;
          if (__builtin_popcount(pend) > 2) {
            const u32x4 pr = __builtin_amdgcn_raw_buffer_load_b128(rs_ws, (lane < K3_KS && ((pend >> lane) & 1u)) ? poff : OOB, 0, AUX_SC1);
            go = (unsigned)__ballot(all_nz(pr)) & pend;
          }
#pragma unroll
          for (int ks = 0; ks < K3_KS; ++ks)
            if (go & (1u << ks)) sl[ks] = __builtin_amdgcn_raw_buffer_load_b128(rs_ws, off, ks * (NB * DM * 4), AUX_SC1);
#pragma unroll
          for (int ks = 0; ks < K3_KS; ++ks)
            if ((go & (1u << ks)) && __all(all_nz(sl[ks]))) pend &= ~(1u << ks);
          if (pend == 0u) break;
          __builtin_amdgcn_s_sleep(1);
        }
        f32x4 hv = f32x4{bf_lo(xx.x), bf_hi(xx.x), bf_lo(xx.y), bf_hi(xx.y)};
#pragma unroll
        for (int ks = 0; ks < K3_KS; ++ks) hv += __builtin_bit_cast(f32x4, sl[ks]);
        *reinterpret_cast<f32x4*>(&s_h[tid * 4]) = hv;
        if (isG) put_terms4(s_A + (tid >= DM / 4 ? 4 * GA - DM * 2 : 0) + tid * 8, GA, hv);
      }
      __syncthreads();
      if (w >= NW - 2) {
        const int rw = w - (NW - 2);
        const float piv = s_h[rw * DM];
        float hv[10], sm = 0.f, sq = 0.f;
#pragma unroll
        for (int i = 0; i < 5; ++i) {
          const float2 t2 = *reinterpret_cast<const float2*>(&s_h[rw * DM + 2 * lane + 128 * i]);
          hv[2 * i] = t2.x - piv; hv[2 * i + 1] = t2.y - piv;
          sm += hv[2 * i] + hv[2 * i + 1]; sq = fmaf(hv[2 * i], hv[2 * i], sq); sq = fmaf(hv[2 * i + 1], hv[2 * i + 1], sq);
        }
        const float dm = wave_sum(sm) * (1.0f / DM);
        const float var = fmaxf(fmaf(-dm, dm, wave_sum(sq) * (1.0f / DM)), 0.f);
        const float rstd = 1.0f / sqrtf(var + LN_EPS);
        if (lane == 0) { s_stat[rw * 2] = piv + dm; s_stat[rw * 2 + 1] = rstd; }
        if (!isG) {
          float rr = 0.f;
#pragma unroll
          for (int i = 0; i < 5; ++i) { rr = fmaf((hv[2 * i] - dm) * rstd, bf_lo(wr2[i]), rr); rr = fmaf((hv[2 * i + 1] - dm) * rstd, bf_hi(wr2[i]), rr); }
          rr = wave_sum(rr);
          if (lane == 0) s_sig[rw] = 1.0f / (1.0f + __expf(-rr));
        }
      } else if (!isG && real) {
        const int off = (WS_ACT + bp * 2 * FF + tid * 4) * 4;
        u32x4 v0 = {0u, 0u, 0u, 0u}, v1 = v0;
        unsigned pend = 3u;
        for (unsigned spin = 0; spin < SPIN_CAP; ++spin) {
          if (pend & 1u) v0 = __builtin_amdgcn_raw_buffer_load_b128(rs_ws, off, 0, AUX_SC1);
          if (pend & 2u) v1 = __builtin_amdgcn_raw_buffer_load_b128(rs_ws, off, FF * 4, AUX_SC1);
          if ((pend & 1u) && all_nz(v0)) pend &= ~1u;
          if ((pend & 2u) && all_nz(v1)) pend &= ~2u;
          if (!__any(pend != 0u)) break;
          __builtin_amdgcn_s_sleep(1);
        }
        put_terms4(s_A + tid * 8, DA, __builtin_bit_cast(f32x4, v0));
        put_terms4(s_A + 4 * DA + tid * 8, DA, __builtin_bit_cast(f32x4, v1));
      }
      if (!isG) __syncthreads();
      f32x4 ac[4];
#pragma unroll
      for (int t = 0; t < 4; ++t) ac[t] = f32x4{0.f, 0.f, 0.f, 0.f};
      {
        const char* ap = s_A + (mc & 7) * (isG ? GA : DA) + (isG ? w : w * 6 + (mc >> 3)) * 64 + mg * 16;
        const int astep = isG ? 8 * 64 : 2 * 64;
#pragma unroll
        for (int st = 0; st < 3; ++st) {
          if (st < 2 || w < 4 || !isG) {
            const u32x4 a = *reinterpret_cast<const u32x4*>(ap + st * astep);
#pragma unroll
            for (int t = 0; t < 4; ++t) ac[t] = mfma16(a, fr[st * 4 + t], ac[t]);
          }
        }
      }
      if (isG ? lane < 32 : (mc >> 3) == (mg >> 1))
        *reinterpret_cast<f32x4*>(&s_red[w * 256 + (isG ? lane * 4 : mg * 32 + (mc & 7) * 4)]) = f32x4{sum3(ac[0]), sum3(ac[1]), sum3(ac[2]), sum3(ac[3])};
      if (lane < 16) *reinterpret_cast<f32x4*>(&s_red[w * 256 + 128 + lane * 4]) = f32x4{ac[0][3], ac[1][3], ac[2][3], ac[3][3]};
      __syncthreads();
      if (tid < 64) {
        const int bb = tid >> 5, f = tid & 31;
        const int i0 = isG ? bb * 64 + f : tid, i1 = isG ? bb * 64 + 32 + f : 64 + tid;
        float s0 = 0.f, s1 = 0.f, c0 = 0.f, c1 = 0.f;
#pragma unroll
        for (int k = 0; k < NW; ++k) { s0 += s_red[k * 256 + i0]; s1 += s_red[k * 256 + i1]; c0 += s_red[k * 256 + 128 + f]; c1 += s_red[k * 256 + 160 + f]; }
        const float mean = s_stat[bb * 2], rstd = s_stat[bb * 2 + 1];
        if (isG) {
          const float gg = (s0 - mean * c0) * rstd, uu = (s1 - mean * c1) * rstd;
          const float a = gg / (1.0f + __expf(-gg)) * uu;
          __builtin_amdgcn_raw_buffer_store_b32(nz_bits(a), rs_ws, real ? (WS_ACT + (bp * 2 + bb) * FF + ct * 32 + f) * 4 : OOB, 0, AUX_SC1);
        } else {
          const float hh = s_h[bb * DM + ct * 32 + f];
          const bf16 ov = __float2bfloat16(hh + ((hh - mean) * rstd + (s0 + s1) * s_sig[bb]));
          __builtin_amdgcn_raw_buffer_store_b16(__builtin_bit_cast(unsigned short, ov), rs_out, real ? ((bp * 2 + bb) * DM + ct * 32 + f) * 2 : OOB, 0, 0);
        }
      }
      if (!real) __syncthreads();
    }
  }
}

extern "C" void kernel_launch(void* const* d_in, const int* in_sizes, int n_in, void* d_out, int out_size, void* d_ws,
                              size_t ws_size, hipStream_t stream) {
  fused_kernel<<<NWG, NW * 64, 0, stream>>>((const bf16*)d_in[0], (const bf16*)d_in[1], (const bf16*)d_in[2], (const bf16*)d_in[3],
                                            (const bf16*)d_in[4], (const bf16*)d_in[6], (const bf16*)d_in[7], (const bf16*)d_in[8],
                                            (const bf16*)d_in[9], (float*)d_ws, (bf16*)d_out);
}
```

```cpp
#define PTAU 675
#define POFF 1100
#include <hip/hip_runtime.h>
#include <hip/hip_bf16.h>
#include <stdint.h>

typedef __hip_bfloat16 bf16;
using u32x4 = __attribute__((ext_vector_type(4))) unsigned;
using u32x2 = __attribute__((ext_vector_type(2))) unsigned;
using f32x4 = __attribute__((ext_vector_type(4))) float;
using f32x2 = __attribute__((ext_vector_type(2))) float;

constexpr int NB = 4, DM = 640, NH = 32, SK = 8192, DH = 192, HD = NH * DH, FF = 1536;
constexpr int NWG = 256, NSEG = 6, NWIN = NB * NH * NSEG / NWG;
constexpr float LN_EPS = 1e-5f;
constexpr float QSCALE = 0.07216878364870322f * 1.4426950408889634f;

constexpr int WS_HP = 0;
constexpr int WS_HC = WS_HP + 2 * NWG;
constexpr int REC = DH + 4;
constexpr int WS_PO = WS_HC + 2 * NWG;
constexpr int WS_Z0 = WS_PO + NB / 2 * NH * NSEG * REC;
constexpr int K3_KS = 12, K3_ROWS = HD / K3_KS;
constexpr int WS_SLAB = WS_PO + NB * NH * NSEG * REC;
constexpr int WS_ACT = WS_SLAB + K3_KS * NB * DM;
constexpr int WS_EF = WS_ACT + NB * FF;
constexpr int ZPW = 448;
constexpr int WS_QI = WS_Z0 + NWG * ZPW;
constexpr int WS_END = WS_QI + NB * NH * DH * 2;
static_assert(WS_EF + NWG <= WS_QI && WS_Z0 % 64 == 0 && WS_SLAB % 4 == 0 && WS_PO % 4 == 0 && ZPW % 64 == 0 && NWIN == 3, "layout");
constexpr int OOB = 0x7ffffff0;

__device__ __forceinline__ float bf_lo(unsigned u) { return __uint_as_float(u << 16); }
__device__ __forceinline__ float bf_hi(unsigned u) { return __uint_as_float(u & 0xffff0000u); }
__device__ __forceinline__ f32x2 bf_pair(unsigned u) { return f32x2{bf_lo(u), bf_hi(u)}; }
__device__ __forceinline__ f32x2 fma2(float a, f32x2 b, f32x2 c) { return __builtin_elementwise_fma(f32x2{a, a}, b, c); }
__device__ __forceinline__ f32x2 fma2(f32x2 a, f32x2 b, f32x2 c) { return __builtin_elementwise_fma(a, b, c); }

template <int CTRL> __device__ __forceinline__ float dpp(float x) {
  return __builtin_bit_cast(float, __builtin_amdgcn_mov_dpp(__builtin_bit_cast(int, x), CTRL, 0xf, 0xf, true));
}
constexpr int DPP_XOR1 = 0xB1, DPP_XOR2 = 0x4E, DPP_HMIRROR = 0x141;

__device__ __forceinline__ float sum_x16_32(float x) {
  auto s = __builtin_amdgcn_permlane16_swap(__float_as_uint(x), __float_as_uint(x), false, false);
  x = __uint_as_float(s[0]) + __uint_as_float(s[1]);
  auto t = __builtin_amdgcn_permlane32_swap(__float_as_uint(x), __float_as_uint(x), false, false);
  return __uint_as_float(t[0]) + __uint_as_float(t[1]);
}
__device__ __forceinline__ float max_x16_32(float x) {
  auto s = __builtin_amdgcn_permlane16_swap(__float_as_uint(x), __float_as_uint(x), false, false);
  x = fmaxf(__uint_as_float(s[0]), __uint_as_float(s[1]));
  auto t = __builtin_amdgcn_permlane32_swap(__float_as_uint(x), __float_as_uint(x), false, false);
  return fmaxf(__uint_as_float(t[0]), __uint_as_float(t[1]));
}
__device__ __forceinline__ float tsum32(float a, float b) {
  auto t = __builtin_amdgcn_permlane32_swap(__float_as_uint(a), __float_as_uint(b), false, false);
  return __uint_as_float(t[0]) + __uint_as_float(t[1]);
}
__device__ __forceinline__ float tsum16(float a, float b) {
  auto t = __builtin_amdgcn_permlane16_swap(__float_as_uint(a), __float_as_uint(b), false, false);
  return __uint_as_float(t[0]) + __uint_as_float(t[1]);
}
constexpr int DPP_ROR8 = 0x128, DPP_BCAST = 0x150;
__device__ __forceinline__ float sum_r(float v) { v += dpp<DPP_ROR8>(v); return sum_x16_32(v); }
__device__ __forceinline__ float max_r(float v) { v = fmaxf(v, dpp<DPP_ROR8>(v)); return max_x16_32(v); }
__device__ __forceinline__ float wave_sum(float v) {
  v += dpp<DPP_XOR1>(v); v += dpp<DPP_XOR2>(v); v += dpp<DPP_HMIRROR>(v);
  return sum_r(v);
}

__device__ __forceinline__ float row16_sum(float v) {
  v += dpp<DPP_XOR1>(v); v += dpp<DPP_XOR2>(v); v += dpp<DPP_HMIRROR>(v);
  return v + dpp<DPP_ROR8>(v);
}

struct Slot { u32x4 a[3]; u32x4 b[3]; };

#define PIN8(a, o) asm volatile("" : "+v"((a)[(o) + 0]), "+v"((a)[(o) + 1]), "+v"((a)[(o) + 2]), "+v"((a)[(o) + 3]), \
                                       "+v"((a)[(o) + 4]), "+v"((a)[(o) + 5]), "+v"((a)[(o) + 6]), "+v"((a)[(o) + 7]))
#define PIN4(a, o) asm volatile("" : "+v"((a)[(o) + 0]), "+v"((a)[(o) + 1]), "+v"((a)[(o) + 2]), "+v"((a)[(o) + 3]))
#define MEMFENCE() do { asm volatile("" ::: "memory"); __builtin_amdgcn_sched_barrier(0); } while (0)
#define DRAIN() asm volatile("s_waitcnt vmcnt(0)" ::: "memory")

__device__ __forceinline__ void k_tile(const u32x4 (&k)[3], const f32x2 (&q)[12], float& gmx, float* dst, int j) {
  f32x2 s0 = {0.f, 0.f}, s1 = s0, s2 = s0;
#pragma unroll
  for (int d = 0; d < 4; ++d) {
    s0 = fma2(bf_pair(k[0][d]), q[d], s0);
    s1 = fma2(bf_pair(k[1][d]), q[4 + d], s1);
    s2 = fma2(bf_pair(k[2][d]), q[8 + d], s2);
  }
  s0 = (s0 + s1) + s2;
  float s = s0[0] + s0[1];
  s += dpp<DPP_XOR1>(s);
  s += dpp<DPP_XOR2>(s);
  s += dpp<DPP_HMIRROR>(s);
  gmx = fmaxf(gmx, s);
  asm volatile("" : "+v"(gmx));
  if (j == 0) *dst = s;
}
__device__ __forceinline__ void v_tile(u32x4 (&v)[3], float p, f32x2 (&acc)[12]) {
#pragma unroll
  for (int i = 0; i < 3; ++i) {
    asm volatile("" : "+v"(v[i]), "+v"(p));
#pragma unroll
    for (int d = 0; d < 4; ++d) acc[i * 4 + d] = fma2(p, bf_pair(v[i][d]), acc[i * 4 + d]);
    PIN4(acc, i * 4);
  }
}

#define QWAIT 32
#define YIELD 1
#define QSLEEP 4
constexpr int AUX_SC1 = 16, AUX_NT = 2;
constexpr unsigned SPIN_CAP = 1u << 17;
__device__ __forceinline__ unsigned nz_bits(float v) { const unsigned u = __float_as_uint(v); return u == 0u ? 0x80000000u : u; }
__device__ __forceinline__ void lds_put(int* p, int v) { __hip_atomic_store(p, v, __ATOMIC_RELAXED, __HIP_MEMORY_SCOPE_WORKGROUP); }
__device__ __forceinline__ int lds_get(int* p) { return __hip_atomic_load(p, __ATOMIC_RELAXED, __HIP_MEMORY_SCOPE_WORKGROUP); }
using bf16x8 = __attribute__((ext_vector_type(8))) short;
__device__ __forceinline__ unsigned pk_lo(unsigned a, unsigned b) { return __builtin_amdgcn_perm(b, a, 0x05040100u); }
__device__ __forceinline__ unsigned pk_hi(unsigned a, unsigned b) { return __builtin_amdgcn_perm(b, a, 0x07060302u); }
__device__ __forceinline__ void split3(float v, unsigned& t0, unsigned& t1, unsigned& t2) {
  t0 = __float_as_uint(v) & 0xffff0000u;
  const float r1 = v - __uint_as_float(t0);
  t1 = __float_as_uint(r1) & 0xffff0000u;
  t2 = __float_as_uint(r1 - __uint_as_float(t1));
}
__device__ __forceinline__ f32x4 mfma16(u32x4 a, u32x4 b, f32x4 c) {
  return __builtin_amdgcn_mfma_f32_16x16x32_bf16(__builtin_bit_cast(bf16x8, a), __builtin_bit_cast(bf16x8, b), c, 0, 0, 0);
}
__device__ __forceinline__ void put_terms4(char* img, int rowbytes, f32x4 v) {
  unsigned t[3][4];
#pragma unroll
  for (int e = 0; e < 4; ++e) split3(v[e], t[0][e], t[1][e], t[2][e]);
#pragma unroll
  for (int k = 0; k < 3; ++k) *reinterpret_cast<u32x2*>(img + k * rowbytes) = u32x2{pk_hi(t[k][0], t[k][1]), pk_hi(t[k][2], t[k][3])};
}
__device__ __forceinline__ bool all_nz(u32x4 v) { return v[0] != 0u && v[1] != 0u && v[2] != 0u && v[3] != 0u; }

__device__ __forceinline__ float sum3(f32x4 v) { return (v[0] + v[1]) + v[2]; }
constexpr int NW = 8, NBUF = 4, NGRP = 64, NT = NGRP / NW;
constexpr int NQP = NH * DH / 64, NQW = 5, QST = DM / 64 / NQW;
constexpr int W_CT = DM / 64, G_TASKS = 2 * (FF / 32), W_TASKS = W_CT * K3_KS, D_TASKS = 2 * (DM / 32);
static_assert(G_TASKS + W_TASKS + D_TASKS == NWG && NW == 8 && K3_ROWS == NW * 64 && NBUF == 4 && SK / 64 == 128, "shape");
#define WSTEP 0
#define KROT 0
#define PJIT 64
#define WPOS 0
#define WSLEEP 4
#define PFINE 1
#define GDELAY 0
#define DDELAY 0
#define XMAP 1
#define ROT 5
__device__ __forceinline__ unsigned fresh_tag(unsigned tags) {
  unsigned t = __builtin_amdgcn_readfirstlane(tags);
  do ++t; while (__any(tags == t));
  return t;
}
struct Turn { int seg, off, t0, cnt, len; };

__global__ void __launch_bounds__(NW * 64) fused_kernel(const bf16* __restrict__ x, const bf16* __restrict__ kc, const bf16* __restrict__ vc,
                                                        const bf16* __restrict__ wq, const bf16* __restrict__ wo, const bf16* __restrict__ wr,
                                                        const bf16* __restrict__ wg, const bf16* __restrict__ wu, const bf16* __restrict__ wd,
                                                        float* ws, bf16* __restrict__ out) {
  __shared__ __attribute__((aligned(16))) u32x4 s_kv[4096];
  __shared__ __attribute__((aligned(16))) float s_buf[NWIN * NW * DH];
  __shared__ __attribute__((aligned(16))) float s_sc[NGRP * 64];
  __shared__ __attribute__((aligned(16))) float s_q[NWIN * DH];
  __shared__ float s_m[NWIN * NW], s_l[NWIN * NW];
  __shared__ float s_sig[2];
  __shared__ int s_qok, s_pcnt, s_ecnt, s_fcnt;
  __shared__ int s_prog[NW];
  __shared__ float s_stat[4];

  const int tid = threadIdx.x;
  int lane = tid & 63, r = lane >> 3, j = lane & 7, mg = lane >> 4, mc = lane & 15;
  int wave = __builtin_amdgcn_readfirstlane(tid >> 6), bid = blockIdx.x;
  const unsigned t_start = (unsigned)__builtin_amdgcn_s_memrealtime();
  int wid = XMAP ? (bid & 7) * 32 + (bid >> 3) : bid;
  const bool prod = bid < NQP;
  const __amdgpu_buffer_rsrc_t rs_ws = __builtin_amdgcn_make_buffer_rsrc(ws, 0, WS_END * 4, 0x00020000);
  const __amdgpu_buffer_rsrc_t rs_k = __builtin_amdgcn_make_buffer_rsrc(const_cast<bf16*>(kc), 0, NB * NH * SK * DH * 2, 0x00020000);
  const __amdgpu_buffer_rsrc_t rs_v = __builtin_amdgcn_make_buffer_rsrc(const_cast<bf16*>(vc), 0, NB * NH * SK * DH * 2, 0x00020000);
  const int voff = r * (DH * 2) + j * 16;
  const int c1 = wid % 3 == 0 ? 22 : 21, c2 = wid % 3 == 1 ? 42 : 43;
  const int rot = (wid * ROT) % 168;
  auto turn = [&](int n) {
    const int g = n * 8 + wave;
    Turn t;
    t.seg = (g >= c1) + (g >= c2);
    const int cl = t.seg == 0 ? 0 : (t.seg == 1 ? c1 : c2), ch = t.seg == 0 ? c1 : (t.seg == 1 ? c2 : NGRP);
    const int lo = max(n * 8, cl), sg = wid + NWG * t.seg, row = sg / NSEG, k = sg - row * NSEG;
    t.cnt = min(n * 8 + 8, ch) - lo;
    t.t0 = (lo - cl) * 8 + g - lo + rot;
    t.len = (ch - cl) * 8;
    t.off = (row * (SK / 64) + k * 21 + (k + 2) / 3) * (64 * DH * 2);
    return t;
  };
  auto gload = [&](Slot& sl, __amdgpu_buffer_rsrc_t rs, const Turn& t, int u) {
    int ta = t.t0 + 2 * u * t.cnt, tb = ta + t.cnt;
    ta -= ta >= t.len ? t.len : 0; tb -= tb >= t.len ? t.len : 0;
#pragma unroll
    for (int i = 0; i < 3; ++i) sl.a[i] = __builtin_amdgcn_raw_buffer_load_b128(rs, voff + i * 128, t.off + ta * 3072, AUX_NT);
    MEMFENCE();
#pragma unroll
    for (int i = 0; i < 3; ++i) sl.b[i] = __builtin_amdgcn_raw_buffer_load_b128(rs, voff + i * 128, t.off + tb * 3072, AUX_NT);
    MEMFENCE();
  };
  unsigned hs_nc = 0u, hs_ep = 0u;
  auto master = [&]() {
    u32x4 hp[2], hc[2];
#pragma unroll
    for (int i = 0; i < 2; ++i) {
      hp[i] = __builtin_amdgcn_raw_buffer_load_b128(rs_ws, (WS_HP + lane * 8 + i * 4) * 4, 0, AUX_SC1);
      hc[i] = __builtin_amdgcn_raw_buffer_load_b128(rs_ws, (WS_HC + lane * 8 + i * 4) * 4, 0, AUX_SC1);
    }
    if (__all(hc[0][1] == hp[0][0] && hc[0][3] == hp[0][2] && hc[1][1] == hp[1][0] && hc[1][3] == hp[1][2]) &&
        __any(hc[0][0] != hp[0][1] || hc[0][2] != hp[0][3] || hc[1][0] != hp[1][1] || hc[1][2] != hp[1][3])) {
#pragma unroll
      for (int i = 0; i < 2; ++i)
        __builtin_amdgcn_raw_buffer_store_b128(u32x4{hp[i][0], hc[i][0], hp[i][2], hc[i][2]}, rs_ws, (WS_HP + lane * 8 + i * 4) * 4, 0, AUX_SC1);
    }
  };
  auto look_at_p = [&]() {
    if (bid == NWG - 1) master();
    const u32x2 pw = __builtin_amdgcn_raw_buffer_load_b64(rs_ws, (WS_HP + bid * 2) * 4, 0, AUX_SC1);
    const unsigned np = __builtin_amdgcn_readfirstlane(pw[0]), ec = __builtin_amdgcn_readfirstlane(pw[1]);
    if (np != hs_ep) { hs_ep = np; __builtin_amdgcn_raw_buffer_store_b64(u32x2{hs_nc, hs_ep}, rs_ws, lane == 0 ? (WS_HC + bid * 2) * 4 : OOB, 0, AUX_SC1); }
    return ec == hs_nc;
  };
  if (tid < NW) s_prog[tid] = 0;
  if (tid == 0) { s_qok = 0; s_pcnt = 0; s_ecnt = 0; s_fcnt = 0; }
  if (wave == 0) {
    __builtin_amdgcn_s_setprio(3);
    int iofs[NWIN];
#pragma unroll
    for (int s = 0; s < NWIN; ++s) iofs[s] = (WS_QI + ((wid + NWG * s) / NSEG * DH + lane) * 2) * 4;
    const int oofs = prod ? (WS_QI + (bid * 64 + lane) * 2) * 4 : OOB;
    u32x2 in[NWIN * 3];
    unsigned ot[NB];
#pragma unroll
    for (int e = 0; e < NWIN * 3; ++e) in[e] = __builtin_amdgcn_raw_buffer_load_b64(rs_ws, iofs[e / 3], (e % 3) * 512, AUX_SC1);
#pragma unroll
    for (int bb = 0; bb < NB; ++bb) ot[bb] = __builtin_amdgcn_raw_buffer_load_b64(rs_ws, oofs, bb * (HD * 8), AUX_SC1)[1];
    u32x4 xr[5], hp[2], hc[2];
#pragma unroll
    for (int i = 0; i < 5; ++i) xr[i] = reinterpret_cast<const u32x4*>(x)[lane * 5 + i];
#pragma unroll
    for (int i = 0; i < 2; ++i) {
      hp[i] = __builtin_amdgcn_raw_buffer_load_b128(rs_ws, bid == NWG - 1 ? (WS_HP + lane * 8 + i * 4) * 4 : OOB, 0, AUX_SC1);
      hc[i] = __builtin_amdgcn_raw_buffer_load_b128(rs_ws, bid == NWG - 1 ? (WS_HC + lane * 8 + i * 4) * 4 : OOB, 0, AUX_SC1);
    }
    {
      const u32x4 z4 = {0u, 0u, 0u, 0u};
#pragma unroll
      for (int i = 0; i < (ZPW + 255) / 256; ++i)
        __builtin_amdgcn_raw_buffer_store_b128(z4, rs_ws, i * 256 + lane * 4 < ZPW ? (WS_Z0 + bid * ZPW + i * 256 + lane * 4) * 4 : OOB, 0, AUX_SC1);
    }
    MEMFENCE();
    __syncthreads();
    MEMFENCE();
    if (bid == NWG - 1) {
      asm volatile("" : "+v"(hc[0]), "+v"(hc[1]));
#pragma unroll
      for (int i = 0; i < 2; ++i) {
#pragma unroll
        for (int e = 0; e < 4; e += 2) { unsigned np = hp[i][e] + 1u; if (np == hc[i][e + 1]) np += 1u; hp[i][e] = np; }
        __builtin_amdgcn_raw_buffer_store_b128(hp[i], rs_ws, (WS_HP + lane * 8 + i * 4) * 4, 0, AUX_SC1);
      }
    }
    unsigned want[NWIN * 3];
#pragma unroll
    for (int e = 0; e < NWIN * 3; ++e) want[e] = fresh_tag(in[e][1]);
    if (prod) {
      float xs = 0.f;
#pragma unroll
      for (int i = 0; i < 20; ++i) xs += bf_lo(xr[i >> 2][i & 3]) + bf_hi(xr[i >> 2][i & 3]);
      const float mean = row16_sum(xs) * (1.0f / DM);
      float xv = 0.f;
#pragma unroll
      for (int i = 0; i < 20; ++i) { const float a = bf_lo(xr[i >> 2][i & 3]) - mean, c = bf_hi(xr[i >> 2][i & 3]) - mean; xv += a * a + c * c; }
      const float rstd = QSCALE / sqrtf(row16_sum(xv) * (1.0f / DM) + LN_EPS);
#pragma unroll
      for (int bb = 0; bb < NB; ++bb) ot[bb] = fresh_tag(ot[bb]);
      for (unsigned spin = 0; spin < (SPIN_CAP << 4) && lds_get(&s_pcnt) < NQW; ++spin) __builtin_amdgcn_s_sleep(1);
      __builtin_amdgcn_fence(__ATOMIC_ACQUIRE, "workgroup");
      float pr[5];
#pragma unroll
      for (int i = 0; i < 5; ++i) {
        pr[i] = 0.f;
#pragma unroll
        for (int k = 0; k < NQW; ++k) pr[i] += s_buf[2 * k * 320 + i * 64 + lane] + s_buf[(2 * k + 1) * 320 + i * 64 + lane];
      }
#pragma unroll
      for (int bb = 0; bb < NB; ++bb) {
        const float qv = (pr[bb] - __int_as_float(__builtin_amdgcn_readlane(__float_as_int(mean), bb * 16)) * pr[4]) *
                         __int_as_float(__builtin_amdgcn_readlane(__float_as_int(rstd), bb * 16));
        __builtin_amdgcn_raw_buffer_store_b64(u32x2{__float_as_uint(qv), ot[bb]}, rs_ws, oofs, bb * (HD * 8), AUX_SC1);
      }
    } else {
      __builtin_amdgcn_s_sleep(QWAIT);
    }
    {
      const u32x2 pw = __builtin_amdgcn_raw_buffer_load_b64(rs_ws, (WS_HP + bid * 2) * 4, 0, AUX_SC1);
      DRAIN();
      hs_ep = __builtin_amdgcn_readfirstlane(pw[0]);
      hs_nc = __builtin_amdgcn_readfirstlane(pw[1]) + 1u;
      __builtin_amdgcn_raw_buffer_store_b64(u32x2{hs_nc, hs_ep}, rs_ws, lane == 0 ? (WS_HC + bid * 2) * 4 : OOB, 0, AUX_SC1);
    }
    bool all_out = false;
    for (unsigned spin = 0; spin < SPIN_CAP; ++spin) {
#pragma unroll
      for (int e = 0; e < NWIN * 3; ++e) in[e] = __builtin_amdgcn_raw_buffer_load_b64(rs_ws, iofs[e / 3], (e % 3) * 512, AUX_SC1);
      bool ok = true;
#pragma unroll
      for (int e = 0; e < NWIN * 3; ++e) ok = ok && in[e][1] == want[e];
      if (all_out || __all(ok)) break;
      if (spin >= 3) all_out = look_at_p();
      __builtin_amdgcn_s_sleep(QSLEEP);
    }
#pragma unroll
    for (int e = 0; e < NWIN * 3; ++e) s_q[e * 64 + lane] = __uint_as_float(in[e][0]);
    __builtin_amdgcn_fence(__ATOMIC_RELEASE, "workgroup");
    lds_put(&s_qok, 1);
    __builtin_amdgcn_s_setprio(0);
  } else if (prod && wave <= NQW) {
    const int k0 = (wave - 1) * (QST * 64) + (mc >> 3) * 32 + mg * 8;
    u32x4 raw[QST * 8], xa[QST];
    const bf16* wb = wq + (size_t)k0 * HD + bid * 64 + (mc & 7) * 8;
#pragma unroll
    for (int st = 0; st < QST; ++st) xa[st] = *reinterpret_cast<const u32x4*>(x + (mc & 3) * DM + k0 + st * 64);
#pragma unroll
    for (int e = 0; e < QST * 8; ++e) raw[e] = *reinterpret_cast<const u32x4*>(wb + (size_t)((e >> 3) * 64 + (e & 7)) * HD);
    MEMFENCE();
    __syncthreads();
    MEMFENCE();
    f32x4 ac[8];
#pragma unroll
    for (int t = 0; t < 8; ++t) ac[t] = f32x4{0.f, 0.f, 0.f, 0.f};
#pragma unroll
    for (int st = 0; st < QST; ++st) {
      const u32x4 a = (mc & 4) == 0 ? xa[st] : u32x4{0x3f803f80u, 0x3f803f80u, 0x3f803f80u, 0x3f803f80u};
#pragma unroll
      for (int c = 0; c < 4; ++c) {
        u32x4 f0, f1;
#pragma unroll
        for (int i = 0; i < 4; ++i) { f0[i] = pk_lo(raw[st * 8 + 2 * i][c], raw[st * 8 + 2 * i + 1][c]); f1[i] = pk_hi(raw[st * 8 + 2 * i][c], raw[st * 8 + 2 * i + 1][c]); }
        ac[2 * c] = mfma16(a, f0, ac[2 * c]);
        ac[2 * c + 1] = mfma16(a, f1, ac[2 * c + 1]);
      }
    }
    if ((mg >> 1) == (mc >> 3)) {
      float* const dst = &s_buf[((wave - 1) * 2 + (mg >> 1)) * 320 + (mc & 7) * 8];
#pragma unroll
      for (int bb = 0; bb < NB; ++bb)
        if (bb == 0 || (mg & 1) == 0) {
          *reinterpret_cast<f32x4*>(dst + ((mg & 1) * 4 + bb) * 64) = f32x4{ac[0][bb], ac[1][bb], ac[2][bb], ac[3][bb]};
          *reinterpret_cast<f32x4*>(dst + ((mg & 1) * 4 + bb) * 64 + 4) = f32x4{ac[4][bb], ac[5][bb], ac[6][bb], ac[7][bb]};
        }
    }
    __builtin_amdgcn_fence(__ATOMIC_RELEASE, "workgroup");
    if (lane == 0) __hip_atomic_fetch_add(&s_pcnt, 1, __ATOMIC_RELAXED, __HIP_MEMORY_SCOPE_WORKGROUP);
  } else {
    __syncthreads();
  }
  MEMFENCE();
  char* const s_A = reinterpret_cast<char*>(s_kv);
  float* const s_red = reinterpret_cast<float*>(&s_kv[2048]);
  float* const s_h = reinterpret_cast<float*>(&s_kv[3072]);
  constexpr int WA = 64 * 2 + 16, GA = DM * 2 + 16, DA = FF * 2 + 16;
#pragma unroll 1
  for (int pass = (13 - wave) >> 3; pass < 2; ++pass) {
  int real = pass;
  asm volatile("" : "+s"(real));
  asm volatile("" : "+s"(wave), "+s"(bid), "+s"(wid), "+v"(lane));
  r = lane >> 3; j = lane & 7;
  u32x2 raw[24];
#pragma unroll
  for (int e = 0; e < 24; ++e) raw[e] = u32x2{0u, 0u};
  unsigned ef = 0u;
  if (real) {
  auto turn_prio = [&](int tn) {
    if (PTAU) {
      const unsigned due = t_start + (unsigned)(tn * PTAU + POFF + (PFINE ? wave * (PTAU / 8) : (wave >> 2) * (PTAU / 2)) + ((bid >> 3) * 13 & 31) * (PJIT / 32));
      for (unsigned spin = 0; spin < 4096 && (int)((unsigned)__builtin_amdgcn_s_memrealtime() - due) < 0; ++spin) __builtin_amdgcn_s_sleep(4);
    }
    if (lane == 0) lds_put(&s_prog[wave], tn);
    int pp = __builtin_amdgcn_readfirstlane(lds_get(&s_prog[wave ^ (NW / 2)]));
    if (WSTEP) {
      for (unsigned spin = 0; spin < 4096 && __any(tn > lds_get(&s_prog[j]) + WSTEP); ++spin) __builtin_amdgcn_s_sleep(4);
      pp = __builtin_amdgcn_readfirstlane(lds_get(&s_prog[wave ^ (NW / 2)]));
    }
    for (unsigned spin = 0; spin < 4096 && tn > pp + YIELD; ++spin) { __builtin_amdgcn_s_sleep(4); pp = __builtin_amdgcn_readfirstlane(lds_get(&s_prog[wave ^ (NW / 2)])); }
    if (tn > pp) __builtin_amdgcn_s_setprio(0); else __builtin_amdgcn_s_setprio(1);
  };
  Slot buf[NBUF];
  f32x2 q[12];
  float gmx = -INFINITY;
  float* const sc = &s_sc[wave * (NT * 64) + r * 8];
  auto load_q = [&](int s) {
#pragma unroll
    for (int i = 0; i < 6; ++i) {
      const f32x4 a = *reinterpret_cast<const f32x4*>(&s_q[s * DH + (i >> 1) * 64 + j * 8 + (i & 1) * 4]);
      q[i * 2] = f32x2{a[0], a[1]}; q[i * 2 + 1] = f32x2{a[2], a[3]};
    }
  };
  const int n0 = KROT ? (c2 - wave + 7) >> 3 : 0;
  Turn tnx = turn((n0 + 2) & 7);
  {
    Slot xt[NBUF];
    const Turn t0 = turn(n0), t1 = turn(n0 + 1);
    __builtin_amdgcn_s_setprio(2);
#pragma unroll
    for (int u = 0; u < NBUF; ++u) gload(buf[u], rs_k, t0, u);
    __builtin_amdgcn_s_setprio(1);
#pragma unroll
    for (int u = 0; u < NBUF; ++u) gload(xt[u], rs_k, t1, u);
    __builtin_amdgcn_s_setprio(0);
    if (lane == 0) lds_put(&s_prog[wave], 1);
    for (unsigned spin = 0; spin < (SPIN_CAP << 4) && lds_get(&s_qok) == 0; ++spin) __builtin_amdgcn_s_sleep(2);
    __builtin_amdgcn_fence(__ATOMIC_ACQUIRE, "workgroup");
    load_q(KROT ? NWIN - 1 : 0);
    MEMFENCE();
#pragma unroll
    for (int u = 0; u < NBUF; ++u) {
      k_tile(buf[u].a, q, gmx, sc + n0 * 64 + 2 * u, j);
      k_tile(buf[u].b, q, gmx, sc + n0 * 64 + 2 * u + 1, j);
      MEMFENCE();
      gload(buf[u], rs_k, tnx, u);
    }
#pragma unroll
    for (int u = 0; u < NBUF; ++u) {
      k_tile(xt[u].a, q, gmx, sc + n0 * 64 + 64 + 2 * u, j);
      k_tile(xt[u].b, q, gmx, sc + n0 * 64 + 64 + 2 * u + 1, j);
      MEMFENCE();
    }
  }
  int seg = KROT ? NWIN - 1 : 0;
  for (int n = 2; n < NT; ++n) {
    turn_prio(n);
    if (tnx.seg != seg) {
      const float gm = max_r(gmx);
      if (lane == 0) s_m[seg * NW + wave] = gm;
      gmx = -INFINITY;
      seg = tnx.seg;
      load_q(seg);
    }
    const __amdgpu_buffer_rsrc_t rs = n == NT - 1 ? rs_v : rs_k;
    tnx = turn(n == NT - 1 ? 0 : (n0 + n + 1) & 7);
    float* const scn = sc + ((n0 + n) & 7) * 64;
    MEMFENCE();
#pragma unroll
    for (int u = 0; u < NBUF; ++u) {
      k_tile(buf[u].a, q, gmx, scn + 2 * u, j);
      k_tile(buf[u].b, q, gmx, scn + 2 * u + 1, j);
      MEMFENCE();
      gload(buf[u], rs, tnx, u);
    }
    if (wave == 0 && (n == 2 || (bid == NWG - 1 && n >= 4))) look_at_p();
  }
  {
    const float gm = max_r(gmx);
    if (lane == 0) s_m[seg * NW + wave] = gm;
  }
  float l = 0.f;
  f32x2 acc[12];
#pragma unroll
  for (int i = 0; i < 12; ++i) acc[i] = f32x2{0.f, 0.f};
  float p[8];
  __builtin_amdgcn_fence(__ATOMIC_RELEASE, "wavefront"); __builtin_amdgcn_wave_barrier(); __builtin_amdgcn_fence(__ATOMIC_ACQUIRE, "wavefront");
  auto v_begin = [&](int n, int s) {
    const float m = s_m[s * NW + wave];
    const f32x4 sa = *reinterpret_cast<const f32x4*>(sc + n * 64), sb = *reinterpret_cast<const f32x4*>(sc + n * 64 + 4);
#pragma unroll
    for (int t = 0; t < 4; ++t) { p[t] = __builtin_amdgcn_exp2f(sa[t] - m); p[4 + t] = __builtin_amdgcn_exp2f(sb[t] - m); }
    l += ((p[0] + p[1]) + (p[2] + p[3])) + ((p[4] + p[5]) + (p[6] + p[7]));
    MEMFENCE();
  };
  auto v_flush = [&](int s) {
    const float lw = sum_r(l);
    float t12[12], t6[6];
#pragma unroll
    for (int e = 0; e < 12; ++e) t12[e] = tsum32(acc[e >> 1][e & 1], acc[6 + (e >> 1)][e & 1]);
#pragma unroll
    for (int e = 0; e < 6; ++e) { t6[e] = tsum16(t12[e], t12[e + 6]); t6[e] += dpp<DPP_ROR8>(t6[e]); }
    if ((lane & 8) == 0) {
#pragma unroll
      for (int e = 0; e < 3; ++e) *reinterpret_cast<f32x2*>(&s_buf[(s * NW + wave) * DH + j * 24 + (lane >> 4) * 6 + e * 2]) = f32x2{t6[2 * e], t6[2 * e + 1]};
    }
    if (lane == 0) s_l[s * NW + wave] = lw;
  };
  auto publish = [&](int s) {
    float M = s_m[s * NW];
#pragma unroll
    for (int w = 1; w < NW; ++w) M = fmaxf(M, s_m[s * NW + w]);
    float L = 0.f;
    f32x4 o = {0.f, 0.f, 0.f, 0.f};
    const int d4 = lane < DH / 4 ? ((lane >> 1) & 7) * 24 + (lane >> 4) * 8 + (lane & 1) * 4 : 0;
#pragma unroll
    for (int w = 0; w < NW; ++w) {
      const float e = __builtin_amdgcn_exp2f(s_m[s * NW + w] - M);
      L = fmaf(e, s_l[s * NW + w], L);
      o += e * *reinterpret_cast<const f32x4*>(&s_buf[(s * NW + w) * DH + d4]);
    }
    if (lane == DH / 4) o = f32x4{M, L, 1.f, 1.f};
    __builtin_amdgcn_raw_buffer_store_b128(u32x4{nz_bits(o[0]), nz_bits(o[1]), nz_bits(o[2]), nz_bits(o[3])}, rs_ws,
                                           lane < REC / 4 ? (WS_PO + (wid + NWG * s) * REC + lane * 4) * 4 : OOB, 0, AUX_SC1);
  };
  for (int n = 0; n < NT - 1; ++n) {
    turn_prio(NT + n);
    seg = tnx.seg;
    v_begin(n, seg);
    tnx = turn(n + 1);
    MEMFENCE();
#pragma unroll
    for (int u = 0; u < NBUF; ++u) {
      v_tile(buf[u].a, p[2 * u], acc);
      v_tile(buf[u].b, p[2 * u + 1], acc);
      MEMFENCE();
      gload(buf[u], rs_v, tnx, u);
    }
    if (wave == 0 && n == 1) {
      for (unsigned spin = 0; spin < SPIN_CAP && !look_at_p(); ++spin) __builtin_amdgcn_s_sleep(4);
    }
    if (tnx.seg != seg) {
      v_flush(seg);
      l = 0.f;
#pragma unroll
      for (int i = 0; i < 12; ++i) acc[i] = f32x2{0.f, 0.f};
      if (seg == 1) {
        __builtin_amdgcn_fence(__ATOMIC_RELEASE, "workgroup");
        int c = 0;
        if (lane == 0) c = __hip_atomic_fetch_add(&s_ecnt, 1, __ATOMIC_RELAXED, __HIP_MEMORY_SCOPE_WORKGROUP);
        if (__builtin_amdgcn_readfirstlane(c) == NW - 1) {
          __builtin_amdgcn_fence(__ATOMIC_ACQUIRE, "workgroup");
#pragma unroll 1
          for (int s = 0; s < 2; ++s) publish(s);
          DRAIN();
          __builtin_amdgcn_raw_buffer_store_b32(1u, rs_ws, lane == 0 ? (WS_EF + wid) * 4 : OOB, 0, AUX_SC1);
        }
      }
      MEMFENCE();
    }
  }
  {
    asm volatile("" : "+s"(bid), "+v"(lane));
    mg = lane >> 4; mc = lane & 15;
    const int xcd = bid & 7, ri = bid >> 3;
    const bool isW = ri < 15, isG = !isW && ri < 27;
    const int tt = isW ? xcd * 15 + ri : (isG ? xcd * 12 + ri - 15 : xcd * 5 + ri - 27);
    const int bp = tt & 1, ct = tt >> 1;
    const int ks1 = tt / W_CT, ct1 = tt - ks1 * W_CT;
    const __amdgpu_buffer_rsrc_t rs_wo = __builtin_amdgcn_make_buffer_rsrc(const_cast<bf16*>(wo), 0, isW ? HD * DM * 2 : 0, 0x00020000);
    const int wofs = ((ks1 * K3_ROWS + wave * 64 + mg * 8) * DM + ct1 * 64 + mc * 4) * 2;
    v_begin(NT - 1, NWIN - 1);
#pragma unroll
    for (int u = 0; u < NBUF; ++u) {
      v_tile(buf[u].a, p[2 * u], acc);
      v_tile(buf[u].b, p[2 * u + 1], acc);
      MEMFENCE();
      if (u == WPOS) {
#pragma unroll
        for (int e = 0; e < 16; ++e) raw[e] = __builtin_amdgcn_raw_buffer_load_b64(rs_wo, wofs, ((e >> 3) * 32 + (e & 7)) * (DM * 2), 0);
        ef = __builtin_amdgcn_raw_buffer_load_b32(rs_ws, (isW && mg < 2 && mc < NSEG) ? (WS_EF + (((mg * NH + (ks1 * K3_ROWS + wave * 64) / DH) * NSEG + mc) & (NWG - 1))) * 4 : OOB, 0, AUX_SC1);
      }
      MEMFENCE();
    }
  }
  __builtin_amdgcn_s_setprio(0);
  v_flush(NWIN - 1);
  {
    __builtin_amdgcn_fence(__ATOMIC_RELEASE, "workgroup");
    int c = 0;
    if (lane == 0) c = __hip_atomic_fetch_add(&s_fcnt, 1, __ATOMIC_RELAXED, __HIP_MEMORY_SCOPE_WORKGROUP);
    if (__builtin_amdgcn_readfirstlane(c) == NW - 1) {
      __builtin_amdgcn_fence(__ATOMIC_ACQUIRE, "workgroup");
      publish(NWIN - 1);
    }
  }

  }
  {
    asm volatile("" : "+s"(wave), "+s"(bid), "+v"(lane));
    r = lane >> 3; j = lane & 7; mg = lane >> 4; mc = lane & 15;
    const int w = wave - (1 - real) * (NW - 2) * (NW - 1 - wave), td = w * 64 + lane;
    const int xcd = bid & 7, ri = bid >> 3;
    const bool isW = ri < 15, isG = !isW && ri < 27;
    const int tt = isW ? xcd * 15 + ri : (isG ? xcd * 12 + ri - 15 : xcd * 5 + ri - 27);
    const int bp = tt & 1, ct = tt >> 1;
    const int ks1 = tt / W_CT, ct1 = tt - ks1 * W_CT;
    const __amdgpu_buffer_rsrc_t rs_out = __builtin_amdgcn_make_buffer_rsrc(out, 0, NB * DM * 2, 0x00020000);
    uint2 xx = {0u, 0u};
    unsigned wr2[5] = {0u, 0u, 0u, 0u, 0u};
    u32x4 fr[12];
#pragma unroll
    for (int i = 0; i < 12; ++i) fr[i] = u32x4{0u, 0u, 0u, 0u};
    auto frags = [&](int nst) {
      PIN8(raw, 0); PIN8(raw, 8); PIN8(raw, 16);
#pragma unroll
      for (int st = 0; st < nst; ++st)
#pragma unroll
        for (int i = 0; i < 4; ++i) {
          const u32x2 r0 = raw[st * 8 + 2 * i], r1 = raw[st * 8 + 2 * i + 1];
          fr[st * 4 + 0][i] = pk_lo(r0[0], r1[0]); fr[st * 4 + 1][i] = pk_hi(r0[0], r1[0]);
          fr[st * 4 + 2][i] = pk_lo(r0[1], r1[1]); fr[st * 4 + 3][i] = pk_hi(r0[1], r1[1]);
        }
      PIN4(fr, 0); PIN4(fr, 4);
      if (nst == 3) PIN4(fr, 8);
    };
    if (isW) {
      char* const img = s_A + w * (16 * WA);
      const int hh = (ks1 * K3_ROWS + w * 64) / DH;
      {
        const int rec = WS_PO + (mg * NH + hh) * NSEG * REC;
        const int offp = (rec + ks1 * K3_ROWS + w * 64 - hh * DH + mc * 4) * 4, offm = (rec + mc * REC + DH) * 4;
        const int offe = (mg < 2 && mc < NSEG) ? (WS_EF + (((mg * NH + hh) * NSEG + mc) & (NWG - 1))) * 4 : OOB;
        u32x4 pv[NSEG];
        u32x2 ml = {0u, 0u};
#pragma unroll
        for (int k = 0; k < NSEG; ++k) pv[k] = u32x4{0u, 0u, 0u, 0u};
        unsigned pend = real ? (1u << NSEG) - 1u + (mc < NSEG ? 1u << NSEG : 0u) : 0u;
        for (unsigned spin = 0; spin < SPIN_CAP; ++spin) {
          const bool early = __all(offe == OOB || ef != 0u);
          const unsigned go = (early || mg >= 2) ? pend : 0u;
          if (!early && real) ef = __builtin_amdgcn_raw_buffer_load_b32(rs_ws, offe, 0, AUX_SC1);
          if (go & (1u << NSEG)) ml = __builtin_amdgcn_raw_buffer_load_b64(rs_ws, offm, 0, AUX_SC1);
#pragma unroll
          for (int k = 0; k < NSEG; ++k)
            if (go & (1u << k)) pv[k] = __builtin_amdgcn_raw_buffer_load_b128(rs_ws, offp, k * REC * 4, AUX_SC1);
          if (spin == 0) frags(2);
          if (ml[0] != 0u && ml[1] != 0u) pend &= ~(1u << NSEG);
#pragma unroll
          for (int k = 0; k < NSEG; ++k)
            if (all_nz(pv[k])) pend &= ~(1u << k);
          if (!__any(pend != 0u)) break;
          if (spin == 0) {
            for (unsigned sp2 = 0; sp2 < (SPIN_CAP << 4) && lds_get(&s_fcnt) < NW; ++sp2) __builtin_amdgcn_s_sleep(1);
          } else if (spin < 6) __builtin_amdgcn_s_sleep(1); else __builtin_amdgcn_s_sleep(WSLEEP);
        }
        const float mm = mc < NSEG ? __uint_as_float(ml[0]) : -INFINITY;
        float M = fmaxf(mm, dpp<DPP_XOR1>(mm)); M = fmaxf(M, dpp<DPP_XOR2>(M)); M = fmaxf(M, dpp<DPP_HMIRROR>(M));
        const float e = __builtin_amdgcn_exp2f(mm - M);
        float L = e * __uint_as_float(ml[1]);
        L += dpp<DPP_XOR1>(L); L += dpp<DPP_XOR2>(L); L += dpp<DPP_HMIRROR>(L);
        const float wt = e / L;
        f32x4 o = dpp<DPP_BCAST + 0>(wt) * __builtin_bit_cast(f32x4, pv[0]);
        o += dpp<DPP_BCAST + 1>(wt) * __builtin_bit_cast(f32x4, pv[1]);
        o += dpp<DPP_BCAST + 2>(wt) * __builtin_bit_cast(f32x4, pv[2]);
        o += dpp<DPP_BCAST + 3>(wt) * __builtin_bit_cast(f32x4, pv[3]);
        o += dpp<DPP_BCAST + 4>(wt) * __builtin_bit_cast(f32x4, pv[4]);
        o += dpp<DPP_BCAST + 5>(wt) * __builtin_bit_cast(f32x4, pv[5]);
        put_terms4(img + mg * 4 * WA + mc * 8, WA, o);
      }
      __builtin_amdgcn_fence(__ATOMIC_RELEASE, "wavefront"); __builtin_amdgcn_wave_barrier(); __builtin_amdgcn_fence(__ATOMIC_ACQUIRE, "wavefront");
      f32x4 ac[4];
#pragma unroll
      for (int t = 0; t < 4; ++t) ac[t] = f32x4{0.f, 0.f, 0.f, 0.f};
#pragma unroll
      for (int st = 0; st < 2; ++st) {
        const u32x4 a = *reinterpret_cast<const u32x4*>(img + mc * WA + st * 64 + mg * 16);
#pragma unroll
        for (int t = 0; t < 4; ++t) ac[t] = mfma16(a, fr[st * 4 + t], ac[t]);
      }
      *reinterpret_cast<f32x4*>(&s_red[w * 256 + lane * 4]) = f32x4{sum3(ac[0]), sum3(ac[1]), sum3(ac[2]), sum3(ac[3])};
      if (real) __syncthreads();
      if (w == 0) {
        f32x4 s0 = f32x4{0.f, 0.f, 0.f, 0.f}, s1 = s0;
#pragma unroll
        for (int k = 0; k < NW; k += 2) { s0 += *reinterpret_cast<const f32x4*>(&s_red[k * 256 + lane * 4]); s1 += *reinterpret_cast<const f32x4*>(&s_red[(k + 1) * 256 + lane * 4]); }
        s0 += s1;
        __builtin_amdgcn_raw_buffer_store_b128(u32x4{nz_bits(s0[0]), nz_bits(s0[1]), nz_bits(s0[2]), nz_bits(s0[3])}, rs_ws,
                                               real ? (WS_SLAB + (ks1 * NB + mg) * DM + ct1 * 64 + mc * 4) * 4 : OOB, 0, AUX_SC1);
      }
    } else {
      if (real) {
        for (unsigned spin = 0; spin < (SPIN_CAP << 4) && lds_get(&s_fcnt) < NW; ++spin) __builtin_amdgcn_s_sleep(1);
        if (isG) { if (GDELAY) __builtin_amdgcn_s_sleep(GDELAY); } else { if (DDELAY) __builtin_amdgcn_s_sleep(DDELAY); }
        const bf16* wb = (isG ? (mc < 8 ? wg : wu) + (size_t)(w * 32 + mg * 8) * FF : wd + (size_t)(w * 192 + (mc >> 3) * 32 + mg * 8) * DM) + ct * 32 + (mc & 7) * 4;
        const int rstr = isG ? FF : DM, o1 = isG ? 256 * FF : 64 * DM, o2 = (isG && w >= 4) ? 0 : 2 * o1;
#pragma unroll
        for (int e = 0; e < 24; ++e) raw[e] = *reinterpret_cast<const u32x2*>(wb + (size_t)((e < 8 ? 0 : (e < 16 ? o1 : o2)) + (e & 7) * rstr));
        xx = *reinterpret_cast<const uint2*>(x + (size_t)bp * 2 * DM + (td < 2 * DM / 4 ? td : 0) * 4);
#pragma unroll
        for (int i = 0; i < 5; ++i) wr2[i] = reinterpret_cast<const unsigned*>(wr)[lane + 64 * i];
        if (isG && td < DM / 8) *reinterpret_cast<u32x4*>(s_A + 3 * GA + td * 16) = u32x4{0x3f803f80u, 0x3f803f80u, 0x3f803f80u, 0x3f803f80u};
      }
      frags(3);
      PIN4(wr2, 0); asm volatile("" : "+v"(wr2[4]), "+v"(xx.x), "+v"(xx.y));
      if (real && td < 2 * DM / 4) {
        const int off = (WS_SLAB + bp * 2 * DM + td * 4) * 4;
        const int poff = (WS_SLAB + bp * 2 * DM + w * 256 + lane * (NB * DM)) * 4;
        u32x4 sl[K3_KS];
#pragma unroll
        for (int ks = 0; ks < K3_KS; ++ks) sl[ks] = u32x4{0u, 0u, 0u, 0u};
        unsigned pend = (1u << K3_KS) - 1u;
        for (unsigned spin = 0; spin < SPIN_CAP; ++spin) {
          unsigned go = pend;
          if (__builtin_popcount(pend) > 2) {
            const u32x4 pr = __builtin_amdgcn_raw_buffer_load_b128(rs_ws, (lane < K3_KS && ((pend >> lane) & 1u)) ? poff : OOB, 0, AUX_SC1);
            go = (unsigned)__ballot(all_nz(pr)) & pend;
          }
#pragma unroll
          for (int ks = 0; ks < K3_KS; ++ks)
            if (go & (1u << ks)) sl[ks] = __builtin_amdgcn_raw_buffer_load_b128(rs_ws, off, ks * (NB * DM * 4), AUX_SC1);
#pragma unroll
          for (int ks = 0; ks < K3_KS; ++ks)
            if ((go & (1u << ks)) && __all(all_nz(sl[ks]))) pend &= ~(1u << ks);
          if (pend == 0u) break;
          __builtin_amdgcn_s_sleep(1);
        }
        f32x4 hv = f32x4{bf_lo(xx.x), bf_hi(xx.x), bf_lo(xx.y), bf_hi(xx.y)};
#pragma unroll
        for (int ks = 0; ks < K3_KS; ++ks) hv += __builtin_bit_cast(f32x4, sl[ks]);
        *reinterpret_cast<f32x4*>(&s_h[td * 4]) = hv;
        if (isG) put_terms4(s_A + (td >= DM / 4 ? 4 * GA - DM * 2 : 0) + td * 8, GA, hv);
      }
      if (real) __syncthreads();
      if (w >= NW - 2) {
        const int rw = w - (NW - 2);
        const float piv = s_h[rw * DM];
        float hv[10], sm = 0.f, sq = 0.f;
#pragma unroll
        for (int i = 0; i < 5; ++i) {
          const float2 t2 = *reinterpret_cast<const float2*>(&s_h[rw * DM + 2 * lane + 128 * i]);
          hv[2 * i] = t2.x - piv; hv[2 * i + 1] = t2.y - piv;
          sm += hv[2 * i] + hv[2 * i + 1]; sq = fmaf(hv[2 * i], hv[2 * i], sq); sq = fmaf(hv[2 * i + 1], hv[2 * i + 1], sq);
        }
        const float dm = wave_sum(sm) * (1.0f / DM);
        const float var = fmaxf(fmaf(-dm, dm, wave_sum(sq) * (1.0f / DM)), 0.f);
        const float rstd = 1.0f / sqrtf(var + LN_EPS);
        if (lane == 0) { s_stat[rw * 2] = piv + dm; s_stat[rw * 2 + 1] = rstd; }
        if (!isG) {
          float rr = 0.f;
#pragma unroll
          for (int i = 0; i < 5; ++i) { rr = fmaf((hv[2 * i] - dm) * rstd, bf_lo(wr2[i]), rr); rr = fmaf((hv[2 * i + 1] - dm) * rstd, bf_hi(wr2[i]), rr); }
          rr = wave_sum(rr);
          if (lane == 0) s_sig[rw] = 1.0f / (1.0f + __expf(-rr));
        }
      } else if (!isG && real) {
        const int off = (WS_ACT + bp * 2 * FF + td * 4) * 4;
        u32x4 v0 = {0u, 0u, 0u, 0u}, v1 = v0;
        unsigned pend = 3u;
        for (unsigned spin = 0; spin < SPIN_CAP; ++spin) {
          if (pend & 1u) v0 = __builtin_amdgcn_raw_buffer_load_b128(rs_ws, off, 0, AUX_SC1);
          if (pend & 2u) v1 = __builtin_amdgcn_raw_buffer_load_b128(rs_ws, off, FF * 4, AUX_SC1);
          if ((pend & 1u) && all_nz(v0)) pend &= ~1u;
          if ((pend & 2u) && all_nz(v1)) pend &= ~2u;
          if (!__any(pend != 0u)) break;
          __builtin_amdgcn_s_sleep(1);
        }
        put_terms4(s_A + td * 8, DA, __builtin_bit_cast(f32x4, v0));
        put_terms4(s_A + 4 * DA + td * 8, DA, __builtin_bit_cast(f32x4, v1));
      }
      if (!isG && real) __syncthreads();
      f32x4 ac[4];
#pragma unroll
      for (int t = 0; t < 4; ++t) ac[t] = f32x4{0.f, 0.f, 0.f, 0.f};
      {
        const char* ap = s_A + (mc & 7) * (isG ? GA : DA) + (isG ? w : w * 6 + (mc >> 3)) * 64 + mg * 16;
        const int astep = isG ? 8 * 64 : 2 * 64;
#pragma unroll
        for (int st = 0; st < 3; ++st) {
          if (st < 2 || w < 4 || !isG) {
            const u32x4 a = *reinterpret_cast<const u32x4*>(ap + st * astep);
#pragma unroll
            for (int t = 0; t < 4; ++t) ac[t] = mfma16(a, fr[st * 4 + t], ac[t]);
          }
        }
      }
      if (isG ? lane < 32 : (mc >> 3) == (mg >> 1))
        *reinterpret_cast<f32x4*>(&s_red[w * 256 + (isG ? lane * 4 : mg * 32 + (mc & 7) * 4)]) = f32x4{sum3(ac[0]), sum3(ac[1]), sum3(ac[2]), sum3(ac[3])};
      if (lane < 16) *reinterpret_cast<f32x4*>(&s_red[w * 256 + 128 + lane * 4]) = f32x4{ac[0][3], ac[1][3], ac[2][3], ac[3][3]};
      if (real) __syncthreads();
      if (w == 0) {
        const int bb = lane >> 5, f = lane & 31;
        const int i0 = isG ? bb * 64 + f : lane, i1 = isG ? bb * 64 + 32 + f : 64 + lane;
        float s0 = 0.f, s1 = 0.f, c0 = 0.f, c1 = 0.f;
#pragma unroll
        for (int k = 0; k < NW; ++k) { s0 += s_red[k * 256 + i0]; s1 += s_red[k * 256 + i1]; c0 += s_red[k * 256 + 128 + f]; c1 += s_red[k * 256 + 160 + f]; }
        const float mean = s_stat[bb * 2], rstd = s_stat[bb * 2 + 1];
        if (isG) {
          const float gg = (s0 - mean * c0) * rstd, uu = (s1 - mean * c1) * rstd;
          const float a = gg / (1.0f + __expf(-gg)) * uu;
          __builtin_amdgcn_raw_buffer_store_b32(nz_bits(a), rs_ws, real ? (WS_ACT + (bp * 2 + bb) * FF + ct * 32 + f) * 4 : OOB, 0, AUX_SC1);
        } else {
          const float hh = s_h[bb * DM + ct * 32 + f];
          const bf16 ov = __float2bfloat16(hh + ((hh - mean) * rstd + (s0 + s1) * s_sig[bb]));
          __builtin_amdgcn_raw_buffer_store_b16(__builtin_bit_cast(unsigned short, ov), rs_out, real ? ((bp * 2 + bb) * DM + ct * 32 + f) * 2 : OOB, 0, 0);
        }
      }
    }
  }
  }
}

extern "C" void kernel_launch(void* const* d_in, const int* in_sizes, int n_in, void* d_out, int out_size, void* d_ws,
                              size_t ws_size, hipStream_t stream) {
  fused_kernel<<<NWG, NW * 64, 0, stream>>>((const bf16*)d_in[0], (const bf16*)d_in[1], (const bf16*)d_in[2], (const bf16*)d_in[3],
                                            (const bf16*)d_in[4], (const bf16*)d_in[6], (const bf16*)d_in[7], (const bf16*)d_in[8],
                                            (const bf16*)d_in[9], (float*)d_ws, (bf16*)d_out);
}
```

```cpp
#define PTAU 685
#define POFF 720
#define ADAPT 60
#define PTOL 350
#include <hip/hip_runtime.h>
#include <hip/hip_bf16.h>
#include <stdint.h>

typedef __hip_bfloat16 bf16;
using u32x4 = __attribute__((ext_vector_type(4))) unsigned;
using u32x2 = __attribute__((ext_vector_type(2))) unsigned;
using f32x4 = __attribute__((ext_vector_type(4))) float;
using f32x2 = __attribute__((ext_vector_type(2))) float;

constexpr int NB = 4, DM = 640, NH = 32, SK = 8192, DH = 192, HD = NH * DH, FF = 1536;
constexpr int NWG = 256, NSEG = 6, NWIN = NB * NH * NSEG / NWG;
constexpr float LN_EPS = 1e-5f;
constexpr float QSCALE = 0.07216878364870322f * 1.4426950408889634f;

#define EFREC 1
#define CSTRIDE 64
constexpr int WS_HP = 0;
constexpr int REC = DH + 4;
constexpr int WS_PO = WS_HP + 4 * NWG;
constexpr int WS_Z0 = WS_PO + NB / 2 * NH * NSEG * REC;
constexpr int K3_KS = 12, K3_ROWS = HD / K3_KS;
constexpr int WS_SLAB = WS_PO + NB * NH * NSEG * REC;
constexpr int WS_ACT = WS_SLAB + K3_KS * NB * DM;
constexpr int WS_EF = WS_ACT + NB * FF;
constexpr int ZPW = 448;
constexpr int WS_QI = WS_Z0 + NWG * ZPW;
constexpr int WS_REP = WS_QI + NB * NH * DH * 2;
constexpr int RSTR = 64;
constexpr int WS_HC = WS_REP + 256 * RSTR, CSTR = CSTRIDE;
constexpr int WS_END = WS_HC + NWG * CSTR;
static_assert(WS_EF + NWG <= WS_QI && WS_Z0 % 64 == 0 && WS_SLAB % 4 == 0 && WS_PO % 4 == 0 && ZPW % 64 == 0 && NWIN == 3, "layout");
#define EFOFS(i) ((WS_PO + (2 * NWG + (i)) * REC + DH + 2) * 4)
constexpr int OOB = 0x7ffffff0;

__device__ __forceinline__ float bf_lo(unsigned u) { return __uint_as_float(u << 16); }
__device__ __forceinline__ float bf_hi(unsigned u) { return __uint_as_float(u & 0xffff0000u); }
__device__ __forceinline__ f32x2 bf_pair(unsigned u) { return f32x2{bf_lo(u), bf_hi(u)}; }
__device__ __forceinline__ f32x2 fma2(float a, f32x2 b, f32x2 c) { return __builtin_elementwise_fma(f32x2{a, a}, b, c); }
__device__ __forceinline__ f32x2 fma2(f32x2 a, f32x2 b, f32x2 c) { return __builtin_elementwise_fma(a, b, c); }

template <int CTRL> __device__ __forceinline__ float dpp(float x) {
  return __builtin_bit_cast(float, __builtin_amdgcn_mov_dpp(__builtin_bit_cast(int, x), CTRL, 0xf, 0xf, true));
}
constexpr int DPP_XOR1 = 0xB1, DPP_XOR2 = 0x4E, DPP_HMIRROR = 0x141;

__device__ __forceinline__ float sum_x16_32(float x) {
  auto s = __builtin_amdgcn_permlane16_swap(__float_as_uint(x), __float_as_uint(x), false, false);
  x = __uint_as_float(s[0]) + __uint_as_float(s[1]);
  auto t = __builtin_amdgcn_permlane32_swap(__float_as_uint(x), __float_as_uint(x), false, false);
  return __uint_as_float(t[0]) + __uint_as_float(t[1]);
}
__device__ __forceinline__ float max_x16_32(float x) {
  auto s = __builtin_amdgcn_permlane16_swap(__float_as_uint(x), __float_as_uint(x), false, false);
  x = fmaxf(__uint_as_float(s[0]), __uint_as_float(s[1]));
  auto t = __builtin_amdgcn_permlane32_swap(__float_as_uint(x), __float_as_uint(x), false, false);
  return fmaxf(__uint_as_float(t[0]), __uint_as_float(t[1]));
}
__device__ __forceinline__ float tsum32(float a, float b) {
  auto t = __builtin_amdgcn_permlane32_swap(__float_as_uint(a), __float_as_uint(b), false, false);
  return __uint_as_float(t[0]) + __uint_as_float(t[1]);
}
__device__ __forceinline__ float tsum16(float a, float b) {
  auto t = __builtin_amdgcn_permlane16_swap(__float_as_uint(a), __float_as_uint(b), false, false);
  return __uint_as_float(t[0]) + __uint_as_float(t[1]);
}
constexpr int DPP_ROR8 = 0x128, DPP_BCAST = 0x150;
__device__ __forceinline__ float sum_r(float v) { v += dpp<DPP_ROR8>(v); return sum_x16_32(v); }
__device__ __forceinline__ float max_r(float v) { v = fmaxf(v, dpp<DPP_ROR8>(v)); return max_x16_32(v); }
__device__ __forceinline__ float wave_sum(float v) {
  v += dpp<DPP_XOR1>(v); v += dpp<DPP_XOR2>(v); v += dpp<DPP_HMIRROR>(v);
  return sum_r(v);
}

__device__ __forceinline__ float row16_sum(float v) {
  v += dpp<DPP_XOR1>(v); v += dpp<DPP_XOR2>(v); v += dpp<DPP_HMIRROR>(v);
  return v + dpp<DPP_ROR8>(v);
}

struct Slot { u32x4 a[3]; u32x4 b[3]; };

#define PIN8(a, o) asm volatile("" : "+v"((a)[(o) + 0]), "+v"((a)[(o) + 1]), "+v"((a)[(o) + 2]), "+v"((a)[(o) + 3]), \
                                       "+v"((a)[(o) + 4]), "+v"((a)[(o) + 5]), "+v"((a)[(o) + 6]), "+v"((a)[(o) + 7]))
#define PIN4(a, o) asm volatile("" : "+v"((a)[(o) + 0]), "+v"((a)[(o) + 1]), "+v"((a)[(o) + 2]), "+v"((a)[(o) + 3]))
#define MEMFENCE() do { asm volatile("" ::: "memory"); __builtin_amdgcn_sched_barrier(0); } while (0)
#define DRAIN() asm volatile("s_waitcnt vmcnt(0)" ::: "memory")

__device__ __forceinline__ void k_tile(const u32x4 (&k)[3], const f32x2 (&q)[12], float& gmx, float* dst, int j) {
  f32x2 s0 = {0.f, 0.f}, s1 = s0, s2 = s0;
#pragma unroll
  for (int d = 0; d < 4; ++d) {
    s0 = fma2(bf_pair(k[0][d]), q[d], s0);
    s1 = fma2(bf_pair(k[1][d]), q[4 + d], s1);
    s2 = fma2(bf_pair(k[2][d]), q[8 + d], s2);
  }
  s0 = (s0 + s1) + s2;
  float s = s0[0] + s0[1];
  s += dpp<DPP_XOR1>(s);
  s += dpp<DPP_XOR2>(s);
  s += dpp<DPP_HMIRROR>(s);
  gmx = fmaxf(gmx, s);
  asm volatile("" : "+v"(gmx));
  if (j == 0) *dst = s;
}
__device__ __forceinline__ void v_tile(u32x4 (&v)[3], float p, f32x2 (&acc)[12]) {
#pragma unroll
  for (int i = 0; i < 3; ++i) {
    asm volatile("" : "+v"(v[i]), "+v"(p));
#pragma unroll
    for (int d = 0; d < 4; ++d) acc[i * 4 + d] = fma2(p, bf_pair(v[i][d]), acc[i * 4 + d]);
    PIN4(acc, i * 4);
  }
}

#define QWAIT 32
#define YIELD 1
#define QSLEEP 4
constexpr int AUX_SC1 = 16, AUX_NT = 2;
constexpr unsigned SPIN_CAP = 1u << 17;
__device__ __forceinline__ unsigned nz_bits(float v) { const unsigned u = __float_as_uint(v); return u == 0u ? 0x80000000u : u; }
__device__ __forceinline__ void lds_put(int* p, int v) { __hip_atomic_store(p, v, __ATOMIC_RELAXED, __HIP_MEMORY_SCOPE_WORKGROUP); }
__device__ __forceinline__ int lds_get(int* p) { return __hip_atomic_load(p, __ATOMIC_RELAXED, __HIP_MEMORY_SCOPE_WORKGROUP); }
using bf16x8 = __attribute__((ext_vector_type(8))) short;
__device__ __forceinline__ unsigned pk_lo(unsigned a, unsigned b) { return __builtin_amdgcn_perm(b, a, 0x05040100u); }
__device__ __forceinline__ unsigned pk_hi(unsigned a, unsigned b) { return __builtin_amdgcn_perm(b, a, 0x07060302u); }
__device__ __forceinline__ void split3(float v, unsigned& t0, unsigned& t1, unsigned& t2) {
  t0 = __float_as_uint(v) & 0xffff0000u;
  const float r1 = v - __uint_as_float(t0);
  t1 = __float_as_uint(r1) & 0xffff0000u;
  t2 = __float_as_uint(r1 - __uint_as_float(t1));
}
__device__ __forceinline__ f32x4 mfma16(u32x4 a, u32x4 b, f32x4 c) {
  return __builtin_amdgcn_mfma_f32_16x16x32_bf16(__builtin_bit_cast(bf16x8, a), __builtin_bit_cast(bf16x8, b), c, 0, 0, 0);
}
__device__ __forceinline__ void put_terms4(char* img, int rowbytes, f32x4 v) {
  unsigned t[3][4];
#pragma unroll
  for (int e = 0; e < 4; ++e) split3(v[e], t[0][e], t[1][e], t[2][e]);
#pragma unroll
  for (int k = 0; k < 3; ++k) *reinterpret_cast<u32x2*>(img + k * rowbytes) = u32x2{pk_hi(t[k][0], t[k][1]), pk_hi(t[k][2], t[k][3])};
}
__device__ __forceinline__ bool all_nz(u32x4 v) { return v[0] != 0u && v[1] != 0u && v[2] != 0u && v[3] != 0u; }

__device__ __forceinline__ float sum3(f32x4 v) { return (v[0] + v[1]) + v[2]; }
constexpr int NW = 8, NBUF = 4, NGRP = 64, NT = NGRP / NW;
constexpr int NQP = NH * DH / 64, NQW = 5, QST = DM / 64 / NQW;
constexpr int W_CT = DM / 64, G_TASKS = 2 * (FF / 32), W_TASKS = W_CT * K3_KS, D_TASKS = 2 * (DM / 32);
static_assert(G_TASKS + W_TASKS + D_TASKS == NWG && NW == 8 && K3_ROWS == NW * 64 && NBUF == 4 && SK / 64 == 128, "shape");
#define WSTEP 0
#define KROT 0
#define PJIT 64
#define WPOS 0
#define WSLEEP 4
#define PFINE 1
#define GDELAY 4
#define DDELAY 64
#define ENDPIN 0
#define AHEAD_W 0
#define AHEAD_G 0
#define WSIG 27
#define LATEISS 1
#define ENDPRIO 1
#define WDELAY 4
#define AFROM 4
#define LMISS 250
#define NOST 0
#define NOLD 0
#define RWAVE 1
#define CONS 1
#define EFROM 11
#define ESL 0
#define WEARLY 7
#define WROLES 15
#define PROBE_MIN 6
#define XMAP 1
#define ROT 5
__device__ __forceinline__ unsigned fresh_tag(unsigned tags) {
  unsigned t = __builtin_amdgcn_readfirstlane(tags);
  do ++t; while (__any(tags == t));
  return t;
}
struct Turn { int seg, off, t0, cnt, len; };

__global__ void __launch_bounds__(NW * 64) fused_kernel(const bf16* __restrict__ x, const bf16* __restrict__ kc, const bf16* __restrict__ vc,
                                                        const bf16* __restrict__ wq, const bf16* __restrict__ wo, const bf16* __restrict__ wr,
                                                        const bf16* __restrict__ wg, const bf16* __restrict__ wu, const bf16* __restrict__ wd,
                                                        float* ws, bf16* __restrict__ out) {
  __shared__ __attribute__((aligned(16))) u32x4 s_kv[4096];
  __shared__ __attribute__((aligned(16))) float s_buf[NWIN * NW * DH];
  __shared__ __attribute__((aligned(16))) float s_sc[NGRP * 64];
  __shared__ __attribute__((aligned(16))) float s_q[NWIN * DH];
  __shared__ float s_m[NWIN * NW], s_l[NWIN * NW];
  __shared__ float s_sig[2];
  __shared__ int s_qok, s_pcnt, s_ecnt, s_fcnt, s_icnt, s_org;
  __shared__ int s_prog[NW];
  __shared__ float s_stat[4];

  const int tid = threadIdx.x;
  int lane = tid & 63, r = lane >> 3, j = lane & 7, mg = lane >> 4, mc = lane & 15;
  int wave = __builtin_amdgcn_readfirstlane(tid >> 6), bid = blockIdx.x;
  const unsigned t_start = (unsigned)__builtin_amdgcn_s_memrealtime();
  int wid = XMAP ? (bid & 7) * 32 + (bid >> 3) : bid;
  const bool prod = bid < NQP;
  const __amdgpu_buffer_rsrc_t rs_ws = __builtin_amdgcn_make_buffer_rsrc(ws, 0, WS_END * 4, 0x00020000);
  const __amdgpu_buffer_rsrc_t rs_k = __builtin_amdgcn_make_buffer_rsrc(const_cast<bf16*>(kc), 0, NB * NH * SK * DH * 2, 0x00020000);
  const __amdgpu_buffer_rsrc_t rs_v = __builtin_amdgcn_make_buffer_rsrc(const_cast<bf16*>(vc), 0, NB * NH * SK * DH * 2, 0x00020000);
  const int voff = r * (DH * 2) + j * 16;
  const int c1 = wid % 3 == 0 ? 22 : 21, c2 = wid % 3 == 1 ? 42 : 43;
  const int rot = (wid * ROT) % 168;
  auto turn = [&](int n) {
    const int g = n * 8 + wave;
    Turn t;
    t.seg = (g >= c1) + (g >= c2);
    const int cl = t.seg == 0 ? 0 : (t.seg == 1 ? c1 : c2), ch = t.seg == 0 ? c1 : (t.seg == 1 ? c2 : NGRP);
    const int lo = max(n * 8, cl), sg = wid + NWG * t.seg, row = sg / NSEG, k = sg - row * NSEG;
    t.cnt = min(n * 8 + 8, ch) - lo;
    t.t0 = (lo - cl) * 8 + g - lo + rot;
    t.len = (ch - cl) * 8;
    t.off = (row * (SK / 64) + k * 21 + (k + 2) / 3) * (64 * DH * 2);
    return t;
  };
  auto gload = [&](Slot& sl, __amdgpu_buffer_rsrc_t rs, const Turn& t, int u) {
    int ta = t.t0 + 2 * u * t.cnt, tb = ta + t.cnt;
    ta -= ta >= t.len ? t.len : 0; tb -= tb >= t.len ? t.len : 0;
#pragma unroll
    for (int i = 0; i < 3; ++i) sl.a[i] = __builtin_amdgcn_raw_buffer_load_b128(rs, voff + i * 128, t.off + ta * 3072, AUX_NT);
    MEMFENCE();
#pragma unroll
    for (int i = 0; i < 3; ++i) sl.b[i] = __builtin_amdgcn_raw_buffer_load_b128(rs, voff + i * 128, t.off + tb * 3072, AUX_NT);
    MEMFENCE();
  };
  unsigned hs_nc = 0u, hs_ep = 0u;
  auto master = [&]() {
    u32x4 hp[2], hc[2];
#pragma unroll
    for (int i = 0; i < 2; ++i) {
      hp[i] = __builtin_amdgcn_raw_buffer_load_b128(rs_ws, (WS_HP + lane * 8 + i * 4) * 4, 0, AUX_SC1);
      const u32x2 c0 = __builtin_amdgcn_raw_buffer_load_b64(rs_ws, (WS_HC + (lane * 4 + i * 2) * CSTR) * 4, 0, AUX_SC1), c1 = __builtin_amdgcn_raw_buffer_load_b64(rs_ws, (WS_HC + (lane * 4 + i * 2 + 1) * CSTR) * 4, 0, AUX_SC1);
      hc[i] = u32x4{c0[0], c0[1], c1[0], c1[1]};
    }
    if (__all(hc[0][1] == hp[0][0] && hc[0][3] == hp[0][2] && hc[1][1] == hp[1][0] && hc[1][3] == hp[1][2]) &&
        __any(hc[0][0] != hp[0][1] || hc[0][2] != hp[0][3] || hc[1][0] != hp[1][1] || hc[1][2] != hp[1][3])) {
#pragma unroll
      for (int i = 0; i < 2; ++i)
        __builtin_amdgcn_raw_buffer_store_b128(u32x4{hp[i][0], hc[i][0], hp[i][2], hc[i][2]}, rs_ws, (WS_HP + lane * 8 + i * 4) * 4, 0, AUX_SC1);
    }
  };
  auto look_at_p = [&]() {
    if (bid == NWG - 1) master();
    const u32x2 pw = __builtin_amdgcn_raw_buffer_load_b64(rs_ws, (WS_HP + bid * 2) * 4, 0, AUX_SC1);
    const unsigned np = __builtin_amdgcn_readfirstlane(pw[0]), ec = __builtin_amdgcn_readfirstlane(pw[1]);
    if (np != hs_ep) { hs_ep = np; __builtin_amdgcn_raw_buffer_store_b64(u32x2{hs_nc, hs_ep}, rs_ws, lane == 0 ? (WS_HC + bid * CSTR) * 4 : OOB, 0, AUX_SC1); }
    return ec == hs_nc;
  };
  if (tid < NW) s_prog[tid] = 0;
  if (tid == 0) { s_qok = 0; s_pcnt = 0; s_ecnt = 0; s_fcnt = 0; s_icnt = 0; s_org = (int)(t_start + POFF); }
  if (wave == 0) {
    __builtin_amdgcn_s_setprio(3);
    int iofs[NWIN];
#pragma unroll
    for (int s = 0; s < NWIN; ++s) iofs[s] = (WS_QI + ((wid + NWG * s) / NSEG * DH + lane) * 2) * 4;
    const int oofs = prod ? (WS_QI + (bid * 64 + lane) * 2) * 4 : OOB;
    u32x2 in[NWIN * 3];
    unsigned ot[NB];
#pragma unroll
    for (int e = 0; e < NWIN * 3; ++e) in[e] = __builtin_amdgcn_raw_buffer_load_b64(rs_ws, iofs[e / 3], (e % 3) * 512, AUX_SC1);
#pragma unroll
    for (int bb = 0; bb < NB; ++bb) ot[bb] = __builtin_amdgcn_raw_buffer_load_b64(rs_ws, oofs, bb * (HD * 8), AUX_SC1)[1];
    u32x4 xr[5], hp[2], hc[2];
#pragma unroll
    for (int i = 0; i < 5; ++i) xr[i] = reinterpret_cast<const u32x4*>(x)[lane * 5 + i];
#pragma unroll
    for (int i = 0; i < 2; ++i) {
      hp[i] = __builtin_amdgcn_raw_buffer_load_b128(rs_ws, bid == NWG - 1 ? (WS_HP + lane * 8 + i * 4) * 4 : OOB, 0, AUX_SC1);
      hc[i][1] = __builtin_amdgcn_raw_buffer_load_b32(rs_ws, bid == NWG - 1 ? (WS_HC + (lane * 4 + i * 2) * CSTR + 1) * 4 : OOB, 0, AUX_SC1);
      hc[i][3] = __builtin_amdgcn_raw_buffer_load_b32(rs_ws, bid == NWG - 1 ? (WS_HC + (lane * 4 + i * 2 + 1) * CSTR + 1) * 4 : OOB, 0, AUX_SC1);
    }
    {
      const u32x4 z4 = {0u, 0u, 0u, 0u};
#pragma unroll
      for (int i = 0; i < (ZPW + 255) / 256; ++i)
        __builtin_amdgcn_raw_buffer_store_b128(z4, rs_ws, i * 256 + lane * 4 < ZPW ? (WS_Z0 + bid * ZPW + i * 256 + lane * 4) * 4 : OOB, 0, AUX_SC1);
    }
    MEMFENCE();
    __syncthreads();
    MEMFENCE();
    if (bid == NWG - 1) {
#pragma unroll
      for (int i = 0; i < 2; ++i) {
#pragma unroll
        for (int e = 0; e < 4; e += 2) { unsigned np = hp[i][e] + 1u; if (np == hc[i][e + 1]) np += 1u; hp[i][e] = np; }
        __builtin_amdgcn_raw_buffer_store_b128(hp[i], rs_ws, (WS_HP + lane * 8 + i * 4) * 4, 0, AUX_SC1);
      }
    }
    unsigned want[NWIN * 3];
#pragma unroll
    for (int e = 0; e < NWIN * 3; ++e) want[e] = fresh_tag(in[e][1]);
    if (prod) {
      float xs = 0.f;
#pragma unroll
      for (int i = 0; i < 20; ++i) xs += bf_lo(xr[i >> 2][i & 3]) + bf_hi(xr[i >> 2][i & 3]);
      const float mean = row16_sum(xs) * (1.0f / DM);
      float xv = 0.f;
#pragma unroll
      for (int i = 0; i < 20; ++i) { const float a = bf_lo(xr[i >> 2][i & 3]) - mean, c = bf_hi(xr[i >> 2][i & 3]) - mean; xv += a * a + c * c; }
      const float rstd = QSCALE / sqrtf(row16_sum(xv) * (1.0f / DM) + LN_EPS);
#pragma unroll
      for (int bb = 0; bb < NB; ++bb) ot[bb] = fresh_tag(ot[bb]);
      for (unsigned spin = 0; spin < (SPIN_CAP << 4) && lds_get(&s_pcnt) < NQW; ++spin) __builtin_amdgcn_s_sleep(1);
      __builtin_amdgcn_fence(__ATOMIC_ACQUIRE, "workgroup");
      float pr[5];
#pragma unroll
      for (int i = 0; i < 5; ++i) {
        pr[i] = 0.f;
#pragma unroll
        for (int k = 0; k < NQW; ++k) pr[i] += s_buf[2 * k * 320 + i * 64 + lane] + s_buf[(2 * k + 1) * 320 + i * 64 + lane];
      }
#pragma unroll
      for (int bb = 0; bb < NB; ++bb) {
        const float qv = (pr[bb] - __int_as_float(__builtin_amdgcn_readlane(__float_as_int(mean), bb * 16)) * pr[4]) *
                         __int_as_float(__builtin_amdgcn_readlane(__float_as_int(rstd), bb * 16));
        __builtin_amdgcn_raw_buffer_store_b64(u32x2{__float_as_uint(qv), ot[bb]}, rs_ws, oofs, bb * (HD * 8), AUX_SC1);
      }
    } else {
      __builtin_amdgcn_s_sleep(QWAIT);
    }
    {
      const u32x2 pw = __builtin_amdgcn_raw_buffer_load_b64(rs_ws, (WS_HP + bid * 2) * 4, 0, AUX_SC1);
      DRAIN();
      hs_ep = __builtin_amdgcn_readfirstlane(pw[0]);
      hs_nc = __builtin_amdgcn_readfirstlane(pw[1]) + 1u;
      __builtin_amdgcn_raw_buffer_store_b64(u32x2{hs_nc, hs_ep}, rs_ws, lane == 0 ? (WS_HC + bid * CSTR) * 4 : OOB, 0, AUX_SC1);
    }
    bool all_out = false;
    for (unsigned spin = 0; spin < SPIN_CAP; ++spin) {
#pragma unroll
      for (int e = 0; e < NWIN * 3; ++e) in[e] = __builtin_amdgcn_raw_buffer_load_b64(rs_ws, iofs[e / 3], (e % 3) * 512, AUX_SC1);
      bool ok = true;
#pragma unroll
      for (int e = 0; e < NWIN * 3; ++e) ok = ok && in[e][1] == want[e];
      if (all_out || __all(ok)) break;
      if (spin >= 3) all_out = look_at_p();
      __builtin_amdgcn_s_sleep(QSLEEP);
    }
#pragma unroll
    for (int e = 0; e < NWIN * 3; ++e) s_q[e * 64 + lane] = __uint_as_float(in[e][0]);
    __builtin_amdgcn_fence(__ATOMIC_RELEASE, "workgroup");
    lds_put(&s_qok, 1);
    __builtin_amdgcn_s_setprio(0);
  } else if (prod && wave <= NQW) {
    const int k0 = (wave - 1) * (QST * 64) + (mc >> 3) * 32 + mg * 8;
    u32x4 raw[QST * 8], xa[QST];
    const bf16* wb = wq + (size_t)k0 * HD + bid * 64 + (mc & 7) * 8;
#pragma unroll
    for (int st = 0; st < QST; ++st) xa[st] = *reinterpret_cast<const u32x4*>(x + (mc & 3) * DM + k0 + st * 64);
#pragma unroll
    for (int e = 0; e < QST * 8; ++e) raw[e] = *reinterpret_cast<const u32x4*>(wb + (size_t)((e >> 3) * 64 + (e & 7)) * HD);
    MEMFENCE();
    __syncthreads();
    MEMFENCE();
    f32x4 ac[8];
#pragma unroll
    for (int t = 0; t < 8; ++t) ac[t] = f32x4{0.f, 0.f, 0.f, 0.f};
#pragma unroll
    for (int st = 0; st < QST; ++st) {
      const u32x4 a = (mc & 4) == 0 ? xa[st] : u32x4{0x3f803f80u, 0x3f803f80u, 0x3f803f80u, 0x3f803f80u};
#pragma unroll
      for (int c = 0; c < 4; ++c) {
        u32x4 f0, f1;
#pragma unroll
        for (int i = 0; i < 4; ++i) { f0[i] = pk_lo(raw[st * 8 + 2 * i][c], raw[st * 8 + 2 * i + 1][c]); f1[i] = pk_hi(raw[st * 8 + 2 * i][c], raw[st * 8 + 2 * i + 1][c]); }
        ac[2 * c] = mfma16(a, f0, ac[2 * c]);
        ac[2 * c + 1] = mfma16(a, f1, ac[2 * c + 1]);
      }
    }
    if ((mg >> 1) == (mc >> 3)) {
      float* const dst = &s_buf[((wave - 1) * 2 + (mg >> 1)) * 320 + (mc & 7) * 8];
#pragma unroll
      for (int bb = 0; bb < NB; ++bb)
        if (bb == 0 || (mg & 1) == 0) {
          *reinterpret_cast<f32x4*>(dst + ((mg & 1) * 4 + bb) * 64) = f32x4{ac[0][bb], ac[1][bb], ac[2][bb], ac[3][bb]};
          *reinterpret_cast<f32x4*>(dst + ((mg & 1) * 4 + bb) * 64 + 4) = f32x4{ac[4][bb], ac[5][bb], ac[6][bb], ac[7][bb]};
        }
    }
    __builtin_amdgcn_fence(__ATOMIC_RELEASE, "workgroup");
    if (lane == 0) __hip_atomic_fetch_add(&s_pcnt, 1, __ATOMIC_RELAXED, __HIP_MEMORY_SCOPE_WORKGROUP);
  } else {
    __syncthreads();
  }
  MEMFENCE();
  char* const s_A = reinterpret_cast<char*>(s_kv);
  float* const s_red = reinterpret_cast<float*>(&s_kv[2048]);
  float* const s_h = reinterpret_cast<float*>(&s_kv[3072]);
  constexpr int WA = 64 * 2 + 16, GA = DM * 2 + 16, DA = FF * 2 + 16;
#pragma unroll 1
  for (int pass = (13 - wave) >> 3; pass < 2; ++pass) {
  int real = pass;
  asm volatile("" : "+s"(real));
  asm volatile("" : "+s"(wave), "+s"(bid), "+s"(wid), "+v"(lane));
  r = lane >> 3; j = lane & 7;
  u32x2 raw[24];
#pragma unroll
  for (int e = 0; e < 24; ++e) raw[e] = u32x2{0u, 0u};
  unsigned ef = 0u;
  auto weights = [&]() {
    asm volatile("" : "+s"(bid), "+v"(lane));
    mg = lane >> 4; mc = lane & 15;
    const int xcd = bid & 7, ri = bid >> 3;
    const bool isW = ri < 15, isG = !isW && ri < 27;
    const int tt = isW ? xcd * 15 + ri : (isG ? xcd * 12 + ri - 15 : xcd * 5 + ri - 27);
    const int bp = tt & 1, ct = tt >> 1;
    const int ks1 = tt / W_CT, ct1 = tt - ks1 * W_CT;
    typedef const __attribute__((address_space(1))) char* gptr;
    const gptr wb = (gptr)(isW ? wo + (size_t)(ks1 * K3_ROWS + wave * 64 + mg * 8) * DM + ct1 * 64 + mc * 4
                               : (isG ? (mc < 8 ? wg : wu) + (size_t)(wave * 32 + mg * 8) * FF : wd + (size_t)(wave * 192 + (mc >> 3) * 32 + mg * 8) * DM) + ct * 32 + (mc & 7) * 4);
    const int rstr = (isG ? FF : DM) * 2, o1 = (isW ? 32 * DM : (isG ? 256 * FF : 64 * DM)) * 2;
#pragma unroll
    for (int e = 0; e < 16; ++e) raw[e] = *(const __attribute__((address_space(1))) u32x2*)(wb + ((e >> 3) * o1 + (e & 7) * rstr));
    if (!isW && (!isG || wave < 4)) {
#pragma unroll
      for (int e = 16; e < 24; ++e) raw[e] = *(const __attribute__((address_space(1))) u32x2*)(wb + (2 * o1 + (e & 7) * rstr));
    }
    MEMFENCE();
  };
  if (real) {
  u32x2 rep = {0u, 0u};
  auto turn_prio = [&](int tn) {
    if (PTAU) {
      if (ADAPT && wave == 0 && tn > AFROM) {
        const int org = lds_get(&s_org);
        const bool ok = rep[0] - t_start < 5000u && rep[1] - t_start < 5000u;
        const float up = row16_sum(ok ? (CONS ? (float)(int)(rep[1] - (unsigned)org) : 0.f) + (float)max((int)(rep[0] - (CONS ? rep[1] : (unsigned)org)) - PTOL, 0) * (ADAPT / 100.f) : LMISS * (ADAPT / 100.f));
        if (lane == 0) lds_put(&s_org, org + max((int)(up * (1.f / 16.f)), 0));
      }
      const unsigned rel = (unsigned)(tn * PTAU + max(tn - EFROM, 0) * ESL + wave * (PTAU / 8) + ((bid >> 3) * 13 & 31) * (PJIT / 32));
      unsigned now = (unsigned)__builtin_amdgcn_s_memrealtime();
      if (ADAPT && !NOST && wave == RWAVE && (bid >> 4) == 7) __builtin_amdgcn_raw_buffer_store_b64(u32x2{now - rel + PTAU + (tn > EFROM ? ESL : 0), (unsigned)lds_get(&s_org)}, rs_ws, lane == 0 ? (WS_REP + ((tn - 1) * 16 + (bid & 15)) * RSTR) * 4 : OOB, 0, AUX_SC1);
      for (unsigned spin = 0; spin < 4096 && (int)(now - rel - (unsigned)__builtin_amdgcn_readfirstlane(lds_get(&s_org))) < 0; ++spin) {
        __builtin_amdgcn_s_sleep(2);
        now = (unsigned)__builtin_amdgcn_s_memrealtime();
      }
      if (ADAPT) {
        if (!NOLD) rep = __builtin_amdgcn_raw_buffer_load_b64(rs_ws, (wave == 0 && lane < 16 && tn >= AFROM) ? (WS_REP + ((tn - 1) * 16 + lane) * RSTR) * 4 : OOB, 0, 0);
      }
    }
    if (lane == 0) lds_put(&s_prog[wave], tn);
    int pp = __builtin_amdgcn_readfirstlane(lds_get(&s_prog[wave ^ (NW / 2)]));
    if (WSTEP) {
      for (unsigned spin = 0; spin < 4096 && __any(tn > lds_get(&s_prog[j]) + WSTEP); ++spin) __builtin_amdgcn_s_sleep(4);
      pp = __builtin_amdgcn_readfirstlane(lds_get(&s_prog[wave ^ (NW / 2)]));
    }
    for (unsigned spin = 0; spin < 4096 && tn > pp + YIELD; ++spin) { __builtin_amdgcn_s_sleep(4); pp = __builtin_amdgcn_readfirstlane(lds_get(&s_prog[wave ^ (NW / 2)])); }
    if (tn > pp) __builtin_amdgcn_s_setprio(0); else __builtin_amdgcn_s_setprio(1);
  };
  Slot buf[NBUF];
  f32x2 q[12];
  float gmx = -INFINITY;
  float* const sc = &s_sc[wave * (NT * 64) + r * 8];
  auto load_q = [&](int s) {
#pragma unroll
    for (int i = 0; i < 6; ++i) {
      const f32x4 a = *reinterpret_cast<const f32x4*>(&s_q[s * DH + (i >> 1) * 64 + j * 8 + (i & 1) * 4]);
      q[i * 2] = f32x2{a[0], a[1]}; q[i * 2 + 1] = f32x2{a[2], a[3]};
    }
  };
  const int n0 = KROT ? (c2 - wave + 7) >> 3 : 0;
  Turn tnx = turn((n0 + 2) & 7);
  {
    Slot xt[NBUF];
    const Turn t0 = turn(n0), t1 = turn(n0 + 1);
    __builtin_amdgcn_s_setprio(2);
#pragma unroll
    for (int u = 0; u < NBUF; ++u) gload(buf[u], rs_k, t0, u);
    __builtin_amdgcn_s_setprio(1);
#pragma unroll
    for (int u = 0; u < NBUF; ++u) gload(xt[u], rs_k, t1, u);
    __builtin_amdgcn_s_setprio(0);
    if (lane == 0) lds_put(&s_prog[wave], 1);
    for (unsigned spin = 0; spin < (SPIN_CAP << 4) && lds_get(&s_qok) == 0; ++spin) __builtin_amdgcn_s_sleep(2);
    __builtin_amdgcn_fence(__ATOMIC_ACQUIRE, "workgroup");
    load_q(KROT ? NWIN - 1 : 0);
    MEMFENCE();
#pragma unroll
    for (int u = 0; u < NBUF; ++u) {
      k_tile(buf[u].a, q, gmx, sc + n0 * 64 + 2 * u, j);
      k_tile(buf[u].b, q, gmx, sc + n0 * 64 + 2 * u + 1, j);
      MEMFENCE();
      gload(buf[u], rs_k, tnx, u);
    }
#pragma unroll
    for (int u = 0; u < NBUF; ++u) {
      k_tile(xt[u].a, q, gmx, sc + n0 * 64 + 64 + 2 * u, j);
      k_tile(xt[u].b, q, gmx, sc + n0 * 64 + 64 + 2 * u + 1, j);
      MEMFENCE();
    }
  }
  int seg = KROT ? NWIN - 1 : 0;
  for (int n = 2; n < NT; ++n) {
    if (!LATEISS) turn_prio(n);
    if (tnx.seg != seg) {
      const float gm = max_r(gmx);
      if (lane == 0) s_m[seg * NW + wave] = gm;
      gmx = -INFINITY;
      seg = tnx.seg;
      load_q(seg);
    }
    const __amdgpu_buffer_rsrc_t rs = n == NT - 1 ? rs_v : rs_k;
    tnx = turn(n == NT - 1 ? 0 : (n0 + n + 1) & 7);
    float* const scn = sc + ((n0 + n) & 7) * 64;
    MEMFENCE();
#pragma unroll
    for (int u = 0; u < NBUF; ++u) {
      k_tile(buf[u].a, q, gmx, scn + 2 * u, j);
      k_tile(buf[u].b, q, gmx, scn + 2 * u + 1, j);
      MEMFENCE();
      if (!LATEISS) gload(buf[u], rs, tnx, u);
    }
    if (LATEISS) {
      turn_prio(n);
#pragma unroll
      for (int u = 0; u < NBUF; ++u) gload(buf[u], rs, tnx, u);
    }
    if (wave == 0 && (n == 2 || (bid == NWG - 1 && n >= 4))) look_at_p();
  }
  {
    const float gm = max_r(gmx);
    if (lane == 0) s_m[seg * NW + wave] = gm;
  }
  float l = 0.f;
  f32x2 acc[12];
#pragma unroll
  for (int i = 0; i < 12; ++i) acc[i] = f32x2{0.f, 0.f};
  float p[8];
  __builtin_amdgcn_fence(__ATOMIC_RELEASE, "wavefront"); __builtin_amdgcn_wave_barrier(); __builtin_amdgcn_fence(__ATOMIC_ACQUIRE, "wavefront");
  auto v_begin = [&](int n, int s) {
    const float m = s_m[s * NW + wave];
    const f32x4 sa = *reinterpret_cast<const f32x4*>(sc + n * 64), sb = *reinterpret_cast<const f32x4*>(sc + n * 64 + 4);
#pragma unroll
    for (int t = 0; t < 4; ++t) { p[t] = __builtin_amdgcn_exp2f(sa[t] - m); p[4 + t] = __builtin_amdgcn_exp2f(sb[t] - m); }
    l += ((p[0] + p[1]) + (p[2] + p[3])) + ((p[4] + p[5]) + (p[6] + p[7]));
    MEMFENCE();
  };
  auto v_flush = [&](int s) {
    const float lw = sum_r(l);
    float t12[12], t6[6];
#pragma unroll
    for (int e = 0; e < 12; ++e) t12[e] = tsum32(acc[e >> 1][e & 1], acc[6 + (e >> 1)][e & 1]);
#pragma unroll
    for (int e = 0; e < 6; ++e) { t6[e] = tsum16(t12[e], t12[e + 6]); t6[e] += dpp<DPP_ROR8>(t6[e]); }
    if ((lane & 8) == 0) {
#pragma unroll
      for (int e = 0; e < 3; ++e) *reinterpret_cast<f32x2*>(&s_buf[(s * NW + wave) * DH + j * 24 + (lane >> 4) * 6 + e * 2]) = f32x2{t6[2 * e], t6[2 * e + 1]};
    }
    if (lane == 0) s_l[s * NW + wave] = lw;
  };
  auto publish = [&](int s) {
    float M = s_m[s * NW];
#pragma unroll
    for (int w = 1; w < NW; ++w) M = fmaxf(M, s_m[s * NW + w]);
    float L = 0.f;
    f32x4 o = {0.f, 0.f, 0.f, 0.f};
    const int d4 = lane < DH / 4 ? ((lane >> 1) & 7) * 24 + (lane >> 4) * 8 + (lane & 1) * 4 : 0;
#pragma unroll
    for (int w = 0; w < NW; ++w) {
      const float e = __builtin_amdgcn_exp2f(s_m[s * NW + w] - M);
      L = fmaf(e, s_l[s * NW + w], L);
      o += e * *reinterpret_cast<const f32x4*>(&s_buf[(s * NW + w) * DH + d4]);
    }
    if (lane == DH / 4) o = f32x4{M, L, 1.f, 1.f};
    __builtin_amdgcn_raw_buffer_store_b128(u32x4{nz_bits(o[0]), nz_bits(o[1]), nz_bits(o[2]), nz_bits(o[3])}, rs_ws,
                                           lane < REC / 4 ? (WS_PO + (wid + NWG * s) * REC + lane * 4) * 4 : OOB, 0, AUX_SC1);
  };
  for (int n = 0; n < NT - 1; ++n) {
    if (!LATEISS) turn_prio(NT + n);
    seg = tnx.seg;
    v_begin(n, seg);
    tnx = turn(n + 1);
    MEMFENCE();
#pragma unroll
    for (int u = 0; u < NBUF; ++u) {
      v_tile(buf[u].a, p[2 * u], acc);
      v_tile(buf[u].b, p[2 * u + 1], acc);
      MEMFENCE();
      if (!LATEISS) gload(buf[u], rs_v, tnx, u);
    }
    if (LATEISS) {
      turn_prio(NT + n);
#pragma unroll
      for (int u = 0; u < NBUF; ++u) gload(buf[u], rs_v, tnx, u);
    }
    if (wave == 0 && n == 1) {
      for (unsigned spin = 0; spin < SPIN_CAP && !look_at_p(); ++spin) __builtin_amdgcn_s_sleep(4);
    }
    if (tnx.seg != seg) {
      v_flush(seg);
      l = 0.f;
#pragma unroll
      for (int i = 0; i < 12; ++i) acc[i] = f32x2{0.f, 0.f};
      if (seg == 1) {
        __builtin_amdgcn_fence(__ATOMIC_RELEASE, "workgroup");
        int c = 0;
        if (lane == 0) c = __hip_atomic_fetch_add(&s_ecnt, 1, __ATOMIC_RELAXED, __HIP_MEMORY_SCOPE_WORKGROUP);
        if (__builtin_amdgcn_readfirstlane(c) == NW - 1) {
          __builtin_amdgcn_fence(__ATOMIC_ACQUIRE, "workgroup");
#pragma unroll 1
          for (int s = 0; s < 2; ++s) publish(s);
          DRAIN();
          __builtin_amdgcn_raw_buffer_store_b32(1u, rs_ws, lane == 0 ? EFOFS(wid) : OOB, 0, AUX_SC1);
        }
      }
      MEMFENCE();
    }
  }
  {
    asm volatile("" : "+s"(bid), "+v"(lane));
    mg = lane >> 4; mc = lane & 15;
    const int ks1 = ((bid & 7) * 15 + (bid >> 3)) / W_CT;
    if (lane == 0) __hip_atomic_fetch_add(&s_icnt, 1, __ATOMIC_RELAXED, __HIP_MEMORY_SCOPE_WORKGROUP);
    if (ENDPRIO) __builtin_amdgcn_s_setprio(3);
    {
      const __amdgpu_buffer_rsrc_t rs_wo = __builtin_amdgcn_make_buffer_rsrc(const_cast<bf16*>(wo), 0, (wave >= WEARLY && (bid >> 3) < 15) ? HD * DM * 2 : 0, 0x00020000);
      const int wofs = ((ks1 * K3_ROWS + wave * 64 + mg * 8) * DM + ((bid & 7) * 15 + (bid >> 3) - ks1 * W_CT) * 64 + mc * 4) * 2;
#pragma unroll
      for (int e = 0; e < 16; ++e) raw[e] = __builtin_amdgcn_raw_buffer_load_b64(rs_wo, wofs, ((e >> 3) * 32 + (e & 7)) * (DM * 2), 0);
    }
    v_begin(NT - 1, NWIN - 1);
    ef = __builtin_amdgcn_raw_buffer_load_b32(rs_ws, ((bid >> 3) < 15 && mg < 2 && mc < NSEG) ? EFOFS(((mg * NH + (ks1 * K3_ROWS + wave * 64) / DH) * NSEG + mc) & (NWG - 1)) : OOB, 0, AUX_SC1);
    MEMFENCE();
#pragma unroll
    for (int u = 0; u < NBUF; ++u) {
      v_tile(buf[u].a, p[2 * u], acc);
      v_tile(buf[u].b, p[2 * u + 1], acc);
      MEMFENCE();
    }
  }
  if (!ENDPRIO) __builtin_amdgcn_s_setprio(0);
  v_flush(NWIN - 1);
  {
    __builtin_amdgcn_fence(__ATOMIC_RELEASE, "workgroup");
    int c = 0;
    if (lane == 0) c = __hip_atomic_fetch_add(&s_fcnt, 1, __ATOMIC_RELAXED, __HIP_MEMORY_SCOPE_WORKGROUP);
    if (__builtin_amdgcn_readfirstlane(c) == NW - 1) {
      __builtin_amdgcn_fence(__ATOMIC_ACQUIRE, "workgroup");
      publish(NWIN - 1);
    }
  }
  __builtin_amdgcn_s_setprio(0);
  if (wave < WEARLY || (bid >> 3) >= 15) {
    int* const cnt = (bid >> 3) < WSIG ? &s_icnt : &s_fcnt;
    for (unsigned spin = 0; spin < (SPIN_CAP << 4) && lds_get(cnt) < NW; ++spin) __builtin_amdgcn_s_sleep(1);
    if ((bid >> 3) >= 27) __builtin_amdgcn_s_sleep(DDELAY);
    else {
      const int nsl = (bid >> 3) < 15 ? WDELAY : GDELAY;
      for (int i = 0; i < nsl && lds_get(&s_fcnt) < NW; ++i) __builtin_amdgcn_s_sleep(3);
    }
    weights();
  }

  }
  {
    asm volatile("" : "+s"(wave), "+s"(bid), "+v"(lane));
    r = lane >> 3; j = lane & 7; mg = lane >> 4; mc = lane & 15;
    const int w = wave - (1 - real) * (NW - 2) * (NW - 1 - wave), td = w * 64 + lane;
    const int xcd = bid & 7, ri = bid >> 3;
    const bool isW = ri < 15, isG = !isW && ri < 27;
    const int tt = isW ? xcd * 15 + ri : (isG ? xcd * 12 + ri - 15 : xcd * 5 + ri - 27);
    const int bp = tt & 1, ct = tt >> 1;
    const int ks1 = tt / W_CT, ct1 = tt - ks1 * W_CT;
    const __amdgpu_buffer_rsrc_t rs_out = __builtin_amdgcn_make_buffer_rsrc(out, 0, NB * DM * 2, 0x00020000);
    uint2 xx = {0u, 0u};
    unsigned wr2[5] = {0u, 0u, 0u, 0u, 0u};
    u32x4 fr[12];
#pragma unroll
    for (int i = 0; i < 12; ++i) fr[i] = u32x4{0u, 0u, 0u, 0u};
    auto frags = [&](int nst) {
      PIN8(raw, 0); PIN8(raw, 8); PIN8(raw, 16);
#pragma unroll
      for (int st = 0; st < nst; ++st)
#pragma unroll
        for (int i = 0; i < 4; ++i) {
          const u32x2 r0 = raw[st * 8 + 2 * i], r1 = raw[st * 8 + 2 * i + 1];
          fr[st * 4 + 0][i] = pk_lo(r0[0], r1[0]); fr[st * 4 + 1][i] = pk_hi(r0[0], r1[0]);
          fr[st * 4 + 2][i] = pk_lo(r0[1], r1[1]); fr[st * 4 + 3][i] = pk_hi(r0[1], r1[1]);
        }
      PIN4(fr, 0); PIN4(fr, 4);
      if (nst == 3) PIN4(fr, 8);
    };
    if (isW) {
      char* const img = s_A + w * (16 * WA);
      const int hh = (ks1 * K3_ROWS + w * 64) / DH;
      {
        const int rec = WS_PO + (mg * NH + hh) * NSEG * REC;
        const int offp = (rec + ks1 * K3_ROWS + w * 64 - hh * DH + mc * 4) * 4, offm = (rec + mc * REC + DH) * 4;
        const int offe = (mg < 2 && mc < NSEG) ? EFOFS(((mg * NH + hh) * NSEG + mc) & (NWG - 1)) : OOB;
        u32x4 pv[NSEG];
        u32x2 ml = {0u, 0u};
#pragma unroll
        for (int k = 0; k < NSEG; ++k) pv[k] = u32x4{0u, 0u, 0u, 0u};
        unsigned pend = real ? (1u << NSEG) - 1u + (mc < NSEG ? 1u << NSEG : 0u) : 0u;
        for (unsigned spin = 0; spin < SPIN_CAP; ++spin) {
          const bool early = __all(offe == OOB || ef != 0u);
          const unsigned go = (early || mg >= 2) ? pend : 0u;
          if (!early && real) ef = __builtin_amdgcn_raw_buffer_load_b32(rs_ws, offe, 0, AUX_SC1);
          if (go & (1u << NSEG)) ml = __builtin_amdgcn_raw_buffer_load_b64(rs_ws, offm, 0, AUX_SC1);
#pragma unroll
          for (int k = 0; k < NSEG; ++k)
            if (go & (1u << k)) pv[k] = __builtin_amdgcn_raw_buffer_load_b128(rs_ws, offp, k * REC * 4, AUX_SC1);
          if (spin == 0) frags(2);
          if (ml[0] != 0u && ml[1] != 0u) pend &= ~(1u << NSEG);
#pragma unroll
          for (int k = 0; k < NSEG; ++k)
            if (all_nz(pv[k])) pend &= ~(1u << k);
          if (!__any(pend != 0u)) break;
          if (spin == 0) {
            for (unsigned sp2 = 0; sp2 < (SPIN_CAP << 4) && lds_get(&s_fcnt) < NW; ++sp2) __builtin_amdgcn_s_sleep(1);
          } else if (spin < 6) __builtin_amdgcn_s_sleep(1); else __builtin_amdgcn_s_sleep(WSLEEP);
        }
        const float mm = mc < NSEG ? __uint_as_float(ml[0]) : -INFINITY;
        float M = fmaxf(mm, dpp<DPP_XOR1>(mm)); M = fmaxf(M, dpp<DPP_XOR2>(M)); M = fmaxf(M, dpp<DPP_HMIRROR>(M));
        const float e = __builtin_amdgcn_exp2f(mm - M);
        float L = e * __uint_as_float(ml[1]);
        L += dpp<DPP_XOR1>(L); L += dpp<DPP_XOR2>(L); L += dpp<DPP_HMIRROR>(L);
        const float wt = e / L;
        f32x4 o = dpp<DPP_BCAST + 0>(wt) * __builtin_bit_cast(f32x4, pv[0]);
        o += dpp<DPP_BCAST + 1>(wt) * __builtin_bit_cast(f32x4, pv[1]);
        o += dpp<DPP_BCAST + 2>(wt) * __builtin_bit_cast(f32x4, pv[2]);
        o += dpp<DPP_BCAST + 3>(wt) * __builtin_bit_cast(f32x4, pv[3]);
        o += dpp<DPP_BCAST + 4>(wt) * __builtin_bit_cast(f32x4, pv[4]);
        o += dpp<DPP_BCAST + 5>(wt) * __builtin_bit_cast(f32x4, pv[5]);
        put_terms4(img + mg * 4 * WA + mc * 8, WA, o);
      }
      __builtin_amdgcn_fence(__ATOMIC_RELEASE, "wavefront"); __builtin_amdgcn_wave_barrier(); __builtin_amdgcn_fence(__ATOMIC_ACQUIRE, "wavefront");
      f32x4 ac[4];
#pragma unroll
      for (int t = 0; t < 4; ++t) ac[t] = f32x4{0.f, 0.f, 0.f, 0.f};
#pragma unroll
      for (int st = 0; st < 2; ++st) {
        const u32x4 a = *reinterpret_cast<const u32x4*>(img + mc * WA + st * 64 + mg * 16);
#pragma unroll
        for (int t = 0; t < 4; ++t) ac[t] = mfma16(a, fr[st * 4 + t], ac[t]);
      }
      *reinterpret_cast<f32x4*>(&s_red[w * 256 + lane * 4]) = f32x4{sum3(ac[0]), sum3(ac[1]), sum3(ac[2]), sum3(ac[3])};
      if (real) __syncthreads();
      if (w == 0) {
        f32x4 s0 = f32x4{0.f, 0.f, 0.f, 0.f}, s1 = s0;
#pragma unroll
        for (int k = 0; k < NW; k += 2) { s0 += *reinterpret_cast<const f32x4*>(&s_red[k * 256 + lane * 4]); s1 += *reinterpret_cast<const f32x4*>(&s_red[(k + 1) * 256 + lane * 4]); }
        s0 += s1;
        __builtin_amdgcn_raw_buffer_store_b128(u32x4{nz_bits(s0[0]), nz_bits(s0[1]), nz_bits(s0[2]), nz_bits(s0[3])}, rs_ws,
                                               real ? (WS_SLAB + (ks1 * NB + mg) * DM + ct1 * 64 + mc * 4) * 4 : OOB, 0, AUX_SC1);
      }
    } else {
      if (real) {
        xx = *reinterpret_cast<const uint2*>(x + (size_t)bp * 2 * DM + (td < 2 * DM / 4 ? td : 0) * 4);
#pragma unroll
        for (int i = 0; i < 5; ++i) wr2[i] = reinterpret_cast<const unsigned*>(wr)[lane + 64 * i];
        if (isG && td < DM / 8) *reinterpret_cast<u32x4*>(s_A + 3 * GA + td * 16) = u32x4{0x3f803f80u, 0x3f803f80u, 0x3f803f80u, 0x3f803f80u};
      }
      frags(3);
      PIN4(wr2, 0); asm volatile("" : "+v"(wr2[4]), "+v"(xx.x), "+v"(xx.y));
      for (unsigned spin = 0; spin < (SPIN_CAP << 4) && real && lds_get(&s_fcnt) < NW; ++spin) __builtin_amdgcn_s_sleep(1);
      if (real && td < 2 * DM / 4) {
        const int off = (WS_SLAB + bp * 2 * DM + td * 4) * 4;
        const int poff = (WS_SLAB + bp * 2 * DM + w * 256 + lane * (NB * DM)) * 4;
        u32x4 sl[K3_KS];
#pragma unroll
        for (int ks = 0; ks < K3_KS; ++ks) sl[ks] = u32x4{0u, 0u, 0u, 0u};
        unsigned pend = (1u << K3_KS) - 1u;
        for (unsigned spin = 0; spin < SPIN_CAP; ++spin) {
          unsigned go = pend;
          if (__builtin_popcount(pend) > PROBE_MIN) {
            const u32x4 pr = __builtin_amdgcn_raw_buffer_load_b128(rs_ws, (lane < K3_KS && ((pend >> lane) & 1u)) ? poff : OOB, 0, AUX_SC1);
            go = (unsigned)__ballot(all_nz(pr)) & pend;
          }
#pragma unroll
          for (int ks = 0; ks < K3_KS; ++ks)
            if (go & (1u << ks)) sl[ks] = __builtin_amdgcn_raw_buffer_load_b128(rs_ws, off, ks * (NB * DM * 4), AUX_SC1);
#pragma unroll
          for (int ks = 0; ks < K3_KS; ++ks)
            if ((go & (1u << ks)) && __all(all_nz(sl[ks]))) pend &= ~(1u << ks);
          if (pend == 0u) break;
          __builtin_amdgcn_s_sleep(1);
        }
        f32x4 hv = f32x4{bf_lo(xx.x), bf_hi(xx.x), bf_lo(xx.y), bf_hi(xx.y)};
#pragma unroll
        for (int ks = 0; ks < K3_KS; ++ks) hv += __builtin_bit_cast(f32x4, sl[ks]);
        *reinterpret_cast<f32x4*>(&s_h[td * 4]) = hv;
        if (isG) put_terms4(s_A + (td >= DM / 4 ? 4 * GA - DM * 2 : 0) + td * 8, GA, hv);
      }
      if (real) __syncthreads();
      if (w >= NW - 2) {
        const int rw = w - (NW - 2);
        const float piv = s_h[rw * DM];
        float hv[10], sm = 0.f, sq = 0.f;
#pragma unroll
        for (int i = 0; i < 5; ++i) {
          const float2 t2 = *reinterpret_cast<const float2*>(&s_h[rw * DM + 2 * lane + 128 * i]);
          hv[2 * i] = t2.x - piv; hv[2 * i + 1] = t2.y - piv;
          sm += hv[2 * i] + hv[2 * i + 1]; sq = fmaf(hv[2 * i], hv[2 * i], sq); sq = fmaf(hv[2 * i + 1], hv[2 * i + 1], sq);
        }
        const float dm = wave_sum(sm) * (1.0f / DM);
        const float var = fmaxf(fmaf(-dm, dm, wave_sum(sq) * (1.0f / DM)), 0.f);
        const float rstd = 1.0f / sqrtf(var + LN_EPS);
        if (lane == 0) { s_stat[rw * 2] = piv + dm; s_stat[rw * 2 + 1] = rstd; }
        if (!isG) {
          float rr = 0.f;
#pragma unroll
          for (int i = 0; i < 5; ++i) { rr = fmaf((hv[2 * i] - dm) * rstd, bf_lo(wr2[i]), rr); rr = fmaf((hv[2 * i + 1] - dm) * rstd, bf_hi(wr2[i]), rr); }
          rr = wave_sum(rr);
          if (lane == 0) s_sig[rw] = 1.0f / (1.0f + __expf(-rr));
        }
      } else if (!isG && real) {
        const int off = (WS_ACT + bp * 2 * FF + td * 4) * 4;
        u32x4 v0 = {0u, 0u, 0u, 0u}, v1 = v0;
        unsigned pend = 3u;
        for (unsigned spin = 0; spin < SPIN_CAP; ++spin) {
          if (pend & 1u) v0 = __builtin_amdgcn_raw_buffer_load_b128(rs_ws, off, 0, AUX_SC1);
          if (pend & 2u) v1 = __builtin_amdgcn_raw_buffer_load_b128(rs_ws, off, FF * 4, AUX_SC1);
          if ((pend & 1u) && all_nz(v0)) pend &= ~1u;
          if ((pend & 2u) && all_nz(v1)) pend &= ~2u;
          if (!__any(pend != 0u)) break;
          __builtin_amdgcn_s_sleep(1);
        }
        put_terms4(s_A + td * 8, DA, __builtin_bit_cast(f32x4, v0));
        put_terms4(s_A + 4 * DA + td * 8, DA, __builtin_bit_cast(f32x4, v1));
      }
      if (!isG && real) __syncthreads();
      f32x4 ac[4];
#pragma unroll
      for (int t = 0; t < 4; ++t) ac[t] = f32x4{0.f, 0.f, 0.f, 0.f};
      {
        const char* ap = s_A + (mc & 7) * (isG ? GA : DA) + (isG ? w : w * 6 + (mc >> 3)) * 64 + mg * 16;
        const int astep = isG ? 8 * 64 : 2 * 64;
#pragma unroll
        for (int st = 0; st < 3; ++st) {
          if (st < 2 || w < 4 || !isG) {
            const u32x4 a = *reinterpret_cast<const u32x4*>(ap + st * astep);
#pragma unroll
            for (int t = 0; t < 4; ++t) ac[t] = mfma16(a, fr[st * 4 + t], ac[t]);
          }
        }
      }
      if (isG ? lane < 32 : (mc >> 3) == (mg >> 1))
        *reinterpret_cast<f32x4*>(&s_red[w * 256 + (isG ? lane * 4 : mg * 32 + (mc & 7) * 4)]) = f32x4{sum3(ac[0]), sum3(ac[1]), sum3(ac[2]), sum3(ac[3])};
      if (lane < 16) *reinterpret_cast<f32x4*>(&s_red[w * 256 + 128 + lane * 4]) = f32x4{ac[0][3], ac[1][3], ac[2][3], ac[3][3]};
      if (real) __syncthreads();
      if (w == 0) {
        const int bb = lane >> 5, f = lane & 31;
        const int i0 = isG ? bb * 64 + f : lane, i1 = isG ? bb * 64 + 32 + f : 64 + lane;
        float s0 = 0.f, s1 = 0.f, c0 = 0.f, c1 = 0.f;
#pragma unroll
        for (int k = 0; k < NW; ++k) { s0 += s_red[k * 256 + i0]; s1 += s_red[k * 256 + i1]; c0 += s_red[k * 256 + 128 + f]; c1 += s_red[k * 256 + 160 + f]; }
        const float mean = s_stat[bb * 2], rstd = s_stat[bb * 2 + 1];
        if (isG) {
          const float gg = (s0 - mean * c0) * rstd, uu = (s1 - mean * c1) * rstd;
          const float a = gg / (1.0f + __expf(-gg)) * uu;
          __builtin_amdgcn_raw_buffer_store_b32(nz_bits(a), rs_ws, real ? (WS_ACT + (bp * 2 + bb) * FF + ct * 32 + f) * 4 : OOB, 0, AUX_SC1);
        } else {
          const float hh = s_h[bb * DM + ct * 32 + f];
          const bf16 ov = __float2bfloat16(hh + ((hh - mean) * rstd + (s0 + s1) * s_sig[bb]));
          __builtin_amdgcn_raw_buffer_store_b16(__builtin_bit_cast(unsigned short, ov), rs_out, real ? ((bp * 2 + bb) * DM + ct * 32 + f) * 2 : OOB, 0, 0);
        }
      }
    }
  }
  }
  if (ENDPIN && wave == 0) for (unsigned spin = 0; spin < 100000 && (int)((unsigned)__builtin_amdgcn_s_memrealtime() - t_start) < ENDPIN; ++spin) __builtin_amdgcn_s_sleep(1);
}

extern "C" void kernel_launch(void* const* d_in, const int* in_sizes, int n_in, void* d_out, int out_size, void* d_ws,
                              size_t ws_size, hipStream_t stream) {
  fused_kernel<<<NWG, NW * 64, 0, stream>>>((const bf16*)d_in[0], (const bf16*)d_in[1], (const bf16*)d_in[2], (const bf16*)d_in[3],
                                            (const bf16*)d_in[4], (const bf16*)d_in[6], (const bf16*)d_in[7], (const bf16*)d_in[8],
                                            (const bf16*)d_in[9], (float*)d_ws, (bf16*)d_out);
}
```

```cpp
#include <hip/hip_runtime.h>
#include <hip/hip_bf16.h>
#include <stdint.h>

typedef __hip_bfloat16 bf16;
using u32x4 = __attribute__((ext_vector_type(4))) unsigned;
using u32x2 = __attribute__((ext_vector_type(2))) unsigned;
using f32x4 = __attribute__((ext_vector_type(4))) float;
using f32x2 = __attribute__((ext_vector_type(2))) float;

constexpr int NB = 4, DM = 640, NH = 32, SK = 8192, DH = 192, HD = NH * DH, FF = 1536;
constexpr int NWG = 256, NSEG = 6, NWIN = NB * NH * NSEG / NWG;
constexpr float LN_EPS = 1e-5f;
constexpr float QSCALE = 0.07216878364870322f * 1.4426950408889634f;

constexpr int WS_HP = 0;
constexpr int REC = DH + 32;
constexpr int WS_PO = WS_HP + 2 * NWG;
constexpr int WS_Z0 = WS_PO + 2 * NWG * REC;
constexpr int K3_KS = 12, K3_ROWS = HD / K3_KS;
constexpr int WS_SLAB = WS_PO + NB * NH * NSEG * REC;
constexpr int WS_ACT = WS_SLAB + K3_KS * NB * DM;
constexpr int ZPW = 384;
constexpr int WS_QI = WS_Z0 + NWG * ZPW;
constexpr int LSTR = 64;
constexpr int WS_REP = WS_QI + NB * NH * DH * 2;
constexpr int WS_HC = WS_REP + 256 * LSTR;
constexpr int WS_END = WS_HC + NWG * LSTR;
constexpr int RSTR = LSTR;
static_assert(WS_ACT + NB * FF <= WS_QI && WS_Z0 % 64 == 0 && WS_PO % 32 == 0 && ZPW % 64 == 0 && NWIN == 3, "layout");
__device__ __forceinline__ int ef_ofs(int i) { return (WS_PO + (2 * NWG + i) * REC + DH + 2) * 4; }
constexpr int OOB = 0x7ffffff0;

__device__ __forceinline__ float bf_lo(unsigned u) { return __uint_as_float(u << 16); }
__device__ __forceinline__ float bf_hi(unsigned u) { return __uint_as_float(u & 0xffff0000u); }
__device__ __forceinline__ f32x2 bf_pair(unsigned u) { return f32x2{bf_lo(u), bf_hi(u)}; }
__device__ __forceinline__ f32x2 fma2(float a, f32x2 b, f32x2 c) { return __builtin_elementwise_fma(f32x2{a, a}, b, c); }
__device__ __forceinline__ f32x2 fma2(f32x2 a, f32x2 b, f32x2 c) { return __builtin_elementwise_fma(a, b, c); }

template <int CTRL> __device__ __forceinline__ float dpp(float x) {
  return __builtin_bit_cast(float, __builtin_amdgcn_mov_dpp(__builtin_bit_cast(int, x), CTRL, 0xf, 0xf, true));
}
constexpr int DPP_XOR1 = 0xB1, DPP_XOR2 = 0x4E, DPP_HMIRROR = 0x141;

__device__ __forceinline__ float sum_x16_32(float x) {
  auto s = __builtin_amdgcn_permlane16_swap(__float_as_uint(x), __float_as_uint(x), false, false);
  x = __uint_as_float(s[0]) + __uint_as_float(s[1]);
  auto t = __builtin_amdgcn_permlane32_swap(__float_as_uint(x), __float_as_uint(x), false, false);
  return __uint_as_float(t[0]) + __uint_as_float(t[1]);
}
__device__ __forceinline__ float max_x16_32(float x) {
  auto s = __builtin_amdgcn_permlane16_swap(__float_as_uint(x), __float_as_uint(x), false, false);
  x = fmaxf(__uint_as_float(s[0]), __uint_as_float(s[1]));
  auto t = __builtin_amdgcn_permlane32_swap(__float_as_uint(x), __float_as_uint(x), false, false);
  return fmaxf(__uint_as_float(t[0]), __uint_as_float(t[1]));
}
__device__ __forceinline__ float tsum32(float a, float b) {
  auto t = __builtin_amdgcn_permlane32_swap(__float_as_uint(a), __float_as_uint(b), false, false);
  return __uint_as_float(t[0]) + __uint_as_float(t[1]);
}
__device__ __forceinline__ float tsum16(float a, float b) {
  auto t = __builtin_amdgcn_permlane16_swap(__float_as_uint(a), __float_as_uint(b), false, false);
  return __uint_as_float(t[0]) + __uint_as_float(t[1]);
}
constexpr int DPP_ROR8 = 0x128, DPP_BCAST = 0x150;
__device__ __forceinline__ float sum_r(float v) { v += dpp<DPP_ROR8>(v); return sum_x16_32(v); }
__device__ __forceinline__ float max_r(float v) { v = fmaxf(v, dpp<DPP_ROR8>(v)); return max_x16_32(v); }
__device__ __forceinline__ float wave_sum(float v) {
  v += dpp<DPP_XOR1>(v); v += dpp<DPP_XOR2>(v); v += dpp<DPP_HMIRROR>(v);
  return sum_r(v);
}

__device__ __forceinline__ float row16_sum(float v) {
  v += dpp<DPP_XOR1>(v); v += dpp<DPP_XOR2>(v); v += dpp<DPP_HMIRROR>(v);
  return v + dpp<DPP_ROR8>(v);
}

struct Slot { u32x4 a[3]; u32x4 b[3]; };

#define PIN8(a, o) asm volatile("" : "+v"((a)[(o) + 0]), "+v"((a)[(o) + 1]), "+v"((a)[(o) + 2]), "+v"((a)[(o) + 3]), \
                                       "+v"((a)[(o) + 4]), "+v"((a)[(o) + 5]), "+v"((a)[(o) + 6]), "+v"((a)[(o) + 7]))
#define PIN4(a, o) asm volatile("" : "+v"((a)[(o) + 0]), "+v"((a)[(o) + 1]), "+v"((a)[(o) + 2]), "+v"((a)[(o) + 3]))
#define MEMFENCE() do { asm volatile("" ::: "memory"); __builtin_amdgcn_sched_barrier(0); } while (0)
#define DRAIN() asm volatile("s_waitcnt vmcnt(0)" ::: "memory")

__device__ __forceinline__ void k_tile(const u32x4 (&k)[3], const f32x2 (&q)[12], float& gmx, float* dst, int j) {
  f32x2 s0 = {0.f, 0.f}, s1 = s0, s2 = s0;
#pragma unroll
  for (int d = 0; d < 4; ++d) {
    s0 = fma2(bf_pair(k[0][d]), q[d], s0);
    s1 = fma2(bf_pair(k[1][d]), q[4 + d], s1);
    s2 = fma2(bf_pair(k[2][d]), q[8 + d], s2);
  }
  s0 = (s0 + s1) + s2;
  float s = s0[0] + s0[1];
  s += dpp<DPP_XOR1>(s);
  s += dpp<DPP_XOR2>(s);
  s += dpp<DPP_HMIRROR>(s);
  gmx = fmaxf(gmx, s);
  asm volatile("" : "+v"(gmx));
  if (j == 0) *dst = s;
}
__device__ __forceinline__ void v_tile(u32x4 (&v)[3], float p, f32x2 (&acc)[12]) {
#pragma unroll
  for (int i = 0; i < 3; ++i) {
    asm volatile("" : "+v"(v[i]), "+v"(p));
#pragma unroll
    for (int d = 0; d < 4; ++d) acc[i * 4 + d] = fma2(p, bf_pair(v[i][d]), acc[i * 4 + d]);
    PIN4(acc, i * 4);
  }
}

#define QWAIT 32
#define QSLEEP 4
#define PTAU 668
#define POFF 160
#define PJIT (PTAU / 8)
#define ADAPT 60
#define AFROM 3
#define LCAP 600
#define LTGT 220
#define DOWN 20
#define WDELAY 4
#define GDELAY 4
#define DDELAY 64
#define WSLEEP 4
#define PROBE_MIN 6
#define ROT 0
#define EARLYG 1
#define WLOOK 0
#define LMODE 0
#define LLO 150
#define LHI 280
#define LSPR 50
#define TLBW 0
#define ILV 0
#define FFWD 160
#define XMAP 2
#define JMUL 1
#define JX 0
#define QW0 0
#define NREH 2
#define AMAX 0
#define EFROM 12
#define ESL 0
#define WSTART 8
#define ENDPIN 0
constexpr int AUX_SC1 = 16, AUX_NT = 2;
constexpr unsigned SPIN_CAP = 1u << 17;
__device__ __forceinline__ unsigned nz_bits(float v) { const unsigned u = __float_as_uint(v); return u == 0u ? 0x80000000u : u; }
__device__ __forceinline__ void lds_put(int* p, int v) { __hip_atomic_store(p, v, __ATOMIC_RELAXED, __HIP_MEMORY_SCOPE_WORKGROUP); }
__device__ __forceinline__ int lds_get(int* p) { return __hip_atomic_load(p, __ATOMIC_RELAXED, __HIP_MEMORY_SCOPE_WORKGROUP); }
using bf16x8 = __attribute__((ext_vector_type(8))) short;
__device__ __forceinline__ unsigned pk_lo(unsigned a, unsigned b) { return __builtin_amdgcn_perm(b, a, 0x05040100u); }
__device__ __forceinline__ unsigned pk_hi(unsigned a, unsigned b) { return __builtin_amdgcn_perm(b, a, 0x07060302u); }
__device__ __forceinline__ void split3(float v, unsigned& t0, unsigned& t1, unsigned& t2) {
  t0 = __float_as_uint(v) & 0xffff0000u;
  const float r1 = v - __uint_as_float(t0);
  t1 = __float_as_uint(r1) & 0xffff0000u;
  t2 = __float_as_uint(r1 - __uint_as_float(t1));
}
__device__ __forceinline__ f32x4 mfma16(u32x4 a, u32x4 b, f32x4 c) {
  return __builtin_amdgcn_mfma_f32_16x16x32_bf16(__builtin_bit_cast(bf16x8, a), __builtin_bit_cast(bf16x8, b), c, 0, 0, 0);
}
__device__ __forceinline__ void put_terms4(char* img, int rowbytes, f32x4 v) {
  unsigned t[3][4];
#pragma unroll
  for (int e = 0; e < 4; ++e) split3(v[e], t[0][e], t[1][e], t[2][e]);
#pragma unroll
  for (int k = 0; k < 3; ++k) *reinterpret_cast<u32x2*>(img + k * rowbytes) = u32x2{pk_hi(t[k][0], t[k][1]), pk_hi(t[k][2], t[k][3])};
}
__device__ __forceinline__ bool all_nz(u32x4 v) { return v[0] != 0u && v[1] != 0u && v[2] != 0u && v[3] != 0u; }

__device__ __forceinline__ float sum3(f32x4 v) { return (v[0] + v[1]) + v[2]; }

constexpr int NW = 8, NBUF = 4, NGRP = 64, NT = NGRP / NW;
constexpr int NQP = NH * DH / 64, NQW = 5, QST = DM / 64 / NQW;
constexpr int SVC = NW - 1;
constexpr int W_CT = DM / 64, G_TASKS = 2 * (FF / 32), W_TASKS = W_CT * K3_KS, D_TASKS = 2 * (DM / 32);
static_assert(G_TASKS + W_TASKS + D_TASKS == NWG && NW == 8 && K3_ROWS == 4 * 128 && NBUF == 4 && SK / 64 == 128, "shape");
__device__ __forceinline__ unsigned fresh_tag(unsigned tags) {
  unsigned t = __builtin_amdgcn_readfirstlane(tags);
  do ++t; while (__any(tags == t));
  return t;
}

__global__ void __launch_bounds__(NW * 64) fused_kernel(const bf16* __restrict__ x, const bf16* __restrict__ kc, const bf16* __restrict__ vc,
                                                        const bf16* __restrict__ wq, const bf16* __restrict__ wo, const bf16* __restrict__ wr,
                                                        const bf16* __restrict__ wg, const bf16* __restrict__ wu, const bf16* __restrict__ wd,
                                                        float* ws, bf16* __restrict__ out) {
  constexpr int WA = 128 * 2 + 16, GA = DM * 2 + 16, DA = FF * 2 + 16;
  __shared__ __attribute__((aligned(16))) char s_A[8 * DA];
  __shared__ __attribute__((aligned(16))) float s_red[6 * 256];
  __shared__ __attribute__((aligned(16))) float s_h[2 * DM];
  __shared__ __attribute__((aligned(16))) float s_buf[NWIN * NW * DH];
  __shared__ __attribute__((aligned(16))) float s_sc[NGRP * 64];
  __shared__ __attribute__((aligned(16))) float s_q[NWIN * DH];
  __shared__ float s_m[NWIN * NW], s_l[NWIN * NW];
  __shared__ float s_sig[2];
  __shared__ int s_qok, s_pcnt, s_ecnt, s_fcnt, s_icnt, s_org;
  __shared__ int s_prog[NW];
  __shared__ float s_stat[4];

  const int tid = threadIdx.x;
  int lane = tid & 63, r = lane >> 3, j = lane & 7, mg = lane >> 4, mc = lane & 15;
  int wave = __builtin_amdgcn_readfirstlane(tid >> 6), bid = blockIdx.x;
  const unsigned t_start = (unsigned)__builtin_amdgcn_s_memrealtime();
  int wid = XMAP == 0 ? bid : (XMAP == 1 ? (bid & 7) * 32 + (bid >> 3) : (XMAP == 2 ? (bid >> 5) * 32 + (bid & 7) * 4 + ((bid >> 3) & 3) : (bid >> 7) * 128 + (bid & 7) * 16 + ((bid >> 3) & 15)));
  const bool prod = bid < NQP;
  const __amdgpu_buffer_rsrc_t rs_ws = __builtin_amdgcn_make_buffer_rsrc(ws, 0, WS_END * 4, 0x00020000);
  const __amdgpu_buffer_rsrc_t rs_k = __builtin_amdgcn_make_buffer_rsrc(const_cast<bf16*>(kc), 0, NB * NH * SK * DH * 2, 0x00020000);
  const __amdgpu_buffer_rsrc_t rs_v = __builtin_amdgcn_make_buffer_rsrc(const_cast<bf16*>(vc), 0, NB * NH * SK * DH * 2, 0x00020000);
  const int voff = r * (DH * 2) + j * 16;
  const int c1 = wid % 3 == 0 ? 22 : 21, c2 = wid % 3 == 1 ? 42 : 43;
  auto seg_of = [&](int n) { return (n * 8 + wave >= c1) + (n * 8 + wave >= c2); };
  auto gload = [&](Slot (&sl)[NBUF], __amdgpu_buffer_rsrc_t rs, int n) {
    const int sn = seg_of(n), sg = wid + NWG * sn, row = sg / NSEG, k = sg - row * NSEG;
    const int off = (row * (SK / 64) + k * 21 + (k + 2) / 3 + n * 8 + wave - (sn == 0 ? 0 : (sn == 1 ? c1 : c2))) * (64 * DH * 2);
#pragma unroll
    for (int u = 0; u < NBUF; ++u) {
#pragma unroll
      for (int i = 0; i < 3; ++i) sl[u].a[i] = __builtin_amdgcn_raw_buffer_load_b128(rs, voff + i * 128, off + 2 * u * 3072, AUX_NT);
      MEMFENCE();
#pragma unroll
      for (int i = 0; i < 3; ++i) sl[u].b[i] = __builtin_amdgcn_raw_buffer_load_b128(rs, voff + i * 128, off + (2 * u + 1) * 3072, AUX_NT);
      MEMFENCE();
    }
  };
  unsigned hs_nc = 0u, hs_ep = 0u;
  auto look_at_p = [&]() {
    if (bid == NWG - 1) {
      u32x4 hp[2], hc[2];
#pragma unroll
      for (int i = 0; i < 2; ++i) {
        hp[i] = __builtin_amdgcn_raw_buffer_load_b128(rs_ws, (WS_HP + lane * 8 + i * 4) * 4, 0, AUX_SC1);
        const u32x2 c0 = __builtin_amdgcn_raw_buffer_load_b64(rs_ws, (WS_HC + (lane * 4 + i * 2) * LSTR) * 4, 0, AUX_SC1);
        const u32x2 c1_ = __builtin_amdgcn_raw_buffer_load_b64(rs_ws, (WS_HC + (lane * 4 + i * 2 + 1) * LSTR) * 4, 0, AUX_SC1);
        hc[i] = u32x4{c0[0], c0[1], c1_[0], c1_[1]};
      }
      if (__all(hc[0][1] == hp[0][0] && hc[0][3] == hp[0][2] && hc[1][1] == hp[1][0] && hc[1][3] == hp[1][2]) &&
          __any(hc[0][0] != hp[0][1] || hc[0][2] != hp[0][3] || hc[1][0] != hp[1][1] || hc[1][2] != hp[1][3])) {
#pragma unroll
        for (int i = 0; i < 2; ++i)
          __builtin_amdgcn_raw_buffer_store_b128(u32x4{hp[i][0], hc[i][0], hp[i][2], hc[i][2]}, rs_ws, (WS_HP + lane * 8 + i * 4) * 4, 0, AUX_SC1);
      }
    }
    const u32x2 pw = __builtin_amdgcn_raw_buffer_load_b64(rs_ws, (WS_HP + bid * 2) * 4, 0, AUX_SC1);
    const unsigned np = __builtin_amdgcn_readfirstlane(pw[0]), ec = __builtin_amdgcn_readfirstlane(pw[1]);
    if (np != hs_ep) { hs_ep = np; __builtin_amdgcn_raw_buffer_store_b64(u32x2{hs_nc, hs_ep}, rs_ws, lane == 0 ? (WS_HC + bid * LSTR) * 4 : OOB, 0, AUX_SC1); }
    return ec == hs_nc;
  };
  if (tid < NW) s_prog[tid] = 1;
  if (tid == 0) { s_qok = 0; s_pcnt = 0; s_ecnt = 0; s_fcnt = 0; s_icnt = 0; s_org = (int)(t_start + POFF); }
  if (wave == SVC) {
    __builtin_amdgcn_s_setprio(3);
    int iofs[NWIN];
#pragma unroll
    for (int s = 0; s < NWIN; ++s) iofs[s] = (WS_QI + ((wid + NWG * s) / NSEG * DH + lane) * 2) * 4;
    const int oofs = prod ? (WS_QI + (bid * 64 + lane) * 2) * 4 : OOB;
    u32x2 in[NWIN * 3];
    unsigned ot[NB], ce[4];
#pragma unroll
    for (int e = 0; e < NWIN * 3; ++e) in[e] = __builtin_amdgcn_raw_buffer_load_b64(rs_ws, iofs[e / 3], (e % 3) * 512, AUX_SC1);
#pragma unroll
    for (int bb = 0; bb < NB; ++bb) ot[bb] = __builtin_amdgcn_raw_buffer_load_b64(rs_ws, oofs, bb * (HD * 8), AUX_SC1)[1];
    u32x4 xr[5], hp[2];
#pragma unroll
    for (int i = 0; i < 5; ++i) xr[i] = reinterpret_cast<const u32x4*>(x)[lane * 5 + i];
#pragma unroll
    for (int i = 0; i < 2; ++i) hp[i] = __builtin_amdgcn_raw_buffer_load_b128(rs_ws, bid == NWG - 1 ? (WS_HP + lane * 8 + i * 4) * 4 : OOB, 0, AUX_SC1);
#pragma unroll
    for (int i = 0; i < 4; ++i) ce[i] = __builtin_amdgcn_raw_buffer_load_b32(rs_ws, bid == NWG - 1 ? (WS_HC + (lane * 4 + i) * LSTR + 1) * 4 : OOB, 0, AUX_SC1);
    {
      const u32x4 z4 = {0u, 0u, 0u, 0u};
#pragma unroll
      for (int i = 0; i < (ZPW + 255) / 256; ++i)
        __builtin_amdgcn_raw_buffer_store_b128(z4, rs_ws, i * 256 + lane * 4 < ZPW ? (WS_Z0 + bid * ZPW + i * 256 + lane * 4) * 4 : OOB, 0, AUX_SC1);
    }
    MEMFENCE();
    __syncthreads();
    MEMFENCE();
    if (bid == NWG - 1) {
#pragma unroll
      for (int i = 0; i < 2; ++i) {
#pragma unroll
        for (int e = 0; e < 2; ++e) { unsigned np = hp[i][2 * e] + 1u; if (np == ce[2 * i + e]) np += 1u; hp[i][2 * e] = np; }
        __builtin_amdgcn_raw_buffer_store_b128(hp[i], rs_ws, (WS_HP + lane * 8 + i * 4) * 4, 0, AUX_SC1);
      }
    }
    unsigned want[NWIN * 3];
#pragma unroll
    for (int e = 0; e < NWIN * 3; ++e) want[e] = fresh_tag(in[e][1]);
    if (prod) {
      float xs = 0.f;
#pragma unroll
      for (int i = 0; i < 20; ++i) xs += bf_lo(xr[i >> 2][i & 3]) + bf_hi(xr[i >> 2][i & 3]);
      const float mean = row16_sum(xs) * (1.0f / DM);
      float xv = 0.f;
#pragma unroll
      for (int i = 0; i < 20; ++i) { const float a = bf_lo(xr[i >> 2][i & 3]) - mean, c = bf_hi(xr[i >> 2][i & 3]) - mean; xv += a * a + c * c; }
      const float rstd = QSCALE / sqrtf(row16_sum(xv) * (1.0f / DM) + LN_EPS);
#pragma unroll
      for (int bb = 0; bb < NB; ++bb) ot[bb] = fresh_tag(ot[bb]);
      for (unsigned spin = 0; spin < (SPIN_CAP << 4) && lds_get(&s_pcnt) < NQW; ++spin) __builtin_amdgcn_s_sleep(1);
      __builtin_amdgcn_fence(__ATOMIC_ACQUIRE, "workgroup");
      float pr[5];
#pragma unroll
      for (int i = 0; i < 5; ++i) {
        pr[i] = 0.f;
#pragma unroll
        for (int k = 0; k < NQW; ++k) pr[i] += s_buf[2 * k * 320 + i * 64 + lane] + s_buf[(2 * k + 1) * 320 + i * 64 + lane];
      }
#pragma unroll
      for (int bb = 0; bb < NB; ++bb) {
        const float qv = (pr[bb] - __int_as_float(__builtin_amdgcn_readlane(__float_as_int(mean), bb * 16)) * pr[4]) *
                         __int_as_float(__builtin_amdgcn_readlane(__float_as_int(rstd), bb * 16));
        __builtin_amdgcn_raw_buffer_store_b64(u32x2{__float_as_uint(qv), ot[bb]}, rs_ws, oofs, bb * (HD * 8), AUX_SC1);
      }
    } else {
      __builtin_amdgcn_s_sleep(QWAIT);
    }
    {
      const u32x2 pw = __builtin_amdgcn_raw_buffer_load_b64(rs_ws, (WS_HP + bid * 2) * 4, 0, AUX_SC1);
      DRAIN();
      hs_ep = __builtin_amdgcn_readfirstlane(pw[0]);
      hs_nc = __builtin_amdgcn_readfirstlane(pw[1]) + 1u;
      __builtin_amdgcn_raw_buffer_store_b64(u32x2{hs_nc, hs_ep}, rs_ws, lane == 0 ? (WS_HC + bid * LSTR) * 4 : OOB, 0, AUX_SC1);
    }
    bool all_out = false;
#pragma unroll 1
    for (unsigned spin = 0; spin < SPIN_CAP; ++spin) {
#pragma unroll
      for (int e = 0; e < NWIN * 3; ++e) in[e] = __builtin_amdgcn_raw_buffer_load_b64(rs_ws, iofs[e / 3], (e % 3) * 512, AUX_SC1);
      bool ok = true;
#pragma unroll
      for (int e = 0; e < NWIN * 3; ++e) ok = ok && in[e][1] == want[e];
      if (all_out || __all(ok)) break;
      if ((spin & 3) == 3) all_out = look_at_p();
      __builtin_amdgcn_s_sleep(QSLEEP);
    }
#pragma unroll
    for (int e = 0; e < NWIN * 3; ++e) s_q[e * 64 + lane] = __uint_as_float(in[e][0]);
    __builtin_amdgcn_fence(__ATOMIC_RELEASE, "workgroup");
    lds_put(&s_qok, 1);
    __builtin_amdgcn_s_setprio(0);
  } else if (prod && wave >= QW0 && wave < QW0 + NQW) {
    const int k0 = (wave - QW0) * (QST * 64) + (mc >> 3) * 32 + mg * 8;
    u32x4 raw[QST * 8], xa[QST];
    const bf16* wb = wq + (size_t)k0 * HD + bid * 64 + (mc & 7) * 8;
#pragma unroll
    for (int st = 0; st < QST; ++st) xa[st] = *reinterpret_cast<const u32x4*>(x + (mc & 3) * DM + k0 + st * 64);
#pragma unroll
    for (int e = 0; e < QST * 8; ++e) raw[e] = *reinterpret_cast<const u32x4*>(wb + (size_t)((e >> 3) * 64 + (e & 7)) * HD);
    MEMFENCE();
    __syncthreads();
    MEMFENCE();
    f32x4 ac[8];
#pragma unroll
    for (int t = 0; t < 8; ++t) ac[t] = f32x4{0.f, 0.f, 0.f, 0.f};
#pragma unroll
    for (int st = 0; st < QST; ++st) {
      const u32x4 a = (mc & 4) == 0 ? xa[st] : u32x4{0x3f803f80u, 0x3f803f80u, 0x3f803f80u, 0x3f803f80u};
#pragma unroll
      for (int c = 0; c < 4; ++c) {
        u32x4 f0, f1;
#pragma unroll
        for (int i = 0; i < 4; ++i) { f0[i] = pk_lo(raw[st * 8 + 2 * i][c], raw[st * 8 + 2 * i + 1][c]); f1[i] = pk_hi(raw[st * 8 + 2 * i][c], raw[st * 8 + 2 * i + 1][c]); }
        ac[2 * c] = mfma16(a, f0, ac[2 * c]);
        ac[2 * c + 1] = mfma16(a, f1, ac[2 * c + 1]);
      }
    }
    if ((mg >> 1) == (mc >> 3)) {
      float* const dst = &s_buf[((wave - QW0) * 2 + (mg >> 1)) * 320 + (mc & 7) * 8];
#pragma unroll
      for (int bb = 0; bb < NB; ++bb)
        if (bb == 0 || (mg & 1) == 0) {
          *reinterpret_cast<f32x4*>(dst + ((mg & 1) * 4 + bb) * 64) = f32x4{ac[0][bb], ac[1][bb], ac[2][bb], ac[3][bb]};
          *reinterpret_cast<f32x4*>(dst + ((mg & 1) * 4 + bb) * 64 + 4) = f32x4{ac[4][bb], ac[5][bb], ac[6][bb], ac[7][bb]};
        }
    }
    __builtin_amdgcn_fence(__ATOMIC_RELEASE, "workgroup");
    if (lane == 0) __hip_atomic_fetch_add(&s_pcnt, 1, __ATOMIC_RELAXED, __HIP_MEMORY_SCOPE_WORKGROUP);
  } else {
    __syncthreads();
  }
  MEMFENCE();
  u32x2 rep = {0u, 0u};
  unsigned t_thru = 0u;
  auto pace = [&](int g) {
    if (wave == 0 && g > AFROM) {
      const int org = lds_get(&s_org);
      const bool ok = rep[0] - t_start + 1000u < 6000u && rep[1] - t_start + 1000u < 6000u;
      const float dorg = row16_sum(ok ? (float)(int)(rep[1] - (unsigned)org) : 0.f);
      const float lt = ok ? (float)min((int)(rep[0] - rep[1]), LCAP) : (float)LCAP;
      const float lx = fminf(lt, dpp<DPP_ROR8>(lt));
      float hi = fmaxf(lx, dpp<DPP_XOR1>(lx)); hi = fmaxf(hi, dpp<DPP_XOR2>(hi)); hi = fmaxf(hi, dpp<DPP_HMIRROR>(hi));
      const float sig = row16_sum(lt) * ((1.f - AMAX / 100.f) / 16.f) + hi * (AMAX / 100.f);
      if (lane == 0) lds_put(&s_org, org + (int)(dorg * (1.f / 16.f) + fmaxf((sig - LTGT) * (ADAPT / 100.f), g > EFROM ? (float)ESL : -(float)DOWN)));
    }
    const unsigned rel = (unsigned)(g * PTAU + wave * (PTAU / 8) + min(g * 8 + wave, 16) * (FFWD / 16) - FFWD + (((bid >> 3) * JMUL + (bid & 7) * JX) & 31) * PJIT / 32);
    unsigned now = (unsigned)__builtin_amdgcn_s_memrealtime();
    if (wave == 1 && (bid >> 4) == 7 && g >= 2)
      __builtin_amdgcn_raw_buffer_store_b64(u32x2{t_thru - rel + PTAU, (unsigned)lds_get(&s_org)}, rs_ws, lane == 0 ? (WS_REP + ((g - 2) * 16 + (bid & 15)) * LSTR) * 4 : OOB, 0, AUX_SC1);
    for (unsigned spin = 0; spin < 4096 && (int)(now - rel - (unsigned)__builtin_amdgcn_readfirstlane(lds_get(&s_org))) < 0; ++spin) {
      __builtin_amdgcn_s_sleep(2);
      now = (unsigned)__builtin_amdgcn_s_memrealtime();
    }
    if (g >= 2) rep = __builtin_amdgcn_raw_buffer_load_b64(rs_ws, (wave == 0 && lane < 16 && g >= AFROM) ? (WS_REP + ((g - 2) * 16 + lane) * LSTR) * 4 : OOB, 0, 0);
    if (lane == 0) lds_put(&s_prog[wave], g);
    int pp = __builtin_amdgcn_readfirstlane(lds_get(&s_prog[wave ^ (NW / 2)]));
    for (unsigned spin = 0; spin < 4096 && g > pp + 1; ++spin) { __builtin_amdgcn_s_sleep(4); pp = __builtin_amdgcn_readfirstlane(lds_get(&s_prog[wave ^ (NW / 2)])); }
    if (g > pp) __builtin_amdgcn_s_setprio(0); else __builtin_amdgcn_s_setprio(1);
  };
  float gmx = -INFINITY;
  auto k_group = [&](const Slot (&sl)[NBUF], int n, int s) {
    f32x2 q[12];
#pragma unroll
    for (int i = 0; i < 6; ++i) {
      const f32x4 a = *reinterpret_cast<const f32x4*>(&s_q[s * DH + (i >> 1) * 64 + j * 8 + (i & 1) * 4]);
      q[i * 2] = f32x2{a[0], a[1]}; q[i * 2 + 1] = f32x2{a[2], a[3]};
    }
    float* const sc = &s_sc[(wave * NT + n) * 64 + r * 8];
    MEMFENCE();
#pragma unroll
    for (int u = 0; u < NBUF; ++u) {
      k_tile(sl[u].a, q, gmx, sc + 2 * u, j);
      k_tile(sl[u].b, q, gmx, sc + 2 * u + 1, j);
      MEMFENCE();
    }
    t_thru = (unsigned)__builtin_amdgcn_s_memrealtime();
  };
  {
    Slot b0[NBUF], b1[NBUF];
    pace(0);
    gload(b0, rs_k, 0);
    pace(1);
    gload(b1, rs_k, 1);
    for (unsigned spin = 0; spin < (SPIN_CAP << 4) && lds_get(&s_qok) == 0; ++spin) __builtin_amdgcn_s_sleep(2);
    __builtin_amdgcn_fence(__ATOMIC_ACQUIRE, "workgroup");
    k_group(b0, 0, 0);
    k_group(b1, 1, 0);
  }
#pragma unroll 1
  for (int pass = 1 - ((0x60 >> wave) & 1); pass < 2; ++pass) {
  int real = pass;
  asm volatile("" : "+s"(real));
  asm volatile("" : "+s"(wave), "+s"(bid), "+s"(wid), "+v"(lane));
  r = lane >> 3; j = lane & 7;
  if (real) {
  Slot buf[NBUF];
  int seg = 0;
  for (int n = 2; n < NT; ++n) {
    pace(n);
    gload(buf, rs_k, n);
    if (seg_of(n) != seg) {
      const float gm = max_r(gmx);
      if (lane == 0) s_m[seg * NW + wave] = gm;
      gmx = -INFINITY;
      ++seg;
    }
    k_group(buf, n, seg);
    if (wave == SVC && (n == 2 || (bid == NWG - 1 && n >= 3))) look_at_p();
  }
  {
    const float gm = max_r(gmx);
    if (lane == 0) s_m[seg * NW + wave] = gm;
  }
  float l = 0.f;
  f32x2 acc[12];
#pragma unroll
  for (int i = 0; i < 12; ++i) acc[i] = f32x2{0.f, 0.f};
  __builtin_amdgcn_fence(__ATOMIC_RELEASE, "wavefront"); __builtin_amdgcn_wave_barrier(); __builtin_amdgcn_fence(__ATOMIC_ACQUIRE, "wavefront");
  for (int n = 0; n < NT; ++n) {
    pace(NT + n);
    gload(buf, rs_v, n);
    if (n == NT - 1) {
      if (lane == 0) __hip_atomic_fetch_add(&s_icnt, 1, __ATOMIC_RELAXED, __HIP_MEMORY_SCOPE_WORKGROUP);
      __builtin_amdgcn_s_setprio(3);
    }
    seg = seg_of(n);
    {
      float p[8];
      const float m = s_m[seg * NW + wave];
      const float* const sc = &s_sc[(wave * NT + n) * 64 + r * 8];
      const f32x4 sa = *reinterpret_cast<const f32x4*>(sc), sb = *reinterpret_cast<const f32x4*>(sc + 4);
#pragma unroll
      for (int t = 0; t < 4; ++t) { p[t] = __builtin_amdgcn_exp2f(sa[t] - m); p[4 + t] = __builtin_amdgcn_exp2f(sb[t] - m); }
      l += ((p[0] + p[1]) + (p[2] + p[3])) + ((p[4] + p[5]) + (p[6] + p[7]));
      MEMFENCE();
#pragma unroll
      for (int u = 0; u < NBUF; ++u) {
        v_tile(buf[u].a, p[2 * u], acc);
        v_tile(buf[u].b, p[2 * u + 1], acc);
        MEMFENCE();
      }
    }
    t_thru = (unsigned)__builtin_amdgcn_s_memrealtime();
    if (wave == SVC && n == 1)
      for (unsigned spin = 0; spin < SPIN_CAP && !look_at_p(); ++spin) __builtin_amdgcn_s_sleep(4);
    if (n == NT - 1 || seg_of(n + 1) != seg) {
      const float lw = sum_r(l);
      float t12[12], t6[6];
#pragma unroll
      for (int e = 0; e < 12; ++e) t12[e] = tsum32(acc[e >> 1][e & 1], acc[6 + (e >> 1)][e & 1]);
#pragma unroll
      for (int e = 0; e < 6; ++e) { t6[e] = tsum16(t12[e], t12[e + 6]); t6[e] += dpp<DPP_ROR8>(t6[e]); }
      if ((lane & 8) == 0) {
#pragma unroll
        for (int e = 0; e < 3; ++e) *reinterpret_cast<f32x2*>(&s_buf[(seg * NW + wave) * DH + j * 24 + (lane >> 4) * 6 + e * 2]) = f32x2{t6[2 * e], t6[2 * e + 1]};
      }
      if (lane == 0) s_l[seg * NW + wave] = lw;
      l = 0.f;
#pragma unroll
      for (int i = 0; i < 12; ++i) acc[i] = f32x2{0.f, 0.f};
      if (seg != 0) {
        __builtin_amdgcn_fence(__ATOMIC_RELEASE, "workgroup");
        int c = 0;
        if (lane == 0) c = __hip_atomic_fetch_add(seg == 1 ? &s_ecnt : &s_fcnt, 1, __ATOMIC_RELAXED, __HIP_MEMORY_SCOPE_WORKGROUP);
        if (__builtin_amdgcn_readfirstlane(c) == NW - 1) {
          __builtin_amdgcn_fence(__ATOMIC_ACQUIRE, "workgroup");
#pragma unroll 1
          for (int s = seg & 2; s <= seg; ++s) {
            float M = s_m[s * NW];
#pragma unroll
            for (int w = 1; w < NW; ++w) M = fmaxf(M, s_m[s * NW + w]);
            float L = 0.f;
            f32x4 o = {0.f, 0.f, 0.f, 0.f};
            const int d4 = lane < DH / 4 ? ((lane >> 1) & 7) * 24 + (lane >> 4) * 8 + (lane & 1) * 4 : 0;
#pragma unroll
            for (int w = 0; w < NW; ++w) {
              const float e = __builtin_amdgcn_exp2f(s_m[s * NW + w] - M);
              L = fmaf(e, s_l[s * NW + w], L);
              o += e * *reinterpret_cast<const f32x4*>(&s_buf[(s * NW + w) * DH + d4]);
            }
            if (lane >= DH / 4) o = f32x4{M, L, 1.f, 1.f};
            __builtin_amdgcn_raw_buffer_store_b128(u32x4{nz_bits(o[0]), nz_bits(o[1]), nz_bits(o[2]), nz_bits(o[3])}, rs_ws,
                                                   lane < REC / 4 ? (WS_PO + (wid + NWG * s) * REC + lane * 4) * 4 : OOB, 0, AUX_SC1);
          }
          if (seg == 2) { MEMFENCE(); lds_put(&s_fcnt, NW + 1); }
          if (seg == 1) {
            DRAIN();
            __builtin_amdgcn_raw_buffer_store_b32(1u, rs_ws, lane == 0 ? ef_ofs(wid) : OOB, 0, AUX_SC1);
          }
        }
      }
      MEMFENCE();
    }
  }
  __builtin_amdgcn_s_setprio(0);
  }
  {
    asm volatile("" : "+s"(wave), "+s"(bid), "+v"(lane));
    r = lane >> 3; j = lane & 7; mg = lane >> 4; mc = lane & 15;
    const int w = real ? wave : (wave - 5) * 7, td = w * 64 + lane;
    const int xcd = bid & 7, ri = bid >> 3;
    const bool isW = ri < 15, isG = !isW && ri < 27;
    const int tt = isW ? xcd * 15 + ri : (isG ? xcd * 12 + ri - 15 : xcd * 5 + ri - 27);
    const int bp = tt & 1, ct = tt >> 1;
    const int ks1 = tt / W_CT, ct1 = tt - ks1 * W_CT;
    const int nmw = isW ? 4 : (isG ? 5 : 6);
    const __amdgpu_buffer_rsrc_t rs_out = __builtin_amdgcn_make_buffer_rsrc(out, 0, NB * DM * 2, 0x00020000);
    u32x4 fr[16];
    u32x2 raw[32];
    auto weights = [&]() {
#pragma unroll
      for (int i = 0; i < 32; ++i) raw[i] = u32x2{0u, 0u};
      if (real && w < nmw) {
        int* const cnt = (isW || isG) ? &s_icnt : &s_fcnt;
        for (unsigned spin = 0; spin < (SPIN_CAP << 4) && lds_get(cnt) < NW; ++spin) __builtin_amdgcn_s_sleep(1);
        if (isW || isG) {
          const int nsl = isW ? WDELAY : GDELAY;
          for (int i = 0; i < nsl && lds_get(&s_fcnt) <= NW; ++i) __builtin_amdgcn_s_sleep(3);
        } else {
          __builtin_amdgcn_s_sleep(DDELAY);
        }
        typedef const __attribute__((address_space(1))) char* gptr;
        gptr wb = (gptr)(isW ? wo + (size_t)(ks1 * K3_ROWS + w * 128 + mg * 8) * DM + ct1 * 64 + mc * 4
                             : (isG ? (mc < 8 ? wg : wu) + (size_t)(w * 128 + mg * 8) * FF : wd + (size_t)(w * 256 + (mc >> 3) * 32 + mg * 8) * DM) + ct * 32 + (mc & 7) * 4);
        const int rstr = (isG ? FF : DM) * 2, sstr = (isW || isG ? 25 : 57) * rstr;
#pragma unroll
        for (int e = 0; e < 32; ++e) {
          raw[e] = *(const __attribute__((address_space(1))) u32x2*)wb;
          wb += (e & 7) == 7 ? sstr : rstr;
          asm volatile("" : "+v"(wb));
        }
        MEMFENCE();
      }
    };
    auto frags = [&]() {
      PIN8(raw, 0); PIN8(raw, 8); PIN8(raw, 16); PIN8(raw, 24);
#pragma unroll
      for (int st = 0; st < 4; ++st)
#pragma unroll
        for (int i = 0; i < 4; ++i) {
          const u32x2 r0 = raw[st * 8 + 2 * i], r1 = raw[st * 8 + 2 * i + 1];
          fr[st * 4 + 0][i] = pk_lo(r0[0], r1[0]); fr[st * 4 + 1][i] = pk_hi(r0[0], r1[0]);
          fr[st * 4 + 2][i] = pk_lo(r0[1], r1[1]); fr[st * 4 + 3][i] = pk_hi(r0[1], r1[1]);
        }
      PIN8(fr, 0); PIN8(fr, 8);
    };
    if (isW) {
      if (w >= 4) { if (real) break; else continue; }
      char* const img = s_A + w * (16 * WA);
      {
        int offp[2], ne[2], sg0[2];
#pragma unroll
        for (int hf = 0; hf < 2; ++hf) {
          const int row = ks1 * K3_ROWS + w * 128 + hf * 64, hh = row / DH;
          sg0[hf] = (mg * NH + hh) * NSEG;
          offp[hf] = (WS_PO + sg0[hf] * REC + row - hh * DH + mc * 4) * 4;
          ne[hf] = min(max(2 * NWG - sg0[hf], 0), NSEG);
        }
        const int sgm = (mc < 8 ? sg0[0] : sg0[1]) + j;
        const unsigned emask = (1u << ne[0]) - 1u + (((1u << ne[1]) - 1u) << NSEG) + (sgm < 2 * NWG ? 1u << (2 * NSEG) : 0u);
        u32x4 pv[2 * NSEG];
        u32x2 ml = {0u, 0u};
#pragma unroll
        for (int k = 0; k < 2 * NSEG; ++k) pv[k] = u32x4{0u, 0u, 0u, 0u};
        unsigned pend = real ? (1u << (2 * NSEG)) - 1u + (j < NSEG ? 1u << (2 * NSEG) : 0u) : 0u;
        auto look = [&](unsigned go) {
          if (go & (1u << (2 * NSEG))) ml = __builtin_amdgcn_raw_buffer_load_b64(rs_ws, (WS_PO + sgm * REC + DH) * 4, 0, AUX_SC1);
#pragma unroll
          for (int k = 0; k < 2 * NSEG; ++k)
            if (go & (1u << k)) pv[k] = __builtin_amdgcn_raw_buffer_load_b128(rs_ws, offp[k / NSEG], (k % NSEG) * REC * 4, AUX_SC1);
        };
        for (unsigned spin = 0; spin < SPIN_CAP && real; ++spin) {
          const bool fl = j < NSEG && sgm < 2 * NWG;
          if (__all(!fl || __builtin_amdgcn_raw_buffer_load_b32(rs_ws, fl ? ef_ofs(sgm & (NWG - 1)) : OOB, 0, AUX_SC1) != 0u)) break;
          __builtin_amdgcn_s_sleep(8);
        }
        weights();
        if (EARLYG) { look(pend & emask); pend &= ~emask; }
        for (unsigned sp2 = 0; sp2 < (SPIN_CAP << 4) && real && lds_get(&s_fcnt) < WSTART; ++sp2) __builtin_amdgcn_s_sleep(1);
        if (WLOOK) __builtin_amdgcn_s_sleep(WLOOK);
        look(pend);
        MEMFENCE();
        frags();
#pragma unroll 1
        for (unsigned spin = 0; spin < SPIN_CAP; ++spin) {
          unsigned got = ml[0] != 0u && ml[1] != 0u;
#pragma unroll
          for (int k = 2 * NSEG - 1; k >= 0; --k) got = got * 2u + (unsigned)all_nz(pv[k]);
          pend &= ~got;
          if (!__any(pend != 0u)) break;
          if (spin >= 8) __builtin_amdgcn_s_sleep(WSLEEP);
          look(pend);
        }
        const float mm = j < NSEG ? __uint_as_float(ml[0]) : -INFINITY;
        float M = fmaxf(mm, dpp<DPP_XOR1>(mm)); M = fmaxf(M, dpp<DPP_XOR2>(M)); M = fmaxf(M, dpp<DPP_HMIRROR>(M));
        const float e = __builtin_amdgcn_exp2f(mm - M);
        float L = e * __uint_as_float(ml[1]);
        L += dpp<DPP_XOR1>(L); L += dpp<DPP_XOR2>(L); L += dpp<DPP_HMIRROR>(L);
        const float wt = e / L;
        f32x4 o0 = dpp<DPP_BCAST + 0>(wt) * __builtin_bit_cast(f32x4, pv[0]), o1 = dpp<DPP_BCAST + 8>(wt) * __builtin_bit_cast(f32x4, pv[6]);
        o0 += dpp<DPP_BCAST + 1>(wt) * __builtin_bit_cast(f32x4, pv[1]); o1 += dpp<DPP_BCAST + 9>(wt) * __builtin_bit_cast(f32x4, pv[7]);
        o0 += dpp<DPP_BCAST + 2>(wt) * __builtin_bit_cast(f32x4, pv[2]); o1 += dpp<DPP_BCAST + 10>(wt) * __builtin_bit_cast(f32x4, pv[8]);
        o0 += dpp<DPP_BCAST + 3>(wt) * __builtin_bit_cast(f32x4, pv[3]); o1 += dpp<DPP_BCAST + 11>(wt) * __builtin_bit_cast(f32x4, pv[9]);
        o0 += dpp<DPP_BCAST + 4>(wt) * __builtin_bit_cast(f32x4, pv[4]); o1 += dpp<DPP_BCAST + 12>(wt) * __builtin_bit_cast(f32x4, pv[10]);
        o0 += dpp<DPP_BCAST + 5>(wt) * __builtin_bit_cast(f32x4, pv[5]); o1 += dpp<DPP_BCAST + 13>(wt) * __builtin_bit_cast(f32x4, pv[11]);
        put_terms4(img + mg * 4 * WA + mc * 8, WA, o0);
        put_terms4(img + mg * 4 * WA + 128 + mc * 8, WA, o1);
      }
      __builtin_amdgcn_fence(__ATOMIC_RELEASE, "wavefront"); __builtin_amdgcn_wave_barrier(); __builtin_amdgcn_fence(__ATOMIC_ACQUIRE, "wavefront");
      f32x4 ac[4];
#pragma unroll
      for (int t = 0; t < 4; ++t) ac[t] = f32x4{0.f, 0.f, 0.f, 0.f};
#pragma unroll
      for (int st = 0; st < 4; ++st) {
        const u32x4 a = *reinterpret_cast<const u32x4*>(img + mc * WA + st * 64 + mg * 16);
#pragma unroll
        for (int t = 0; t < 4; ++t) ac[t] = mfma16(a, fr[st * 4 + t], ac[t]);
      }
      *reinterpret_cast<f32x4*>(&s_red[w * 256 + lane * 4]) = f32x4{sum3(ac[0]), sum3(ac[1]), sum3(ac[2]), sum3(ac[3])};
      if (real) __syncthreads();
      if (w == 0) {
        const f32x4 s0 = (*reinterpret_cast<const f32x4*>(&s_red[lane * 4]) + *reinterpret_cast<const f32x4*>(&s_red[256 + lane * 4])) +
                         (*reinterpret_cast<const f32x4*>(&s_red[512 + lane * 4]) + *reinterpret_cast<const f32x4*>(&s_red[768 + lane * 4]));
        __builtin_amdgcn_raw_buffer_store_b128(u32x4{nz_bits(s0[0]), nz_bits(s0[1]), nz_bits(s0[2]), nz_bits(s0[3])}, rs_ws,
                                               real ? (WS_SLAB + (ks1 * NB + mg) * DM + ct1 * 64 + mc * 4) * 4 : OOB, 0, AUX_SC1);
      }
    } else {
      uint2 xx = {0u, 0u};
      unsigned wr2[5] = {0u, 0u, 0u, 0u, 0u};
      if (real) {
        xx = *reinterpret_cast<const uint2*>(x + (size_t)bp * 2 * DM + (td < 2 * DM / 4 ? td : 0) * 4);
#pragma unroll
        for (int i = 0; i < 5; ++i) wr2[i] = reinterpret_cast<const unsigned*>(wr)[lane + 64 * i];
        unsigned one = 0x3f803f80u;
        asm volatile("" : "+v"(one));
        if (isG && td < DM / 8) *reinterpret_cast<u32x4*>(s_A + 3 * GA + td * 16) = u32x4{one, one, one, one};
      }
      weights();
      frags();
      PIN4(wr2, 0); asm volatile("" : "+v"(wr2[4]), "+v"(xx.x), "+v"(xx.y));
      if (real && td < 2 * DM / 4) {
        const int off = (WS_SLAB + bp * 2 * DM + td * 4) * 4;
        const int poff = (WS_SLAB + bp * 2 * DM + w * 256 + lane * (NB * DM)) * 4;
        u32x4 sl[K3_KS];
#pragma unroll
        for (int ks = 0; ks < K3_KS; ++ks) sl[ks] = u32x4{0u, 0u, 0u, 0u};
        unsigned pend = (1u << K3_KS) - 1u;
        for (unsigned spin = 0; spin < SPIN_CAP; ++spin) {
          unsigned go = pend;
          if (__builtin_popcount(pend) > PROBE_MIN) {
            const u32x4 pr = __builtin_amdgcn_raw_buffer_load_b128(rs_ws, (lane < K3_KS && ((pend >> lane) & 1u)) ? poff : OOB, 0, AUX_SC1);
            go = (unsigned)__ballot(all_nz(pr)) & pend;
          }
#pragma unroll
          for (int ks = 0; ks < K3_KS; ++ks)
            if (go & (1u << ks)) sl[ks] = __builtin_amdgcn_raw_buffer_load_b128(rs_ws, off, ks * (NB * DM * 4), AUX_SC1);
#pragma unroll
          for (int ks = 0; ks < K3_KS; ++ks)
            if ((go & (1u << ks)) && __all(all_nz(sl[ks]))) pend &= ~(1u << ks);
          if (pend == 0u) break;
          __builtin_amdgcn_s_sleep(1);
        }
        f32x4 hv = f32x4{bf_lo(xx.x), bf_hi(xx.x), bf_lo(xx.y), bf_hi(xx.y)};
#pragma unroll
        for (int ks = 0; ks < K3_KS; ++ks) hv += __builtin_bit_cast(f32x4, sl[ks]);
        *reinterpret_cast<f32x4*>(&s_h[td * 4]) = hv;
        if (isG) put_terms4(s_A + (td >= DM / 4 ? 4 * GA - DM * 2 : 0) + td * 8, GA, hv);
      }
      if (real) __syncthreads();
      if (w >= NW - 2) {
        const int rw = w - (NW - 2);
        const float piv = s_h[rw * DM];
        float hv[10], sm = 0.f, sq = 0.f;
#pragma unroll
        for (int i = 0; i < 5; ++i) {
          const float2 t2 = *reinterpret_cast<const float2*>(&s_h[rw * DM + 2 * lane + 128 * i]);
          hv[2 * i] = t2.x - piv; hv[2 * i + 1] = t2.y - piv;
          sm += hv[2 * i] + hv[2 * i + 1]; sq = fmaf(hv[2 * i], hv[2 * i], sq); sq = fmaf(hv[2 * i + 1], hv[2 * i + 1], sq);
        }
        const float dm = wave_sum(sm) * (1.0f / DM);
        const float var = fmaxf(fmaf(-dm, dm, wave_sum(sq) * (1.0f / DM)), 0.f);
        const float rstd = 1.0f / sqrtf(var + LN_EPS);
        if (lane == 0) { s_stat[rw * 2] = piv + dm; s_stat[rw * 2 + 1] = rstd; }
        if (!isG) {
          float rr = 0.f;
#pragma unroll
          for (int i = 0; i < 5; ++i) { rr = fmaf((hv[2 * i] - dm) * rstd, bf_lo(wr2[i]), rr); rr = fmaf((hv[2 * i + 1] - dm) * rstd, bf_hi(wr2[i]), rr); }
          rr = wave_sum(rr);
          if (lane == 0) s_sig[rw] = 1.0f / (1.0f + __expf(-rr));
        }
      } else if (!isG && real) {
        const int off = (WS_ACT + bp * 2 * FF + td * 4) * 4;
        u32x4 v0 = {0u, 0u, 0u, 0u}, v1 = v0;
        unsigned pend = 3u;
        for (unsigned spin = 0; spin < SPIN_CAP; ++spin) {
          if (pend & 1u) v0 = __builtin_amdgcn_raw_buffer_load_b128(rs_ws, off, 0, AUX_SC1);
          if (pend & 2u) v1 = __builtin_amdgcn_raw_buffer_load_b128(rs_ws, off, FF * 4, AUX_SC1);
          if ((pend & 1u) && all_nz(v0)) pend &= ~1u;
          if ((pend & 2u) && all_nz(v1)) pend &= ~2u;
          if (!__any(pend != 0u)) break;
          __builtin_amdgcn_s_sleep(1);
        }
        put_terms4(s_A + td * 8, DA, __builtin_bit_cast(f32x4, v0));
        put_terms4(s_A + 4 * DA + td * 8, DA, __builtin_bit_cast(f32x4, v1));
      }
      if (!isG && real) __syncthreads();
      if (w < nmw) {
        f32x4 ac[4];
#pragma unroll
        for (int t = 0; t < 4; ++t) ac[t] = f32x4{0.f, 0.f, 0.f, 0.f};
        const char* ap = s_A + (mc & 7) * (isG ? GA : DA) + (isG ? w * 4 : w * 8 + (mc >> 3)) * 64 + mg * 16;
        const int astep = isG ? 64 : 2 * 64;
#pragma unroll
        for (int st = 0; st < 4; ++st) {
          const u32x4 a = *reinterpret_cast<const u32x4*>(ap + st * astep);
#pragma unroll
          for (int t = 0; t < 4; ++t) ac[t] = mfma16(a, fr[st * 4 + t], ac[t]);
        }
        if (isG ? lane < 32 : (mc >> 3) == (mg >> 1))
          *reinterpret_cast<f32x4*>(&s_red[w * 256 + (isG ? lane * 4 : mg * 32 + (mc & 7) * 4)]) = f32x4{sum3(ac[0]), sum3(ac[1]), sum3(ac[2]), sum3(ac[3])};
        if (lane < 16) *reinterpret_cast<f32x4*>(&s_red[w * 256 + 128 + lane * 4]) = f32x4{ac[0][3], ac[1][3], ac[2][3], ac[3][3]};
      }
      if (real) __syncthreads();
      if (w == 0) {
        const int bb = lane >> 5, f = lane & 31;
        const int i0 = isG ? bb * 64 + f : lane, i1 = isG ? bb * 64 + 32 + f : 64 + lane;
        float s0 = 0.f, s1 = 0.f, c0 = 0.f, c1_ = 0.f;
#pragma unroll
        for (int k = 0; k < 5; ++k) { s0 += s_red[k * 256 + i0]; s1 += s_red[k * 256 + i1]; c0 += s_red[k * 256 + 128 + f]; c1_ += s_red[k * 256 + 160 + f]; }
        const float mean = s_stat[bb * 2], rstd = s_stat[bb * 2 + 1];
        if (isG) {
          const float gg = (s0 - mean * c0) * rstd, uu = (s1 - mean * c1_) * rstd;
          const float a = gg / (1.0f + __expf(-gg)) * uu;
          __builtin_amdgcn_raw_buffer_store_b32(nz_bits(a), rs_ws, real ? (WS_ACT + (bp * 2 + bb) * FF + ct * 32 + f) * 4 : OOB, 0, AUX_SC1);
        } else {
          s0 += s_red[5 * 256 + i0]; s1 += s_red[5 * 256 + i1];
          const float hh = s_h[bb * DM + ct * 32 + f];
          const bf16 ov = __float2bfloat16(hh + ((hh - mean) * rstd + (s0 + s1) * s_sig[bb]));
          __builtin_amdgcn_raw_buffer_store_b16(__builtin_bit_cast(unsigned short, ov), rs_out, real ? ((bp * 2 + bb) * DM + ct * 32 + f) * 2 : OOB, 0, 0);
        }
      }
    }
  }
  }
  if (ENDPIN && wave == 0) for (unsigned spin = 0; spin < 100000 && (int)((unsigned)__builtin_amdgcn_s_memrealtime() - t_start) < ENDPIN; ++spin) __builtin_amdgcn_s_sleep(1);
}

extern "C" void kernel_launch(void* const* d_in, const int* in_sizes, int n_in, void* d_out, int out_size, void* d_ws,
                              size_t ws_size, hipStream_t stream) {
  fused_kernel<<<NWG, NW * 64, 0, stream>>>((const bf16*)d_in[0], (const bf16*)d_in[1], (const bf16*)d_in[2], (const bf16*)d_in[3],
                                            (const bf16*)d_in[4], (const bf16*)d_in[6], (const bf16*)d_in[7], (const bf16*)d_in[8],
                                            (const bf16*)d_in[9], (float*)d_ws, (bf16*)d_out);
}
```

```cpp
#define ESL 6
#include <hip/hip_runtime.h>
#include <hip/hip_bf16.h>
#include <stdint.h>

typedef __hip_bfloat16 bf16;
using u32x4 = __attribute__((ext_vector_type(4))) unsigned;
using u32x2 = __attribute__((ext_vector_type(2))) unsigned;
using f32x4 = __attribute__((ext_vector_type(4))) float;
using f32x2 = __attribute__((ext_vector_type(2))) float;

constexpr int NB = 4, DM = 640, NH = 32, SK = 8192, DH = 192, HD = NH * DH, FF = 1536;
constexpr int NWG = 256, NSEG = 6, NWIN = NB * NH * NSEG / NWG;
constexpr float LN_EPS = 1e-5f;
constexpr float QSCALE = 0.07216878364870322f * 1.4426950408889634f;

constexpr int WS_HP = 0;
constexpr int REC = DH + 32;
constexpr int WS_PO = WS_HP + 2 * NWG;
constexpr int WS_Z0 = WS_PO + 2 * NWG * REC;
constexpr int K3_KS = 12, K3_ROWS = HD / K3_KS;
constexpr int WS_SLAB = WS_PO + NB * NH * NSEG * REC;
constexpr int WS_ACT = WS_SLAB + K3_KS * NB * DM;
constexpr int WS_BASE = WS_ACT + NB * FF;
constexpr int ZPW = 416;
constexpr int WS_QI = WS_Z0 + NWG * ZPW;
constexpr int LSTR = 64;
constexpr int WS_REP = WS_QI + NB * NH * DH * 2;
constexpr int WS_HC = WS_REP + 256 * LSTR;
constexpr int WS_END = WS_HC + NWG * LSTR;
constexpr int RSTR = LSTR;
static_assert(WS_BASE + NB * DM * 2 <= WS_QI && WS_Z0 % 64 == 0 && WS_PO % 32 == 0 && ZPW % 32 == 0 && NWIN == 3, "layout");
__device__ __forceinline__ int ef_ofs(int i) { return (WS_PO + (2 * NWG + i) * REC + DH + 2) * 4; }
constexpr int OOB = 0x7ffffff0;

__device__ __forceinline__ float bf_lo(unsigned u) { return __uint_as_float(u << 16); }
__device__ __forceinline__ float bf_hi(unsigned u) { return __uint_as_float(u & 0xffff0000u); }
__device__ __forceinline__ f32x2 bf_pair(unsigned u) { return f32x2{bf_lo(u), bf_hi(u)}; }
__device__ __forceinline__ f32x2 fma2(float a, f32x2 b, f32x2 c) { return __builtin_elementwise_fma(f32x2{a, a}, b, c); }
__device__ __forceinline__ f32x2 fma2(f32x2 a, f32x2 b, f32x2 c) { return __builtin_elementwise_fma(a, b, c); }

template <int CTRL> __device__ __forceinline__ float dpp(float x) {
  return __builtin_bit_cast(float, __builtin_amdgcn_mov_dpp(__builtin_bit_cast(int, x), CTRL, 0xf, 0xf, true));
}
constexpr int DPP_XOR1 = 0xB1, DPP_XOR2 = 0x4E, DPP_HMIRROR = 0x141;

__device__ __forceinline__ float sum_x16_32(float x) {
  auto s = __builtin_amdgcn_permlane16_swap(__float_as_uint(x), __float_as_uint(x), false, false);
  x = __uint_as_float(s[0]) + __uint_as_float(s[1]);
  auto t = __builtin_amdgcn_permlane32_swap(__float_as_uint(x), __float_as_uint(x), false, false);
  return __uint_as_float(t[0]) + __uint_as_float(t[1]);
}
__device__ __forceinline__ float max_x16_32(float x) {
  auto s = __builtin_amdgcn_permlane16_swap(__float_as_uint(x), __float_as_uint(x), false, false);
  x = fmaxf(__uint_as_float(s[0]), __uint_as_float(s[1]));
  auto t = __builtin_amdgcn_permlane32_swap(__float_as_uint(x), __float_as_uint(x), false, false);
  return fmaxf(__uint_as_float(t[0]), __uint_as_float(t[1]));
}
__device__ __forceinline__ float tsum32(float a, float b) {
  auto t = __builtin_amdgcn_permlane32_swap(__float_as_uint(a), __float_as_uint(b), false, false);
  return __uint_as_float(t[0]) + __uint_as_float(t[1]);
}
__device__ __forceinline__ float tsum16(float a, float b) {
  auto t = __builtin_amdgcn_permlane16_swap(__float_as_uint(a), __float_as_uint(b), false, false);
  return __uint_as_float(t[0]) + __uint_as_float(t[1]);
}
constexpr int DPP_ROR8 = 0x128, DPP_BCAST = 0x150;
__device__ __forceinline__ float sum_r(float v) { v += dpp<DPP_ROR8>(v); return sum_x16_32(v); }
__device__ __forceinline__ float max_r(float v) { v = fmaxf(v, dpp<DPP_ROR8>(v)); return max_x16_32(v); }
__device__ __forceinline__ float wave_sum(float v) {
  v += dpp<DPP_XOR1>(v); v += dpp<DPP_XOR2>(v); v += dpp<DPP_HMIRROR>(v);
  return sum_r(v);
}

__device__ __forceinline__ float row16_sum(float v) {
  v += dpp<DPP_XOR1>(v); v += dpp<DPP_XOR2>(v); v += dpp<DPP_HMIRROR>(v);
  return v + dpp<DPP_ROR8>(v);
}

struct Slot { u32x4 a[3]; u32x4 b[3]; };

#define PIN8(a, o) asm volatile("" : "+v"((a)[(o) + 0]), "+v"((a)[(o) + 1]), "+v"((a)[(o) + 2]), "+v"((a)[(o) + 3]), \
                                       "+v"((a)[(o) + 4]), "+v"((a)[(o) + 5]), "+v"((a)[(o) + 6]), "+v"((a)[(o) + 7]))
#define PIN4(a, o) asm volatile("" : "+v"((a)[(o) + 0]), "+v"((a)[(o) + 1]), "+v"((a)[(o) + 2]), "+v"((a)[(o) + 3]))
#define MEMFENCE() do { asm volatile("" ::: "memory"); __builtin_amdgcn_sched_barrier(0); } while (0)
#define DRAIN() asm volatile("s_waitcnt vmcnt(0)" ::: "memory")

__device__ __forceinline__ void k_tile(const u32x4 (&k)[3], const f32x2 (&q)[12], float& gmx, float* dst, int j) {
  f32x2 s0 = {0.f, 0.f}, s1 = s0, s2 = s0;
#pragma unroll
  for (int d = 0; d < 4; ++d) {
    s0 = fma2(bf_pair(k[0][d]), q[d], s0);
    s1 = fma2(bf_pair(k[1][d]), q[4 + d], s1);
    s2 = fma2(bf_pair(k[2][d]), q[8 + d], s2);
  }
  s0 = (s0 + s1) + s2;
  float s = s0[0] + s0[1];
  s += dpp<DPP_XOR1>(s);
  s += dpp<DPP_XOR2>(s);
  s += dpp<DPP_HMIRROR>(s);
  gmx = fmaxf(gmx, s);
  asm volatile("" : "+v"(gmx));
  if (j == 0) *dst = s;
}
__device__ __forceinline__ void v_tile(u32x4 (&v)[3], float p, f32x2 (&acc)[12]) {
#pragma unroll
  for (int i = 0; i < 3; ++i) {
    asm volatile("" : "+v"(v[i]), "+v"(p));
#pragma unroll
    for (int d = 0; d < 4; ++d) acc[i * 4 + d] = fma2(p, bf_pair(v[i][d]), acc[i * 4 + d]);
    PIN4(acc, i * 4);
  }
}

#define BURST 1
#define JW 65
#define SPREAD 3
#define PROBE_T 1
#define DLOOK 0
#define QNT 0
#define NEARLY 2
#define PLATE 1
#define ZLATE 1
#define QWAIT 32
#define QSLEEP 4
#define PTAU 668
#define POFF 100
#define PJIT (BURST * PTAU / 8)
#define ADAPT 60
#define AFROM 3
#define LCAP 600
#define LTGT 220
#define DOWN 30
#define WDELAY 4
#define GDELAY 4
#define DDELAY 64
#define WSLEEP 4
#define PROBE_MIN 4
#define ROT 0
#define EARLYG 1
#define WLOOK 0
#define LMODE 0
#define LLO 150
#define LHI 280
#define LSPR 50
#define TLBW 0
#define ILV 0
#define FFWD 96
#define XMAP 2
#define JMUL 1
#define JX 0
#define QW0 2
#define NREH 2
#define AMAX 0
#define EFROM 12
#define WSTART 8
#define ENDPIN 0
constexpr int AUX_SC1 = 16, AUX_NT = 2;
constexpr unsigned SPIN_CAP = 1u << 17;
__device__ __forceinline__ unsigned nz_bits(float v) { const unsigned u = __float_as_uint(v); return u == 0u ? 0x80000000u : u; }
__device__ __forceinline__ void lds_put(int* p, int v) { __hip_atomic_store(p, v, __ATOMIC_RELAXED, __HIP_MEMORY_SCOPE_WORKGROUP); }
__device__ __forceinline__ int lds_get(int* p) { return __hip_atomic_load(p, __ATOMIC_RELAXED, __HIP_MEMORY_SCOPE_WORKGROUP); }
using bf16x8 = __attribute__((ext_vector_type(8))) short;
__device__ __forceinline__ unsigned pk_lo(unsigned a, unsigned b) { return __builtin_amdgcn_perm(b, a, 0x05040100u); }
__device__ __forceinline__ unsigned pk_hi(unsigned a, unsigned b) { return __builtin_amdgcn_perm(b, a, 0x07060302u); }
__device__ __forceinline__ void split3(float v, unsigned& t0, unsigned& t1, unsigned& t2) {
  t0 = __float_as_uint(v) & 0xffff0000u;
  const float r1 = v - __uint_as_float(t0);
  t1 = __float_as_uint(r1) & 0xffff0000u;
  t2 = __float_as_uint(r1 - __uint_as_float(t1));
}
__device__ __forceinline__ f32x4 mfma16(u32x4 a, u32x4 b, f32x4 c) {
  return __builtin_amdgcn_mfma_f32_16x16x32_bf16(__builtin_bit_cast(bf16x8, a), __builtin_bit_cast(bf16x8, b), c, 0, 0, 0);
}
__device__ __forceinline__ void put_terms4(char* img, int rowbytes, f32x4 v) {
  unsigned t[3][4];
#pragma unroll
  for (int e = 0; e < 4; ++e) split3(v[e], t[0][e], t[1][e], t[2][e]);
#pragma unroll
  for (int k = 0; k < 3; ++k) *reinterpret_cast<u32x2*>(img + k * rowbytes) = u32x2{pk_hi(t[k][0], t[k][1]), pk_hi(t[k][2], t[k][3])};
}
__device__ __forceinline__ bool all_nz(u32x4 v) { return v[0] != 0u && v[1] != 0u && v[2] != 0u && v[3] != 0u; }

__device__ __forceinline__ float sum3(f32x4 v) { return (v[0] + v[1]) + v[2]; }

constexpr int NW = 8, NBUF = 4, NGRP = 64, NT = NGRP / NW;
constexpr int NQP = NH * DH / 64, NQW = 5, QST = DM / 64 / NQW;
constexpr int SVC = NW - 1;
constexpr int W_CT = DM / 64, G_TASKS = 2 * (FF / 32), W_TASKS = W_CT * K3_KS, D_TASKS = 2 * (DM / 32);
static_assert(G_TASKS + W_TASKS + D_TASKS == NWG && NW == 8 && K3_ROWS == 4 * 128 && NBUF == 4 && SK / 64 == 128, "shape");
__device__ __forceinline__ unsigned fresh_tag(unsigned tags) {
  unsigned t = __builtin_amdgcn_readfirstlane(tags);
  do ++t; while (__any(tags == t));
  return t;
}

__global__ void __launch_bounds__(NW * 64) fused_kernel(const bf16* __restrict__ x, const bf16* __restrict__ kc, const bf16* __restrict__ vc,
                                                        const bf16* __restrict__ wq, const bf16* __restrict__ wo, const bf16* __restrict__ wr,
                                                        const bf16* __restrict__ wg, const bf16* __restrict__ wu, const bf16* __restrict__ wd,
                                                        float* ws, bf16* __restrict__ out) {
  constexpr int WA = 128 * 2 + 16, GA = DM * 2 + 16, DA = FF * 2 + 16;
  __shared__ __attribute__((aligned(16))) char s_A[8 * DA];
  __shared__ __attribute__((aligned(16))) float s_red[6 * 256];
  __shared__ __attribute__((aligned(16))) float s_h[2 * DM];
  __shared__ __attribute__((aligned(16))) float s_buf[NWIN * NW * DH];
  __shared__ __attribute__((aligned(16))) float s_sc[NGRP * 64];
  __shared__ __attribute__((aligned(16))) float s_q[NWIN * DH];
  __shared__ float s_m[NWIN * NW], s_l[NWIN * NW];
  __shared__ int s_qok, s_pcnt, s_ecnt, s_fcnt, s_icnt, s_org, s_hcnt;
  __shared__ int s_prog[NW];
  __shared__ float s_stat[4];

  const int tid = threadIdx.x;
  int lane = tid & 63, r = lane >> 3, j = lane & 7, mg = lane >> 4, mc = lane & 15;
  int wave = __builtin_amdgcn_readfirstlane(tid >> 6), bid = blockIdx.x;
  const unsigned t_start = (unsigned)__builtin_amdgcn_s_memrealtime();
  int wid = XMAP == 0 ? bid : (XMAP == 1 ? (bid & 7) * 32 + (bid >> 3) : (XMAP == 2 ? (bid >> 5) * 32 + (bid & 7) * 4 + ((bid >> 3) & 3) : (bid >> 7) * 128 + (bid & 7) * 16 + ((bid >> 3) & 15)));
  const bool prod = bid < NQP;
  const __amdgpu_buffer_rsrc_t rs_ws = __builtin_amdgcn_make_buffer_rsrc(ws, 0, WS_END * 4, 0x00020000);
  const __amdgpu_buffer_rsrc_t rs_k = __builtin_amdgcn_make_buffer_rsrc(const_cast<bf16*>(kc), 0, NB * NH * SK * DH * 2, 0x00020000);
  const __amdgpu_buffer_rsrc_t rs_v = __builtin_amdgcn_make_buffer_rsrc(const_cast<bf16*>(vc), 0, NB * NH * SK * DH * 2, 0x00020000);
  const int voff = r * (DH * 2) + j * 16;
  const int c1 = wid % 3 == 0 ? 22 : 21, c2 = wid % 3 == 1 ? 42 : 43;
  auto seg_of = [&](int n) { return (n * 8 + wave >= c1) + (n * 8 + wave >= c2); };
  auto gload = [&](Slot (&sl)[NBUF], __amdgpu_buffer_rsrc_t rs, int n) {
    const int sn = seg_of(n), sg = wid + NWG * sn, row = sg / NSEG, k = sg - row * NSEG;
    const int off = (row * (SK / 64) + k * 21 + (k + 2) / 3 + n * 8 + wave - (sn == 0 ? 0 : (sn == 1 ? c1 : c2))) * (64 * DH * 2);
#pragma unroll
    for (int u = 0; u < NBUF; ++u) {
#pragma unroll
      for (int i = 0; i < 3; ++i) sl[u].a[i] = __builtin_amdgcn_raw_buffer_load_b128(rs, voff + i * 128, off + 2 * u * 3072, AUX_NT);
      MEMFENCE();
#pragma unroll
      for (int i = 0; i < 3; ++i) sl[u].b[i] = __builtin_amdgcn_raw_buffer_load_b128(rs, voff + i * 128, off + (2 * u + 1) * 3072, AUX_NT);
      if (SPREAD && u < NBUF - 1) __builtin_amdgcn_s_sleep(SPREAD);
      MEMFENCE();
    }
  };
  asm volatile("" :: "s"(ws), "s"(x), "s"(wq), "s"(kc));
  Slot b0[NBUF];
  const bool early = wave < NEARLY && !(prod && PLATE);
  if (early) gload(b0, rs_k, 0);
  unsigned hs_nc = 0u, hs_ep = 0u;
  auto look_at_p = [&]() {
    if (bid == NWG - 1) {
      u32x4 hp[2], hc[2];
#pragma unroll
      for (int i = 0; i < 2; ++i) {
        hp[i] = __builtin_amdgcn_raw_buffer_load_b128(rs_ws, (WS_HP + lane * 8 + i * 4) * 4, 0, AUX_SC1);
        const u32x2 c0 = __builtin_amdgcn_raw_buffer_load_b64(rs_ws, (WS_HC + (lane * 4 + i * 2) * LSTR) * 4, 0, AUX_SC1);
        const u32x2 c1_ = __builtin_amdgcn_raw_buffer_load_b64(rs_ws, (WS_HC + (lane * 4 + i * 2 + 1) * LSTR) * 4, 0, AUX_SC1);
        hc[i] = u32x4{c0[0], c0[1], c1_[0], c1_[1]};
      }
      if (__all(hc[0][1] == hp[0][0] && hc[0][3] == hp[0][2] && hc[1][1] == hp[1][0] && hc[1][3] == hp[1][2]) &&
          __any(hc[0][0] != hp[0][1] || hc[0][2] != hp[0][3] || hc[1][0] != hp[1][1] || hc[1][2] != hp[1][3])) {
#pragma unroll
        for (int i = 0; i < 2; ++i)
          __builtin_amdgcn_raw_buffer_store_b128(u32x4{hp[i][0], hc[i][0], hp[i][2], hc[i][2]}, rs_ws, (WS_HP + lane * 8 + i * 4) * 4, 0, AUX_SC1);
      }
    }
    const u32x2 pw = __builtin_amdgcn_raw_buffer_load_b64(rs_ws, (WS_HP + bid * 2) * 4, 0, AUX_SC1);
    const unsigned np = __builtin_amdgcn_readfirstlane(pw[0]), ec = __builtin_amdgcn_readfirstlane(pw[1]);
    if (np != hs_ep) { hs_ep = np; __builtin_amdgcn_raw_buffer_store_b64(u32x2{hs_nc, hs_ep}, rs_ws, lane == 0 ? (WS_HC + bid * LSTR) * 4 : OOB, 0, AUX_SC1); }
    return ec == hs_nc;
  };
  auto zero = [&]() {
    const u32x4 z4 = {0u, 0u, 0u, 0u};
#pragma unroll
    for (int i = 0; i < (ZPW + 255) / 256; ++i)
      __builtin_amdgcn_raw_buffer_store_b128(z4, rs_ws, i * 256 + lane * 4 < ZPW ? (WS_Z0 + bid * ZPW + i * 256 + lane * 4) * 4 : OOB, 0, AUX_SC1);
  };
  auto answer = [&]() {
    if (ZLATE) zero();
    const u32x2 pw = __builtin_amdgcn_raw_buffer_load_b64(rs_ws, (WS_HP + bid * 2) * 4, 0, AUX_SC1);
    DRAIN();
    hs_ep = __builtin_amdgcn_readfirstlane(pw[0]);
    hs_nc = __builtin_amdgcn_readfirstlane(pw[1]) + 1u;
    __builtin_amdgcn_raw_buffer_store_b64(u32x2{hs_nc, hs_ep}, rs_ws, lane == 0 ? (WS_HC + bid * LSTR) * 4 : OOB, 0, AUX_SC1);
  };
  bool answered = false;
  if (tid < NW) s_prog[tid] = 1;
  if (tid == 0) { s_qok = 0; s_pcnt = 0; s_ecnt = 0; s_fcnt = 0; s_icnt = 0; s_hcnt = 0; s_org = (int)(t_start + POFF); }
  if (wave == SVC) {
    __builtin_amdgcn_s_setprio(3);
    int iofs[NWIN];
#pragma unroll
    for (int s = 0; s < NWIN; ++s) iofs[s] = (WS_QI + ((wid + NWG * s) / NSEG * DH + lane) * 2) * 4;
    const int oofs = prod ? (WS_QI + (bid * 64 + lane) * 2) * 4 : OOB;
    u32x2 in[NWIN * 3];
    unsigned ot[NB], ce[4];
#pragma unroll
    for (int e = 0; e < NWIN * 3; ++e) in[e] = __builtin_amdgcn_raw_buffer_load_b64(rs_ws, iofs[e / 3], (e % 3) * 512, AUX_SC1);
#pragma unroll
    for (int bb = 0; bb < NB; ++bb) ot[bb] = __builtin_amdgcn_raw_buffer_load_b64(rs_ws, oofs, bb * (HD * 8), AUX_SC1)[1];
    u32x4 xr[5], hp[2];
#pragma unroll
    for (int i = 0; i < 5; ++i) xr[i] = reinterpret_cast<const u32x4*>(x)[lane * 5 + i];
#pragma unroll
    for (int i = 0; i < 2; ++i) hp[i] = __builtin_amdgcn_raw_buffer_load_b128(rs_ws, bid == NWG - 1 ? (WS_HP + lane * 8 + i * 4) * 4 : OOB, 0, AUX_SC1);
#pragma unroll
    for (int i = 0; i < 4; ++i) ce[i] = __builtin_amdgcn_raw_buffer_load_b32(rs_ws, bid == NWG - 1 ? (WS_HC + (lane * 4 + i) * LSTR + 1) * 4 : OOB, 0, AUX_SC1);
    if (!ZLATE) zero();
    MEMFENCE();
    __syncthreads();
    MEMFENCE();
    if (bid == NWG - 1) {
#pragma unroll
      for (int i = 0; i < 2; ++i) {
#pragma unroll
        for (int e = 0; e < 2; ++e) { unsigned np = hp[i][2 * e] + 1u; if (np == ce[2 * i + e]) np += 1u; hp[i][2 * e] = np; }
        __builtin_amdgcn_raw_buffer_store_b128(hp[i], rs_ws, (WS_HP + lane * 8 + i * 4) * 4, 0, AUX_SC1);
      }
    }
    unsigned want[NWIN * 3];
#pragma unroll
    for (int e = 0; e < NWIN * 3; ++e) want[e] = fresh_tag(in[e][1]);
    if (prod) {
      float xs = 0.f;
#pragma unroll
      for (int i = 0; i < 20; ++i) xs += bf_lo(xr[i >> 2][i & 3]) + bf_hi(xr[i >> 2][i & 3]);
      const float mean = row16_sum(xs) * (1.0f / DM);
      float xv = 0.f;
#pragma unroll
      for (int i = 0; i < 20; ++i) { const float a = bf_lo(xr[i >> 2][i & 3]) - mean, c = bf_hi(xr[i >> 2][i & 3]) - mean; xv += a * a + c * c; }
      const float rstd = QSCALE / sqrtf(row16_sum(xv) * (1.0f / DM) + LN_EPS);
#pragma unroll
      for (int bb = 0; bb < NB; ++bb) ot[bb] = fresh_tag(ot[bb]);
      for (unsigned spin = 0; spin < (SPIN_CAP << 4) && lds_get(&s_pcnt) < NQW; ++spin) __builtin_amdgcn_s_sleep(1);
      __builtin_amdgcn_fence(__ATOMIC_ACQUIRE, "workgroup");
      float pr[5];
#pragma unroll
      for (int i = 0; i < 5; ++i) {
        pr[i] = 0.f;
#pragma unroll
        for (int k = 0; k < NQW; ++k) pr[i] += s_buf[2 * k * 320 + i * 64 + lane] + s_buf[(2 * k + 1) * 320 + i * 64 + lane];
      }
#pragma unroll
      for (int bb = 0; bb < NB; ++bb) {
        const float qv = (pr[bb] - __int_as_float(__builtin_amdgcn_readlane(__float_as_int(mean), bb * 16)) * pr[4]) *
                         __int_as_float(__builtin_amdgcn_readlane(__float_as_int(rstd), bb * 16));
        __builtin_amdgcn_raw_buffer_store_b64(u32x2{__float_as_uint(qv), ot[bb]}, rs_ws, oofs, bb * (HD * 8), AUX_SC1);
      }
    } else {
      __builtin_amdgcn_s_sleep(QWAIT);
    }
    bool all_out = false;
#pragma unroll 1
    for (unsigned spin = 0; spin < SPIN_CAP; ++spin) {
#pragma unroll
      for (int e = 0; e < NWIN * 3; ++e) in[e] = __builtin_amdgcn_raw_buffer_load_b64(rs_ws, iofs[e / 3], (e % 3) * 512, AUX_SC1);
      bool ok = true;
#pragma unroll
      for (int e = 0; e < NWIN * 3; ++e) ok = ok && in[e][1] == want[e];
      if (all_out || __all(ok)) break;
      if (spin == 15) { answer(); answered = true; }
      if (spin > 15 && (spin & 3) == 3) all_out = look_at_p();
      if (QSLEEP) __builtin_amdgcn_s_sleep(QSLEEP);
    }
#pragma unroll
    for (int e = 0; e < NWIN * 3; ++e) s_q[e * 64 + lane] = __uint_as_float(in[e][0]);
    __builtin_amdgcn_fence(__ATOMIC_RELEASE, "workgroup");
    lds_put(&s_qok, 1);
    __builtin_amdgcn_s_setprio(0);
  } else if (prod && wave >= QW0 && wave < QW0 + NQW) {
    const int k0 = (wave - QW0) * (QST * 64) + (mc >> 3) * 32 + mg * 8;
    u32x4 raw[QST * 8], xa[QST];
    const bf16* wb = wq + (size_t)k0 * HD + bid * 64 + (mc & 7) * 8;
#pragma unroll
    for (int st = 0; st < QST; ++st) xa[st] = *reinterpret_cast<const u32x4*>(x + (mc & 3) * DM + k0 + st * 64);
#pragma unroll
    for (int e = 0; e < QST * 8; ++e) raw[e] = QNT ? __builtin_nontemporal_load(reinterpret_cast<const u32x4*>(wb + (size_t)((e >> 3) * 64 + (e & 7)) * HD)) : *reinterpret_cast<const u32x4*>(wb + (size_t)((e >> 3) * 64 + (e & 7)) * HD);
    MEMFENCE();
    __syncthreads();
    MEMFENCE();
    f32x4 ac[8];
#pragma unroll
    for (int t = 0; t < 8; ++t) ac[t] = f32x4{0.f, 0.f, 0.f, 0.f};
#pragma unroll
    for (int st = 0; st < QST; ++st) {
      const u32x4 a = (mc & 4) == 0 ? xa[st] : u32x4{0x3f803f80u, 0x3f803f80u, 0x3f803f80u, 0x3f803f80u};
#pragma unroll
      for (int c = 0; c < 4; ++c) {
        u32x4 f0, f1;
#pragma unroll
        for (int i = 0; i < 4; ++i) { f0[i] = pk_lo(raw[st * 8 + 2 * i][c], raw[st * 8 + 2 * i + 1][c]); f1[i] = pk_hi(raw[st * 8 + 2 * i][c], raw[st * 8 + 2 * i + 1][c]); }
        ac[2 * c] = mfma16(a, f0, ac[2 * c]);
        ac[2 * c + 1] = mfma16(a, f1, ac[2 * c + 1]);
      }
    }
    if ((mg >> 1) == (mc >> 3)) {
      float* const dst = &s_buf[((wave - QW0) * 2 + (mg >> 1)) * 320 + (mc & 7) * 8];
#pragma unroll
      for (int bb = 0; bb < NB; ++bb)
        if (bb == 0 || (mg & 1) == 0) {
          *reinterpret_cast<f32x4*>(dst + ((mg & 1) * 4 + bb) * 64) = f32x4{ac[0][bb], ac[1][bb], ac[2][bb], ac[3][bb]};
          *reinterpret_cast<f32x4*>(dst + ((mg & 1) * 4 + bb) * 64 + 4) = f32x4{ac[4][bb], ac[5][bb], ac[6][bb], ac[7][bb]};
        }
    }
    __builtin_amdgcn_fence(__ATOMIC_RELEASE, "workgroup");
    if (lane == 0) __hip_atomic_fetch_add(&s_pcnt, 1, __ATOMIC_RELAXED, __HIP_MEMORY_SCOPE_WORKGROUP);
  } else {
    __syncthreads();
  }
  MEMFENCE();
  u32x2 rep = {0u, 0u};
  unsigned t_thru = 0u;
  auto pace = [&](int g) {
    if (wave == 0 && g > AFROM) {
      const int org = lds_get(&s_org);
      const bool ok = rep[0] - t_start + 1000u < 6000u && rep[1] - t_start + 1000u < 6000u;
      const float dorg = row16_sum(ok ? (float)(int)(rep[1] - (unsigned)org) : 0.f);
      const float lt = ok ? (float)min((int)(rep[0] - rep[1]), LCAP) : (float)LCAP;
      const float lx = fminf(lt, dpp<DPP_ROR8>(lt));
      float hi = fmaxf(lx, dpp<DPP_XOR1>(lx)); hi = fmaxf(hi, dpp<DPP_XOR2>(hi)); hi = fmaxf(hi, dpp<DPP_HMIRROR>(hi));
      const float sig = row16_sum(lt) * ((1.f - AMAX / 100.f) / 16.f) + hi * (AMAX / 100.f);
      if (lane == 0) lds_put(&s_org, org + (int)(dorg * (1.f / 16.f) + fmaxf((sig - LTGT) * (ADAPT / 100.f), g > EFROM ? (float)ESL : -(float)DOWN)));
    }
    const unsigned rel = (unsigned)(g * PTAU + (wave / BURST * BURST) * (PTAU / 8) + min(g * 8 + wave, 16) * (FFWD / 16) - FFWD + (JW ? ((wid * JW) & 255) * PJIT / 256 : (((bid >> 3) * JMUL + (bid & 7) * JX) & 31) * PJIT / 32));
    unsigned now = (unsigned)__builtin_amdgcn_s_memrealtime();
    if (wave == 1 && (bid >> 4) == 7 && g >= 2)
      __builtin_amdgcn_raw_buffer_store_b64(u32x2{t_thru - rel + PTAU, (unsigned)lds_get(&s_org)}, rs_ws, lane == 0 ? (WS_REP + ((g - 2) * 16 + (bid & 15)) * LSTR) * 4 : OOB, 0, AUX_SC1);
    for (unsigned spin = 0; spin < 4096 && (int)(now - rel - (unsigned)__builtin_amdgcn_readfirstlane(lds_get(&s_org))) < 0; ++spin) {
      __builtin_amdgcn_s_sleep(2);
      now = (unsigned)__builtin_amdgcn_s_memrealtime();
    }
    if (g >= 2) rep = __builtin_amdgcn_raw_buffer_load_b64(rs_ws, (wave == 0 && lane < 16 && g >= AFROM) ? (WS_REP + ((g - 2) * 16 + lane) * LSTR) * 4 : OOB, 0, 0);
    if (lane == 0) lds_put(&s_prog[wave], g);
    int pp = __builtin_amdgcn_readfirstlane(lds_get(&s_prog[wave ^ (NW / 2)]));
    for (unsigned spin = 0; spin < 4096 && g > pp + 1; ++spin) { __builtin_amdgcn_s_sleep(4); pp = __builtin_amdgcn_readfirstlane(lds_get(&s_prog[wave ^ (NW / 2)])); }
    if (g > pp) __builtin_amdgcn_s_setprio(0); else __builtin_amdgcn_s_setprio(1);
  };
  float gmx = -INFINITY;
  auto k_group = [&](const Slot (&sl)[NBUF], int n, int s) {
    f32x2 q[12];
#pragma unroll
    for (int i = 0; i < 6; ++i) {
      const f32x4 a = *reinterpret_cast<const f32x4*>(&s_q[s * DH + (i >> 1) * 64 + j * 8 + (i & 1) * 4]);
      q[i * 2] = f32x2{a[0], a[1]}; q[i * 2 + 1] = f32x2{a[2], a[3]};
    }
    float* const sc = &s_sc[(wave * NT + n) * 64 + r * 8];
    MEMFENCE();
#pragma unroll
    for (int u = 0; u < NBUF; ++u) {
      k_tile(sl[u].a, q, gmx, sc + 2 * u, j);
      k_tile(sl[u].b, q, gmx, sc + 2 * u + 1, j);
      MEMFENCE();
    }
    t_thru = (unsigned)__builtin_amdgcn_s_memrealtime();
  };
  {
    Slot b1[NBUF];
    if (!early) {
      pace(0);
      gload(b0, rs_k, 0);
    }
    pace(1);
    gload(b1, rs_k, 1);
    for (unsigned spin = 0; spin < (SPIN_CAP << 4) && lds_get(&s_qok) == 0; ++spin) __builtin_amdgcn_s_sleep(2);
    __builtin_amdgcn_fence(__ATOMIC_ACQUIRE, "workgroup");
    k_group(b0, 0, 0);
    k_group(b1, 1, 0);
    if (wave == SVC && !answered) answer();
  }
#pragma unroll 1
  for (int pass = 1 - ((0x60 >> wave) & 1); pass < 2; ++pass) {
  int real = pass;
  asm volatile("" : "+s"(real));
  asm volatile("" : "+s"(wave), "+s"(bid), "+s"(wid), "+v"(lane));
  r = lane >> 3; j = lane & 7;
  if (real) {
  Slot buf[NBUF];
  int seg = 0;
  for (int n = 2; n < NT; ++n) {
    pace(n);
    gload(buf, rs_k, n);
    if (seg_of(n) != seg) {
      const float gm = max_r(gmx);
      if (lane == 0) s_m[seg * NW + wave] = gm;
      gmx = -INFINITY;
      ++seg;
    }
    k_group(buf, n, seg);
    if (wave == SVC && bid == NWG - 1 && n >= 3) look_at_p();
  }
  {
    const float gm = max_r(gmx);
    if (lane == 0) s_m[seg * NW + wave] = gm;
  }
  float l = 0.f;
  f32x2 acc[12];
#pragma unroll
  for (int i = 0; i < 12; ++i) acc[i] = f32x2{0.f, 0.f};
  __builtin_amdgcn_fence(__ATOMIC_RELEASE, "wavefront"); __builtin_amdgcn_wave_barrier(); __builtin_amdgcn_fence(__ATOMIC_ACQUIRE, "wavefront");
  for (int n = 0; n < NT; ++n) {
    pace(NT + n);
    gload(buf, rs_v, n);
    if (n == NT - 1) {
      if (lane == 0) __hip_atomic_fetch_add(&s_icnt, 1, __ATOMIC_RELAXED, __HIP_MEMORY_SCOPE_WORKGROUP);
      __builtin_amdgcn_s_setprio(3);
    }
    seg = seg_of(n);
    {
      float p[8];
      const float m = s_m[seg * NW + wave];
      const float* const sc = &s_sc[(wave * NT + n) * 64 + r * 8];
      const f32x4 sa = *reinterpret_cast<const f32x4*>(sc), sb = *reinterpret_cast<const f32x4*>(sc + 4);
#pragma unroll
      for (int t = 0; t < 4; ++t) { p[t] = __builtin_amdgcn_exp2f(sa[t] - m); p[4 + t] = __builtin_amdgcn_exp2f(sb[t] - m); }
      l += ((p[0] + p[1]) + (p[2] + p[3])) + ((p[4] + p[5]) + (p[6] + p[7]));
      MEMFENCE();
#pragma unroll
      for (int u = 0; u < NBUF; ++u) {
        v_tile(buf[u].a, p[2 * u], acc);
        v_tile(buf[u].b, p[2 * u + 1], acc);
        MEMFENCE();
      }
    }
    t_thru = (unsigned)__builtin_amdgcn_s_memrealtime();
    if (wave == SVC && n == 1)
      for (unsigned spin = 0; spin < SPIN_CAP && !look_at_p(); ++spin) __builtin_amdgcn_s_sleep(4);
    if (n == NT - 1 || seg_of(n + 1) != seg) {
      const float lw = sum_r(l);
      float t12[12], t6[6];
#pragma unroll
      for (int e = 0; e < 12; ++e) t12[e] = tsum32(acc[e >> 1][e & 1], acc[6 + (e >> 1)][e & 1]);
#pragma unroll
      for (int e = 0; e < 6; ++e) { t6[e] = tsum16(t12[e], t12[e + 6]); t6[e] += dpp<DPP_ROR8>(t6[e]); }
      if ((lane & 8) == 0) {
#pragma unroll
        for (int e = 0; e < 3; ++e) *reinterpret_cast<f32x2*>(&s_buf[(seg * NW + wave) * DH + j * 24 + (lane >> 4) * 6 + e * 2]) = f32x2{t6[2 * e], t6[2 * e + 1]};
      }
      if (lane == 0) s_l[seg * NW + wave] = lw;
      l = 0.f;
#pragma unroll
      for (int i = 0; i < 12; ++i) acc[i] = f32x2{0.f, 0.f};
      if (seg != 0) {
        __builtin_amdgcn_fence(__ATOMIC_RELEASE, "workgroup");
        int c = 0;
        if (lane == 0) c = __hip_atomic_fetch_add(seg == 1 ? &s_ecnt : &s_fcnt, 1, __ATOMIC_RELAXED, __HIP_MEMORY_SCOPE_WORKGROUP);
        if (__builtin_amdgcn_readfirstlane(c) == NW - 1) {
          __builtin_amdgcn_fence(__ATOMIC_ACQUIRE, "workgroup");
#pragma unroll 1
          for (int s = seg & 2; s <= seg; ++s) {
            float M = s_m[s * NW];
#pragma unroll
            for (int w = 1; w < NW; ++w) M = fmaxf(M, s_m[s * NW + w]);
            float L = 0.f;
            f32x4 o = {0.f, 0.f, 0.f, 0.f};
            const int d4 = lane < DH / 4 ? ((lane >> 1) & 7) * 24 + (lane >> 4) * 8 + (lane & 1) * 4 : 0;
#pragma unroll
            for (int w = 0; w < NW; ++w) {
              const float e = __builtin_amdgcn_exp2f(s_m[s * NW + w] - M);
              L = fmaf(e, s_l[s * NW + w], L);
              o += e * *reinterpret_cast<const f32x4*>(&s_buf[(s * NW + w) * DH + d4]);
            }
            if (lane >= DH / 4) o = f32x4{M, L, 1.f, 1.f};
            __builtin_amdgcn_raw_buffer_store_b128(u32x4{nz_bits(o[0]), nz_bits(o[1]), nz_bits(o[2]), nz_bits(o[3])}, rs_ws,
                                                   lane < REC / 4 ? (WS_PO + (wid + NWG * s) * REC + lane * 4) * 4 : OOB, 0, AUX_SC1);
          }
          if (seg == 2) { MEMFENCE(); lds_put(&s_fcnt, NW + 1); }
          if (seg == 1) {
            DRAIN();
            __builtin_amdgcn_raw_buffer_store_b32(1u, rs_ws, lane == 0 ? ef_ofs(wid) : OOB, 0, AUX_SC1);
          }
        }
      }
      MEMFENCE();
    }
  }
  __builtin_amdgcn_s_setprio(0);
  }
  {
    asm volatile("" : "+s"(wave), "+s"(bid), "+v"(lane));
    r = lane >> 3; j = lane & 7; mg = lane >> 4; mc = lane & 15;
    const int w = real ? wave : (wave - 5) * 7, td = w * 64 + lane;
    const int xcd = bid & 7, ri = bid >> 3;
    const bool isW = ri < 15, isG = !isW && ri < 27;
    const int tt = isW ? xcd * 15 + ri : (isG ? xcd * 12 + ri - 15 : xcd * 5 + ri - 27);
    const int bp = tt & 1, ct = tt >> 1;
    const int ks1 = tt / W_CT, ct1 = tt - ks1 * W_CT;
    const int nmw = isW ? 4 : (isG ? 5 : 6);
    const __amdgpu_buffer_rsrc_t rs_out = __builtin_amdgcn_make_buffer_rsrc(out, 0, NB * DM * 2, 0x00020000);
    u32x4 fr[16];
    u32x2 raw[32];
    auto weights = [&]() {
#pragma unroll
      for (int i = 0; i < 32; ++i) raw[i] = u32x2{0u, 0u};
      if (real && w < nmw) {
        int* const cnt = (isW || isG) ? &s_icnt : &s_fcnt;
        for (unsigned spin = 0; spin < (SPIN_CAP << 4) && lds_get(cnt) < NW; ++spin) __builtin_amdgcn_s_sleep(1);
        if (isW || isG) {
          const int nsl = isW ? WDELAY : GDELAY;
          for (int i = 0; i < nsl && lds_get(&s_fcnt) <= NW; ++i) __builtin_amdgcn_s_sleep(3);
        } else {
          __builtin_amdgcn_s_sleep(DDELAY);
        }
        typedef const __attribute__((address_space(1))) char* gptr;
        gptr wb = (gptr)(isW ? wo + (size_t)(ks1 * K3_ROWS + w * 128 + mg * 8) * DM + ct1 * 64 + mc * 4
                             : (isG ? (mc < 8 ? wg : wu) + (size_t)(w * 128 + mg * 8) * FF : wd + (size_t)(w * 256 + (mc >> 3) * 32 + mg * 8) * DM) + ct * 32 + (mc & 7) * 4);
        const int rstr = (isG ? FF : DM) * 2, sstr = (isW || isG ? 25 : 57) * rstr;
#pragma unroll
        for (int e = 0; e < 32; ++e) {
          raw[e] = *(const __attribute__((address_space(1))) u32x2*)wb;
          wb += (e & 7) == 7 ? sstr : rstr;
          asm volatile("" : "+v"(wb));
        }
        MEMFENCE();
      }
    };
    auto frags = [&]() {
      PIN8(raw, 0); PIN8(raw, 8); PIN8(raw, 16); PIN8(raw, 24);
#pragma unroll
      for (int st = 0; st < 4; ++st)
#pragma unroll
        for (int i = 0; i < 4; ++i) {
          const u32x2 r0 = raw[st * 8 + 2 * i], r1 = raw[st * 8 + 2 * i + 1];
          fr[st * 4 + 0][i] = pk_lo(r0[0], r1[0]); fr[st * 4 + 1][i] = pk_hi(r0[0], r1[0]);
          fr[st * 4 + 2][i] = pk_lo(r0[1], r1[1]); fr[st * 4 + 3][i] = pk_hi(r0[1], r1[1]);
        }
      PIN8(fr, 0); PIN8(fr, 8);
    };
    if (isW) {
      if (w >= 4) { if (real) break; else continue; }
      char* const img = s_A + w * (16 * WA);
      {
        int offp[2], ne[2], sg0[2];
#pragma unroll
        for (int hf = 0; hf < 2; ++hf) {
          const int row = ks1 * K3_ROWS + w * 128 + hf * 64, hh = row / DH;
          sg0[hf] = (mg * NH + hh) * NSEG;
          offp[hf] = (WS_PO + sg0[hf] * REC + row - hh * DH + mc * 4) * 4;
          ne[hf] = min(max(2 * NWG - sg0[hf], 0), NSEG);
        }
        const int sgm = (mc < 8 ? sg0[0] : sg0[1]) + j;
        const unsigned emask = (1u << ne[0]) - 1u + (((1u << ne[1]) - 1u) << NSEG) + (sgm < 2 * NWG ? 1u << (2 * NSEG) : 0u);
        u32x4 pv[2 * NSEG];
        u32x2 ml = {0u, 0u};
#pragma unroll
        for (int k = 0; k < 2 * NSEG; ++k) pv[k] = u32x4{0u, 0u, 0u, 0u};
        unsigned pend = real ? (1u << (2 * NSEG)) - 1u + (j < NSEG ? 1u << (2 * NSEG) : 0u) : 0u;
        auto look = [&](unsigned go) {
          if (go & (1u << (2 * NSEG))) ml = __builtin_amdgcn_raw_buffer_load_b64(rs_ws, (WS_PO + sgm * REC + DH) * 4, 0, AUX_SC1);
#pragma unroll
          for (int k = 0; k < 2 * NSEG; ++k)
            if (go & (1u << k)) pv[k] = __builtin_amdgcn_raw_buffer_load_b128(rs_ws, offp[k / NSEG], (k % NSEG) * REC * 4, AUX_SC1);
        };
        for (unsigned spin = 0; spin < SPIN_CAP && real; ++spin) {
          const bool fl = j < NSEG && sgm < 2 * NWG;
          if (__all(!fl || __builtin_amdgcn_raw_buffer_load_b32(rs_ws, fl ? ef_ofs(sgm & (NWG - 1)) : OOB, 0, AUX_SC1) != 0u)) break;
          __builtin_amdgcn_s_sleep(8);
        }
        weights();
        if (EARLYG) { look(pend & emask); pend &= ~emask; }
        for (unsigned sp2 = 0; sp2 < (SPIN_CAP << 4) && real && lds_get(&s_fcnt) < WSTART; ++sp2) __builtin_amdgcn_s_sleep(1);
        if (WLOOK) __builtin_amdgcn_s_sleep(WLOOK);
        look(pend);
        MEMFENCE();
        frags();
#pragma unroll 1
        for (unsigned spin = 0; spin < SPIN_CAP; ++spin) {
          unsigned got = ml[0] != 0u && ml[1] != 0u;
#pragma unroll
          for (int k = 2 * NSEG - 1; k >= 0; --k) got = got * 2u + (unsigned)all_nz(pv[k]);
          pend &= ~got;
          if (!__any(pend != 0u)) break;
          if (spin >= 8) __builtin_amdgcn_s_sleep(WSLEEP);
          look(pend);
        }
        const float mm = j < NSEG ? __uint_as_float(ml[0]) : -INFINITY;
        float M = fmaxf(mm, dpp<DPP_XOR1>(mm)); M = fmaxf(M, dpp<DPP_XOR2>(M)); M = fmaxf(M, dpp<DPP_HMIRROR>(M));
        const float e = __builtin_amdgcn_exp2f(mm - M);
        float L = e * __uint_as_float(ml[1]);
        L += dpp<DPP_XOR1>(L); L += dpp<DPP_XOR2>(L); L += dpp<DPP_HMIRROR>(L);
        const float wt = e * __builtin_amdgcn_rcpf(L);
        f32x4 o0 = dpp<DPP_BCAST + 0>(wt) * __builtin_bit_cast(f32x4, pv[0]), o1 = dpp<DPP_BCAST + 8>(wt) * __builtin_bit_cast(f32x4, pv[6]);
        o0 += dpp<DPP_BCAST + 1>(wt) * __builtin_bit_cast(f32x4, pv[1]); o1 += dpp<DPP_BCAST + 9>(wt) * __builtin_bit_cast(f32x4, pv[7]);
        o0 += dpp<DPP_BCAST + 2>(wt) * __builtin_bit_cast(f32x4, pv[2]); o1 += dpp<DPP_BCAST + 10>(wt) * __builtin_bit_cast(f32x4, pv[8]);
        o0 += dpp<DPP_BCAST + 3>(wt) * __builtin_bit_cast(f32x4, pv[3]); o1 += dpp<DPP_BCAST + 11>(wt) * __builtin_bit_cast(f32x4, pv[9]);
        o0 += dpp<DPP_BCAST + 4>(wt) * __builtin_bit_cast(f32x4, pv[4]); o1 += dpp<DPP_BCAST + 12>(wt) * __builtin_bit_cast(f32x4, pv[10]);
        o0 += dpp<DPP_BCAST + 5>(wt) * __builtin_bit_cast(f32x4, pv[5]); o1 += dpp<DPP_BCAST + 13>(wt) * __builtin_bit_cast(f32x4, pv[11]);
        put_terms4(img + mg * 4 * WA + mc * 8, WA, o0);
        put_terms4(img + mg * 4 * WA + 128 + mc * 8, WA, o1);
      }
      __builtin_amdgcn_fence(__ATOMIC_RELEASE, "wavefront"); __builtin_amdgcn_wave_barrier(); __builtin_amdgcn_fence(__ATOMIC_ACQUIRE, "wavefront");
      f32x4 ac[4];
#pragma unroll
      for (int t = 0; t < 4; ++t) ac[t] = f32x4{0.f, 0.f, 0.f, 0.f};
#pragma unroll
      for (int st = 0; st < 4; ++st) {
        const u32x4 a = *reinterpret_cast<const u32x4*>(img + mc * WA + st * 64 + mg * 16);
#pragma unroll
        for (int t = 0; t < 4; ++t) ac[t] = mfma16(a, fr[st * 4 + t], ac[t]);
      }
      *reinterpret_cast<f32x4*>(&s_red[w * 256 + lane * 4]) = f32x4{sum3(ac[0]), sum3(ac[1]), sum3(ac[2]), sum3(ac[3])};
      if (real) __syncthreads();
      if (w == 0) {
        const f32x4 s0 = (*reinterpret_cast<const f32x4*>(&s_red[lane * 4]) + *reinterpret_cast<const f32x4*>(&s_red[256 + lane * 4])) +
                         (*reinterpret_cast<const f32x4*>(&s_red[512 + lane * 4]) + *reinterpret_cast<const f32x4*>(&s_red[768 + lane * 4]));
        __builtin_amdgcn_raw_buffer_store_b128(u32x4{nz_bits(s0[0]), nz_bits(s0[1]), nz_bits(s0[2]), nz_bits(s0[3])}, rs_ws,
                                               real ? (WS_SLAB + (ks1 * NB + mg) * DM + ct1 * 64 + mc * 4) * 4 : OOB, 0, AUX_SC1);
      }
    } else {
      const int hcol = (lane >> 5) * DM + w * 128 + (lane & 31) * 4;
      uint2 xx = {0u, 0u};
      unsigned wr2[5] = {0u, 0u, 0u, 0u, 0u};
      if (real) {
        xx = *reinterpret_cast<const uint2*>(x + (size_t)bp * 2 * DM + (w < 5 ? hcol : 0));
#pragma unroll
        for (int i = 0; i < 5; ++i) wr2[i] = reinterpret_cast<const unsigned*>(wr)[lane + 64 * i];
        unsigned one = 0x3f803f80u;
        asm volatile("" : "+v"(one));
        if (isG && w < 5 && lane < 16) *reinterpret_cast<u32x4*>(s_A + 3 * GA + w * 256 + lane * 16) = u32x4{one, one, one, one};
      }
      weights();
      frags();
      PIN4(wr2, 0); asm volatile("" : "+v"(wr2[4]), "+v"(xx.x), "+v"(xx.y));
      const int bb = lane >> 5, f = lane & 31;
      int a_img = (mc & 7) * (isG ? GA : DA) + (isG ? w * 4 : w * 8 + (mc >> 3)) * 64 + mg * 16;
      int a_red = w * 256 + (isG ? lane * 4 : mg * 32 + (mc & 7) * 4), i0 = isG ? bb * 64 + f : lane, i1 = isG ? bb * 64 + 32 + f : 64 + lane;
      int o_res = !real ? OOB : (isG ? (WS_ACT + (bp * 2 + bb) * FF + ct * 32 + f) * 4 : ((bp * 2 + bb) * DM + ct * 32 + f) * 2);
      asm volatile("" : "+v"(a_img), "+v"(a_red), "+v"(i0), "+v"(i1), "+v"(o_res));
      if (isG && w < 5) {
        const int off = (WS_SLAB + bp * 2 * DM + hcol) * 4;
        const int poff = (WS_SLAB + bp * 2 * DM + w * 128 + (mg & 1) * 64 + mc * (NB * DM)) * 4;
        u32x4 sl[K3_KS];
#pragma unroll
        for (int ks = 0; ks < K3_KS; ++ks) sl[ks] = u32x4{0u, 0u, 0u, 0u};
        unsigned pend = real ? (1u << K3_KS) - 1u : 0u;
        unsigned todo = pend, go = 0u;
        for (unsigned spin = 0; spin < SPIN_CAP; ++spin) {
#pragma unroll
          for (int ks = 0; ks < K3_KS; ++ks)
            if (go & (1u << ks)) sl[ks] = __builtin_amdgcn_raw_buffer_load_b128(rs_ws, off, ks * (NB * DM * 4), AUX_SC1);
          todo &= ~go;
          if (todo != 0u) {
            const unsigned pr = __builtin_amdgcn_raw_buffer_load_b32(rs_ws, (mg < 2 && mc < K3_KS && ((todo >> mc) & 1u)) ? poff : OOB, 0, AUX_SC1);
            go = (unsigned)__ballot(pr != 0u);
            go = go & go >> 16 & todo;
            if (__builtin_popcount(todo & ~go) <= PROBE_MIN) go = todo;
          } else {
            unsigned got = 0u;
#pragma unroll
            for (int ks = K3_KS - 1; ks >= 0; --ks) got = got * 2u + (unsigned)__all(all_nz(sl[ks]));
            go = pend & ~got;
            if (go == 0u) break;
          }
        }
        f32x4 hv = f32x4{bf_lo(xx.x), bf_hi(xx.x), bf_lo(xx.y), bf_hi(xx.y)};
#pragma unroll
        for (int ks = 0; ks < K3_KS; ++ks) hv += __builtin_bit_cast(f32x4, sl[ks]);
        *reinterpret_cast<f32x4*>(&s_h[hcol]) = hv;
        __builtin_amdgcn_fence(__ATOMIC_RELEASE, "workgroup");
        if (lane == 0 && real) __hip_atomic_fetch_add(&s_hcnt, 1, __ATOMIC_RELAXED, __HIP_MEMORY_SCOPE_WORKGROUP);
        put_terms4(s_A + (lane >> 5) * 4 * GA + w * 256 + (lane & 31) * 8, GA, hv);
      }
      unsigned bs = 0u, sg = 0u;
      if (isG && w >= NW - 2) {
        const int rw = w - (NW - 2);
        for (unsigned spin = 0; spin < (SPIN_CAP << 6) && real && lds_get(&s_hcnt) < 5; ++spin) {}
        __builtin_amdgcn_fence(__ATOMIC_ACQUIRE, "workgroup");
        const float piv = s_h[rw * DM];
        float hv[10], sm = 0.f, sq = 0.f;
#pragma unroll
        for (int i = 0; i < 5; ++i) {
          const float2 t2 = *reinterpret_cast<const float2*>(&s_h[rw * DM + 2 * lane + 128 * i]);
          hv[2 * i] = t2.x; hv[2 * i + 1] = t2.y;
        }
        PIN8(hv, 0); asm volatile("" : "+v"(hv[8]), "+v"(hv[9]));
#pragma unroll
        for (int i = 0; i < 10; ++i) { hv[i] -= piv; sm += hv[i]; sq = fmaf(hv[i], hv[i], sq); }
        const float dm = wave_sum(sm) * (1.0f / DM);
        const float var = fmaxf(fmaf(-dm, dm, wave_sum(sq) * (1.0f / DM)), 0.f);
        const float rstd = __builtin_amdgcn_rsqf(var + LN_EPS);
        if (lane == 0) { s_stat[rw * 2] = piv + dm; s_stat[rw * 2 + 1] = rstd; }
        if (real) __syncthreads();
        if (ct < DM / 32) {
          float rr = 0.f;
#pragma unroll
          for (int i = 0; i < 5; ++i) { rr = fmaf((hv[2 * i] - dm) * rstd, bf_lo(wr2[i]), rr); rr = fmaf((hv[2 * i + 1] - dm) * rstd, bf_hi(wr2[i]), rr); }
          rr = wave_sum(rr);
          const float hh = s_h[rw * DM + ct * 32 + f];
          __builtin_amdgcn_raw_buffer_store_b32(nz_bits(bb ? 1.0f / (1.0f + __expf(-rr)) : hh + (hh - (piv + dm)) * rstd), rs_ws,
                                                real ? (WS_BASE + ((bp * 2 + rw) * (DM / 32) + ct) * 64 + lane) * 4 : OOB, 0, AUX_SC1);
        }
        continue;
      } else if (!isG && w < 6) {
        const int off = (WS_ACT + bp * 2 * FF + td * 4) * 4;
        u32x4 v0 = {0u, 0u, 0u, 0u}, v1 = v0;
        const int offb = (WS_BASE + ((bp * 2 + bb) * (DM / 32) + ct) * 64 + f) * 4;
        unsigned pend = real ? (w == 0 ? 7u : 3u) : 0u;
        if (DLOOK && real) { const unsigned t1 = (unsigned)lds_get(&s_org) + 16 * PTAU + DLOOK; for (unsigned spin = 0; spin < 4096 && (int)((unsigned)__builtin_amdgcn_s_memrealtime() - t1) < 0; ++spin) __builtin_amdgcn_s_sleep(1); }
        for (unsigned spin = 0; spin < SPIN_CAP; ++spin) {
          if (pend & 1u) v0 = __builtin_amdgcn_raw_buffer_load_b128(rs_ws, off, 0, AUX_SC1);
          if (pend & 2u) v1 = __builtin_amdgcn_raw_buffer_load_b128(rs_ws, off, FF * 4, AUX_SC1);
          if (pend & 4u) { bs = __builtin_amdgcn_raw_buffer_load_b32(rs_ws, offb, 0, AUX_SC1); sg = __builtin_amdgcn_raw_buffer_load_b32(rs_ws, offb, 128, AUX_SC1); }
          pend &= ~((unsigned)all_nz(v0) + 2u * (unsigned)all_nz(v1) + 4u * (unsigned)(bs != 0u && sg != 0u));
          if (!__any(pend != 0u)) break;
        }
        put_terms4(s_A + td * 8, DA, __builtin_bit_cast(f32x4, v0));
        put_terms4(s_A + 4 * DA + td * 8, DA, __builtin_bit_cast(f32x4, v1));
      }
      if (w < nmw) {
        __builtin_amdgcn_fence(__ATOMIC_RELEASE, "wavefront"); __builtin_amdgcn_wave_barrier(); __builtin_amdgcn_fence(__ATOMIC_ACQUIRE, "wavefront");
        f32x4 ac[4];
#pragma unroll
        for (int t = 0; t < 4; ++t) ac[t] = f32x4{0.f, 0.f, 0.f, 0.f};
        const int astep = isG ? 64 : 2 * 64;
        u32x4 av[4];
#pragma unroll
        for (int st = 0; st < 4; ++st) av[st] = *reinterpret_cast<const u32x4*>(s_A + a_img + st * astep);
#pragma unroll
        for (int st = 0; st < 4; ++st)
#pragma unroll
          for (int t = 0; t < 4; ++t) ac[t] = mfma16(av[st], fr[st * 4 + t], ac[t]);
        if (isG ? lane < 32 : (mc >> 3) == (mg >> 1)) *reinterpret_cast<f32x4*>(&s_red[a_red]) = f32x4{sum3(ac[0]), sum3(ac[1]), sum3(ac[2]), sum3(ac[3])};
        if (lane < 16) *reinterpret_cast<f32x4*>(&s_red[w * 256 + 128 + lane * 4]) = f32x4{ac[0][3], ac[1][3], ac[2][3], ac[3][3]};
      }
      if (real) __syncthreads();
      if (w == 0) {
        float pr[24];
#pragma unroll
        for (int k = 0; k < 6; ++k) { pr[k] = s_red[k * 256 + i0]; pr[6 + k] = s_red[k * 256 + i1]; }
#pragma unroll
        for (int k = 0; k < 5; ++k) { pr[12 + k] = s_red[k * 256 + 128 + f]; pr[17 + k] = s_red[k * 256 + 160 + f]; }
        pr[22] = 0.f; pr[23] = 0.f;
        float mean = s_stat[bb * 2], rstd = s_stat[bb * 2 + 1];
        PIN8(pr, 0); PIN8(pr, 8); PIN8(pr, 16); asm volatile("" : "+v"(mean), "+v"(rstd));
        const float s0 = ((pr[0] + pr[1]) + (pr[2] + pr[3])) + pr[4], s1 = ((pr[6] + pr[7]) + (pr[8] + pr[9])) + pr[10];
        if (isG) {
          const float c0 = ((pr[12] + pr[13]) + (pr[14] + pr[15])) + pr[16], c1_ = ((pr[17] + pr[18]) + (pr[19] + pr[20])) + pr[21];
          const float gg = (s0 - mean * c0) * rstd, uu = (s1 - mean * c1_) * rstd;
          const float a = gg * __builtin_amdgcn_rcpf(1.0f + __expf(-gg)) * uu;
          __builtin_amdgcn_raw_buffer_store_b32(nz_bits(a), rs_ws, o_res, 0, AUX_SC1);
        } else {
          const bf16 ov = __float2bfloat16(fmaf((s0 + pr[5]) + (s1 + pr[11]), __uint_as_float(sg), __uint_as_float(bs)));
          __builtin_amdgcn_raw_buffer_store_b16(__builtin_bit_cast(unsigned short, ov), rs_out, o_res, 0, 0);
        }
      }
    }
  }
  }
  if (ENDPIN && wave == 0) for (unsigned spin = 0; spin < 100000 && (int)((unsigned)__builtin_amdgcn_s_memrealtime() - t_start) < ENDPIN; ++spin) __builtin_amdgcn_s_sleep(1);
}

extern "C" void kernel_launch(void* const* d_in, const int* in_sizes, int n_in, void* d_out, int out_size, void* d_ws,
                              size_t ws_size, hipStream_t stream) {
  fused_kernel<<<NWG, NW * 64, 0, stream>>>((const bf16*)d_in[0], (const bf16*)d_in[1], (const bf16*)d_in[2], (const bf16*)d_in[3],
                                            (const bf16*)d_in[4], (const bf16*)d_in[6], (const bf16*)d_in[7], (const bf16*)d_in[8],
                                            (const bf16*)d_in[9], (float*)d_ws, (bf16*)d_out);
}
```

```cpp
#include <hip/hip_runtime.h>
#include <hip/hip_bf16.h>
#include <stdint.h>

typedef __hip_bfloat16 bf16;
using u32x4 = __attribute__((ext_vector_type(4))) unsigned;
using u32x2 = __attribute__((ext_vector_type(2))) unsigned;
using f32x4 = __attribute__((ext_vector_type(4))) float;
using f32x2 = __attribute__((ext_vector_type(2))) float;

constexpr int NB = 4, DM = 640, NH = 32, SK = 8192, DH = 192, HD = NH * DH, FF = 1536;
constexpr int NWG = 256, NSEG = 6, NWIN = NB * NH * NSEG / NWG;
constexpr float LN_EPS = 1e-5f;
constexpr float QSCALE = 0.07216878364870322f * 1.4426950408889634f;

constexpr int WS_HP = 0;
constexpr int REC = DH + 32;
constexpr int WS_PO = WS_HP + 2 * NWG;
constexpr int WS_Z0 = WS_PO + 2 * NWG * REC;
constexpr int K3_KS = 12, K3_ROWS = HD / K3_KS;
constexpr int WS_SLAB = WS_PO + NB * NH * NSEG * REC;
constexpr int WS_ACT = WS_SLAB + K3_KS * NB * DM;
constexpr int WS_BASE = WS_ACT + NB * FF;
constexpr int ZPW = 416;
constexpr int WS_QI = WS_Z0 + NWG * ZPW;
constexpr int LSTR = 64;
constexpr int WS_REP = WS_QI + NB * NH * DH * 2;
constexpr int WS_HC = WS_REP + 256 * LSTR;
constexpr int WS_END = WS_HC + NWG * LSTR;
static_assert(WS_BASE + NB * DM * 2 <= WS_QI && WS_Z0 % 64 == 0 && WS_PO % 32 == 0 && ZPW % 32 == 0 && NWIN == 3, "layout");
__device__ __forceinline__ int ef_ofs(int i) { return (WS_PO + (2 * NWG + i) * REC + DH + 2) * 4; }
constexpr int OOB = 0x7ffffff0;

__device__ __forceinline__ float bf_lo(unsigned u) { return __uint_as_float(u << 16); }
__device__ __forceinline__ float bf_hi(unsigned u) { return __uint_as_float(u & 0xffff0000u); }
__device__ __forceinline__ f32x2 bf_pair(unsigned u) { return f32x2{bf_lo(u), bf_hi(u)}; }
__device__ __forceinline__ f32x2 fma2(float a, f32x2 b, f32x2 c) { return __builtin_elementwise_fma(f32x2{a, a}, b, c); }
__device__ __forceinline__ f32x2 fma2(f32x2 a, f32x2 b, f32x2 c) { return __builtin_elementwise_fma(a, b, c); }

template <int CTRL> __device__ __forceinline__ float dpp(float x) {
  return __builtin_bit_cast(float, __builtin_amdgcn_mov_dpp(__builtin_bit_cast(int, x), CTRL, 0xf, 0xf, true));
}
constexpr int DPP_XOR1 = 0xB1, DPP_XOR2 = 0x4E, DPP_HMIRROR = 0x141;

__device__ __forceinline__ float sum_x16_32(float x) {
  auto s = __builtin_amdgcn_permlane16_swap(__float_as_uint(x), __float_as_uint(x), false, false);
  x = __uint_as_float(s[0]) + __uint_as_float(s[1]);
  auto t = __builtin_amdgcn_permlane32_swap(__float_as_uint(x), __float_as_uint(x), false, false);
  return __uint_as_float(t[0]) + __uint_as_float(t[1]);
}
__device__ __forceinline__ float max_x16_32(float x) {
  auto s = __builtin_amdgcn_permlane16_swap(__float_as_uint(x), __float_as_uint(x), false, false);
  x = fmaxf(__uint_as_float(s[0]), __uint_as_float(s[1]));
  auto t = __builtin_amdgcn_permlane32_swap(__float_as_uint(x), __float_as_uint(x), false, false);
  return fmaxf(__uint_as_float(t[0]), __uint_as_float(t[1]));
}
__device__ __forceinline__ float tsum32(float a, float b) {
  auto t = __builtin_amdgcn_permlane32_swap(__float_as_uint(a), __float_as_uint(b), false, false);
  return __uint_as_float(t[0]) + __uint_as_float(t[1]);
}
__device__ __forceinline__ float tsum16(float a, float b) {
  auto t = __builtin_amdgcn_permlane16_swap(__float_as_uint(a), __float_as_uint(b), false, false);
  return __uint_as_float(t[0]) + __uint_as_float(t[1]);
}
constexpr int DPP_ROR8 = 0x128, DPP_BCAST = 0x150;
__device__ __forceinline__ float sum_r(float v) { v += dpp<DPP_ROR8>(v); return sum_x16_32(v); }
__device__ __forceinline__ float max_r(float v) { v = fmaxf(v, dpp<DPP_ROR8>(v)); return max_x16_32(v); }
__device__ __forceinline__ float wave_sum(float v) {
  v += dpp<DPP_XOR1>(v); v += dpp<DPP_XOR2>(v); v += dpp<DPP_HMIRROR>(v);
  return sum_r(v);
}

__device__ __forceinline__ float row16_sum(float v) {
  v += dpp<DPP_XOR1>(v); v += dpp<DPP_XOR2>(v); v += dpp<DPP_HMIRROR>(v);
  return v + dpp<DPP_ROR8>(v);
}

struct Slot { u32x4 a[3]; u32x4 b[3]; };

#define PIN8(a, o) asm volatile("" : "+v"((a)[(o) + 0]), "+v"((a)[(o) + 1]), "+v"((a)[(o) + 2]), "+v"((a)[(o) + 3]), \
                                       "+v"((a)[(o) + 4]), "+v"((a)[(o) + 5]), "+v"((a)[(o) + 6]), "+v"((a)[(o) + 7]))
#define PIN4(a, o) asm volatile("" : "+v"((a)[(o) + 0]), "+v"((a)[(o) + 1]), "+v"((a)[(o) + 2]), "+v"((a)[(o) + 3]))
#define MEMFENCE() do { asm volatile("" ::: "memory"); __builtin_amdgcn_sched_barrier(0); } while (0)
#define DRAIN() asm volatile("s_waitcnt vmcnt(0)" ::: "memory")

__device__ __forceinline__ void k_tile(const u32x4 (&k)[3], const f32x2 (&q)[12], float& gmx, float* dst, int j) {
  f32x2 s0 = {0.f, 0.f}, s1 = s0, s2 = s0;
#pragma unroll
  for (int d = 0; d < 4; ++d) {
    s0 = fma2(bf_pair(k[0][d]), q[d], s0);
    s1 = fma2(bf_pair(k[1][d]), q[4 + d], s1);
    s2 = fma2(bf_pair(k[2][d]), q[8 + d], s2);
  }
  s0 = (s0 + s1) + s2;
  float s = s0[0] + s0[1];
  s += dpp<DPP_XOR1>(s);
  s += dpp<DPP_XOR2>(s);
  s += dpp<DPP_HMIRROR>(s);
  gmx = fmaxf(gmx, s);
  asm volatile("" : "+v"(gmx));
  if (j == 0) *dst = s;
}
__device__ __forceinline__ void v_tile(u32x4 (&v)[3], float p, f32x2 (&acc)[12]) {
#pragma unroll
  for (int i = 0; i < 3; ++i) {
    asm volatile("" : "+v"(v[i]), "+v"(p));
#pragma unroll
    for (int d = 0; d < 4; ++d) acc[i * 4 + d] = fma2(p, bf_pair(v[i][d]), acc[i * 4 + d]);
    PIN4(acc, i * 4);
  }
}

constexpr int PTAU = 668;
constexpr int POFF = 100, FFWD = 96;
constexpr int LTGT = 220, LCAP = 600;
constexpr int DOWN = 30, AFROM = 3, EFROM = 12;
constexpr int PROBE_MIN = 4;
constexpr int AUX_SC1 = 16, AUX_NT = 2;
constexpr unsigned SPIN_CAP = 1u << 17;
__device__ __forceinline__ unsigned nz_bits(float v) { const unsigned u = __float_as_uint(v); return u == 0u ? 0x80000000u : u; }
__device__ __forceinline__ void lds_put(int* p, int v) { __hip_atomic_store(p, v, __ATOMIC_RELAXED, __HIP_MEMORY_SCOPE_WORKGROUP); }
__device__ __forceinline__ int lds_get(int* p) { return __hip_atomic_load(p, __ATOMIC_RELAXED, __HIP_MEMORY_SCOPE_WORKGROUP); }
using bf16x8 = __attribute__((ext_vector_type(8))) short;
__device__ __forceinline__ unsigned pk_lo(unsigned a, unsigned b) { return __builtin_amdgcn_perm(b, a, 0x05040100u); }
__device__ __forceinline__ unsigned pk_hi(unsigned a, unsigned b) { return __builtin_amdgcn_perm(b, a, 0x07060302u); }
__device__ __forceinline__ void split3(float v, unsigned& t0, unsigned& t1, unsigned& t2) {
  t0 = __float_as_uint(v) & 0xffff0000u;
  const float r1 = v - __uint_as_float(t0);
  t1 = __float_as_uint(r1) & 0xffff0000u;
  t2 = __float_as_uint(r1 - __uint_as_float(t1));
}
__device__ __forceinline__ f32x4 mfma16(u32x4 a, u32x4 b, f32x4 c) {
  return __builtin_amdgcn_mfma_f32_16x16x32_bf16(__builtin_bit_cast(bf16x8, a), __builtin_bit_cast(bf16x8, b), c, 0, 0, 0);
}
__device__ __forceinline__ void put_terms4(char* img, int rowbytes, f32x4 v) {
  unsigned t[3][4];
#pragma unroll
  for (int e = 0; e < 4; ++e) split3(v[e], t[0][e], t[1][e], t[2][e]);
#pragma unroll
  for (int k = 0; k < 3; ++k) *reinterpret_cast<u32x2*>(img + k * rowbytes) = u32x2{pk_hi(t[k][0], t[k][1]), pk_hi(t[k][2], t[k][3])};
}
__device__ __forceinline__ bool all_nz(u32x4 v) { return v[0] != 0u && v[1] != 0u && v[2] != 0u && v[3] != 0u; }

__device__ __forceinline__ float sum3(f32x4 v) { return (v[0] + v[1]) + v[2]; }

constexpr int NW = 8, NBUF = 4, NGRP = 64, NT = NGRP / NW;
constexpr int NQP = NH * DH / 64, NQW = 5, QW0 = 2, QST = DM / 64 / NQW;
constexpr int SVC = NW - 1;
constexpr int W_CT = DM / 64, G_TASKS = 2 * (FF / 32), W_TASKS = W_CT * K3_KS, D_TASKS = 2 * (DM / 32);
static_assert(G_TASKS + W_TASKS + D_TASKS == NWG && NW == 8 && K3_ROWS == 4 * 128 && NBUF == 4 && SK / 64 == 128, "shape");
__device__ __forceinline__ unsigned fresh_tag(unsigned tags) {
  unsigned t = __builtin_amdgcn_readfirstlane(tags);
  do ++t; while (__any(tags == t));
  return t;
}

__global__ void __launch_bounds__(NW * 64, 2) fused_kernel(const bf16* __restrict__ x, const bf16* __restrict__ kc, const bf16* __restrict__ vc,
                                                        const bf16* __restrict__ wq, const bf16* __restrict__ wo, const bf16* __restrict__ wr,
                                                        const bf16* __restrict__ wg, const bf16* __restrict__ wu, const bf16* __restrict__ wd,
                                                        float* ws, bf16* __restrict__ out) {
  constexpr int WA = 128 * 2 + 16, GA = DM * 2 + 16, DA = FF * 2 + 16;
  __shared__ __attribute__((aligned(16))) char s_A[8 * DA];
  __shared__ __attribute__((aligned(16))) float s_red[6 * 256];
  __shared__ __attribute__((aligned(16))) float s_h[2 * DM];
  __shared__ __attribute__((aligned(16))) float s_buf[NWIN * NW * DH];
  __shared__ __attribute__((aligned(16))) float s_sc[NGRP * 64];
  __shared__ __attribute__((aligned(16))) float s_q[NWIN * DH];
  __shared__ float s_m[NWIN * NW], s_l[NWIN * NW];
  __shared__ int s_qok, s_pcnt, s_ecnt, s_fcnt, s_icnt, s_org, s_hcnt;
  __shared__ int s_prog[NW];
  __shared__ float s_stat[4];

  const int tid = threadIdx.x;
  int lane = tid & 63, r = lane >> 3, j = lane & 7, mg = lane >> 4, mc = lane & 15;
  int wave = __builtin_amdgcn_readfirstlane(tid >> 6), bid = blockIdx.x;
  const unsigned t_start = (unsigned)__builtin_amdgcn_s_memrealtime();
  int wid = (bid >> 5) * 32 + (bid & 7) * 4 + ((bid >> 3) & 3);
  const bool prod = bid < NQP;
  const __amdgpu_buffer_rsrc_t rs_ws = __builtin_amdgcn_make_buffer_rsrc(ws, 0, WS_END * 4, 0x00020000);
  const __amdgpu_buffer_rsrc_t rs_k = __builtin_amdgcn_make_buffer_rsrc(const_cast<bf16*>(kc), 0, NB * NH * SK * DH * 2, 0x00020000);
  const __amdgpu_buffer_rsrc_t rs_v = __builtin_amdgcn_make_buffer_rsrc(const_cast<bf16*>(vc), 0, NB * NH * SK * DH * 2, 0x00020000);
  const int voff = r * (DH * 2) + j * 16;
  const int c1 = wid % 3 == 0 ? 22 : 21, c2 = wid % 3 == 1 ? 42 : 43;
  auto seg_of = [&](int n) { return (n * 8 + wave >= c1) + (n * 8 + wave >= c2); };
  auto gload = [&](Slot (&sl)[NBUF], __amdgpu_buffer_rsrc_t rs, int n) {
    const int sn = seg_of(n), sg = wid + NWG * sn, row = sg / NSEG, k = sg - row * NSEG;
    const int off = (row * (SK / 64) + k * 21 + (k + 2) / 3 + n * 8 + wave - (sn == 0 ? 0 : (sn == 1 ? c1 : c2))) * (64 * DH * 2);
#pragma unroll
    for (int u = 0; u < NBUF; ++u) {
#pragma unroll
      for (int i = 0; i < 3; ++i) sl[u].a[i] = __builtin_amdgcn_raw_buffer_load_b128(rs, voff + i * 128, off + 2 * u * 3072, AUX_NT);
      MEMFENCE();
#pragma unroll
      for (int i = 0; i < 3; ++i) sl[u].b[i] = __builtin_amdgcn_raw_buffer_load_b128(rs, voff + i * 128, off + (2 * u + 1) * 3072, AUX_NT);
      if (u < NBUF - 1) __builtin_amdgcn_s_sleep(3);
      MEMFENCE();
    }
  };
  asm volatile("" :: "s"(ws), "s"(x), "s"(wq), "s"(kc));
  Slot b0[NBUF];
  const bool early = wave < QW0 && !prod;
  if (early) gload(b0, rs_k, 0);
  unsigned hs_nc = 0u, hs_ep = 0u;
  auto look_at_p = [&]() {
    if (bid == NWG - 1) {
      u32x4 hp[2], hc[2];
#pragma unroll
      for (int i = 0; i < 2; ++i) {
        hp[i] = __builtin_amdgcn_raw_buffer_load_b128(rs_ws, (WS_HP + lane * 8 + i * 4) * 4, 0, AUX_SC1);
        const u32x2 c0 = __builtin_amdgcn_raw_buffer_load_b64(rs_ws, (WS_HC + (lane * 4 + i * 2) * LSTR) * 4, 0, AUX_SC1);
        const u32x2 c1_ = __builtin_amdgcn_raw_buffer_load_b64(rs_ws, (WS_HC + (lane * 4 + i * 2 + 1) * LSTR) * 4, 0, AUX_SC1);
        hc[i] = u32x4{c0[0], c0[1], c1_[0], c1_[1]};
      }
      if (__all(hc[0][1] == hp[0][0] && hc[0][3] == hp[0][2] && hc[1][1] == hp[1][0] && hc[1][3] == hp[1][2]) &&
          __any(hc[0][0] != hp[0][1] || hc[0][2] != hp[0][3] || hc[1][0] != hp[1][1] || hc[1][2] != hp[1][3])) {
#pragma unroll
        for (int i = 0; i < 2; ++i)
          __builtin_amdgcn_raw_buffer_store_b128(u32x4{hp[i][0], hc[i][0], hp[i][2], hc[i][2]}, rs_ws, (WS_HP + lane * 8 + i * 4) * 4, 0, AUX_SC1);
      }
    }
    const u32x2 pw = __builtin_amdgcn_raw_buffer_load_b64(rs_ws, (WS_HP + bid * 2) * 4, 0, AUX_SC1);
    const unsigned np = __builtin_amdgcn_readfirstlane(pw[0]), ec = __builtin_amdgcn_readfirstlane(pw[1]);
    if (np != hs_ep) { hs_ep = np; __builtin_amdgcn_raw_buffer_store_b64(u32x2{hs_nc, hs_ep}, rs_ws, lane == 0 ? (WS_HC + bid * LSTR) * 4 : OOB, 0, AUX_SC1); }
    return ec == hs_nc;
  };
  auto answer = [&]() {
    const u32x4 z4 = {0u, 0u, 0u, 0u};
#pragma unroll
    for (int i = 0; i < (ZPW + 255) / 256; ++i)
      __builtin_amdgcn_raw_buffer_store_b128(z4, rs_ws, i * 256 + lane * 4 < ZPW ? (WS_Z0 + bid * ZPW + i * 256 + lane * 4) * 4 : OOB, 0, AUX_SC1);
    const u32x2 pw = __builtin_amdgcn_raw_buffer_load_b64(rs_ws, (WS_HP + bid * 2) * 4, 0, AUX_SC1);
    DRAIN();
    hs_ep = __builtin_amdgcn_readfirstlane(pw[0]);
    hs_nc = __builtin_amdgcn_readfirstlane(pw[1]) + 1u;
    __builtin_amdgcn_raw_buffer_store_b64(u32x2{hs_nc, hs_ep}, rs_ws, lane == 0 ? (WS_HC + bid * LSTR) * 4 : OOB, 0, AUX_SC1);
  };
  bool answered = false;
  if (tid < NW) s_prog[tid] = 1;
  if (tid == 0) { s_qok = 0; s_pcnt = 0; s_ecnt = 0; s_fcnt = 0; s_icnt = 0; s_hcnt = 0; s_org = (int)(t_start + POFF); }
  if (wave == SVC) {
    __builtin_amdgcn_s_setprio(3);
    int iofs[NWIN];
#pragma unroll
    for (int s = 0; s < NWIN; ++s) iofs[s] = (WS_QI + ((wid + NWG * s) / NSEG * DH + lane) * 2) * 4;
    const int oofs = prod ? (WS_QI + (bid * 64 + lane) * 2) * 4 : OOB;
    u32x2 in[NWIN * 3];
    unsigned ot[NB], ce[4];
#pragma unroll
    for (int e = 0; e < NWIN * 3; ++e) in[e] = __builtin_amdgcn_raw_buffer_load_b64(rs_ws, iofs[e / 3], (e % 3) * 512, AUX_SC1);
#pragma unroll
    for (int bb = 0; bb < NB; ++bb) ot[bb] = __builtin_amdgcn_raw_buffer_load_b64(rs_ws, oofs, bb * (HD * 8), AUX_SC1)[1];
    u32x4 xr[5], hp[2];
#pragma unroll
    for (int i = 0; i < 5; ++i) xr[i] = reinterpret_cast<const u32x4*>(x)[lane * 5 + i];
#pragma unroll
    for (int i = 0; i < 2; ++i) hp[i] = __builtin_amdgcn_raw_buffer_load_b128(rs_ws, bid == NWG - 1 ? (WS_HP + lane * 8 + i * 4) * 4 : OOB, 0, AUX_SC1);
#pragma unroll
    for (int i = 0; i < 4; ++i) ce[i] = __builtin_amdgcn_raw_buffer_load_b32(rs_ws, bid == NWG - 1 ? (WS_HC + (lane * 4 + i) * LSTR + 1) * 4 : OOB, 0, AUX_SC1);
    MEMFENCE();
    __syncthreads();
    MEMFENCE();
    if (bid == NWG - 1) {
#pragma unroll
      for (int i = 0; i < 2; ++i) {
#pragma unroll
        for (int e = 0; e < 2; ++e) { unsigned np = hp[i][2 * e] + 1u; if (np == ce[2 * i + e]) np += 1u; hp[i][2 * e] = np; }
        __builtin_amdgcn_raw_buffer_store_b128(hp[i], rs_ws, (WS_HP + lane * 8 + i * 4) * 4, 0, AUX_SC1);
      }
    }
    unsigned want[NWIN * 3];
#pragma unroll
    for (int e = 0; e < NWIN * 3; ++e) want[e] = fresh_tag(in[e][1]);
    if (prod) {
      float xs = 0.f;
#pragma unroll
      for (int i = 0; i < 20; ++i) xs += bf_lo(xr[i >> 2][i & 3]) + bf_hi(xr[i >> 2][i & 3]);
      const float mean = row16_sum(xs) * (1.0f / DM);
      float xv = 0.f;
#pragma unroll
      for (int i = 0; i < 20; ++i) { const float a = bf_lo(xr[i >> 2][i & 3]) - mean, c = bf_hi(xr[i >> 2][i & 3]) - mean; xv += a * a + c * c; }
      const float rstd = QSCALE / sqrtf(row16_sum(xv) * (1.0f / DM) + LN_EPS);
#pragma unroll
      for (int bb = 0; bb < NB; ++bb) ot[bb] = fresh_tag(ot[bb]);
      for (unsigned spin = 0; spin < (SPIN_CAP << 4) && lds_get(&s_pcnt) < NQW; ++spin) __builtin_amdgcn_s_sleep(1);
      __builtin_amdgcn_fence(__ATOMIC_ACQUIRE, "workgroup");
      float pr[5];
#pragma unroll
      for (int i = 0; i < 5; ++i) {
        pr[i] = 0.f;
#pragma unroll
        for (int k = 0; k < NQW; ++k) pr[i] += s_buf[2 * k * 320 + i * 64 + lane] + s_buf[(2 * k + 1) * 320 + i * 64 + lane];
      }
#pragma unroll
      for (int bb = 0; bb < NB; ++bb) {
        const float qv = (pr[bb] - __int_as_float(__builtin_amdgcn_readlane(__float_as_int(mean), bb * 16)) * pr[4]) *
                         __int_as_float(__builtin_amdgcn_readlane(__float_as_int(rstd), bb * 16));
        __builtin_amdgcn_raw_buffer_store_b64(u32x2{__float_as_uint(qv), ot[bb]}, rs_ws, oofs, bb * (HD * 8), AUX_SC1);
      }
    } else {
      __builtin_amdgcn_s_sleep(32);
    }
    bool all_out = false;
#pragma unroll 1
    for (unsigned spin = 0; spin < SPIN_CAP; ++spin) {
#pragma unroll
      for (int e = 0; e < NWIN * 3; ++e) in[e] = __builtin_amdgcn_raw_buffer_load_b64(rs_ws, iofs[e / 3], (e % 3) * 512, AUX_SC1);
      bool ok = true;
#pragma unroll
      for (int e = 0; e < NWIN * 3; ++e) ok = ok && in[e][1] == want[e];
      if (all_out || __all(ok)) break;
      if (spin == 15) { answer(); answered = true; }
      if (spin > 15 && (spin & 3) == 3) all_out = look_at_p();
      __builtin_amdgcn_s_sleep(4);
    }
#pragma unroll
    for (int e = 0; e < NWIN * 3; ++e) s_q[e * 64 + lane] = __uint_as_float(in[e][0]);
    __builtin_amdgcn_fence(__ATOMIC_RELEASE, "workgroup");
    lds_put(&s_qok, 1);
    __builtin_amdgcn_s_setprio(0);
  } else if (prod && wave >= QW0 && wave < QW0 + NQW) {
    const int k0 = (wave - QW0) * (QST * 64) + (mc >> 3) * 32 + mg * 8;
    u32x4 raw[QST * 8], xa[QST];
    const bf16* wb = wq + (size_t)k0 * HD + bid * 64 + (mc & 7) * 8;
#pragma unroll
    for (int st = 0; st < QST; ++st) xa[st] = *reinterpret_cast<const u32x4*>(x + (mc & 3) * DM + k0 + st * 64);
#pragma unroll
    for (int e = 0; e < QST * 8; ++e) raw[e] = *reinterpret_cast<const u32x4*>(wb + (size_t)((e >> 3) * 64 + (e & 7)) * HD);
    MEMFENCE();
    __syncthreads();
    MEMFENCE();
    f32x4 ac[8];
#pragma unroll
    for (int t = 0; t < 8; ++t) ac[t] = f32x4{0.f, 0.f, 0.f, 0.f};
#pragma unroll
    for (int st = 0; st < QST; ++st) {
      const u32x4 a = (mc & 4) == 0 ? xa[st] : u32x4{0x3f803f80u, 0x3f803f80u, 0x3f803f80u, 0x3f803f80u};
#pragma unroll
      for (int c = 0; c < 4; ++c) {
        u32x4 f0, f1;
#pragma unroll
        for (int i = 0; i < 4; ++i) { f0[i] = pk_lo(raw[st * 8 + 2 * i][c], raw[st * 8 + 2 * i + 1][c]); f1[i] = pk_hi(raw[st * 8 + 2 * i][c], raw[st * 8 + 2 * i + 1][c]); }
        ac[2 * c] = mfma16(a, f0, ac[2 * c]);
        ac[2 * c + 1] = mfma16(a, f1, ac[2 * c + 1]);
      }
    }
    if ((mg >> 1) == (mc >> 3)) {
      float* const dst = &s_buf[((wave - QW0) * 2 + (mg >> 1)) * 320 + (mc & 7) * 8];
#pragma unroll
      for (int bb = 0; bb < NB; ++bb)
        if (bb == 0 || (mg & 1) == 0) {
          *reinterpret_cast<f32x4*>(dst + ((mg & 1) * 4 + bb) * 64) = f32x4{ac[0][bb], ac[1][bb], ac[2][bb], ac[3][bb]};
          *reinterpret_cast<f32x4*>(dst + ((mg & 1) * 4 + bb) * 64 + 4) = f32x4{ac[4][bb], ac[5][bb], ac[6][bb], ac[7][bb]};
        }
    }
    __builtin_amdgcn_fence(__ATOMIC_RELEASE, "workgroup");
    if (lane == 0) __hip_atomic_fetch_add(&s_pcnt, 1, __ATOMIC_RELAXED, __HIP_MEMORY_SCOPE_WORKGROUP);
  } else {
    __syncthreads();
  }
  MEMFENCE();
  u32x2 rep = {0u, 0u};
  unsigned t_thru = 0u;
  auto pace = [&](int g) {
    if (wave == 0 && g > AFROM) {
      const int org = lds_get(&s_org);
      const bool ok = rep[0] - t_start + 1000u < 6000u && rep[1] - t_start + 1000u < 6000u;
      const float dorg = row16_sum(ok ? (float)(int)(rep[1] - (unsigned)org) : 0.f);
      const float lt = ok ? (float)min((int)(rep[0] - rep[1]), LCAP) : (float)LCAP;
      const float lat = row16_sum(lt) * (1.f / 16.f);
      if (lane == 0) lds_put(&s_org, org + (int)(dorg * (1.f / 16.f) + fmaxf((lat - LTGT) * 0.6f, g > EFROM ? 0.f : -(float)DOWN)));
    }
    const unsigned rel = (unsigned)(g * PTAU + wave * (PTAU / 8) + min(g * 8 + wave, 16) * (FFWD / 16) - FFWD + ((wid * 65) & 255) * (PTAU / 8) / 256);
    unsigned now = (unsigned)__builtin_amdgcn_s_memrealtime();
    if (wave == 1 && (bid >> 4) == 7 && g >= 2)
      __builtin_amdgcn_raw_buffer_store_b64(u32x2{t_thru - rel + PTAU, (unsigned)lds_get(&s_org)}, rs_ws, lane == 0 ? (WS_REP + ((g - 2) * 16 + (bid & 15)) * LSTR) * 4 : OOB, 0, AUX_SC1);
    for (unsigned spin = 0; spin < 4096 && (int)(now - rel - (unsigned)__builtin_amdgcn_readfirstlane(lds_get(&s_org))) < 0; ++spin) {
      __builtin_amdgcn_s_sleep(2);
      now = (unsigned)__builtin_amdgcn_s_memrealtime();
    }
    if (g >= 2) rep = __builtin_amdgcn_raw_buffer_load_b64(rs_ws, (wave == 0 && lane < 16 && g >= AFROM) ? (WS_REP + ((g - 2) * 16 + lane) * LSTR) * 4 : OOB, 0, 0);
    if (lane == 0) lds_put(&s_prog[wave], g);
    int pp = __builtin_amdgcn_readfirstlane(lds_get(&s_prog[wave ^ (NW / 2)]));
    for (unsigned spin = 0; spin < 4096 && g > pp + 1; ++spin) { __builtin_amdgcn_s_sleep(4); pp = __builtin_amdgcn_readfirstlane(lds_get(&s_prog[wave ^ (NW / 2)])); }
    if (g > pp) __builtin_amdgcn_s_setprio(0); else __builtin_amdgcn_s_setprio(1);
  };
  float gmx = -INFINITY;
  auto k_group = [&](const Slot (&sl)[NBUF], int n, int s) {
    f32x2 q[12];
#pragma unroll
    for (int i = 0; i < 6; ++i) {
      const f32x4 a = *reinterpret_cast<const f32x4*>(&s_q[s * DH + (i >> 1) * 64 + j * 8 + (i & 1) * 4]);
      q[i * 2] = f32x2{a[0], a[1]}; q[i * 2 + 1] = f32x2{a[2], a[3]};
    }
    float* const sc = &s_sc[(wave * NT + n) * 64 + r * 8];
    MEMFENCE();
#pragma unroll
    for (int u = 0; u < NBUF; ++u) {
      k_tile(sl[u].a, q, gmx, sc + 2 * u, j);
      k_tile(sl[u].b, q, gmx, sc + 2 * u + 1, j);
      MEMFENCE();
    }
    t_thru = (unsigned)__builtin_amdgcn_s_memrealtime();
  };
  {
    Slot b1[NBUF];
    if (!early) {
      pace(0);
      gload(b0, rs_k, 0);
    }
    pace(1);
    gload(b1, rs_k, 1);
    for (unsigned spin = 0; spin < (SPIN_CAP << 4) && lds_get(&s_qok) == 0; ++spin) __builtin_amdgcn_s_sleep(2);
    __builtin_amdgcn_fence(__ATOMIC_ACQUIRE, "workgroup");
    k_group(b0, 0, 0);
    k_group(b1, 1, 0);
    if (wave == SVC && !answered) answer();
  }
#pragma unroll 1
  for (int pass = 1 - ((0x60 >> wave) & 1); pass < 2; ++pass) {
  int real = pass;
  asm volatile("" : "+s"(real));
  asm volatile("" : "+s"(wave), "+s"(bid), "+s"(wid), "+v"(lane));
  r = lane >> 3; j = lane & 7;
  if (real) {
  Slot buf[NBUF];
  int seg = 0;
  bool through = false;
  for (int n = 2; n < NT; ++n) {
    pace(n);
    gload(buf, rs_k, n);
    if (seg_of(n) != seg) {
      const float gm = max_r(gmx);
      if (lane == 0) s_m[seg * NW + wave] = gm;
      gmx = -INFINITY;
      ++seg;
    }
    k_group(buf, n, seg);
    if (wave == SVC && bid == NWG - 1 && n >= 3 && !through) through = look_at_p();
  }
  {
    const float gm = max_r(gmx);
    if (lane == 0) s_m[seg * NW + wave] = gm;
  }
  float l = 0.f;
  f32x2 acc[12];
#pragma unroll
  for (int i = 0; i < 12; ++i) acc[i] = f32x2{0.f, 0.f};
  __builtin_amdgcn_fence(__ATOMIC_RELEASE, "wavefront"); __builtin_amdgcn_wave_barrier(); __builtin_amdgcn_fence(__ATOMIC_ACQUIRE, "wavefront");
  for (int n = 0; n < NT; ++n) {
    pace(NT + n);
    gload(buf, rs_v, n);
    if (n == NT - 1) {
      if (lane == 0) __hip_atomic_fetch_add(&s_icnt, 1, __ATOMIC_RELAXED, __HIP_MEMORY_SCOPE_WORKGROUP);
      __builtin_amdgcn_s_setprio(3);
    }
    seg = seg_of(n);
    {
      float p[8];
      const float m = s_m[seg * NW + wave];
      const float* const sc = &s_sc[(wave * NT + n) * 64 + r * 8];
      const f32x4 sa = *reinterpret_cast<const f32x4*>(sc), sb = *reinterpret_cast<const f32x4*>(sc + 4);
#pragma unroll
      for (int t = 0; t < 4; ++t) { p[t] = __builtin_amdgcn_exp2f(sa[t] - m); p[4 + t] = __builtin_amdgcn_exp2f(sb[t] - m); }
      l += ((p[0] + p[1]) + (p[2] + p[3])) + ((p[4] + p[5]) + (p[6] + p[7]));
      MEMFENCE();
#pragma unroll
      for (int u = 0; u < NBUF; ++u) {
        v_tile(buf[u].a, p[2 * u], acc);
        v_tile(buf[u].b, p[2 * u + 1], acc);
        MEMFENCE();
      }
    }
    t_thru = (unsigned)__builtin_amdgcn_s_memrealtime();
    if (wave == SVC && n == 1)
      for (unsigned spin = 0; spin < SPIN_CAP && !look_at_p(); ++spin) __builtin_amdgcn_s_sleep(4);
    if (n == NT - 1 || seg_of(n + 1) != seg) {
      const float lw = sum_r(l);
      float t12[12], t6[6];
#pragma unroll
      for (int e = 0; e < 12; ++e) t12[e] = tsum32(acc[e >> 1][e & 1], acc[6 + (e >> 1)][e & 1]);
#pragma unroll
      for (int e = 0; e < 6; ++e) { t6[e] = tsum16(t12[e], t12[e + 6]); t6[e] += dpp<DPP_ROR8>(t6[e]); }
      if ((lane & 8) == 0) {
#pragma unroll
        for (int e = 0; e < 3; ++e) *reinterpret_cast<f32x2*>(&s_buf[(seg * NW + wave) * DH + j * 24 + (lane >> 4) * 6 + e * 2]) = f32x2{t6[2 * e], t6[2 * e + 1]};
      }
      if (lane == 0) s_l[seg * NW + wave] = lw;
      l = 0.f;
#pragma unroll
      for (int i = 0; i < 12; ++i) acc[i] = f32x2{0.f, 0.f};
      if (seg != 0) {
        __builtin_amdgcn_fence(__ATOMIC_RELEASE, "workgroup");
        int c = 0;
        if (lane == 0) c = __hip_atomic_fetch_add(seg == 1 ? &s_ecnt : &s_fcnt, 1, __ATOMIC_RELAXED, __HIP_MEMORY_SCOPE_WORKGROUP);
        if (__builtin_amdgcn_readfirstlane(c) == NW - 1) {
          __builtin_amdgcn_fence(__ATOMIC_ACQUIRE, "workgroup");
#pragma unroll 1
          for (int s = seg & 2; s <= seg; ++s) {
            float M = s_m[s * NW];
#pragma unroll
            for (int w = 1; w < NW; ++w) M = fmaxf(M, s_m[s * NW + w]);
            float L = 0.f;
            f32x4 o = {0.f, 0.f, 0.f, 0.f};
            const int d4 = lane < DH / 4 ? ((lane >> 1) & 7) * 24 + (lane >> 4) * 8 + (lane & 1) * 4 : 0;
#pragma unroll
            for (int w = 0; w < NW; ++w) {
              const float e = __builtin_amdgcn_exp2f(s_m[s * NW + w] - M);
              L = fmaf(e, s_l[s * NW + w], L);
              o += e * *reinterpret_cast<const f32x4*>(&s_buf[(s * NW + w) * DH + d4]);
            }
            if (lane >= DH / 4) o = f32x4{M, L, 1.f, 1.f};
            __builtin_amdgcn_raw_buffer_store_b128(u32x4{nz_bits(o[0]), nz_bits(o[1]), nz_bits(o[2]), nz_bits(o[3])}, rs_ws,
                                                   lane < REC / 4 ? (WS_PO + (wid + NWG * s) * REC + lane * 4) * 4 : OOB, 0, AUX_SC1);
          }
          if (seg == 2) { MEMFENCE(); lds_put(&s_fcnt, NW + 1); }
          if (seg == 1) {
            DRAIN();
            __builtin_amdgcn_raw_buffer_store_b32(1u, rs_ws, lane == 0 ? ef_ofs(wid) : OOB, 0, AUX_SC1);
          }
        }
      }
      MEMFENCE();
    }
  }
  __builtin_amdgcn_s_setprio(0);
  }
  {
    asm volatile("" : "+s"(wave), "+s"(bid), "+v"(lane));
    r = lane >> 3; j = lane & 7; mg = lane >> 4; mc = lane & 15;
    const int w = real ? wave : (wave - 5) * 7, td = w * 64 + lane;
    const int xcd = bid & 7, ri = bid >> 3;
    const bool isW = ri < 15, isG = !isW && ri < 27;
    const int tt = isW ? xcd * 15 + ri : (isG ? xcd * 12 + ri - 15 : xcd * 5 + ri - 27);
    const int bp = tt & 1, ct = tt >> 1;
    const int ks1 = tt / W_CT, ct1 = tt - ks1 * W_CT;
    const int nmw = isW ? 4 : (isG ? 5 : 6);
    const __amdgpu_buffer_rsrc_t rs_out = __builtin_amdgcn_make_buffer_rsrc(out, 0, NB * DM * 2, 0x00020000);
    u32x4 fr[16];
    u32x2 raw[32];
    auto weights = [&]() {
#pragma unroll
      for (int i = 0; i < 32; ++i) raw[i] = u32x2{0u, 0u};
      if (real && w < nmw) {
        int* const cnt = (isW || isG) ? &s_icnt : &s_fcnt;
        for (unsigned spin = 0; spin < (SPIN_CAP << 4) && lds_get(cnt) < NW; ++spin) __builtin_amdgcn_s_sleep(1);
        if (isW || isG) {
          for (int i = 0; i < 4 && lds_get(&s_fcnt) <= NW; ++i) __builtin_amdgcn_s_sleep(3);
        } else {
          __builtin_amdgcn_s_sleep(64);
        }
        typedef const __attribute__((address_space(1))) char* gptr;
        gptr wb = (gptr)(isW ? wo + (size_t)(ks1 * K3_ROWS + w * 128 + mg * 8) * DM + ct1 * 64 + mc * 4
                             : (isG ? (mc < 8 ? wg : wu) + (size_t)(w * 128 + mg * 8) * FF : wd + (size_t)(w * 256 + (mc >> 3) * 32 + mg * 8) * DM) + ct * 32 + (mc & 7) * 4);
        const int rstr = (isG ? FF : DM) * 2, sstr = (isW || isG ? 25 : 57) * rstr;
#pragma unroll
        for (int e = 0; e < 32; ++e) {
          raw[e] = *(const __attribute__((address_space(1))) u32x2*)wb;
          wb += (e & 7) == 7 ? sstr : rstr;
          asm volatile("" : "+v"(wb));
        }
        MEMFENCE();
      }
    };
    auto frags = [&]() {
      PIN8(raw, 0); PIN8(raw, 8); PIN8(raw, 16); PIN8(raw, 24);
#pragma unroll
      for (int st = 0; st < 4; ++st)
#pragma unroll
        for (int i = 0; i < 4; ++i) {
          const u32x2 r0 = raw[st * 8 + 2 * i], r1 = raw[st * 8 + 2 * i + 1];
          fr[st * 4 + 0][i] = pk_lo(r0[0], r1[0]); fr[st * 4 + 1][i] = pk_hi(r0[0], r1[0]);
          fr[st * 4 + 2][i] = pk_lo(r0[1], r1[1]); fr[st * 4 + 3][i] = pk_hi(r0[1], r1[1]);
        }
      PIN8(fr, 0); PIN8(fr, 8);
    };
    if (isW) {
      if (w >= 4) { if (real) break; else continue; }
      char* const img = s_A + w * (16 * WA);
      {
        int offp[2], ne[2], sg0[2];
#pragma unroll
        for (int hf = 0; hf < 2; ++hf) {
          const int row = ks1 * K3_ROWS + w * 128 + hf * 64, hh = row / DH;
          sg0[hf] = (mg * NH + hh) * NSEG;
          offp[hf] = (WS_PO + sg0[hf] * REC + row - hh * DH + mc * 4) * 4;
          ne[hf] = min(max(2 * NWG - sg0[hf], 0), NSEG);
        }
        const int sgm = (mc < 8 ? sg0[0] : sg0[1]) + j;
        const unsigned emask = (1u << ne[0]) - 1u + (((1u << ne[1]) - 1u) << NSEG) + (sgm < 2 * NWG ? 1u << (2 * NSEG) : 0u);
        u32x4 pv[2 * NSEG];
        u32x2 ml = {0u, 0u};
#pragma unroll
        for (int k = 0; k < 2 * NSEG; ++k) pv[k] = u32x4{0u, 0u, 0u, 0u};
        unsigned pend = real ? (1u << (2 * NSEG)) - 1u + (j < NSEG ? 1u << (2 * NSEG) : 0u) : 0u;
        auto look = [&](unsigned go) {
          if (go & (1u << (2 * NSEG))) ml = __builtin_amdgcn_raw_buffer_load_b64(rs_ws, (WS_PO + sgm * REC + DH) * 4, 0, AUX_SC1);
#pragma unroll
          for (int k = 0; k < 2 * NSEG; ++k)
            if (go & (1u << k)) pv[k] = __builtin_amdgcn_raw_buffer_load_b128(rs_ws, offp[k / NSEG], (k % NSEG) * REC * 4, AUX_SC1);
        };
        for (unsigned spin = 0; spin < (SPIN_CAP << 4) && real && lds_get(&s_ecnt) < NW; ++spin) __builtin_amdgcn_s_sleep(4);
        for (unsigned spin = 0; spin < SPIN_CAP && real; ++spin) {
          const bool fl = j < NSEG && sgm < 2 * NWG;
          if (__all(!fl || __builtin_amdgcn_raw_buffer_load_b32(rs_ws, fl ? ef_ofs(sgm & (NWG - 1)) : OOB, 0, AUX_SC1) != 0u)) break;
          __builtin_amdgcn_s_sleep(8);
        }
        weights();
        look(pend & emask);
        pend &= ~emask;
        for (unsigned sp2 = 0; sp2 < (SPIN_CAP << 4) && real && lds_get(&s_fcnt) < NW; ++sp2) __builtin_amdgcn_s_sleep(1);
        look(pend);
        MEMFENCE();
        frags();
#pragma unroll 1
        for (unsigned spin = 0; spin < SPIN_CAP; ++spin) {
          unsigned got = ml[0] != 0u && ml[1] != 0u;
#pragma unroll
          for (int k = 2 * NSEG - 1; k >= 0; --k) got = got * 2u + (unsigned)all_nz(pv[k]);
          pend &= ~got;
          if (!__any(pend != 0u)) break;
          if (spin >= 8) __builtin_amdgcn_s_sleep(4);
          look(pend);
        }
        const float mm = j < NSEG ? __uint_as_float(ml[0]) : -INFINITY;
        float M = fmaxf(mm, dpp<DPP_XOR1>(mm)); M = fmaxf(M, dpp<DPP_XOR2>(M)); M = fmaxf(M, dpp<DPP_HMIRROR>(M));
        const float e = __builtin_amdgcn_exp2f(mm - M);
        float L = e * __uint_as_float(ml[1]);
        L += dpp<DPP_XOR1>(L); L += dpp<DPP_XOR2>(L); L += dpp<DPP_HMIRROR>(L);
        const float wt = e * __builtin_amdgcn_rcpf(L);
        f32x4 o0 = dpp<DPP_BCAST + 0>(wt) * __builtin_bit_cast(f32x4, pv[0]), o1 = dpp<DPP_BCAST + 8>(wt) * __builtin_bit_cast(f32x4, pv[6]);
        o0 += dpp<DPP_BCAST + 1>(wt) * __builtin_bit_cast(f32x4, pv[1]); o1 += dpp<DPP_BCAST + 9>(wt) * __builtin_bit_cast(f32x4, pv[7]);
        o0 += dpp<DPP_BCAST + 2>(wt) * __builtin_bit_cast(f32x4, pv[2]); o1 += dpp<DPP_BCAST + 10>(wt) * __builtin_bit_cast(f32x4, pv[8]);
        o0 += dpp<DPP_BCAST + 3>(wt) * __builtin_bit_cast(f32x4, pv[3]); o1 += dpp<DPP_BCAST + 11>(wt) * __builtin_bit_cast(f32x4, pv[9]);
        o0 += dpp<DPP_BCAST + 4>(wt) * __builtin_bit_cast(f32x4, pv[4]); o1 += dpp<DPP_BCAST + 12>(wt) * __builtin_bit_cast(f32x4, pv[10]);
        o0 += dpp<DPP_BCAST + 5>(wt) * __builtin_bit_cast(f32x4, pv[5]); o1 += dpp<DPP_BCAST + 13>(wt) * __builtin_bit_cast(f32x4, pv[11]);
        put_terms4(img + mg * 4 * WA + mc * 8, WA, o0);
        put_terms4(img + mg * 4 * WA + 128 + mc * 8, WA, o1);
      }
      __builtin_amdgcn_fence(__ATOMIC_RELEASE, "wavefront"); __builtin_amdgcn_wave_barrier(); __builtin_amdgcn_fence(__ATOMIC_ACQUIRE, "wavefront");
      f32x4 ac[4];
#pragma unroll
      for (int t = 0; t < 4; ++t) ac[t] = f32x4{0.f, 0.f, 0.f, 0.f};
#pragma unroll
      for (int st = 0; st < 4; ++st) {
        const u32x4 a = *reinterpret_cast<const u32x4*>(img + mc * WA + st * 64 + mg * 16);
#pragma unroll
        for (int t = 0; t < 4; ++t) ac[t] = mfma16(a, fr[st * 4 + t], ac[t]);
      }
      *reinterpret_cast<f32x4*>(&s_red[w * 256 + lane * 4]) = f32x4{sum3(ac[0]), sum3(ac[1]), sum3(ac[2]), sum3(ac[3])};
      if (real) __syncthreads();
      if (w == 0) {
        const f32x4 s0 = (*reinterpret_cast<const f32x4*>(&s_red[lane * 4]) + *reinterpret_cast<const f32x4*>(&s_red[256 + lane * 4])) +
                         (*reinterpret_cast<const f32x4*>(&s_red[512 + lane * 4]) + *reinterpret_cast<const f32x4*>(&s_red[768 + lane * 4]));
        __builtin_amdgcn_raw_buffer_store_b128(u32x4{nz_bits(s0[0]), nz_bits(s0[1]), nz_bits(s0[2]), nz_bits(s0[3])}, rs_ws,
                                               real ? (WS_SLAB + (ks1 * NB + mg) * DM + ct1 * 64 + mc * 4) * 4 : OOB, 0, AUX_SC1);
      }
    } else {
      const int hcol = (lane >> 5) * DM + w * 128 + (lane & 31) * 4;
      uint2 xx = {0u, 0u};
      unsigned wr2[5] = {0u, 0u, 0u, 0u, 0u};
      if (real) {
        xx = *reinterpret_cast<const uint2*>(x + (size_t)bp * 2 * DM + (w < 5 ? hcol : 0));
#pragma unroll
        for (int i = 0; i < 5; ++i) wr2[i] = reinterpret_cast<const unsigned*>(wr)[lane + 64 * i];
        unsigned one = 0x3f803f80u;
        asm volatile("" : "+v"(one));
        if (isG && w < 5 && lane < 16) *reinterpret_cast<u32x4*>(s_A + 3 * GA + w * 256 + lane * 16) = u32x4{one, one, one, one};
      }
      weights();
      frags();
      PIN4(wr2, 0); asm volatile("" : "+v"(wr2[4]), "+v"(xx.x), "+v"(xx.y));
      const int bb = lane >> 5, f = lane & 31;
      int a_img = (mc & 7) * (isG ? GA : DA) + (isG ? w * 4 : w * 8 + (mc >> 3)) * 64 + mg * 16;
      int a_red = w * 256 + (isG ? lane * 4 : mg * 32 + (mc & 7) * 4), i0 = isG ? bb * 64 + f : lane, i1 = isG ? bb * 64 + 32 + f : 64 + lane;
      int o_res = !real ? OOB : (isG ? (WS_ACT + (bp * 2 + bb) * FF + ct * 32 + f) * 4 : ((bp * 2 + bb) * DM + ct * 32 + f) * 2);
      asm volatile("" : "+v"(a_img), "+v"(a_red), "+v"(i0), "+v"(i1), "+v"(o_res));
      if (isG && w < 5) {
        const int off = (WS_SLAB + bp * 2 * DM + hcol) * 4;
        const int poff = (WS_SLAB + bp * 2 * DM + w * 128 + (mg & 1) * 64 + mc * (NB * DM)) * 4;
        u32x4 sl[K3_KS];
#pragma unroll
        for (int ks = 0; ks < K3_KS; ++ks) sl[ks] = u32x4{0u, 0u, 0u, 0u};
        unsigned pend = real ? (1u << K3_KS) - 1u : 0u;
        unsigned todo = pend, go = 0u;
        for (unsigned spin = 0; spin < SPIN_CAP; ++spin) {
#pragma unroll
          for (int ks = 0; ks < K3_KS; ++ks)
            if (go & (1u << ks)) sl[ks] = __builtin_amdgcn_raw_buffer_load_b128(rs_ws, off, ks * (NB * DM * 4), AUX_SC1);
          todo &= ~go;
          if (todo != 0u) {
            const unsigned pr = __builtin_amdgcn_raw_buffer_load_b32(rs_ws, (mg < 2 && mc < K3_KS && ((todo >> mc) & 1u)) ? poff : OOB, 0, AUX_SC1);
            go = (unsigned)__ballot(pr != 0u);
            go = go & go >> 16 & todo;
            if (__builtin_popcount(todo & ~go) <= PROBE_MIN) go = todo;
          } else {
            unsigned got = 0u;
#pragma unroll
            for (int ks = K3_KS - 1; ks >= 0; --ks) got = got * 2u + (unsigned)__all(all_nz(sl[ks]));
            go = pend & ~got;
            if (go == 0u) break;
          }
        }
        f32x4 hv = f32x4{bf_lo(xx.x), bf_hi(xx.x), bf_lo(xx.y), bf_hi(xx.y)};
#pragma unroll
        for (int ks = 0; ks < K3_KS; ++ks) hv += __builtin_bit_cast(f32x4, sl[ks]);
        *reinterpret_cast<f32x4*>(&s_h[hcol]) = hv;
        __builtin_amdgcn_fence(__ATOMIC_RELEASE, "workgroup");
        if (lane == 0 && real) __hip_atomic_fetch_add(&s_hcnt, 1, __ATOMIC_RELAXED, __HIP_MEMORY_SCOPE_WORKGROUP);
        put_terms4(s_A + (lane >> 5) * 4 * GA + w * 256 + (lane & 31) * 8, GA, hv);
      }
      unsigned bs = 0u, sg = 0u;
      if (isG && w >= NW - 2) {
        const int rw = w - (NW - 2);
        for (unsigned spin = 0; spin < (SPIN_CAP << 6) && real && lds_get(&s_hcnt) < 5; ++spin) {}
        __builtin_amdgcn_fence(__ATOMIC_ACQUIRE, "workgroup");
        const float piv = s_h[rw * DM];
        float hv[10], sm = 0.f, sq = 0.f;
#pragma unroll
        for (int i = 0; i < 5; ++i) {
          const float2 t2 = *reinterpret_cast<const float2*>(&s_h[rw * DM + 2 * lane + 128 * i]);
          hv[2 * i] = t2.x; hv[2 * i + 1] = t2.y;
        }
        PIN8(hv, 0); asm volatile("" : "+v"(hv[8]), "+v"(hv[9]));
#pragma unroll
        for (int i = 0; i < 10; ++i) { hv[i] -= piv; sm += hv[i]; sq = fmaf(hv[i], hv[i], sq); }
        const float dm = wave_sum(sm) * (1.0f / DM);
        const float var = fmaxf(fmaf(-dm, dm, wave_sum(sq) * (1.0f / DM)), 0.f);
        const float rstd = __builtin_amdgcn_rsqf(var + LN_EPS);
        if (lane == 0) { s_stat[rw * 2] = piv + dm; s_stat[rw * 2 + 1] = rstd; }
        if (real) __syncthreads();
        if (ct < DM / 32) {
          float rr = 0.f;
#pragma unroll
          for (int i = 0; i < 5; ++i) { rr = fmaf((hv[2 * i] - dm) * rstd, bf_lo(wr2[i]), rr); rr = fmaf((hv[2 * i + 1] - dm) * rstd, bf_hi(wr2[i]), rr); }
          rr = wave_sum(rr);
          const float hh = s_h[rw * DM + ct * 32 + f];
          __builtin_amdgcn_raw_buffer_store_b32(nz_bits(bb ? 1.0f / (1.0f + __expf(-rr)) : hh + (hh - (piv + dm)) * rstd), rs_ws,
                                                real ? (WS_BASE + ((bp * 2 + rw) * (DM / 32) + ct) * 64 + lane) * 4 : OOB, 0, AUX_SC1);
        }
        continue;
      } else if (!isG && w < 6) {
        const int off = (WS_ACT + bp * 2 * FF + td * 4) * 4;
        u32x4 v0 = {0u, 0u, 0u, 0u}, v1 = v0;
        const int offb = (WS_BASE + ((bp * 2 + bb) * (DM / 32) + ct) * 64 + f) * 4;
        unsigned pend = real ? (w == 0 ? 7u : 3u) : 0u;
        for (unsigned spin = 0; spin < SPIN_CAP; ++spin) {
          if (pend & 1u) v0 = __builtin_amdgcn_raw_buffer_load_b128(rs_ws, off, 0, AUX_SC1);
          if (pend & 2u) v1 = __builtin_amdgcn_raw_buffer_load_b128(rs_ws, off, FF * 4, AUX_SC1);
          if (pend & 4u) { bs = __builtin_amdgcn_raw_buffer_load_b32(rs_ws, offb, 0, AUX_SC1); sg = __builtin_amdgcn_raw_buffer_load_b32(rs_ws, offb, 128, AUX_SC1); }
          pend &= ~((unsigned)all_nz(v0) + 2u * (unsigned)all_nz(v1) + 4u * (unsigned)(bs != 0u && sg != 0u));
          if (!__any(pend != 0u)) break;
        }
        put_terms4(s_A + td * 8, DA, __builtin_bit_cast(f32x4, v0));
        put_terms4(s_A + 4 * DA + td * 8, DA, __builtin_bit_cast(f32x4, v1));
      }
      if (w < nmw) {
        __builtin_amdgcn_fence(__ATOMIC_RELEASE, "wavefront"); __builtin_amdgcn_wave_barrier(); __builtin_amdgcn_fence(__ATOMIC_ACQUIRE, "wavefront");
        f32x4 ac[4];
#pragma unroll
        for (int t = 0; t < 4; ++t) ac[t] = f32x4{0.f, 0.f, 0.f, 0.f};
        const int astep = isG ? 64 : 2 * 64;
        u32x4 av[4];
#pragma unroll
        for (int st = 0; st < 4; ++st) av[st] = *reinterpret_cast<const u32x4*>(s_A + a_img + st * astep);
#pragma unroll
        for (int st = 0; st < 4; ++st)
#pragma unroll
          for (int t = 0; t < 4; ++t) ac[t] = mfma16(av[st], fr[st * 4 + t], ac[t]);
        if (isG ? lane < 32 : (mc >> 3) == (mg >> 1)) *reinterpret_cast<f32x4*>(&s_red[a_red]) = f32x4{sum3(ac[0]), sum3(ac[1]), sum3(ac[2]), sum3(ac[3])};
        if (lane < 16) *reinterpret_cast<f32x4*>(&s_red[w * 256 + 128 + lane * 4]) = f32x4{ac[0][3], ac[1][3], ac[2][3], ac[3][3]};
      }
      if (real) __syncthreads();
      if (w == 0) {
        float pr[24];
#pragma unroll
        for (int k = 0; k < 6; ++k) { pr[k] = s_red[k * 256 + i0]; pr[6 + k] = s_red[k * 256 + i1]; }
#pragma unroll
        for (int k = 0; k < 5; ++k) { pr[12 + k] = s_red[k * 256 + 128 + f]; pr[17 + k] = s_red[k * 256 + 160 + f]; }
        pr[22] = s_stat[bb * 2]; pr[23] = s_stat[bb * 2 + 1];
        PIN8(pr, 0); PIN8(pr, 8); PIN8(pr, 16);
        const float mean = pr[22], rstd = pr[23];
        const float s0 = ((pr[0] + pr[1]) + (pr[2] + pr[3])) + pr[4], s1 = ((pr[6] + pr[7]) + (pr[8] + pr[9])) + pr[10];
        if (isG) {
          const float c0 = ((pr[12] + pr[13]) + (pr[14] + pr[15])) + pr[16], c1_ = ((pr[17] + pr[18]) + (pr[19] + pr[20])) + pr[21];
          const float gg = (s0 - mean * c0) * rstd, uu = (s1 - mean * c1_) * rstd;
          const float a = gg * __builtin_amdgcn_rcpf(1.0f + __expf(-gg)) * uu;
          __builtin_amdgcn_raw_buffer_store_b32(nz_bits(a), rs_ws, o_res, 0, AUX_SC1);
        } else {
          const bf16 ov = __float2bfloat16(fmaf((s0 + pr[5]) + (s1 + pr[11]), __uint_as_float(sg), __uint_as_float(bs)));
          __builtin_amdgcn_raw_buffer_store_b16(__builtin_bit_cast(unsigned short, ov), rs_out, o_res, 0, 0);
        }
      }
    }
  }
  }
}

extern "C" void kernel_launch(void* const* d_in, const int* in_sizes, int n_in, void* d_out, int out_size, void* d_ws,
                              size_t ws_size, hipStream_t stream) {
  fused_kernel<<<NWG, NW * 64, 0, stream>>>((const bf16*)d_in[0], (const bf16*)d_in[1], (const bf16*)d_in[2], (const bf16*)d_in[3],
                                            (const bf16*)d_in[4], (const bf16*)d_in[6], (const bf16*)d_in[7], (const bf16*)d_in[8],
                                            (const bf16*)d_in[9], (float*)d_ws, (bf16*)d_out);
}
```

```cpp
#include <hip/hip_runtime.h>
#include <hip/hip_bf16.h>
#include <stdint.h>

typedef __hip_bfloat16 bf16;
using u32x4 = __attribute__((ext_vector_type(4))) unsigned;
using u32x2 = __attribute__((ext_vector_type(2))) unsigned;
using f32x4 = __attribute__((ext_vector_type(4))) float;
using f32x2 = __attribute__((ext_vector_type(2))) float;

constexpr int NB = 4, DM = 640, NH = 32, SK = 8192, DH = 192, HD = NH * DH, FF = 1536;
constexpr int NWG = 256, NSEG = 6, NWIN = NB * NH * NSEG / NWG;
constexpr float LN_EPS = 1e-5f;
constexpr float QSCALE = 0.07216878364870322f * 1.4426950408889634f;

constexpr int WS_HP = 0;
constexpr int REC = DH + 32;
constexpr int WS_PO = WS_HP + 2 * NWG;
constexpr int WS_Z0 = WS_PO + 2 * NWG * REC;
constexpr int K3_KS = 12, K3_ROWS = HD / K3_KS;
constexpr int WS_SLAB = WS_PO + NB * NH * NSEG * REC;
constexpr int WS_ACT = WS_SLAB + K3_KS * NB * DM;
constexpr int WS_BASE = WS_ACT + NB * FF * 3 / 2;
constexpr int ZPW = 416;
constexpr int WS_QI = WS_Z0 + NWG * ZPW;
constexpr int LSTR = 64;
constexpr int WS_REP = WS_QI + NB * NH * DH * 2;
constexpr int WS_HC = WS_REP + 256 * LSTR;
constexpr int WS_END = WS_HC + NWG * LSTR;
static_assert(WS_BASE + NB * DM * 2 <= WS_QI && WS_Z0 % 64 == 0 && WS_PO % 32 == 0 && ZPW % 32 == 0 && NWIN == 3, "layout");
__device__ __forceinline__ int ef_ofs(int i) { return (WS_PO + (2 * NWG + i) * REC + DH + 2) * 4; }
constexpr int OOB = 0x7ffffff0;

__device__ __forceinline__ float bf_lo(unsigned u) { return __uint_as_float(u << 16); }
__device__ __forceinline__ float bf_hi(unsigned u) { return __uint_as_float(u & 0xffff0000u); }
__device__ __forceinline__ f32x2 bf_pair(unsigned u) { return f32x2{bf_lo(u), bf_hi(u)}; }
__device__ __forceinline__ f32x2 fma2(float a, f32x2 b, f32x2 c) { return __builtin_elementwise_fma(f32x2{a, a}, b, c); }
__device__ __forceinline__ f32x2 fma2(f32x2 a, f32x2 b, f32x2 c) { return __builtin_elementwise_fma(a, b, c); }

template <int CTRL> __device__ __forceinline__ float dpp(float x) {
  return __builtin_bit_cast(float, __builtin_amdgcn_mov_dpp(__builtin_bit_cast(int, x), CTRL, 0xf, 0xf, true));
}
constexpr int DPP_XOR1 = 0xB1, DPP_XOR2 = 0x4E, DPP_HMIRROR = 0x141;

__device__ __forceinline__ float sum_x16_32(float x) {
  auto s = __builtin_amdgcn_permlane16_swap(__float_as_uint(x), __float_as_uint(x), false, false);
  x = __uint_as_float(s[0]) + __uint_as_float(s[1]);
  auto t = __builtin_amdgcn_permlane32_swap(__float_as_uint(x), __float_as_uint(x), false, false);
  return __uint_as_float(t[0]) + __uint_as_float(t[1]);
}
__device__ __forceinline__ float max_x16_32(float x) {
  auto s = __builtin_amdgcn_permlane16_swap(__float_as_uint(x), __float_as_uint(x), false, false);
  x = fmaxf(__uint_as_float(s[0]), __uint_as_float(s[1]));
  auto t = __builtin_amdgcn_permlane32_swap(__float_as_uint(x), __float_as_uint(x), false, false);
  return fmaxf(__uint_as_float(t[0]), __uint_as_float(t[1]));
}
__device__ __forceinline__ float tsum32(float a, float b) {
  auto t = __builtin_amdgcn_permlane32_swap(__float_as_uint(a), __float_as_uint(b), false, false);
  return __uint_as_float(t[0]) + __uint_as_float(t[1]);
}
__device__ __forceinline__ float tsum16(float a, float b) {
  auto t = __builtin_amdgcn_permlane16_swap(__float_as_uint(a), __float_as_uint(b), false, false);
  return __uint_as_float(t[0]) + __uint_as_float(t[1]);
}
constexpr int DPP_ROR8 = 0x128, DPP_BCAST = 0x150;
__device__ __forceinline__ float sum_r(float v) { v += dpp<DPP_ROR8>(v); return sum_x16_32(v); }
__device__ __forceinline__ float max_r(float v) { v = fmaxf(v, dpp<DPP_ROR8>(v)); return max_x16_32(v); }
__device__ __forceinline__ float wave_sum(float v) {
  v += dpp<DPP_XOR1>(v); v += dpp<DPP_XOR2>(v); v += dpp<DPP_HMIRROR>(v);
  return sum_r(v);
}

__device__ __forceinline__ float row16_sum(float v) {
  v += dpp<DPP_XOR1>(v); v += dpp<DPP_XOR2>(v); v += dpp<DPP_HMIRROR>(v);
  return v + dpp<DPP_ROR8>(v);
}

struct Slot { u32x4 a[3]; u32x4 b[3]; };

#define PIN8(a, o) asm volatile("" : "+v"((a)[(o) + 0]), "+v"((a)[(o) + 1]), "+v"((a)[(o) + 2]), "+v"((a)[(o) + 3]), \
                                       "+v"((a)[(o) + 4]), "+v"((a)[(o) + 5]), "+v"((a)[(o) + 6]), "+v"((a)[(o) + 7]))
#define PIN4(a, o) asm volatile("" : "+v"((a)[(o) + 0]), "+v"((a)[(o) + 1]), "+v"((a)[(o) + 2]), "+v"((a)[(o) + 3]))
#define MEMFENCE() do { asm volatile("" ::: "memory"); __builtin_amdgcn_sched_barrier(0); } while (0)
#define DRAIN() asm volatile("s_waitcnt vmcnt(0)" ::: "memory")

__device__ __forceinline__ void k_tile(const u32x4 (&k)[3], const f32x2 (&q)[12], float& gmx, float* dst, int j) {
  f32x2 s0 = {0.f, 0.f}, s1 = s0, s2 = s0;
#pragma unroll
  for (int d = 0; d < 4; ++d) {
    s0 = fma2(bf_pair(k[0][d]), q[d], s0);
    s1 = fma2(bf_pair(k[1][d]), q[4 + d], s1);
    s2 = fma2(bf_pair(k[2][d]), q[8 + d], s2);
  }
  s0 = (s0 + s1) + s2;
  float s = s0[0] + s0[1];
  s += dpp<DPP_XOR1>(s);
  s += dpp<DPP_XOR2>(s);
  s += dpp<DPP_HMIRROR>(s);
  gmx = fmaxf(gmx, s);
  asm volatile("" : "+v"(gmx));
  if (j == 0) *dst = s;
}
__device__ __forceinline__ void v_tile(u32x4 (&v)[3], float p, f32x2 (&acc)[12]) {
#pragma unroll
  for (int i = 0; i < 3; ++i) {
    asm volatile("" : "+v"(v[i]), "+v"(p));
#pragma unroll
    for (int d = 0; d < 4; ++d) acc[i * 4 + d] = fma2(p, bf_pair(v[i][d]), acc[i * 4 + d]);
    PIN4(acc, i * 4);
  }
}

constexpr int PTAU = 668;
constexpr int POFF = 100, FFWD = 96;
constexpr int LTGT = 220, LCAP = 600;
constexpr int DOWN = 30, AFROM = 3, EFROM = 12;
constexpr int PROBE_MIN = 10;
constexpr int AUX_SC1 = 16, AUX_NT = 2;
constexpr unsigned SPIN_CAP = 1u << 17;
__device__ __forceinline__ unsigned nz_bits(float v) { const unsigned u = __float_as_uint(v); return u == 0u ? 0x80000000u : u; }
__device__ __forceinline__ void lds_put(int* p, int v) { __hip_atomic_store(p, v, __ATOMIC_RELAXED, __HIP_MEMORY_SCOPE_WORKGROUP); }
__device__ __forceinline__ int lds_get(int* p) { return __hip_atomic_load(p, __ATOMIC_RELAXED, __HIP_MEMORY_SCOPE_WORKGROUP); }
using bf16x8 = __attribute__((ext_vector_type(8))) short;
__device__ __forceinline__ unsigned pk_lo(unsigned a, unsigned b) { return __builtin_amdgcn_perm(b, a, 0x05040100u); }
__device__ __forceinline__ unsigned pk_hi(unsigned a, unsigned b) { return __builtin_amdgcn_perm(b, a, 0x07060302u); }
__device__ __forceinline__ void split3(float v, unsigned& t0, unsigned& t1, unsigned& t2) {
  t0 = __float_as_uint(v) & 0xffff0000u;
  const float r1 = v - __uint_as_float(t0);
  t1 = __float_as_uint(r1) & 0xffff0000u;
  t2 = __float_as_uint(r1 - __uint_as_float(t1));
}
__device__ __forceinline__ f32x4 mfma16(u32x4 a, u32x4 b, f32x4 c) {
  return __builtin_amdgcn_mfma_f32_16x16x32_bf16(__builtin_bit_cast(bf16x8, a), __builtin_bit_cast(bf16x8, b), c, 0, 0, 0);
}
__device__ __forceinline__ void put_terms4(char* img, int rowbytes, f32x4 v) {
  unsigned t[3][4];
#pragma unroll
  for (int e = 0; e < 4; ++e) split3(v[e], t[0][e], t[1][e], t[2][e]);
#pragma unroll
  for (int k = 0; k < 3; ++k) *reinterpret_cast<u32x2*>(img + k * rowbytes) = u32x2{pk_hi(t[k][0], t[k][1]), pk_hi(t[k][2], t[k][3])};
}
__device__ __forceinline__ bool all_nz(u32x4 v) { return v[0] != 0u && v[1] != 0u && v[2] != 0u && v[3] != 0u; }

__device__ __forceinline__ float sum3(f32x4 v) { return (v[0] + v[1]) + v[2]; }

constexpr int NW = 8, NBUF = 4, NGRP = 64, NT = NGRP / NW;
constexpr int NQP = NH * DH / 64, NQW = 5, QW0 = 2, QST = DM / 64 / NQW;
constexpr int SVC = NW - 1;
constexpr int W_CT = DM / 64, G_TASKS = NB * (FF / 64), W_TASKS = W_CT * K3_KS, D_TASKS = 2 * (DM / 32);
static_assert(G_TASKS + W_TASKS + D_TASKS == NWG && NW == 8 && K3_ROWS == 4 * 128 && NBUF == 4 && SK / 64 == 128, "shape");
__device__ __forceinline__ unsigned fresh_tag(unsigned tags) {
  unsigned t = __builtin_amdgcn_readfirstlane(tags);
  do ++t; while (__any(tags == t));
  return t;
}

__global__ void __launch_bounds__(NW * 64, 2) fused_kernel(const bf16* __restrict__ x, const bf16* __restrict__ kc, const bf16* __restrict__ vc,
                                                        const bf16* __restrict__ wq, const bf16* __restrict__ wo, const bf16* __restrict__ wr,
                                                        const bf16* __restrict__ wg, const bf16* __restrict__ wu, const bf16* __restrict__ wd,
                                                        float* ws, bf16* __restrict__ out) {
  constexpr int WA = 128 * 2 + 16;
  __shared__ __attribute__((aligned(16))) char s_A[4 * 16 * WA];
  __shared__ __attribute__((aligned(16))) float s_red[6 * 256];
  __shared__ __attribute__((aligned(16))) float s_h[DM];
  __shared__ __attribute__((aligned(16))) float s_buf[NWIN * NW * DH];
  __shared__ __attribute__((aligned(16))) float s_sc[NGRP * 64];
  __shared__ __attribute__((aligned(16))) float s_q[NWIN * DH];
  __shared__ float s_m[NWIN * NW], s_l[NWIN * NW];
  __shared__ int s_qok, s_pcnt, s_ecnt, s_fcnt, s_icnt, s_org, s_hcnt;
  __shared__ int s_prog[NW];
  __shared__ float s_stat[4];

  const int tid = threadIdx.x;
  int lane = tid & 63, r = lane >> 3, j = lane & 7, mg = lane >> 4, mc = lane & 15;
  int wave = __builtin_amdgcn_readfirstlane(tid >> 6), bid = blockIdx.x;
  const unsigned t_start = (unsigned)__builtin_amdgcn_s_memrealtime();
  const int wid = (bid >> 5) * 32 + (bid & 7) * 4 + ((bid >> 3) & 3);
  const bool prod = bid < NQP;
  const __amdgpu_buffer_rsrc_t rs_ws = __builtin_amdgcn_make_buffer_rsrc(ws, 0, WS_END * 4, 0x00020000);
  const __amdgpu_buffer_rsrc_t rs_k = __builtin_amdgcn_make_buffer_rsrc(const_cast<bf16*>(kc), 0, NB * NH * SK * DH * 2, 0x00020000);
  const __amdgpu_buffer_rsrc_t rs_v = __builtin_amdgcn_make_buffer_rsrc(const_cast<bf16*>(vc), 0, NB * NH * SK * DH * 2, 0x00020000);
  const int voff = r * (DH * 2) + j * 16;
  const int c1 = wid % 3 == 0 ? 22 : 21, c2 = wid % 3 == 1 ? 42 : 43;
  auto seg_of = [&](int n) { return (n * 8 + wave >= c1) + (n * 8 + wave >= c2); };
  auto gload = [&](Slot (&sl)[NBUF], __amdgpu_buffer_rsrc_t rs, int n) {
    const int sn = seg_of(n), sg = wid + NWG * sn, row = sg / NSEG, k = sg - row * NSEG;
    const int off = (row * (SK / 64) + k * 21 + (k + 2) / 3 + n * 8 + wave - (sn == 0 ? 0 : (sn == 1 ? c1 : c2))) * (64 * DH * 2);
#pragma unroll
    for (int u = 0; u < NBUF; ++u) {
#pragma unroll
      for (int i = 0; i < 3; ++i) sl[u].a[i] = __builtin_amdgcn_raw_buffer_load_b128(rs, voff + i * 128, off + 2 * u * 3072, AUX_NT);
      MEMFENCE();
#pragma unroll
      for (int i = 0; i < 3; ++i) sl[u].b[i] = __builtin_amdgcn_raw_buffer_load_b128(rs, voff + i * 128, off + (2 * u + 1) * 3072, AUX_NT);
      if (u < NBUF - 1) __builtin_amdgcn_s_sleep(3);
      MEMFENCE();
    }
  };
  asm volatile("" :: "s"(ws), "s"(x), "s"(wq), "s"(kc));
  Slot b0[NBUF];
  const bool early = wave < QW0 && !prod;
  if (early) gload(b0, rs_k, 0);
  unsigned hs_nc = 0u, hs_ep = 0u;
  auto look_at_p = [&]() {
    if (bid == NWG - 1) {
      u32x4 hp[2], hc[2];
#pragma unroll
      for (int i = 0; i < 2; ++i) {
        hp[i] = __builtin_amdgcn_raw_buffer_load_b128(rs_ws, (WS_HP + lane * 8 + i * 4) * 4, 0, AUX_SC1);
        const u32x2 c0 = __builtin_amdgcn_raw_buffer_load_b64(rs_ws, (WS_HC + (lane * 4 + i * 2) * LSTR) * 4, 0, AUX_SC1);
        const u32x2 c1_ = __builtin_amdgcn_raw_buffer_load_b64(rs_ws, (WS_HC + (lane * 4 + i * 2 + 1) * LSTR) * 4, 0, AUX_SC1);
        hc[i] = u32x4{c0[0], c0[1], c1_[0], c1_[1]};
      }
      if (__all(hc[0][1] == hp[0][0] && hc[0][3] == hp[0][2] && hc[1][1] == hp[1][0] && hc[1][3] == hp[1][2]) &&
          __any(hc[0][0] != hp[0][1] || hc[0][2] != hp[0][3] || hc[1][0] != hp[1][1] || hc[1][2] != hp[1][3])) {
#pragma unroll
        for (int i = 0; i < 2; ++i)
          __builtin_amdgcn_raw_buffer_store_b128(u32x4{hp[i][0], hc[i][0], hp[i][2], hc[i][2]}, rs_ws, (WS_HP + lane * 8 + i * 4) * 4, 0, AUX_SC1);
      }
    }
    const u32x2 pw = __builtin_amdgcn_raw_buffer_load_b64(rs_ws, (WS_HP + bid * 2) * 4, 0, AUX_SC1);
    const unsigned np = __builtin_amdgcn_readfirstlane(pw[0]), ec = __builtin_amdgcn_readfirstlane(pw[1]);
    if (np != hs_ep) { hs_ep = np; __builtin_amdgcn_raw_buffer_store_b64(u32x2{hs_nc, hs_ep}, rs_ws, lane == 0 ? (WS_HC + bid * LSTR) * 4 : OOB, 0, AUX_SC1); }
    return ec == hs_nc;
  };
  auto answer = [&]() {
    const u32x4 z4 = {0u, 0u, 0u, 0u};
#pragma unroll
    for (int i = 0; i < (ZPW + 255) / 256; ++i)
      __builtin_amdgcn_raw_buffer_store_b128(z4, rs_ws, i * 256 + lane * 4 < ZPW ? (WS_Z0 + bid * ZPW + i * 256 + lane * 4) * 4 : OOB, 0, AUX_SC1);
    const u32x2 pw = __builtin_amdgcn_raw_buffer_load_b64(rs_ws, (WS_HP + bid * 2) * 4, 0, AUX_SC1);
    DRAIN();
    hs_ep = __builtin_amdgcn_readfirstlane(pw[0]);
    hs_nc = __builtin_amdgcn_readfirstlane(pw[1]) + 1u;
    __builtin_amdgcn_raw_buffer_store_b64(u32x2{hs_nc, hs_ep}, rs_ws, lane == 0 ? (WS_HC + bid * LSTR) * 4 : OOB, 0, AUX_SC1);
  };
  bool answered = false;
  if (tid < NW) s_prog[tid] = 1;
  if (tid == 0) { s_qok = 0; s_pcnt = 0; s_ecnt = 0; s_fcnt = 0; s_icnt = 0; s_hcnt = 0; s_org = (int)(t_start + POFF); }
  if (wave == SVC) {
    __builtin_amdgcn_s_setprio(3);
    int iofs[NWIN];
#pragma unroll
    for (int s = 0; s < NWIN; ++s) iofs[s] = (WS_QI + ((wid + NWG * s) / NSEG * DH + lane) * 2) * 4;
    const int oofs = prod ? (WS_QI + (bid * 64 + lane) * 2) * 4 : OOB;
    u32x2 in[NWIN * 3];
    unsigned ot[NB], ce[4];
#pragma unroll
    for (int e = 0; e < NWIN * 3; ++e) in[e] = __builtin_amdgcn_raw_buffer_load_b64(rs_ws, iofs[e / 3], (e % 3) * 512, AUX_SC1);
#pragma unroll
    for (int bb = 0; bb < NB; ++bb) ot[bb] = __builtin_amdgcn_raw_buffer_load_b64(rs_ws, oofs, bb * (HD * 8), AUX_SC1)[1];
    u32x4 xr[5], hp[2];
#pragma unroll
    for (int i = 0; i < 5; ++i) xr[i] = reinterpret_cast<const u32x4*>(x)[lane * 5 + i];
#pragma unroll
    for (int i = 0; i < 2; ++i) hp[i] = __builtin_amdgcn_raw_buffer_load_b128(rs_ws, bid == NWG - 1 ? (WS_HP + lane * 8 + i * 4) * 4 : OOB, 0, AUX_SC1);
#pragma unroll
    for (int i = 0; i < 4; ++i) ce[i] = __builtin_amdgcn_raw_buffer_load_b32(rs_ws, bid == NWG - 1 ? (WS_HC + (lane * 4 + i) * LSTR + 1) * 4 : OOB, 0, AUX_SC1);
    MEMFENCE();
    __syncthreads();
    MEMFENCE();
    if (bid == NWG - 1) {
#pragma unroll
      for (int i = 0; i < 2; ++i) {
#pragma unroll
        for (int e = 0; e < 2; ++e) { unsigned np = hp[i][2 * e] + 1u; if (np == ce[2 * i + e]) np += 1u; hp[i][2 * e] = np; }
        __builtin_amdgcn_raw_buffer_store_b128(hp[i], rs_ws, (WS_HP + lane * 8 + i * 4) * 4, 0, AUX_SC1);
      }
    }
    unsigned want[NWIN * 3];
#pragma unroll
    for (int e = 0; e < NWIN * 3; ++e) want[e] = fresh_tag(in[e][1]);
    if (prod) {
      float xs = 0.f;
#pragma unroll
      for (int i = 0; i < 20; ++i) xs += bf_lo(xr[i >> 2][i & 3]) + bf_hi(xr[i >> 2][i & 3]);
      const float mean = row16_sum(xs) * (1.0f / DM);
      float xv = 0.f;
#pragma unroll
      for (int i = 0; i < 20; ++i) { const float a = bf_lo(xr[i >> 2][i & 3]) - mean, c = bf_hi(xr[i >> 2][i & 3]) - mean; xv += a * a + c * c; }
      const float rstd = QSCALE / sqrtf(row16_sum(xv) * (1.0f / DM) + LN_EPS);
#pragma unroll
      for (int bb = 0; bb < NB; ++bb) ot[bb] = fresh_tag(ot[bb]);
      for (unsigned spin = 0; spin < (SPIN_CAP << 4) && lds_get(&s_pcnt) < NQW; ++spin) __builtin_amdgcn_s_sleep(1);
      __builtin_amdgcn_fence(__ATOMIC_ACQUIRE, "workgroup");
      float pr[5];
#pragma unroll
      for (int i = 0; i < 5; ++i) {
        pr[i] = 0.f;
#pragma unroll
        for (int k = 0; k < NQW; ++k) pr[i] += s_buf[2 * k * 320 + i * 64 + lane] + s_buf[(2 * k + 1) * 320 + i * 64 + lane];
      }
#pragma unroll
      for (int bb = 0; bb < NB; ++bb) {
        const float qv = (pr[bb] - __int_as_float(__builtin_amdgcn_readlane(__float_as_int(mean), bb * 16)) * pr[4]) *
                         __int_as_float(__builtin_amdgcn_readlane(__float_as_int(rstd), bb * 16));
        __builtin_amdgcn_raw_buffer_store_b64(u32x2{__float_as_uint(qv), ot[bb]}, rs_ws, oofs, bb * (HD * 8), AUX_SC1);
      }
    } else {
      __builtin_amdgcn_s_sleep(32);
    }
    bool all_out = false;
#pragma unroll 1
    for (unsigned spin = 0; spin < SPIN_CAP; ++spin) {
#pragma unroll
      for (int e = 0; e < NWIN * 3; ++e) in[e] = __builtin_amdgcn_raw_buffer_load_b64(rs_ws, iofs[e / 3], (e % 3) * 512, AUX_SC1);
      bool ok = true;
#pragma unroll
      for (int e = 0; e < NWIN * 3; ++e) ok = ok && in[e][1] == want[e];
      if (all_out || __all(ok)) break;
      if (spin == 15) { answer(); answered = true; }
      if (spin > 15 && (spin & 3) == 3) all_out = look_at_p();
      __builtin_amdgcn_s_sleep(4);
    }
#pragma unroll
    for (int e = 0; e < NWIN * 3; ++e) s_q[e * 64 + lane] = __uint_as_float(in[e][0]);
    __builtin_amdgcn_fence(__ATOMIC_RELEASE, "workgroup");
    lds_put(&s_qok, 1);
    __builtin_amdgcn_s_setprio(0);
  } else if (prod && wave >= QW0 && wave < QW0 + NQW) {
    const int k0 = (wave - QW0) * (QST * 64) + (mc >> 3) * 32 + mg * 8;
    u32x4 raw[QST * 8], xa[QST];
    const bf16* wb = wq + (size_t)k0 * HD + bid * 64 + (mc & 7) * 8;
#pragma unroll
    for (int st = 0; st < QST; ++st) xa[st] = *reinterpret_cast<const u32x4*>(x + (mc & 3) * DM + k0 + st * 64);
#pragma unroll
    for (int e = 0; e < QST * 8; ++e) raw[e] = *reinterpret_cast<const u32x4*>(wb + (size_t)((e >> 3) * 64 + (e & 7)) * HD);
    MEMFENCE();
    __syncthreads();
    MEMFENCE();
    f32x4 ac[8];
#pragma unroll
    for (int t = 0; t < 8; ++t) ac[t] = f32x4{0.f, 0.f, 0.f, 0.f};
#pragma unroll
    for (int st = 0; st < QST; ++st) {
      const u32x4 a = (mc & 4) == 0 ? xa[st] : u32x4{0x3f803f80u, 0x3f803f80u, 0x3f803f80u, 0x3f803f80u};
#pragma unroll
      for (int c = 0; c < 4; ++c) {
        u32x4 f0, f1;
#pragma unroll
        for (int i = 0; i < 4; ++i) { f0[i] = pk_lo(raw[st * 8 + 2 * i][c], raw[st * 8 + 2 * i + 1][c]); f1[i] = pk_hi(raw[st * 8 + 2 * i][c], raw[st * 8 + 2 * i + 1][c]); }
        ac[2 * c] = mfma16(a, f0, ac[2 * c]);
        ac[2 * c + 1] = mfma16(a, f1, ac[2 * c + 1]);
      }
    }
    if ((mg >> 1) == (mc >> 3)) {
      float* const dst = &s_buf[((wave - QW0) * 2 + (mg >> 1)) * 320 + (mc & 7) * 8];
#pragma unroll
      for (int bb = 0; bb < NB; ++bb)
        if (bb == 0 || (mg & 1) == 0) {
          *reinterpret_cast<f32x4*>(dst + ((mg & 1) * 4 + bb) * 64) = f32x4{ac[0][bb], ac[1][bb], ac[2][bb], ac[3][bb]};
          *reinterpret_cast<f32x4*>(dst + ((mg & 1) * 4 + bb) * 64 + 4) = f32x4{ac[4][bb], ac[5][bb], ac[6][bb], ac[7][bb]};
        }
    }
    __builtin_amdgcn_fence(__ATOMIC_RELEASE, "workgroup");
    if (lane == 0) __hip_atomic_fetch_add(&s_pcnt, 1, __ATOMIC_RELAXED, __HIP_MEMORY_SCOPE_WORKGROUP);
  } else {
    __syncthreads();
  }
  MEMFENCE();
  u32x2 rep = {0u, 0u};
  unsigned t_thru = 0u;
  auto pace = [&](int g) {
    if (wave == 0 && g > AFROM) {
      const int org = lds_get(&s_org);
      const bool ok = rep[0] - t_start + 1000u < 6000u && rep[1] - t_start + 1000u < 6000u;
      const float dorg = row16_sum(ok ? (float)(int)(rep[1] - (unsigned)org) : 0.f);
      const float lt = ok ? (float)min((int)(rep[0] - rep[1]), LCAP) : (float)LCAP;
      const float lat = row16_sum(lt) * (1.f / 16.f);
      if (lane == 0) lds_put(&s_org, org + (int)(dorg * (1.f / 16.f) + fmaxf((lat - LTGT) * 0.6f, g > EFROM ? 0.f : -(float)DOWN)));
    }
    const unsigned rel = (unsigned)(g * PTAU + wave * (PTAU / 8) + min(g * 8 + wave, 16) * (FFWD / 16) - FFWD + ((wid * 65) & 255) * (PTAU / 8) / 256);
    unsigned now = (unsigned)__builtin_amdgcn_s_memrealtime();
    if (wave == 1 && (bid >> 4) == 7 && g >= 2)
      __builtin_amdgcn_raw_buffer_store_b64(u32x2{t_thru - rel + PTAU, (unsigned)lds_get(&s_org)}, rs_ws, lane == 0 ? (WS_REP + ((g - 2) * 16 + (bid & 15)) * LSTR) * 4 : OOB, 0, AUX_SC1);
    for (unsigned spin = 0; spin < 4096 && (int)(now - rel - (unsigned)__builtin_amdgcn_readfirstlane(lds_get(&s_org))) < 0; ++spin) {
      __builtin_amdgcn_s_sleep(2);
      now = (unsigned)__builtin_amdgcn_s_memrealtime();
    }
    if (g >= 2) rep = __builtin_amdgcn_raw_buffer_load_b64(rs_ws, (wave == 0 && lane < 16 && g >= AFROM) ? (WS_REP + ((g - 2) * 16 + lane) * LSTR) * 4 : OOB, 0, 0);
    if (lane == 0) lds_put(&s_prog[wave], g);
    int pp = __builtin_amdgcn_readfirstlane(lds_get(&s_prog[wave ^ (NW / 2)]));
    for (unsigned spin = 0; spin < 4096 && g > pp + 1; ++spin) { __builtin_amdgcn_s_sleep(4); pp = __builtin_amdgcn_readfirstlane(lds_get(&s_prog[wave ^ (NW / 2)])); }
    if (g > pp) __builtin_amdgcn_s_setprio(0); else __builtin_amdgcn_s_setprio(1);
  };
  float gmx = -INFINITY;
  auto k_group = [&](const Slot (&sl)[NBUF], int n, int s) {
    f32x2 q[12];
#pragma unroll
    for (int i = 0; i < 6; ++i) {
      const f32x4 a = *reinterpret_cast<const f32x4*>(&s_q[s * DH + (i >> 1) * 64 + j * 8 + (i & 1) * 4]);
      q[i * 2] = f32x2{a[0], a[1]}; q[i * 2 + 1] = f32x2{a[2], a[3]};
    }
    float* const sc = &s_sc[(wave * NT + n) * 64 + r * 8];
    MEMFENCE();
#pragma unroll
    for (int u = 0; u < NBUF; ++u) {
      k_tile(sl[u].a, q, gmx, sc + 2 * u, j);
      k_tile(sl[u].b, q, gmx, sc + 2 * u + 1, j);
      MEMFENCE();
    }
    t_thru = (unsigned)__builtin_amdgcn_s_memrealtime();
  };
  {
    Slot b1[NBUF];
    if (!early) {
      pace(0);
      gload(b0, rs_k, 0);
    }
    pace(1);
    gload(b1, rs_k, 1);
    for (unsigned spin = 0; spin < (SPIN_CAP << 4) && lds_get(&s_qok) == 0; ++spin) __builtin_amdgcn_s_sleep(2);
    __builtin_amdgcn_fence(__ATOMIC_ACQUIRE, "workgroup");
    k_group(b0, 0, 0);
    k_group(b1, 1, 0);
    if (wave == SVC && !answered) answer();
  }
  Slot buf[NBUF];
  int seg = 0;
  bool through = false;
  for (int n = 2; n < NT; ++n) {
    pace(n);
    gload(buf, rs_k, n);
    if (seg_of(n) != seg) {
      const float gm = max_r(gmx);
      if (lane == 0) s_m[seg * NW + wave] = gm;
      gmx = -INFINITY;
      ++seg;
    }
    k_group(buf, n, seg);
    if (wave == SVC && bid == NWG - 1 && n >= 3 && !through) through = look_at_p();
  }
  {
    const float gm = max_r(gmx);
    if (lane == 0) s_m[seg * NW + wave] = gm;
  }
  float l = 0.f;
  f32x2 acc[12];
#pragma unroll
  for (int i = 0; i < 12; ++i) acc[i] = f32x2{0.f, 0.f};
  __builtin_amdgcn_fence(__ATOMIC_RELEASE, "wavefront"); __builtin_amdgcn_wave_barrier(); __builtin_amdgcn_fence(__ATOMIC_ACQUIRE, "wavefront");
  for (int n = 0; n < NT; ++n) {
    pace(NT + n);
    gload(buf, rs_v, n);
    if (n == NT - 1) {
      if (lane == 0) __hip_atomic_fetch_add(&s_icnt, 1, __ATOMIC_RELAXED, __HIP_MEMORY_SCOPE_WORKGROUP);
      __builtin_amdgcn_s_setprio(3);
    }
    seg = seg_of(n);
    {
      float p[8];
      const float m = s_m[seg * NW + wave];
      const float* const sc = &s_sc[(wave * NT + n) * 64 + r * 8];
      const f32x4 sa = *reinterpret_cast<const f32x4*>(sc), sb = *reinterpret_cast<const f32x4*>(sc + 4);
#pragma unroll
      for (int t = 0; t < 4; ++t) { p[t] = __builtin_amdgcn_exp2f(sa[t] - m); p[4 + t] = __builtin_amdgcn_exp2f(sb[t] - m); }
      l += ((p[0] + p[1]) + (p[2] + p[3])) + ((p[4] + p[5]) + (p[6] + p[7]));
      MEMFENCE();
#pragma unroll
      for (int u = 0; u < NBUF; ++u) {
        v_tile(buf[u].a, p[2 * u], acc);
        v_tile(buf[u].b, p[2 * u + 1], acc);
        MEMFENCE();
      }
    }
    t_thru = (unsigned)__builtin_amdgcn_s_memrealtime();
    if (wave == SVC && n == 1)
      for (unsigned spin = 0; spin < SPIN_CAP && !look_at_p(); ++spin) __builtin_amdgcn_s_sleep(4);
    if (n == NT - 1 || seg_of(n + 1) != seg) {
      const float lw = sum_r(l);
      float t12[12], t6[6];
#pragma unroll
      for (int e = 0; e < 12; ++e) t12[e] = tsum32(acc[e >> 1][e & 1], acc[6 + (e >> 1)][e & 1]);
#pragma unroll
      for (int e = 0; e < 6; ++e) { t6[e] = tsum16(t12[e], t12[e + 6]); t6[e] += dpp<DPP_ROR8>(t6[e]); }
      if ((lane & 8) == 0) {
#pragma unroll
        for (int e = 0; e < 3; ++e) *reinterpret_cast<f32x2*>(&s_buf[(seg * NW + wave) * DH + j * 24 + (lane >> 4) * 6 + e * 2]) = f32x2{t6[2 * e], t6[2 * e + 1]};
      }
      if (lane == 0) s_l[seg * NW + wave] = lw;
      l = 0.f;
#pragma unroll
      for (int i = 0; i < 12; ++i) acc[i] = f32x2{0.f, 0.f};
      if (seg != 0) {
        __builtin_amdgcn_fence(__ATOMIC_RELEASE, "workgroup");
        int c = 0;
        if (lane == 0) c = __hip_atomic_fetch_add(seg == 1 ? &s_ecnt : &s_fcnt, 1, __ATOMIC_RELAXED, __HIP_MEMORY_SCOPE_WORKGROUP);
        if (__builtin_amdgcn_readfirstlane(c) == NW - 1) {
          __builtin_amdgcn_fence(__ATOMIC_ACQUIRE, "workgroup");
#pragma unroll 1
          for (int s = seg & 2; s <= seg; ++s) {
            float M = s_m[s * NW];
#pragma unroll
            for (int w = 1; w < NW; ++w) M = fmaxf(M, s_m[s * NW + w]);
            float L = 0.f;
            f32x4 o = {0.f, 0.f, 0.f, 0.f};
            const int d4 = lane < DH / 4 ? ((lane >> 1) & 7) * 24 + (lane >> 4) * 8 + (lane & 1) * 4 : 0;
#pragma unroll
            for (int w = 0; w < NW; ++w) {
              const float e = __builtin_amdgcn_exp2f(s_m[s * NW + w] - M);
              L = fmaf(e, s_l[s * NW + w], L);
              o += e * *reinterpret_cast<const f32x4*>(&s_buf[(s * NW + w) * DH + d4]);
            }
            if (lane >= DH / 4) o = f32x4{M, L, 1.f, 1.f};
            __builtin_amdgcn_raw_buffer_store_b128(u32x4{nz_bits(o[0]), nz_bits(o[1]), nz_bits(o[2]), nz_bits(o[3])}, rs_ws,
                                                   lane < REC / 4 ? (WS_PO + (wid + NWG * s) * REC + lane * 4) * 4 : OOB, 0, AUX_SC1);
          }
          if (seg == 2) { MEMFENCE(); lds_put(&s_fcnt, NW + 1); }
          if (seg == 1) {
            DRAIN();
            __builtin_amdgcn_raw_buffer_store_b32(1u, rs_ws, lane == 0 ? ef_ofs(wid) : OOB, 0, AUX_SC1);
          }
        }
      }
      MEMFENCE();
    }
  }
  __builtin_amdgcn_s_setprio(0);

  {
    asm volatile("" : "+s"(wave), "+s"(bid), "+v"(lane));
    r = lane >> 3; j = lane & 7; mg = lane >> 4; mc = lane & 15;
    const int w = wave;
    const int xcd = bid & 7, ri = bid >> 3;
    const bool isW = ri < 15, isG = !isW && ri < 27;
    const int tt = isW ? xcd * 15 + ri : (isG ? xcd * 12 + ri - 15 : xcd * 5 + ri - 27);
    const int ks1 = tt / W_CT, ct1 = tt - ks1 * W_CT;
    typedef const __attribute__((address_space(1))) char* gptr;
    auto behind_stream = [&]() {
      for (unsigned spin = 0; spin < (SPIN_CAP << 4) && lds_get(&s_icnt) < NW; ++spin) __builtin_amdgcn_s_sleep(1);
      for (int i = 0; i < 4 && lds_get(&s_fcnt) <= NW; ++i) __builtin_amdgcn_s_sleep(3);
    };
    u32x4 fr[16];
    u32x2 raw[32];
    auto weights = [&](const bf16* src, int skip) {
      gptr wb = (gptr)src;
#pragma unroll
      for (int e = 0; e < 32; ++e) {
        raw[e] = *(const __attribute__((address_space(1))) u32x2*)wb;
        wb += ((e & 7) == 7 ? skip : 1) * (DM * 2);
        asm volatile("" : "+v"(wb));
      }
      MEMFENCE();
    };
    auto frags = [&]() {
      PIN8(raw, 0); PIN8(raw, 8); PIN8(raw, 16); PIN8(raw, 24);
#pragma unroll
      for (int st = 0; st < 4; ++st)
#pragma unroll
        for (int i = 0; i < 4; ++i) {
          const u32x2 r0 = raw[st * 8 + 2 * i], r1 = raw[st * 8 + 2 * i + 1];
          fr[st * 4 + 0][i] = pk_lo(r0[0], r1[0]); fr[st * 4 + 1][i] = pk_hi(r0[0], r1[0]);
          fr[st * 4 + 2][i] = pk_lo(r0[1], r1[1]); fr[st * 4 + 3][i] = pk_hi(r0[1], r1[1]);
        }
      PIN8(fr, 0); PIN8(fr, 8);
    };
    if (isW) {
      if (w >= 4) return;
      char* const img = s_A + w * (16 * WA);
      {
        int offp[2], ne[2], sg0[2];
#pragma unroll
        for (int hf = 0; hf < 2; ++hf) {
          const int row = ks1 * K3_ROWS + w * 128 + hf * 64, hh = row / DH;
          sg0[hf] = (mg * NH + hh) * NSEG;
          offp[hf] = (WS_PO + sg0[hf] * REC + row - hh * DH + mc * 4) * 4;
          ne[hf] = min(max(2 * NWG - sg0[hf], 0), NSEG);
        }
        const int sgm = (mc < 8 ? sg0[0] : sg0[1]) + j;
        const unsigned emask = (1u << ne[0]) - 1u + (((1u << ne[1]) - 1u) << NSEG) + (sgm < 2 * NWG ? 1u << (2 * NSEG) : 0u);
        u32x4 pv[2 * NSEG];
        u32x2 ml = {0u, 0u};
#pragma unroll
        for (int k = 0; k < 2 * NSEG; ++k) pv[k] = u32x4{0u, 0u, 0u, 0u};
        unsigned pend = (1u << (2 * NSEG)) - 1u + (j < NSEG ? 1u << (2 * NSEG) : 0u);
        auto look = [&](unsigned go) {
          if (go & (1u << (2 * NSEG))) ml = __builtin_amdgcn_raw_buffer_load_b64(rs_ws, (WS_PO + sgm * REC + DH) * 4, 0, AUX_SC1);
#pragma unroll
          for (int k = 0; k < 2 * NSEG; ++k)
            if (go & (1u << k)) pv[k] = __builtin_amdgcn_raw_buffer_load_b128(rs_ws, offp[k / NSEG], (k % NSEG) * REC * 4, AUX_SC1);
        };
        for (unsigned spin = 0; spin < (SPIN_CAP << 4) && lds_get(&s_ecnt) < NW; ++spin) __builtin_amdgcn_s_sleep(4);
        for (unsigned spin = 0; spin < SPIN_CAP; ++spin) {
          const bool fl = j < NSEG && sgm < 2 * NWG;
          if (__all(!fl || __builtin_amdgcn_raw_buffer_load_b32(rs_ws, fl ? ef_ofs(sgm & (NWG - 1)) : OOB, 0, AUX_SC1) != 0u)) break;
          __builtin_amdgcn_s_sleep(8);
        }
        behind_stream();
        weights(wo + (size_t)(ks1 * K3_ROWS + w * 128 + mg * 8) * DM + ct1 * 64 + mc * 4, 25);
        look(pend & emask);
        pend &= ~emask;
        for (unsigned sp2 = 0; sp2 < (SPIN_CAP << 4) && lds_get(&s_fcnt) < NW; ++sp2) __builtin_amdgcn_s_sleep(1);
        look(pend);
        MEMFENCE();
        frags();
#pragma unroll 1
        for (unsigned spin = 0; spin < SPIN_CAP; ++spin) {
          unsigned got = ml[0] != 0u && ml[1] != 0u;
#pragma unroll
          for (int k = 2 * NSEG - 1; k >= 0; --k) got = got * 2u + (unsigned)all_nz(pv[k]);
          pend &= ~got;
          if (!__any(pend != 0u)) break;
          if (spin >= 8) __builtin_amdgcn_s_sleep(4);
          look(pend);
        }
        const float mm = j < NSEG ? __uint_as_float(ml[0]) : -INFINITY;
        float M = fmaxf(mm, dpp<DPP_XOR1>(mm)); M = fmaxf(M, dpp<DPP_XOR2>(M)); M = fmaxf(M, dpp<DPP_HMIRROR>(M));
        const float e = __builtin_amdgcn_exp2f(mm - M);
        float L = e * __uint_as_float(ml[1]);
        L += dpp<DPP_XOR1>(L); L += dpp<DPP_XOR2>(L); L += dpp<DPP_HMIRROR>(L);
        const float wt = e * __builtin_amdgcn_rcpf(L);
        f32x4 o0 = dpp<DPP_BCAST + 0>(wt) * __builtin_bit_cast(f32x4, pv[0]), o1 = dpp<DPP_BCAST + 8>(wt) * __builtin_bit_cast(f32x4, pv[6]);
        o0 += dpp<DPP_BCAST + 1>(wt) * __builtin_bit_cast(f32x4, pv[1]); o1 += dpp<DPP_BCAST + 9>(wt) * __builtin_bit_cast(f32x4, pv[7]);
        o0 += dpp<DPP_BCAST + 2>(wt) * __builtin_bit_cast(f32x4, pv[2]); o1 += dpp<DPP_BCAST + 10>(wt) * __builtin_bit_cast(f32x4, pv[8]);
        o0 += dpp<DPP_BCAST + 3>(wt) * __builtin_bit_cast(f32x4, pv[3]); o1 += dpp<DPP_BCAST + 11>(wt) * __builtin_bit_cast(f32x4, pv[9]);
        o0 += dpp<DPP_BCAST + 4>(wt) * __builtin_bit_cast(f32x4, pv[4]); o1 += dpp<DPP_BCAST + 12>(wt) * __builtin_bit_cast(f32x4, pv[10]);
        o0 += dpp<DPP_BCAST + 5>(wt) * __builtin_bit_cast(f32x4, pv[5]); o1 += dpp<DPP_BCAST + 13>(wt) * __builtin_bit_cast(f32x4, pv[11]);
        put_terms4(img + mg * 4 * WA + mc * 8, WA, o0);
        put_terms4(img + mg * 4 * WA + 128 + mc * 8, WA, o1);
      }
      __builtin_amdgcn_fence(__ATOMIC_RELEASE, "wavefront"); __builtin_amdgcn_wave_barrier(); __builtin_amdgcn_fence(__ATOMIC_ACQUIRE, "wavefront");
      f32x4 ac[4];
#pragma unroll
      for (int t = 0; t < 4; ++t) ac[t] = f32x4{0.f, 0.f, 0.f, 0.f};
#pragma unroll
      for (int st = 0; st < 4; ++st) {
        const u32x4 a = *reinterpret_cast<const u32x4*>(img + mc * WA + st * 64 + mg * 16);
#pragma unroll
        for (int t = 0; t < 4; ++t) ac[t] = mfma16(a, fr[st * 4 + t], ac[t]);
      }
      *reinterpret_cast<f32x4*>(&s_red[w * 256 + lane * 4]) = f32x4{sum3(ac[0]), sum3(ac[1]), sum3(ac[2]), sum3(ac[3])};
      __syncthreads();
      if (w == 0) {
        const f32x4 s0 = (*reinterpret_cast<const f32x4*>(&s_red[lane * 4]) + *reinterpret_cast<const f32x4*>(&s_red[256 + lane * 4])) +
                         (*reinterpret_cast<const f32x4*>(&s_red[512 + lane * 4]) + *reinterpret_cast<const f32x4*>(&s_red[768 + lane * 4]));
        __builtin_amdgcn_raw_buffer_store_b128(u32x4{nz_bits(s0[0]), nz_bits(s0[1]), nz_bits(s0[2]), nz_bits(s0[3])}, rs_ws,
                                               (WS_SLAB + (ks1 * NB + mg) * DM + ct1 * 64 + mc * 4) * 4, 0, AUX_SC1);
      }
    } else if (isG) {
      const int gb = tt & 3, gt = tt >> 2;
      if (w < 5) {
        char* const img = s_A + w * (4 * WA);
        const int hcol = w * 128 + (lane & 31) * 4;
        uint2 xx = {0u, 0u};
        u32x4 fg[32];
        {
          u32x4 rg[32];
          if (lane < 16) *reinterpret_cast<u32x4*>(img + 3 * WA + lane * 16) = u32x4{0x3f803f80u, 0x3f803f80u, 0x3f803f80u, 0x3f803f80u};
          if (lane < 32) xx = *reinterpret_cast<const uint2*>(x + gb * DM + hcol);
          behind_stream();
          gptr wb = (gptr)((mc < 8 ? wg : wu) + (size_t)(w * 128 + mg * 8) * FF + gt * 64 + (mc & 7) * 8);
#pragma unroll
          for (int e = 0; e < 32; ++e) {
            rg[e] = *(const __attribute__((address_space(1))) u32x4*)wb;
            wb += ((e & 7) == 7 ? 25 : 1) * (FF * 2);
            asm volatile("" : "+v"(wb));
          }
          MEMFENCE();
#pragma unroll
          for (int st = 0; st < 4; ++st) {
            PIN8(rg, st * 8);
#pragma unroll
            for (int c = 0; c < 4; ++c)
#pragma unroll
              for (int i = 0; i < 4; ++i) {
                fg[st * 8 + 2 * c][i] = pk_lo(rg[st * 8 + 2 * i][c], rg[st * 8 + 2 * i + 1][c]);
                fg[st * 8 + 2 * c + 1][i] = pk_hi(rg[st * 8 + 2 * i][c], rg[st * 8 + 2 * i + 1][c]);
              }
            PIN8(fg, st * 8);
            MEMFENCE();
          }
        }
        asm volatile("" : "+v"(xx.x), "+v"(xx.y));
        const int off = (WS_SLAB + ((lane >> 5) * (K3_KS / 2) * NB + gb) * DM + hcol) * 4;
        const int poff = (WS_SLAB + ((mg >> 1) * (K3_KS / 2) + mc) * (NB * DM) + gb * DM + w * 128 + (mg & 1) * 64) * 4;
        u32x4 sl[K3_KS / 2];
#pragma unroll
        for (int ks = 0; ks < K3_KS / 2; ++ks) sl[ks] = u32x4{0u, 0u, 0u, 0u};
        const unsigned pend = (1u << (K3_KS / 2)) - 1u;
        unsigned todo = pend, go = 0u;
        for (unsigned spin = 0; spin < SPIN_CAP; ++spin) {
#pragma unroll
          for (int ks = 0; ks < K3_KS / 2; ++ks)
            if (go & (1u << ks)) sl[ks] = __builtin_amdgcn_raw_buffer_load_b128(rs_ws, off, ks * (NB * DM * 4), AUX_SC1);
          todo &= ~go;
          if (todo != 0u) {
            const unsigned pr = __builtin_amdgcn_raw_buffer_load_b32(rs_ws, (mc < K3_KS / 2 && ((todo >> mc) & 1u)) ? poff : OOB, 0, AUX_SC1);
            const unsigned long long bl = __ballot(pr != 0u);
            go = (unsigned)(bl & bl >> 16 & bl >> 32 & bl >> 48) & todo;
            if (__builtin_popcount(todo) * 4 - __builtin_popcountll(bl) <= PROBE_MIN) go = todo;
          } else {
            unsigned got = 0u;
#pragma unroll
            for (int ks = K3_KS / 2 - 1; ks >= 0; --ks) got = got * 2u + (unsigned)__all(all_nz(sl[ks]));
            go = pend & ~got;
            if (go == 0u) break;
          }
        }
        f32x4 hv = f32x4{bf_lo(xx.x), bf_hi(xx.x), bf_lo(xx.y), bf_hi(xx.y)};
#pragma unroll
        for (int ks = 0; ks < K3_KS / 2; ++ks) hv += __builtin_bit_cast(f32x4, sl[ks]);
#pragma unroll
        for (int e = 0; e < 4; ++e) hv[e] = tsum32(hv[e], hv[e]);
        if (lane < 32) {
          *reinterpret_cast<f32x4*>(&s_h[hcol]) = hv;
          put_terms4(img + lane * 8, WA, hv);
        }
        __builtin_amdgcn_fence(__ATOMIC_RELEASE, "workgroup");
        __builtin_amdgcn_wave_barrier();
        if (lane == 0) __hip_atomic_fetch_add(&s_hcnt, 1, __ATOMIC_RELAXED, __HIP_MEMORY_SCOPE_WORKGROUP);
        __builtin_amdgcn_fence(__ATOMIC_ACQUIRE, "wavefront");
        f32x4 ac[8];
#pragma unroll
        for (int t = 0; t < 8; ++t) ac[t] = f32x4{0.f, 0.f, 0.f, 0.f};
        u32x4 av[4];
#pragma unroll
        for (int st = 0; st < 4; ++st) av[st] = *reinterpret_cast<const u32x4*>(img + (mc & 3) * WA + st * 64 + mg * 16);
#pragma unroll
        for (int st = 0; st < 4; ++st)
#pragma unroll
          for (int t = 0; t < 8; ++t) ac[t] = mfma16(av[st], fg[st * 8 + t], ac[t]);
        const int hf = (mg & 1) * 4;
        const f32x4 a0 = hf ? ac[4] : ac[0], a1 = hf ? ac[5] : ac[1], a2 = hf ? ac[6] : ac[2], a3 = hf ? ac[7] : ac[3];
        *reinterpret_cast<f32x4*>(&s_red[w * 256 + (mg >> 1) * 128 + mc * 8 + hf]) =
            mg < 2 ? f32x4{sum3(a0), sum3(a1), sum3(a2), sum3(a3)} : f32x4{a0[3], a1[3], a2[3], a3[3]};
      } else if (w == NW - 1) {
        unsigned wr2[5];
#pragma unroll
        for (int i = 0; i < 5; ++i) wr2[i] = reinterpret_cast<const unsigned*>(wr)[lane + 64 * i];
        PIN4(wr2, 0); asm volatile("" : "+v"(wr2[4]));
        for (unsigned spin = 0; spin < (SPIN_CAP << 6) && lds_get(&s_hcnt) < 5; ++spin) {}
        __builtin_amdgcn_fence(__ATOMIC_ACQUIRE, "workgroup");
        const float piv = s_h[0];
        float hv[10], sm = 0.f, sq = 0.f;
#pragma unroll
        for (int i = 0; i < 5; ++i) {
          const float2 t2 = *reinterpret_cast<const float2*>(&s_h[2 * lane + 128 * i]);
          hv[2 * i] = t2.x; hv[2 * i + 1] = t2.y;
        }
        PIN8(hv, 0); asm volatile("" : "+v"(hv[8]), "+v"(hv[9]));
#pragma unroll
        for (int i = 0; i < 10; ++i) { hv[i] -= piv; sm += hv[i]; sq = fmaf(hv[i], hv[i], sq); }
        const float dm = wave_sum(sm) * (1.0f / DM);
        const float var = fmaxf(fmaf(-dm, dm, wave_sum(sq) * (1.0f / DM)), 0.f);
        const float rstd = __builtin_amdgcn_rsqf(var + LN_EPS);
        if (lane == 0) { s_stat[0] = piv + dm; s_stat[1] = rstd; }
        __syncthreads();
        if (gt < DM / 32) {
          float rr = 0.f;
#pragma unroll
          for (int i = 0; i < 5; ++i) { rr = fmaf((hv[2 * i] - dm) * rstd, bf_lo(wr2[i]), rr); rr = fmaf((hv[2 * i + 1] - dm) * rstd, bf_hi(wr2[i]), rr); }
          rr = wave_sum(rr);
          const float hh = s_h[gt * 32 + (lane & 31)];
          __builtin_amdgcn_raw_buffer_store_b32(nz_bits(lane >= 32 ? 1.0f / (1.0f + __expf(-rr)) : hh + (hh - (piv + dm)) * rstd), rs_ws,
                                                (WS_BASE + (gb * (DM / 32) + gt) * 64 + lane) * 4, 0, AUX_SC1);
        }
        return;
      } else {
        return;
      }
      int o_res = (WS_ACT + (gb * (FF / 64) + gt) * 96 + (lane & 1) * 32 + (lane >> 1)) * 4;
      asm volatile("" : "+v"(o_res));
      __syncthreads();
      if (w == 0) {
        float pr[24];
#pragma unroll
        for (int k = 0; k < 20; ++k) pr[k] = s_red[(k % 5) * 256 + (k / 5) * 64 + lane];
        pr[20] = s_stat[0]; pr[21] = s_stat[1]; pr[22] = 0.f; pr[23] = 0.f;
        PIN8(pr, 0); PIN8(pr, 8); PIN8(pr, 16);
        float sm[4];
#pragma unroll
        for (int k = 0; k < 4; ++k) sm[k] = ((pr[5 * k] + pr[5 * k + 1]) + (pr[5 * k + 2] + pr[5 * k + 3])) + pr[5 * k + 4];
        const float gg = (sm[0] - pr[20] * sm[2]) * pr[21], uu = (sm[1] - pr[20] * sm[3]) * pr[21];
        unsigned t[3], wd2[3];
        split3(gg * __builtin_amdgcn_rcpf(1.0f + __expf(-gg)) * uu, t[0], t[1], t[2]);
#pragma unroll
        for (int k = 0; k < 3; ++k) {
          const unsigned o = __float_as_uint(dpp<DPP_XOR1>(__uint_as_float(t[k])));
          wd2[k] = nz_bits(__uint_as_float((lane & 1) ? pk_hi(o, t[k]) : pk_hi(t[k], o)));
        }
        __builtin_amdgcn_raw_buffer_store_b32((lane & 1) ? wd2[1] : wd2[0], rs_ws, o_res, 0, AUX_SC1);
        __builtin_amdgcn_raw_buffer_store_b32(wd2[2], rs_ws, (lane & 1) ? OOB : o_res, 256, AUX_SC1);
      }
    } else {
      if (w >= 6) return;
      const int bp = tt & 1, ct = tt >> 1, bb = lane >> 5, f = lane & 31;
      for (unsigned spin = 0; spin < (SPIN_CAP << 4) && lds_get(&s_fcnt) < NW; ++spin) __builtin_amdgcn_s_sleep(1);
      __builtin_amdgcn_s_sleep(64);
      weights(wd + (size_t)(w * 256 + (mc >> 3) * 32 + mg * 8) * DM + ct * 32 + (mc & 7) * 4, 57);
      frags();
      const __amdgpu_buffer_rsrc_t rs_out = __builtin_amdgcn_make_buffer_rsrc(out, 0, NB * DM * 2, 0x00020000);
      int a_red = w * 256 + mg * 32 + (mc & 7) * 4, o_res = ((bp * 2 + bb) * DM + ct * 32 + f) * 2;
      asm volatile("" : "+v"(a_red), "+v"(o_res));
      const int off = (WS_ACT + (((bp * 2 + ((mc >> 2) & 1)) * (FF / 64) + w * 4) * 3 + (mc & 3)) * 32 + (mc >> 3) * 16 + mg * 4) * 4;
      const int offb = (WS_BASE + ((bp * 2 + bb) * (DM / 32) + ct) * 64 + f) * 4;
      u32x4 av[4];
#pragma unroll
      for (int st = 0; st < 4; ++st) av[st] = u32x4{0u, 0u, 0u, 0u};
      unsigned bs = 0u, sg = 0u;
      unsigned pend = ((mc & 3) == 3 ? 0u : 15u) + (w == 0 ? 16u : 0u);
      for (unsigned spin = 0; spin < SPIN_CAP; ++spin) {
#pragma unroll
        for (int st = 0; st < 4; ++st)
          if (pend & (1u << st)) av[st] = __builtin_amdgcn_raw_buffer_load_b128(rs_ws, off, st * 384, AUX_SC1);
        if (pend & 16u) { bs = __builtin_amdgcn_raw_buffer_load_b32(rs_ws, offb, 0, AUX_SC1); sg = __builtin_amdgcn_raw_buffer_load_b32(rs_ws, offb, 128, AUX_SC1); }
        unsigned got = bs != 0u && sg != 0u;
#pragma unroll
        for (int st = 3; st >= 0; --st) got = got * 2u + (unsigned)all_nz(av[st]);
        pend &= ~got;
        if (!__any(pend != 0u)) break;
      }
      f32x4 ac[4];
#pragma unroll
      for (int t = 0; t < 4; ++t) ac[t] = f32x4{0.f, 0.f, 0.f, 0.f};
#pragma unroll
      for (int st = 0; st < 4; ++st)
#pragma unroll
        for (int t = 0; t < 4; ++t) ac[t] = mfma16(av[st], fr[st * 4 + t], ac[t]);
      if ((mc >> 3) == (mg >> 1)) *reinterpret_cast<f32x4*>(&s_red[a_red]) = f32x4{sum3(ac[0]), sum3(ac[1]), sum3(ac[2]), sum3(ac[3])};
      __syncthreads();
      if (w == 0) {
        float pr[12];
#pragma unroll
        for (int k = 0; k < 12; ++k) pr[k] = s_red[(k >> 1) * 256 + (k & 1) * 64 + lane];
        PIN8(pr, 0); PIN4(pr, 8);
        const float y = (((pr[0] + pr[1]) + (pr[2] + pr[3])) + ((pr[4] + pr[5]) + (pr[6] + pr[7]))) + ((pr[8] + pr[9]) + (pr[10] + pr[11]));
        const bf16 ov = __float2bfloat16(fmaf(y, __uint_as_float(sg), __uint_as_float(bs)));
        __builtin_amdgcn_raw_buffer_store_b16(__builtin_bit_cast(unsigned short, ov), rs_out, o_res, 0, 0);
      }
    }
  }
}

extern "C" void kernel_launch(void* const* d_in, const int* in_sizes, int n_in, void* d_out, int out_size, void* d_ws,
                              size_t ws_size, hipStream_t stream) {
  fused_kernel<<<NWG, NW * 64, 0, stream>>>((const bf16*)d_in[0], (const bf16*)d_in[1], (const bf16*)d_in[2], (const bf16*)d_in[3],
                                            (const bf16*)d_in[4], (const bf16*)d_in[6], (const bf16*)d_in[7], (const bf16*)d_in[8],
                                            (const bf16*)d_in[9], (float*)d_ws, (bf16*)d_out);
}
```

```cpp
#include <hip/hip_runtime.h>
#include <hip/hip_bf16.h>
#include <stdint.h>

typedef __hip_bfloat16 bf16;
using u32x4 = __attribute__((ext_vector_type(4))) unsigned;
using u32x2 = __attribute__((ext_vector_type(2))) unsigned;
using f32x4 = __attribute__((ext_vector_type(4))) float;
using f32x2 = __attribute__((ext_vector_type(2))) float;

constexpr int NB = 4, DM = 640, NH = 32, SK = 8192, DH = 192, HD = NH * DH, FF = 1536;
constexpr int NWG = 256, NSEG = 6, NWIN = NB * NH * NSEG / NWG;
constexpr float LN_EPS = 1e-5f;
constexpr float QSCALE = 0.07216878364870322f * 1.4426950408889634f;

constexpr int WS_HP = 0;
constexpr int REC = DH + 32;
constexpr int WS_PO = WS_HP + 2 * NWG;
constexpr int WS_Z0 = WS_PO + 2 * NWG * REC;
constexpr int K3_KS = 12, K3_ROWS = HD / K3_KS;
constexpr int WS_SLAB = WS_PO + NB * NH * NSEG * REC;
constexpr int WS_ACT = WS_SLAB + K3_KS * NB * DM;
constexpr int WS_BASE = WS_ACT + NB * FF * 3 / 2;
constexpr int ZPW = 416;
constexpr int WS_QI = WS_Z0 + NWG * ZPW;
constexpr int LSTR = 64;
constexpr int WS_REP = WS_QI + NB * NH * DH * 2;
constexpr int WS_HC = WS_REP + 256 * LSTR;
constexpr int WS_END = WS_HC + NWG * LSTR;
static_assert(WS_BASE + NB * DM * 2 <= WS_QI && WS_Z0 % 64 == 0 && WS_PO % 32 == 0 && ZPW % 32 == 0 && NWIN == 3, "layout");
__device__ __forceinline__ int ef_ofs(int i) { return (WS_PO + (2 * NWG + i) * REC + DH + 2) * 4; }
constexpr int OOB = 0x7ffffff0;

__device__ __forceinline__ float bf_lo(unsigned u) { return __uint_as_float(u << 16); }
__device__ __forceinline__ float bf_hi(unsigned u) { return __uint_as_float(u & 0xffff0000u); }
__device__ __forceinline__ f32x2 bf_pair(unsigned u) { return f32x2{bf_lo(u), bf_hi(u)}; }
__device__ __forceinline__ f32x2 fma2(float a, f32x2 b, f32x2 c) { return __builtin_elementwise_fma(f32x2{a, a}, b, c); }
__device__ __forceinline__ f32x2 fma2(f32x2 a, f32x2 b, f32x2 c) { return __builtin_elementwise_fma(a, b, c); }

template <int CTRL> __device__ __forceinline__ float dpp(float x) {
  return __builtin_bit_cast(float, __builtin_amdgcn_mov_dpp(__builtin_bit_cast(int, x), CTRL, 0xf, 0xf, true));
}
constexpr int DPP_XOR1 = 0xB1, DPP_XOR2 = 0x4E, DPP_HMIRROR = 0x141;

__device__ __forceinline__ float sum_x16_32(float x) {
  auto s = __builtin_amdgcn_permlane16_swap(__float_as_uint(x), __float_as_uint(x), false, false);
  x = __uint_as_float(s[0]) + __uint_as_float(s[1]);
  auto t = __builtin_amdgcn_permlane32_swap(__float_as_uint(x), __float_as_uint(x), false, false);
  return __uint_as_float(t[0]) + __uint_as_float(t[1]);
}
__device__ __forceinline__ float max_x16_32(float x) {
  auto s = __builtin_amdgcn_permlane16_swap(__float_as_uint(x), __float_as_uint(x), false, false);
  x = fmaxf(__uint_as_float(s[0]), __uint_as_float(s[1]));
  auto t = __builtin_amdgcn_permlane32_swap(__float_as_uint(x), __float_as_uint(x), false, false);
  return fmaxf(__uint_as_float(t[0]), __uint_as_float(t[1]));
}
__device__ __forceinline__ float tsum32(float a, float b) {
  auto t = __builtin_amdgcn_permlane32_swap(__float_as_uint(a), __float_as_uint(b), false, false);
  return __uint_as_float(t[0]) + __uint_as_float(t[1]);
}
__device__ __forceinline__ float tsum16(float a, float b) {
  auto t = __builtin_amdgcn_permlane16_swap(__float_as_uint(a), __float_as_uint(b), false, false);
  return __uint_as_float(t[0]) + __uint_as_float(t[1]);
}
constexpr int DPP_ROR8 = 0x128, DPP_BCAST = 0x150;
__device__ __forceinline__ float sum_r(float v) { v += dpp<DPP_ROR8>(v); return sum_x16_32(v); }
__device__ __forceinline__ float max_r(float v) { v = fmaxf(v, dpp<DPP_ROR8>(v)); return max_x16_32(v); }
__device__ __forceinline__ float wave_sum(float v) {
  v += dpp<DPP_XOR1>(v); v += dpp<DPP_XOR2>(v); v += dpp<DPP_HMIRROR>(v);
  return sum_r(v);
}

__device__ __forceinline__ float row16_sum(float v) {
  v += dpp<DPP_XOR1>(v); v += dpp<DPP_XOR2>(v); v += dpp<DPP_HMIRROR>(v);
  return v + dpp<DPP_ROR8>(v);
}

struct Slot { u32x4 a[3]; u32x4 b[3]; };

#define PIN8(a, o) asm volatile("" : "+v"((a)[(o) + 0]), "+v"((a)[(o) + 1]), "+v"((a)[(o) + 2]), "+v"((a)[(o) + 3]), \
                                       "+v"((a)[(o) + 4]), "+v"((a)[(o) + 5]), "+v"((a)[(o) + 6]), "+v"((a)[(o) + 7]))
#define PIN4(a, o) asm volatile("" : "+v"((a)[(o) + 0]), "+v"((a)[(o) + 1]), "+v"((a)[(o) + 2]), "+v"((a)[(o) + 3]))
#define MEMFENCE() do { asm volatile("" ::: "memory"); __builtin_amdgcn_sched_barrier(0); } while (0)
#define DRAIN() asm volatile("s_waitcnt vmcnt(0)" ::: "memory")

__device__ __forceinline__ void k_tile(const u32x4 (&k)[3], const f32x2 (&q)[12], float& gmx, float* dst, int j) {
  f32x2 s0 = {0.f, 0.f}, s1 = s0, s2 = s0;
#pragma unroll
  for (int d = 0; d < 4; ++d) {
    s0 = fma2(bf_pair(k[0][d]), q[d], s0);
    s1 = fma2(bf_pair(k[1][d]), q[4 + d], s1);
    s2 = fma2(bf_pair(k[2][d]), q[8 + d], s2);
  }
  s0 = (s0 + s1) + s2;
  float s = s0[0] + s0[1];
  s += dpp<DPP_XOR1>(s);
  s += dpp<DPP_XOR2>(s);
  s += dpp<DPP_HMIRROR>(s);
  gmx = fmaxf(gmx, s);
  asm volatile("" : "+v"(gmx));
  if (j == 0) *dst = s;
}
__device__ __forceinline__ void v_tile(u32x4 (&v)[3], float p, f32x2 (&acc)[12]) {
#pragma unroll
  for (int i = 0; i < 3; ++i) {
    asm volatile("" : "+v"(v[i]), "+v"(p));
#pragma unroll
    for (int d = 0; d < 4; ++d) acc[i * 4 + d] = fma2(p, bf_pair(v[i][d]), acc[i * 4 + d]);
    PIN4(acc, i * 4);
  }
}

#define TAU 656
#define OFF0 130
#define FF0 128
#define TGT 220
#define DWN 30
#define JW 65
#define AMAX 20
#define EFR 12
#define FFN 16
#define ESL 4
constexpr int PTAU = TAU;
constexpr int POFF = OFF0, FFWD = FF0;
constexpr int LTGT = TGT, LCAP = 600;
constexpr int DOWN = DWN, AFROM = 3, EFROM = EFR;
constexpr int PROBE_MIN = 10;
constexpr int AUX_SC1 = 16, AUX_NT = 2;
constexpr unsigned SPIN_CAP = 1u << 17;
__device__ __forceinline__ unsigned nz_bits(float v) { const unsigned u = __float_as_uint(v); return u == 0u ? 0x80000000u : u; }
__device__ __forceinline__ void lds_put(int* p, int v) { __hip_atomic_store(p, v, __ATOMIC_RELAXED, __HIP_MEMORY_SCOPE_WORKGROUP); }
__device__ __forceinline__ int lds_get(int* p) { return __hip_atomic_load(p, __ATOMIC_RELAXED, __HIP_MEMORY_SCOPE_WORKGROUP); }
using bf16x8 = __attribute__((ext_vector_type(8))) short;
__device__ __forceinline__ unsigned pk_lo(unsigned a, unsigned b) { return __builtin_amdgcn_perm(b, a, 0x05040100u); }
__device__ __forceinline__ unsigned pk_hi(unsigned a, unsigned b) { return __builtin_amdgcn_perm(b, a, 0x07060302u); }
__device__ __forceinline__ void split3(float v, unsigned& t0, unsigned& t1, unsigned& t2) {
  t0 = __float_as_uint(v) & 0xffff0000u;
  const float r1 = v - __uint_as_float(t0);
  t1 = __float_as_uint(r1) & 0xffff0000u;
  t2 = __float_as_uint(r1 - __uint_as_float(t1));
}
__device__ __forceinline__ f32x4 mfma16(u32x4 a, u32x4 b, f32x4 c) {
  return __builtin_amdgcn_mfma_f32_16x16x32_bf16(__builtin_bit_cast(bf16x8, a), __builtin_bit_cast(bf16x8, b), c, 0, 0, 0);
}
__device__ __forceinline__ void put_terms4(char* img, int rowbytes, f32x4 v) {
  unsigned t[3][4];
#pragma unroll
  for (int e = 0; e < 4; ++e) split3(v[e], t[0][e], t[1][e], t[2][e]);
#pragma unroll
  for (int k = 0; k < 3; ++k) *reinterpret_cast<u32x2*>(img + k * rowbytes) = u32x2{pk_hi(t[k][0], t[k][1]), pk_hi(t[k][2], t[k][3])};
}
__device__ __forceinline__ bool all_nz(u32x4 v) { return v[0] != 0u && v[1] != 0u && v[2] != 0u && v[3] != 0u; }

__device__ __forceinline__ float sum3(f32x4 v) { return (v[0] + v[1]) + v[2]; }

constexpr int NW = 8, NBUF = 4, NGRP = 64, NT = NGRP / NW;
constexpr int NQP = NH * DH / 64, NQW = 5, QW0 = 2, QST = DM / 64 / NQW;
constexpr int SVC = NW - 1;
constexpr int W_CT = DM / 64, G_TASKS = NB * (FF / 64), W_TASKS = W_CT * K3_KS, D_TASKS = 2 * (DM / 32);
static_assert(G_TASKS + W_TASKS + D_TASKS == NWG && NW == 8 && K3_ROWS == 4 * 128 && NBUF == 4 && SK / 64 == 128, "shape");
__device__ __forceinline__ unsigned fresh_tag(unsigned tags) {
  unsigned t = __builtin_amdgcn_readfirstlane(tags);
  do ++t; while (__any(tags == t));
  return t;
}

__global__ void __launch_bounds__(NW * 64, 2) fused_kernel(const bf16* __restrict__ x, const bf16* __restrict__ kc, const bf16* __restrict__ vc,
                                                        const bf16* __restrict__ wq, const bf16* __restrict__ wo, const bf16* __restrict__ wr,
                                                        const bf16* __restrict__ wg, const bf16* __restrict__ wu, const bf16* __restrict__ wd,
                                                        float* ws, bf16* __restrict__ out) {
  constexpr int WA = 128 * 2 + 16;
  __shared__ __attribute__((aligned(16))) char s_A[4 * 16 * WA];
  __shared__ __attribute__((aligned(16))) float s_red[6 * 256];
  __shared__ __attribute__((aligned(16))) float s_h[DM];
  __shared__ __attribute__((aligned(16))) float s_buf[NWIN * NW * DH];
  __shared__ __attribute__((aligned(16))) float s_sc[NGRP * 64];
  __shared__ __attribute__((aligned(16))) float s_q[NWIN * DH];
  __shared__ float s_m[NWIN * NW], s_l[NWIN * NW];
  __shared__ int s_qok, s_pcnt, s_ecnt, s_fcnt, s_icnt, s_org, s_hcnt;
  __shared__ int s_prog[NW];
  __shared__ float s_stat[4];

  const int tid = threadIdx.x;
  int lane = tid & 63, r = lane >> 3, j = lane & 7, mg = lane >> 4, mc = lane & 15;
  int wave = __builtin_amdgcn_readfirstlane(tid >> 6), bid = blockIdx.x;
  const unsigned t_start = (unsigned)__builtin_amdgcn_s_memrealtime();
  const int wid = (bid >> 5) * 32 + (bid & 7) * 4 + ((bid >> 3) & 3);
  const bool prod = bid < NQP;
  const __amdgpu_buffer_rsrc_t rs_ws = __builtin_amdgcn_make_buffer_rsrc(ws, 0, WS_END * 4, 0x00020000);
  const __amdgpu_buffer_rsrc_t rs_k = __builtin_amdgcn_make_buffer_rsrc(const_cast<bf16*>(kc), 0, NB * NH * SK * DH * 2, 0x00020000);
  const __amdgpu_buffer_rsrc_t rs_v = __builtin_amdgcn_make_buffer_rsrc(const_cast<bf16*>(vc), 0, NB * NH * SK * DH * 2, 0x00020000);
  const int voff = r * (DH * 2) + j * 16;
  const int c1 = wid % 3 == 0 ? 22 : 21, c2 = wid % 3 == 1 ? 42 : 43;
  auto seg_of = [&](int n) { return (n * 8 + wave >= c1) + (n * 8 + wave >= c2); };
  auto gload = [&](Slot (&sl)[NBUF], __amdgpu_buffer_rsrc_t rs, int n) {
    const int sn = seg_of(n), sg = wid + NWG * sn, row = sg / NSEG, k = sg - row * NSEG;
    const int off = (row * (SK / 64) + (k >> 1) * 43 + (n * 8 + wave - (sn == 0 ? 0 : (sn == 1 ? c1 : c2))) * 2 + ((k ^ (k >> 1)) & 1)) * (64 * DH * 2);
#pragma unroll
    for (int u = 0; u < NBUF; ++u) {
#pragma unroll
      for (int i = 0; i < 3; ++i) sl[u].a[i] = __builtin_amdgcn_raw_buffer_load_b128(rs, voff + i * 128, off + 2 * u * 3072, AUX_NT);
      MEMFENCE();
#pragma unroll
      for (int i = 0; i < 3; ++i) sl[u].b[i] = __builtin_amdgcn_raw_buffer_load_b128(rs, voff + i * 128, off + (2 * u + 1) * 3072, AUX_NT);
      if (u < NBUF - 1) __builtin_amdgcn_s_sleep(3);
      MEMFENCE();
    }
  };
  asm volatile("" :: "s"(ws), "s"(x), "s"(wq), "s"(kc));
  Slot b0[NBUF];
  const bool early = wave < QW0 && !prod;
  if (early) gload(b0, rs_k, 0);
  unsigned hs_nc = 0u, hs_ep = 0u;
  auto look_at_p = [&]() {
    if (bid == NWG - 1) {
      u32x4 hp[2], hc[2];
#pragma unroll
      for (int i = 0; i < 2; ++i) {
        hp[i] = __builtin_amdgcn_raw_buffer_load_b128(rs_ws, (WS_HP + lane * 8 + i * 4) * 4, 0, AUX_SC1);
        const u32x2 c0 = __builtin_amdgcn_raw_buffer_load_b64(rs_ws, (WS_HC + (lane * 4 + i * 2) * LSTR) * 4, 0, AUX_SC1);
        const u32x2 c1_ = __builtin_amdgcn_raw_buffer_load_b64(rs_ws, (WS_HC + (lane * 4 + i * 2 + 1) * LSTR) * 4, 0, AUX_SC1);
        hc[i] = u32x4{c0[0], c0[1], c1_[0], c1_[1]};
      }
      if (__all(hc[0][1] == hp[0][0] && hc[0][3] == hp[0][2] && hc[1][1] == hp[1][0] && hc[1][3] == hp[1][2]) &&
          __any(hc[0][0] != hp[0][1] || hc[0][2] != hp[0][3] || hc[1][0] != hp[1][1] || hc[1][2] != hp[1][3])) {
#pragma unroll
        for (int i = 0; i < 2; ++i)
          __builtin_amdgcn_raw_buffer_store_b128(u32x4{hp[i][0], hc[i][0], hp[i][2], hc[i][2]}, rs_ws, (WS_HP + lane * 8 + i * 4) * 4, 0, AUX_SC1);
      }
    }
    const u32x2 pw = __builtin_amdgcn_raw_buffer_load_b64(rs_ws, (WS_HP + bid * 2) * 4, 0, AUX_SC1);
    const unsigned np = __builtin_amdgcn_readfirstlane(pw[0]), ec = __builtin_amdgcn_readfirstlane(pw[1]);
    if (np != hs_ep) { hs_ep = np; __builtin_amdgcn_raw_buffer_store_b64(u32x2{hs_nc, hs_ep}, rs_ws, lane == 0 ? (WS_HC + bid * LSTR) * 4 : OOB, 0, AUX_SC1); }
    return ec == hs_nc;
  };
  auto answer = [&]() {
    const u32x4 z4 = {0u, 0u, 0u, 0u};
#pragma unroll
    for (int i = 0; i < (ZPW + 255) / 256; ++i)
      __builtin_amdgcn_raw_buffer_store_b128(z4, rs_ws, i * 256 + lane * 4 < ZPW ? (WS_Z0 + bid * ZPW + i * 256 + lane * 4) * 4 : OOB, 0, AUX_SC1);
    const u32x2 pw = __builtin_amdgcn_raw_buffer_load_b64(rs_ws, (WS_HP + bid * 2) * 4, 0, AUX_SC1);
    DRAIN();
    hs_ep = __builtin_amdgcn_readfirstlane(pw[0]);
    hs_nc = __builtin_amdgcn_readfirstlane(pw[1]) + 1u;
    __builtin_amdgcn_raw_buffer_store_b64(u32x2{hs_nc, hs_ep}, rs_ws, lane == 0 ? (WS_HC + bid * LSTR) * 4 : OOB, 0, AUX_SC1);
  };
  bool answered = false;
  if (tid < NW) s_prog[tid] = 1;
  if (tid == 0) { s_qok = 0; s_pcnt = 0; s_ecnt = 0; s_fcnt = 0; s_icnt = 0; s_hcnt = 0; s_org = (int)(t_start + POFF); }
  if (wave == SVC) {
    __builtin_amdgcn_s_setprio(3);
    int iofs[NWIN];
#pragma unroll
    for (int s = 0; s < NWIN; ++s) iofs[s] = (WS_QI + ((wid + NWG * s) / NSEG * DH + lane) * 2) * 4;
    const int oofs = prod ? (WS_QI + (bid * 64 + lane) * 2) * 4 : OOB;
    u32x2 in[NWIN * 3];
    unsigned ot[NB], ce[4];
#pragma unroll
    for (int e = 0; e < NWIN * 3; ++e) in[e] = __builtin_amdgcn_raw_buffer_load_b64(rs_ws, iofs[e / 3], (e % 3) * 512, AUX_SC1);
#pragma unroll
    for (int bb = 0; bb < NB; ++bb) ot[bb] = __builtin_amdgcn_raw_buffer_load_b64(rs_ws, oofs, bb * (HD * 8), AUX_SC1)[1];
    u32x4 xr[5], hp[2];
#pragma unroll
    for (int i = 0; i < 5; ++i) xr[i] = reinterpret_cast<const u32x4*>(x)[lane * 5 + i];
#pragma unroll
    for (int i = 0; i < 2; ++i) hp[i] = __builtin_amdgcn_raw_buffer_load_b128(rs_ws, bid == NWG - 1 ? (WS_HP + lane * 8 + i * 4) * 4 : OOB, 0, AUX_SC1);
#pragma unroll
    for (int i = 0; i < 4; ++i) ce[i] = __builtin_amdgcn_raw_buffer_load_b32(rs_ws, bid == NWG - 1 ? (WS_HC + (lane * 4 + i) * LSTR + 1) * 4 : OOB, 0, AUX_SC1);
    MEMFENCE();
    __syncthreads();
    MEMFENCE();
    if (bid == NWG - 1) {
#pragma unroll
      for (int i = 0; i < 2; ++i) {
#pragma unroll
        for (int e = 0; e < 2; ++e) { unsigned np = hp[i][2 * e] + 1u; if (np == ce[2 * i + e]) np += 1u; hp[i][2 * e] = np; }
        __builtin_amdgcn_raw_buffer_store_b128(hp[i], rs_ws, (WS_HP + lane * 8 + i * 4) * 4, 0, AUX_SC1);
      }
    }
    unsigned want[NWIN * 3];
#pragma unroll
    for (int e = 0; e < NWIN * 3; ++e) want[e] = fresh_tag(in[e][1]);
    if (prod) {
      float xs = 0.f;
#pragma unroll
      for (int i = 0; i < 20; ++i) xs += bf_lo(xr[i >> 2][i & 3]) + bf_hi(xr[i >> 2][i & 3]);
      const float mean = row16_sum(xs) * (1.0f / DM);
      float xv = 0.f;
#pragma unroll
      for (int i = 0; i < 20; ++i) { const float a = bf_lo(xr[i >> 2][i & 3]) - mean, c = bf_hi(xr[i >> 2][i & 3]) - mean; xv += a * a + c * c; }
      const float rstd = QSCALE / sqrtf(row16_sum(xv) * (1.0f / DM) + LN_EPS);
#pragma unroll
      for (int bb = 0; bb < NB; ++bb) ot[bb] = fresh_tag(ot[bb]);
      for (unsigned spin = 0; spin < (SPIN_CAP << 4) && lds_get(&s_pcnt) < NQW; ++spin) __builtin_amdgcn_s_sleep(1);
      __builtin_amdgcn_fence(__ATOMIC_ACQUIRE, "workgroup");
      float pr[5];
#pragma unroll
      for (int i = 0; i < 5; ++i) {
        pr[i] = 0.f;
#pragma unroll
        for (int k = 0; k < NQW; ++k) pr[i] += s_buf[2 * k * 320 + i * 64 + lane] + s_buf[(2 * k + 1) * 320 + i * 64 + lane];
      }
#pragma unroll
      for (int bb = 0; bb < NB; ++bb) {
        const float qv = (pr[bb] - __int_as_float(__builtin_amdgcn_readlane(__float_as_int(mean), bb * 16)) * pr[4]) *
                         __int_as_float(__builtin_amdgcn_readlane(__float_as_int(rstd), bb * 16));
        __builtin_amdgcn_raw_buffer_store_b64(u32x2{__float_as_uint(qv), ot[bb]}, rs_ws, oofs, bb * (HD * 8), AUX_SC1);
      }
    } else {
      __builtin_amdgcn_s_sleep(32);
    }
    bool all_out = false;
#pragma unroll 1
    for (unsigned spin = 0; spin < SPIN_CAP; ++spin) {
#pragma unroll
      for (int e = 0; e < NWIN * 3; ++e) in[e] = __builtin_amdgcn_raw_buffer_load_b64(rs_ws, iofs[e / 3], (e % 3) * 512, AUX_SC1);
      bool ok = true;
#pragma unroll
      for (int e = 0; e < NWIN * 3; ++e) ok = ok && in[e][1] == want[e];
      if (all_out || __all(ok)) break;
      if (spin == 15) { answer(); answered = true; }
      if (spin > 15 && (spin & 3) == 3) all_out = look_at_p();
      __builtin_amdgcn_s_sleep(4);
    }
#pragma unroll
    for (int e = 0; e < NWIN * 3; ++e) s_q[e * 64 + lane] = __uint_as_float(in[e][0]);
    __builtin_amdgcn_fence(__ATOMIC_RELEASE, "workgroup");
    lds_put(&s_qok, 1);
    __builtin_amdgcn_s_setprio(0);
  } else if (prod && wave >= QW0 && wave < QW0 + NQW) {
    const int k0 = (wave - QW0) * (QST * 64) + (mc >> 3) * 32 + mg * 8;
    u32x4 raw[QST * 8], xa[QST];
    const bf16* wb = wq + (size_t)k0 * HD + bid * 64 + (mc & 7) * 8;
#pragma unroll
    for (int st = 0; st < QST; ++st) xa[st] = *reinterpret_cast<const u32x4*>(x + (mc & 3) * DM + k0 + st * 64);
#pragma unroll
    for (int e = 0; e < QST * 8; ++e) raw[e] = *reinterpret_cast<const u32x4*>(wb + (size_t)((e >> 3) * 64 + (e & 7)) * HD);
    MEMFENCE();
    __syncthreads();
    MEMFENCE();
    f32x4 ac[8];
#pragma unroll
    for (int t = 0; t < 8; ++t) ac[t] = f32x4{0.f, 0.f, 0.f, 0.f};
#pragma unroll
    for (int st = 0; st < QST; ++st) {
      const u32x4 a = (mc & 4) == 0 ? xa[st] : u32x4{0x3f803f80u, 0x3f803f80u, 0x3f803f80u, 0x3f803f80u};
#pragma unroll
      for (int c = 0; c < 4; ++c) {
        u32x4 f0, f1;
#pragma unroll
        for (int i = 0; i < 4; ++i) { f0[i] = pk_lo(raw[st * 8 + 2 * i][c], raw[st * 8 + 2 * i + 1][c]); f1[i] = pk_hi(raw[st * 8 + 2 * i][c], raw[st * 8 + 2 * i + 1][c]); }
        ac[2 * c] = mfma16(a, f0, ac[2 * c]);
        ac[2 * c + 1] = mfma16(a, f1, ac[2 * c + 1]);
      }
    }
    if ((mg >> 1) == (mc >> 3)) {
      float* const dst = &s_buf[((wave - QW0) * 2 + (mg >> 1)) * 320 + (mc & 7) * 8];
#pragma unroll
      for (int bb = 0; bb < NB; ++bb)
        if (bb == 0 || (mg & 1) == 0) {
          *reinterpret_cast<f32x4*>(dst + ((mg & 1) * 4 + bb) * 64) = f32x4{ac[0][bb], ac[1][bb], ac[2][bb], ac[3][bb]};
          *reinterpret_cast<f32x4*>(dst + ((mg & 1) * 4 + bb) * 64 + 4) = f32x4{ac[4][bb], ac[5][bb], ac[6][bb], ac[7][bb]};
        }
    }
    __builtin_amdgcn_fence(__ATOMIC_RELEASE, "workgroup");
    if (lane == 0) __hip_atomic_fetch_add(&s_pcnt, 1, __ATOMIC_RELAXED, __HIP_MEMORY_SCOPE_WORKGROUP);
  } else {
    __syncthreads();
  }
  MEMFENCE();
  u32x2 rep = {0u, 0u};
  unsigned t_thru = 0u;
  auto pace = [&](int g) {
    if (wave == 0 && g > AFROM) {
      const int org = lds_get(&s_org);
      const bool ok = rep[0] - t_start + 1000u < 6000u && rep[1] - t_start + 1000u < 6000u;
      const float dorg = row16_sum(ok ? (float)(int)(rep[1] - (unsigned)org) : 0.f);
      const float lt = ok ? (float)min((int)(rep[0] - rep[1]), LCAP) : (float)LCAP;
      const float lx = fminf(lt, dpp<DPP_ROR8>(lt));
      float hi = fmaxf(lx, dpp<DPP_XOR1>(lx)); hi = fmaxf(hi, dpp<DPP_XOR2>(hi)); hi = fmaxf(hi, dpp<DPP_HMIRROR>(hi));
      const float lat = row16_sum(lt) * ((1.f - AMAX / 100.f) / 16.f) + hi * (AMAX / 100.f);
      if (lane == 0) lds_put(&s_org, org + (int)(dorg * (1.f / 16.f) + fmaxf((lat - LTGT) * 0.6f, g > EFROM ? (float)ESL : -(float)DOWN)));
    }
    const unsigned rel = (unsigned)(g * PTAU + wave * (PTAU / 8) + min(g * 8 + wave, FFN) * (FFWD / FFN) - FFWD + ((wid * JW) & 255) * (PTAU / 8) / 256);
    unsigned now = (unsigned)__builtin_amdgcn_s_memrealtime();
    if (wave == 1 && (bid >> 4) == 7 && g >= 2)
      __builtin_amdgcn_raw_buffer_store_b64(u32x2{t_thru - rel + PTAU, (unsigned)lds_get(&s_org)}, rs_ws, lane == 0 ? (WS_REP + ((g - 2) * 16 + (bid & 15)) * LSTR) * 4 : OOB, 0, AUX_SC1);
    for (unsigned spin = 0; spin < 4096 && (int)(now - rel - (unsigned)__builtin_amdgcn_readfirstlane(lds_get(&s_org))) < 0; ++spin) {
      __builtin_amdgcn_s_sleep(2);
      now = (unsigned)__builtin_amdgcn_s_memrealtime();
    }
    if (g >= 2) rep = __builtin_amdgcn_raw_buffer_load_b64(rs_ws, (wave == 0 && lane < 16 && g >= AFROM) ? (WS_REP + ((g - 2) * 16 + lane) * LSTR) * 4 : OOB, 0, 0);
    if (lane == 0) lds_put(&s_prog[wave], g);
    int pp = __builtin_amdgcn_readfirstlane(lds_get(&s_prog[wave ^ (NW / 2)]));
    for (unsigned spin = 0; spin < 4096 && g > pp + 1; ++spin) { __builtin_amdgcn_s_sleep(4); pp = __builtin_amdgcn_readfirstlane(lds_get(&s_prog[wave ^ (NW / 2)])); }
    if (g > pp) __builtin_amdgcn_s_setprio(0); else __builtin_amdgcn_s_setprio(1);
  };
  float gmx = -INFINITY;
  auto k_group = [&](const Slot (&sl)[NBUF], int n, int s) {
    f32x2 q[12];
#pragma unroll
    for (int i = 0; i < 6; ++i) {
      const f32x4 a = *reinterpret_cast<const f32x4*>(&s_q[s * DH + (i >> 1) * 64 + j * 8 + (i & 1) * 4]);
      q[i * 2] = f32x2{a[0], a[1]}; q[i * 2 + 1] = f32x2{a[2], a[3]};
    }
    float* const sc = &s_sc[(wave * NT + n) * 64 + r * 8];
    MEMFENCE();
#pragma unroll
    for (int u = 0; u < NBUF; ++u) {
      k_tile(sl[u].a, q, gmx, sc + 2 * u, j);
      k_tile(sl[u].b, q, gmx, sc + 2 * u + 1, j);
      MEMFENCE();
    }
    t_thru = (unsigned)__builtin_amdgcn_s_memrealtime();
  };
  {
    Slot b1[NBUF];
    if (!early) {
      pace(0);
      gload(b0, rs_k, 0);
    }
    pace(1);
    gload(b1, rs_k, 1);
    for (unsigned spin = 0; spin < (SPIN_CAP << 4) && lds_get(&s_qok) == 0; ++spin) __builtin_amdgcn_s_sleep(2);
    __builtin_amdgcn_fence(__ATOMIC_ACQUIRE, "workgroup");
    k_group(b0, 0, 0);
    k_group(b1, 1, 0);
    if (wave == SVC && !answered) answer();
  }
  Slot buf[NBUF];
  int seg = 0;
  bool through = false;
  for (int n = 2; n < NT; ++n) {
    pace(n);
    gload(buf, rs_k, n);
    if (seg_of(n) != seg) {
      const float gm = max_r(gmx);
      if (lane == 0) s_m[seg * NW + wave] = gm;
      gmx = -INFINITY;
      ++seg;
    }
    k_group(buf, n, seg);
    if (wave == SVC && bid == NWG - 1 && n >= 3 && !through) through = look_at_p();
  }
  {
    const float gm = max_r(gmx);
    if (lane == 0) s_m[seg * NW + wave] = gm;
  }
  float l = 0.f;
  f32x2 acc[12];
#pragma unroll
  for (int i = 0; i < 12; ++i) acc[i] = f32x2{0.f, 0.f};
  __builtin_amdgcn_fence(__ATOMIC_RELEASE, "wavefront"); __builtin_amdgcn_wave_barrier(); __builtin_amdgcn_fence(__ATOMIC_ACQUIRE, "wavefront");
  for (int n = 0; n < NT; ++n) {
    pace(NT + n);
    gload(buf, rs_v, n);
    if (n == NT - 1) {
      if (lane == 0) __hip_atomic_fetch_add(&s_icnt, 1, __ATOMIC_RELAXED, __HIP_MEMORY_SCOPE_WORKGROUP);
      __builtin_amdgcn_s_setprio(3);
    }
    seg = seg_of(n);
    {
      float p[8];
      const float m = s_m[seg * NW + wave];
      const float* const sc = &s_sc[(wave * NT + n) * 64 + r * 8];
      const f32x4 sa = *reinterpret_cast<const f32x4*>(sc), sb = *reinterpret_cast<const f32x4*>(sc + 4);
#pragma unroll
      for (int t = 0; t < 4; ++t) { p[t] = __builtin_amdgcn_exp2f(sa[t] - m); p[4 + t] = __builtin_amdgcn_exp2f(sb[t] - m); }
      l += ((p[0] + p[1]) + (p[2] + p[3])) + ((p[4] + p[5]) + (p[6] + p[7]));
      MEMFENCE();
#pragma unroll
      for (int u = 0; u < NBUF; ++u) {
        v_tile(buf[u].a, p[2 * u], acc);
        v_tile(buf[u].b, p[2 * u + 1], acc);
        MEMFENCE();
      }
    }
    t_thru = (unsigned)__builtin_amdgcn_s_memrealtime();
    if (wave == SVC && n == 1)
      for (unsigned spin = 0; spin < SPIN_CAP && !look_at_p(); ++spin) __builtin_amdgcn_s_sleep(4);
    if (n == NT - 1 || seg_of(n + 1) != seg) {
      const float lw = sum_r(l);
      float t12[12], t6[6];
#pragma unroll
      for (int e = 0; e < 12; ++e) t12[e] = tsum32(acc[e >> 1][e & 1], acc[6 + (e >> 1)][e & 1]);
#pragma unroll
      for (int e = 0; e < 6; ++e) { t6[e] = tsum16(t12[e], t12[e + 6]); t6[e] += dpp<DPP_ROR8>(t6[e]); }
      if ((lane & 8) == 0) {
#pragma unroll
        for (int e = 0; e < 3; ++e) *reinterpret_cast<f32x2*>(&s_buf[(seg * NW + wave) * DH + j * 24 + (lane >> 4) * 6 + e * 2]) = f32x2{t6[2 * e], t6[2 * e + 1]};
      }
      if (lane == 0) s_l[seg * NW + wave] = lw;
      l = 0.f;
#pragma unroll
      for (int i = 0; i < 12; ++i) acc[i] = f32x2{0.f, 0.f};
      if (seg != 0) {
        __builtin_amdgcn_fence(__ATOMIC_RELEASE, "workgroup");
        int c = 0;
        if (lane == 0) c = __hip_atomic_fetch_add(seg == 1 ? &s_ecnt : &s_fcnt, 1, __ATOMIC_RELAXED, __HIP_MEMORY_SCOPE_WORKGROUP);
        if (__builtin_amdgcn_readfirstlane(c) == NW - 1) {
          __builtin_amdgcn_fence(__ATOMIC_ACQUIRE, "workgroup");
#pragma unroll 1
          for (int s = seg & 2; s <= seg; ++s) {
            float M = s_m[s * NW];
#pragma unroll
            for (int w = 1; w < NW; ++w) M = fmaxf(M, s_m[s * NW + w]);
            float L = 0.f;
            f32x4 o = {0.f, 0.f, 0.f, 0.f};
            const int d4 = lane < DH / 4 ? ((lane >> 1) & 7) * 24 + (lane >> 4) * 8 + (lane & 1) * 4 : 0;
#pragma unroll
            for (int w = 0; w < NW; ++w) {
              const float e = __builtin_amdgcn_exp2f(s_m[s * NW + w] - M);
              L = fmaf(e, s_l[s * NW + w], L);
              o += e * *reinterpret_cast<const f32x4*>(&s_buf[(s * NW + w) * DH + d4]);
            }
            if (lane >= DH / 4) o = f32x4{M, L, 1.f, 1.f};
            __builtin_amdgcn_raw_buffer_store_b128(u32x4{nz_bits(o[0]), nz_bits(o[1]), nz_bits(o[2]), nz_bits(o[3])}, rs_ws,
                                                   lane < REC / 4 ? (WS_PO + (wid + NWG * s) * REC + lane * 4) * 4 : OOB, 0, AUX_SC1);
          }
          if (seg == 2) { MEMFENCE(); lds_put(&s_fcnt, NW + 1); }
          if (seg == 1) {
            DRAIN();
            __builtin_amdgcn_raw_buffer_store_b32(1u, rs_ws, lane == 0 ? ef_ofs(wid) : OOB, 0, AUX_SC1);
          }
        }
      }
      MEMFENCE();
    }
  }
  __builtin_amdgcn_s_setprio(0);

  {
    asm volatile("" : "+s"(wave), "+s"(bid), "+v"(lane));
    r = lane >> 3; j = lane & 7; mg = lane >> 4; mc = lane & 15;
    const int w = wave;
    const int xcd = bid & 7, ri = bid >> 3;
    const bool isW = ri < 15, isG = !isW && ri < 27;
    const int tt = isW ? xcd * 15 + ri : (isG ? xcd * 12 + ri - 15 : xcd * 5 + ri - 27);
    const int ks1 = tt / W_CT, ct1 = tt - ks1 * W_CT;
    typedef const __attribute__((address_space(1))) char* gptr;
    auto behind_stream = [&]() {
      for (unsigned spin = 0; spin < (SPIN_CAP << 4) && lds_get(&s_icnt) < NW; ++spin) __builtin_amdgcn_s_sleep(1);
      for (int i = 0; i < 4 && lds_get(&s_fcnt) <= NW; ++i) __builtin_amdgcn_s_sleep(3);
    };
    u32x4 fr[16];
    u32x2 raw[32];
    auto weights = [&](const bf16* src, int skip) {
      gptr wb = (gptr)src;
#pragma unroll
      for (int e = 0; e < 32; ++e) {
        raw[e] = *(const __attribute__((address_space(1))) u32x2*)wb;
        wb += ((e & 7) == 7 ? skip : 1) * (DM * 2);
        asm volatile("" : "+v"(wb));
      }
      MEMFENCE();
    };
    auto frags = [&]() {
      PIN8(raw, 0); PIN8(raw, 8); PIN8(raw, 16); PIN8(raw, 24);
#pragma unroll
      for (int st = 0; st < 4; ++st)
#pragma unroll
        for (int i = 0; i < 4; ++i) {
          const u32x2 r0 = raw[st * 8 + 2 * i], r1 = raw[st * 8 + 2 * i + 1];
          fr[st * 4 + 0][i] = pk_lo(r0[0], r1[0]); fr[st * 4 + 1][i] = pk_hi(r0[0], r1[0]);
          fr[st * 4 + 2][i] = pk_lo(r0[1], r1[1]); fr[st * 4 + 3][i] = pk_hi(r0[1], r1[1]);
        }
      PIN8(fr, 0); PIN8(fr, 8);
    };
    if (isW) {
      if (w >= 4) return;
      char* const img = s_A + w * (16 * WA);
      {
        int offp[2], ne[2], sg0[2];
#pragma unroll
        for (int hf = 0; hf < 2; ++hf) {
          const int row = ks1 * K3_ROWS + w * 128 + hf * 64, hh = row / DH;
          sg0[hf] = (mg * NH + hh) * NSEG;
          offp[hf] = (WS_PO + sg0[hf] * REC + row - hh * DH + mc * 4) * 4;
          ne[hf] = min(max(2 * NWG - sg0[hf], 0), NSEG);
        }
        const int sgm = (mc < 8 ? sg0[0] : sg0[1]) + j;
        const unsigned emask = (1u << ne[0]) - 1u + (((1u << ne[1]) - 1u) << NSEG) + (sgm < 2 * NWG ? 1u << (2 * NSEG) : 0u);
        u32x4 pv[2 * NSEG];
        u32x2 ml = {0u, 0u};
#pragma unroll
        for (int k = 0; k < 2 * NSEG; ++k) pv[k] = u32x4{0u, 0u, 0u, 0u};
        unsigned pend = (1u << (2 * NSEG)) - 1u + (j < NSEG ? 1u << (2 * NSEG) : 0u);
        auto look = [&](unsigned go) {
          if (go & (1u << (2 * NSEG))) ml = __builtin_amdgcn_raw_buffer_load_b64(rs_ws, (WS_PO + sgm * REC + DH) * 4, 0, AUX_SC1);
#pragma unroll
          for (int k = 0; k < 2 * NSEG; ++k)
            if (go & (1u << k)) pv[k] = __builtin_amdgcn_raw_buffer_load_b128(rs_ws, offp[k / NSEG], (k % NSEG) * REC * 4, AUX_SC1);
        };
        for (unsigned spin = 0; spin < (SPIN_CAP << 4) && lds_get(&s_ecnt) < NW; ++spin) __builtin_amdgcn_s_sleep(4);
        for (unsigned spin = 0; spin < SPIN_CAP; ++spin) {
          const bool fl = j < NSEG && sgm < 2 * NWG;
          if (__all(!fl || __builtin_amdgcn_raw_buffer_load_b32(rs_ws, fl ? ef_ofs(sgm & (NWG - 1)) : OOB, 0, AUX_SC1) != 0u)) break;
          __builtin_amdgcn_s_sleep(8);
        }
        behind_stream();
        weights(wo + (size_t)(ks1 * K3_ROWS + w * 128 + mg * 8) * DM + ct1 * 64 + mc * 4, 25);
        look(pend & emask);
        pend &= ~emask;
        for (unsigned sp2 = 0; sp2 < (SPIN_CAP << 4) && lds_get(&s_fcnt) < NW; ++sp2) __builtin_amdgcn_s_sleep(1);
        look(pend);
        MEMFENCE();
        frags();
#pragma unroll 1
        for (unsigned spin = 0; spin < SPIN_CAP; ++spin) {
          unsigned got = ml[0] != 0u && ml[1] != 0u;
#pragma unroll
          for (int k = 2 * NSEG - 1; k >= 0; --k) got = got * 2u + (unsigned)all_nz(pv[k]);
          pend &= ~got;
          if (!__any(pend != 0u)) break;
          if (spin >= 8) __builtin_amdgcn_s_sleep(4);
          look(pend);
        }
        const float mm = j < NSEG ? __uint_as_float(ml[0]) : -INFINITY;
        float M = fmaxf(mm, dpp<DPP_XOR1>(mm)); M = fmaxf(M, dpp<DPP_XOR2>(M)); M = fmaxf(M, dpp<DPP_HMIRROR>(M));
        const float e = __builtin_amdgcn_exp2f(mm - M);
        float L = e * __uint_as_float(ml[1]);
        L += dpp<DPP_XOR1>(L); L += dpp<DPP_XOR2>(L); L += dpp<DPP_HMIRROR>(L);
        const float wt = e * __builtin_amdgcn_rcpf(L);
        f32x4 o0 = dpp<DPP_BCAST + 0>(wt) * __builtin_bit_cast(f32x4, pv[0]), o1 = dpp<DPP_BCAST + 8>(wt) * __builtin_bit_cast(f32x4, pv[6]);
        o0 += dpp<DPP_BCAST + 1>(wt) * __builtin_bit_cast(f32x4, pv[1]); o1 += dpp<DPP_BCAST + 9>(wt) * __builtin_bit_cast(f32x4, pv[7]);
        o0 += dpp<DPP_BCAST + 2>(wt) * __builtin_bit_cast(f32x4, pv[2]); o1 += dpp<DPP_BCAST + 10>(wt) * __builtin_bit_cast(f32x4, pv[8]);
        o0 += dpp<DPP_BCAST + 3>(wt) * __builtin_bit_cast(f32x4, pv[3]); o1 += dpp<DPP_BCAST + 11>(wt) * __builtin_bit_cast(f32x4, pv[9]);
        o0 += dpp<DPP_BCAST + 4>(wt) * __builtin_bit_cast(f32x4, pv[4]); o1 += dpp<DPP_BCAST + 12>(wt) * __builtin_bit_cast(f32x4, pv[10]);
        o0 += dpp<DPP_BCAST + 5>(wt) * __builtin_bit_cast(f32x4, pv[5]); o1 += dpp<DPP_BCAST + 13>(wt) * __builtin_bit_cast(f32x4, pv[11]);
        put_terms4(img + mg * 4 * WA + mc * 8, WA, o0);
        put_terms4(img + mg * 4 * WA + 128 + mc * 8, WA, o1);
      }
      __builtin_amdgcn_fence(__ATOMIC_RELEASE, "wavefront"); __builtin_amdgcn_wave_barrier(); __builtin_amdgcn_fence(__ATOMIC_ACQUIRE, "wavefront");
      f32x4 ac[4];
#pragma unroll
      for (int t = 0; t < 4; ++t) ac[t] = f32x4{0.f, 0.f, 0.f, 0.f};
#pragma unroll
      for (int st = 0; st < 4; ++st) {
        const u32x4 a = *reinterpret_cast<const u32x4*>(img + mc * WA + st * 64 + mg * 16);
#pragma unroll
        for (int t = 0; t < 4; ++t) ac[t] = mfma16(a, fr[st * 4 + t], ac[t]);
      }
      *reinterpret_cast<f32x4*>(&s_red[w * 256 + lane * 4]) = f32x4{sum3(ac[0]), sum3(ac[1]), sum3(ac[2]), sum3(ac[3])};
      __syncthreads();
      if (w == 0) {
        const f32x4 s0 = (*reinterpret_cast<const f32x4*>(&s_red[lane * 4]) + *reinterpret_cast<const f32x4*>(&s_red[256 + lane * 4])) +
                         (*reinterpret_cast<const f32x4*>(&s_red[512 + lane * 4]) + *reinterpret_cast<const f32x4*>(&s_red[768 + lane * 4]));
        __builtin_amdgcn_raw_buffer_store_b128(u32x4{nz_bits(s0[0]), nz_bits(s0[1]), nz_bits(s0[2]), nz_bits(s0[3])}, rs_ws,
                                               (WS_SLAB + (ks1 * NB + mg) * DM + ct1 * 64 + mc * 4) * 4, 0, AUX_SC1);
      }
    } else if (isG) {
      const int gb = tt & 3, gt = tt >> 2;
      if (w < 5) {
        char* const img = s_A + w * (4 * WA);
        const int hcol = w * 128 + (lane & 31) * 4;
        uint2 xx = {0u, 0u};
        u32x4 fg[32];
        {
          u32x4 rg[32];
          if (lane < 16) *reinterpret_cast<u32x4*>(img + 3 * WA + lane * 16) = u32x4{0x3f803f80u, 0x3f803f80u, 0x3f803f80u, 0x3f803f80u};
          if (lane < 32) xx = *reinterpret_cast<const uint2*>(x + gb * DM + hcol);
          behind_stream();
          gptr wb = (gptr)((mc < 8 ? wg : wu) + (size_t)(w * 128 + mg * 8) * FF + gt * 64 + (mc & 7) * 8);
#pragma unroll
          for (int e = 0; e < 32; ++e) {
            rg[e] = *(const __attribute__((address_space(1))) u32x4*)wb;
            wb += ((e & 7) == 7 ? 25 : 1) * (FF * 2);
            asm volatile("" : "+v"(wb));
          }
          MEMFENCE();
#pragma unroll
          for (int st = 0; st < 4; ++st) {
            PIN8(rg, st * 8);
#pragma unroll
            for (int c = 0; c < 4; ++c)
#pragma unroll
              for (int i = 0; i < 4; ++i) {
                fg[st * 8 + 2 * c][i] = pk_lo(rg[st * 8 + 2 * i][c], rg[st * 8 + 2 * i + 1][c]);
                fg[st * 8 + 2 * c + 1][i] = pk_hi(rg[st * 8 + 2 * i][c], rg[st * 8 + 2 * i + 1][c]);
              }
            PIN8(fg, st * 8);
            MEMFENCE();
          }
        }
        asm volatile("" : "+v"(xx.x), "+v"(xx.y));
        const int off = (WS_SLAB + ((lane >> 5) * (K3_KS / 2) * NB + gb) * DM + hcol) * 4;
        const int poff = (WS_SLAB + ((mg >> 1) * (K3_KS / 2) + mc) * (NB * DM) + gb * DM + w * 128 + (mg & 1) * 64) * 4;
        u32x4 sl[K3_KS / 2];
#pragma unroll
        for (int ks = 0; ks < K3_KS / 2; ++ks) sl[ks] = u32x4{0u, 0u, 0u, 0u};
        const unsigned pend = (1u << (K3_KS / 2)) - 1u;
        unsigned todo = pend, go = 0u;
        for (unsigned spin = 0; spin < SPIN_CAP; ++spin) {
#pragma unroll
          for (int ks = 0; ks < K3_KS / 2; ++ks)
            if (go & (1u << ks)) sl[ks] = __builtin_amdgcn_raw_buffer_load_b128(rs_ws, off, ks * (NB * DM * 4), AUX_SC1);
          todo &= ~go;
          if (todo != 0u) {
            const unsigned pr = __builtin_amdgcn_raw_buffer_load_b32(rs_ws, (mc < K3_KS / 2 && ((todo >> mc) & 1u)) ? poff : OOB, 0, AUX_SC1);
            const unsigned long long bl = __ballot(pr != 0u);
            go = (unsigned)(bl & bl >> 16 & bl >> 32 & bl >> 48) & todo;
            if (__builtin_popcount(todo) * 4 - __builtin_popcountll(bl) <= PROBE_MIN) go = todo;
          } else {
            unsigned got = 0u;
#pragma unroll
            for (int ks = K3_KS / 2 - 1; ks >= 0; --ks) got = got * 2u + (unsigned)__all(all_nz(sl[ks]));
            go = pend & ~got;
            if (go == 0u) break;
          }
        }
        f32x4 hv = f32x4{bf_lo(xx.x), bf_hi(xx.x), bf_lo(xx.y), bf_hi(xx.y)};
#pragma unroll
        for (int ks = 0; ks < K3_KS / 2; ++ks) hv += __builtin_bit_cast(f32x4, sl[ks]);
#pragma unroll
        for (int e = 0; e < 4; ++e) hv[e] = tsum32(hv[e], hv[e]);
        if (lane < 32) {
          *reinterpret_cast<f32x4*>(&s_h[hcol]) = hv;
          put_terms4(img + lane * 8, WA, hv);
        }
        __builtin_amdgcn_fence(__ATOMIC_RELEASE, "workgroup");
        __builtin_amdgcn_wave_barrier();
        if (lane == 0) __hip_atomic_fetch_add(&s_hcnt, 1, __ATOMIC_RELAXED, __HIP_MEMORY_SCOPE_WORKGROUP);
        __builtin_amdgcn_fence(__ATOMIC_ACQUIRE, "wavefront");
        f32x4 ac[8];
#pragma unroll
        for (int t = 0; t < 8; ++t) ac[t] = f32x4{0.f, 0.f, 0.f, 0.f};
        u32x4 av[4];
#pragma unroll
        for (int st = 0; st < 4; ++st) av[st] = *reinterpret_cast<const u32x4*>(img + (mc & 3) * WA + st * 64 + mg * 16);
#pragma unroll
        for (int st = 0; st < 4; ++st)
#pragma unroll
          for (int t = 0; t < 8; ++t) ac[t] = mfma16(av[st], fg[st * 8 + t], ac[t]);
        const int hf = (mg & 1) * 4;
        const f32x4 a0 = hf ? ac[4] : ac[0], a1 = hf ? ac[5] : ac[1], a2 = hf ? ac[6] : ac[2], a3 = hf ? ac[7] : ac[3];
        *reinterpret_cast<f32x4*>(&s_red[w * 256 + (mg >> 1) * 128 + mc * 8 + hf]) =
            mg < 2 ? f32x4{sum3(a0), sum3(a1), sum3(a2), sum3(a3)} : f32x4{a0[3], a1[3], a2[3], a3[3]};
      } else if (w == NW - 1) {
        unsigned wr2[5];
#pragma unroll
        for (int i = 0; i < 5; ++i) wr2[i] = reinterpret_cast<const unsigned*>(wr)[lane + 64 * i];
        PIN4(wr2, 0); asm volatile("" : "+v"(wr2[4]));
        for (unsigned spin = 0; spin < (SPIN_CAP << 6) && lds_get(&s_hcnt) < 5; ++spin) {}
        __builtin_amdgcn_fence(__ATOMIC_ACQUIRE, "workgroup");
        const float piv = s_h[0];
        float hv[10], sm = 0.f, sq = 0.f;
#pragma unroll
        for (int i = 0; i < 5; ++i) {
          const float2 t2 = *reinterpret_cast<const float2*>(&s_h[2 * lane + 128 * i]);
          hv[2 * i] = t2.x; hv[2 * i + 1] = t2.y;
        }
        PIN8(hv, 0); asm volatile("" : "+v"(hv[8]), "+v"(hv[9]));
#pragma unroll
        for (int i = 0; i < 10; ++i) { hv[i] -= piv; sm += hv[i]; sq = fmaf(hv[i], hv[i], sq); }
        const float dm = wave_sum(sm) * (1.0f / DM);
        const float var = fmaxf(fmaf(-dm, dm, wave_sum(sq) * (1.0f / DM)), 0.f);
        const float rstd = __builtin_amdgcn_rsqf(var + LN_EPS);
        if (lane == 0) { s_stat[0] = piv + dm; s_stat[1] = rstd; }
        __syncthreads();
        if (gt < DM / 32) {
          float rr = 0.f;
#pragma unroll
          for (int i = 0; i < 5; ++i) { rr = fmaf((hv[2 * i] - dm) * rstd, bf_lo(wr2[i]), rr); rr = fmaf((hv[2 * i + 1] - dm) * rstd, bf_hi(wr2[i]), rr); }
          rr = wave_sum(rr);
          const float hh = s_h[gt * 32 + (lane & 31)];
          __builtin_amdgcn_raw_buffer_store_b32(nz_bits(lane >= 32 ? 1.0f / (1.0f + __expf(-rr)) : hh + (hh - (piv + dm)) * rstd), rs_ws,
                                                (WS_BASE + (gb * (DM / 32) + gt) * 64 + lane) * 4, 0, AUX_SC1);
        }
        return;
      } else {
        return;
      }
      int o_res = (WS_ACT + (gb * (FF / 64) + gt) * 96 + (lane & 1) * 32 + (lane >> 1)) * 4;
      asm volatile("" : "+v"(o_res));
      __syncthreads();
      if (w == 0) {
        float pr[24];
#pragma unroll
        for (int k = 0; k < 20; ++k) pr[k] = s_red[(k % 5) * 256 + (k / 5) * 64 + lane];
        pr[20] = s_stat[0]; pr[21] = s_stat[1]; pr[22] = 0.f; pr[23] = 0.f;
        PIN8(pr, 0); PIN8(pr, 8); PIN8(pr, 16);
        float sm[4];
#pragma unroll
        for (int k = 0; k < 4; ++k) sm[k] = ((pr[5 * k] + pr[5 * k + 1]) + (pr[5 * k + 2] + pr[5 * k + 3])) + pr[5 * k + 4];
        const float gg = (sm[0] - pr[20] * sm[2]) * pr[21], uu = (sm[1] - pr[20] * sm[3]) * pr[21];
        unsigned t[3], wd2[3];
        split3(gg * __builtin_amdgcn_rcpf(1.0f + __expf(-gg)) * uu, t[0], t[1], t[2]);
#pragma unroll
        for (int k = 0; k < 3; ++k) {
          const unsigned o = __float_as_uint(dpp<DPP_XOR1>(__uint_as_float(t[k])));
          wd2[k] = nz_bits(__uint_as_float((lane & 1) ? pk_hi(o, t[k]) : pk_hi(t[k], o)));
        }
        __builtin_amdgcn_raw_buffer_store_b32((lane & 1) ? wd2[1] : wd2[0], rs_ws, o_res, 0, AUX_SC1);
        __builtin_amdgcn_raw_buffer_store_b32(wd2[2], rs_ws, (lane & 1) ? OOB : o_res, 256, AUX_SC1);
      }
    } else {
      if (w >= 6) return;
      const int bp = tt & 1, ct = tt >> 1, bb = lane >> 5, f = lane & 31;
      for (unsigned spin = 0; spin < (SPIN_CAP << 4) && lds_get(&s_fcnt) < NW; ++spin) __builtin_amdgcn_s_sleep(1);
      __builtin_amdgcn_s_sleep(64);
      weights(wd + (size_t)(w * 256 + (mc >> 3) * 32 + mg * 8) * DM + ct * 32 + (mc & 7) * 4, 57);
      frags();
      const __amdgpu_buffer_rsrc_t rs_out = __builtin_amdgcn_make_buffer_rsrc(out, 0, NB * DM * 2, 0x00020000);
      int a_red = w * 256 + mg * 32 + (mc & 7) * 4, o_res = ((bp * 2 + bb) * DM + ct * 32 + f) * 2;
      asm volatile("" : "+v"(a_red), "+v"(o_res));
      const int off = (WS_ACT + (((bp * 2 + ((mc >> 2) & 1)) * (FF / 64) + w * 4) * 3 + (mc & 3)) * 32 + (mc >> 3) * 16 + mg * 4) * 4;
      const int offb = (WS_BASE + ((bp * 2 + bb) * (DM / 32) + ct) * 64 + f) * 4;
      u32x4 av[4];
#pragma unroll
      for (int st = 0; st < 4; ++st) av[st] = u32x4{0u, 0u, 0u, 0u};
      unsigned bs = 0u, sg = 0u;
      unsigned pend = ((mc & 3) == 3 ? 0u : 15u) + (w == 0 ? 16u : 0u);
      for (unsigned spin = 0; spin < SPIN_CAP; ++spin) {
#pragma unroll
        for (int st = 0; st < 4; ++st)
          if (pend & (1u << st)) av[st] = __builtin_amdgcn_raw_buffer_load_b128(rs_ws, off, st * 384, AUX_SC1);
        if (pend & 16u) { bs = __builtin_amdgcn_raw_buffer_load_b32(rs_ws, offb, 0, AUX_SC1); sg = __builtin_amdgcn_raw_buffer_load_b32(rs_ws, offb, 128, AUX_SC1); }
        unsigned got = bs != 0u && sg != 0u;
#pragma unroll
        for (int st = 3; st >= 0; --st) got = got * 2u + (unsigned)all_nz(av[st]);
        pend &= ~got;
        if (!__any(pend != 0u)) break;
      }
      f32x4 ac[4];
#pragma unroll
      for (int t = 0; t < 4; ++t) ac[t] = f32x4{0.f, 0.f, 0.f, 0.f};
#pragma unroll
      for (int st = 0; st < 4; ++st)
#pragma unroll
        for (int t = 0; t < 4; ++t) ac[t] = mfma16(av[st], fr[st * 4 + t], ac[t]);
      if ((mc >> 3) == (mg >> 1)) *reinterpret_cast<f32x4*>(&s_red[a_red]) = f32x4{sum3(ac[0]), sum3(ac[1]), sum3(ac[2]), sum3(ac[3])};
      __syncthreads();
      if (w == 0) {
        float pr[12];
#pragma unroll
        for (int k = 0; k < 12; ++k) pr[k] = s_red[(k >> 1) * 256 + (k & 1) * 64 + lane];
        PIN8(pr, 0); PIN4(pr, 8);
        const float y = (((pr[0] + pr[1]) + (pr[2] + pr[3])) + ((pr[4] + pr[5]) + (pr[6] + pr[7]))) + ((pr[8] + pr[9]) + (pr[10] + pr[11]));
        const bf16 ov = __float2bfloat16(fmaf(y, __uint_as_float(sg), __uint_as_float(bs)));
        __builtin_amdgcn_raw_buffer_store_b16(__builtin_bit_cast(unsigned short, ov), rs_out, o_res, 0, 0);
      }
    }
  }
}

extern "C" void kernel_launch(void* const* d_in, const int* in_sizes, int n_in, void* d_out, int out_size, void* d_ws,
                              size_t ws_size, hipStream_t stream) {
  fused_kernel<<<NWG, NW * 64, 0, stream>>>((const bf16*)d_in[0], (const bf16*)d_in[1], (const bf16*)d_in[2], (const bf16*)d_in[3],
                                            (const bf16*)d_in[4], (const bf16*)d_in[6], (const bf16*)d_in[7], (const bf16*)d_in[8],
                                            (const bf16*)d_in[9], (float*)d_ws, (bf16*)d_out);
}
```

```cpp
#include <hip/hip_runtime.h>
#include <hip/hip_bf16.h>
#include <stdint.h>

typedef __hip_bfloat16 bf16;
using u32x4 = __attribute__((ext_vector_type(4))) unsigned;
using u32x2 = __attribute__((ext_vector_type(2))) unsigned;
using f32x4 = __attribute__((ext_vector_type(4))) float;
using f32x2 = __attribute__((ext_vector_type(2))) float;

constexpr int NB = 4, DM = 640, NH = 32, SK = 8192, DH = 192, HD = NH * DH, FF = 1536;
constexpr int NWG = 256, NSEG = 6, NWIN = NB * NH * NSEG / NWG;
constexpr float LN_EPS = 1e-5f;
constexpr float QSCALE = 0.07216878364870322f * 1.4426950408889634f;

constexpr int WS_HP = 0;
constexpr int REC = DH + 32;
constexpr int WS_PO = WS_HP + 2 * NWG;
constexpr int WS_Z0 = WS_PO + 2 * NWG * REC;
constexpr int K3_KS = 12, K3_ROWS = HD / K3_KS;
constexpr int WS_SLAB = WS_PO + NB * NH * NSEG * REC;
constexpr int WS_ACT = WS_SLAB + K3_KS * NB * DM;
constexpr int WS_BASE = WS_ACT + NB * FF * 3 / 2;
constexpr int ZPW = 416;
constexpr int WS_QI = WS_Z0 + NWG * ZPW;
constexpr int LSTR = 64;
constexpr int WS_REP = WS_QI + NB * NH * DH * 2;
constexpr int WS_HC = WS_REP + 256 * LSTR;
constexpr int WS_END = WS_HC + NWG * LSTR;
static_assert(WS_BASE + NB * DM * 2 <= WS_QI && WS_Z0 % 64 == 0 && WS_PO % 32 == 0 && ZPW % 32 == 0 && NWIN == 3, "layout");
__device__ __forceinline__ int ef_ofs(int i) { return (WS_PO + (2 * NWG + i) * REC + DH + 2) * 4; }
constexpr int OOB = 0x7ffffff0;

__device__ __forceinline__ float bf_lo(unsigned u) { return __uint_as_float(u << 16); }
__device__ __forceinline__ float bf_hi(unsigned u) { return __uint_as_float(u & 0xffff0000u); }
__device__ __forceinline__ f32x2 bf_pair(unsigned u) { return f32x2{bf_lo(u), bf_hi(u)}; }
__device__ __forceinline__ f32x2 fma2(float a, f32x2 b, f32x2 c) { return __builtin_elementwise_fma(f32x2{a, a}, b, c); }
__device__ __forceinline__ f32x2 fma2(f32x2 a, f32x2 b, f32x2 c) { return __builtin_elementwise_fma(a, b, c); }

template <int CTRL> __device__ __forceinline__ float dpp(float x) {
  return __builtin_bit_cast(float, __builtin_amdgcn_mov_dpp(__builtin_bit_cast(int, x), CTRL, 0xf, 0xf, true));
}
constexpr int DPP_XOR1 = 0xB1, DPP_XOR2 = 0x4E, DPP_HMIRROR = 0x141;

__device__ __forceinline__ float sum_x16_32(float x) {
  auto s = __builtin_amdgcn_permlane16_swap(__float_as_uint(x), __float_as_uint(x), false, false);
  x = __uint_as_float(s[0]) + __uint_as_float(s[1]);
  auto t = __builtin_amdgcn_permlane32_swap(__float_as_uint(x), __float_as_uint(x), false, false);
  return __uint_as_float(t[0]) + __uint_as_float(t[1]);
}
__device__ __forceinline__ float max_x16_32(float x) {
  auto s = __builtin_amdgcn_permlane16_swap(__float_as_uint(x), __float_as_uint(x), false, false);
  x = fmaxf(__uint_as_float(s[0]), __uint_as_float(s[1]));
  auto t = __builtin_amdgcn_permlane32_swap(__float_as_uint(x), __float_as_uint(x), false, false);
  return fmaxf(__uint_as_float(t[0]), __uint_as_float(t[1]));
}
__device__ __forceinline__ float tsum32(float a, float b) {
  auto t = __builtin_amdgcn_permlane32_swap(__float_as_uint(a), __float_as_uint(b), false, false);
  return __uint_as_float(t[0]) + __uint_as_float(t[1]);
}
__device__ __forceinline__ float tsum16(float a, float b) {
  auto t = __builtin_amdgcn_permlane16_swap(__float_as_uint(a), __float_as_uint(b), false, false);
  return __uint_as_float(t[0]) + __uint_as_float(t[1]);
}
constexpr int DPP_ROR8 = 0x128, DPP_BCAST = 0x150;
__device__ __forceinline__ float sum_r(float v) { v += dpp<DPP_ROR8>(v); return sum_x16_32(v); }
__device__ __forceinline__ float max_r(float v) { v = fmaxf(v, dpp<DPP_ROR8>(v)); return max_x16_32(v); }
__device__ __forceinline__ float wave_sum(float v) {
  v += dpp<DPP_XOR1>(v); v += dpp<DPP_XOR2>(v); v += dpp<DPP_HMIRROR>(v);
  return sum_r(v);
}

__device__ __forceinline__ float row16_sum(float v) {
  v += dpp<DPP_XOR1>(v); v += dpp<DPP_XOR2>(v); v += dpp<DPP_HMIRROR>(v);
  return v + dpp<DPP_ROR8>(v);
}

struct Slot { u32x4 a[3]; u32x4 b[3]; };

#define PIN8(a, o) asm volatile("" : "+v"((a)[(o) + 0]), "+v"((a)[(o) + 1]), "+v"((a)[(o) + 2]), "+v"((a)[(o) + 3]), \
                                       "+v"((a)[(o) + 4]), "+v"((a)[(o) + 5]), "+v"((a)[(o) + 6]), "+v"((a)[(o) + 7]))
#define PIN4(a, o) asm volatile("" : "+v"((a)[(o) + 0]), "+v"((a)[(o) + 1]), "+v"((a)[(o) + 2]), "+v"((a)[(o) + 3]))
#define MEMFENCE() do { asm volatile("" ::: "memory"); __builtin_amdgcn_sched_barrier(0); } while (0)
#define DRAIN() asm volatile("s_waitcnt vmcnt(0)" ::: "memory")

__device__ __forceinline__ void k_tile(const u32x4 (&k)[3], const f32x2 (&q)[12], float& gmx, float* dst, int j) {
  f32x2 s0 = {0.f, 0.f}, s1 = s0, s2 = s0;
#pragma unroll
  for (int d = 0; d < 4; ++d) {
    s0 = fma2(bf_pair(k[0][d]), q[d], s0);
    s1 = fma2(bf_pair(k[1][d]), q[4 + d], s1);
    s2 = fma2(bf_pair(k[2][d]), q[8 + d], s2);
  }
  s0 = (s0 + s1) + s2;
  float s = s0[0] + s0[1];
  s += dpp<DPP_XOR1>(s);
  s += dpp<DPP_XOR2>(s);
  s += dpp<DPP_HMIRROR>(s);
  gmx = fmaxf(gmx, s);
  asm volatile("" : "+v"(gmx));
  if (j == 0) *dst = s;
}
__device__ __forceinline__ void v_tile(u32x4 (&v)[3], float p, f32x2 (&acc)[12]) {
#pragma unroll
  for (int i = 0; i < 3; ++i) {
    asm volatile("" : "+v"(v[i]), "+v"(p));
#pragma unroll
    for (int d = 0; d < 4; ++d) acc[i * 4 + d] = fma2(p, bf_pair(v[i][d]), acc[i * 4 + d]);
    PIN4(acc, i * 4);
  }
}

constexpr int PTAU = 660;
constexpr int POFF = 120, FFWD = 112;
constexpr int LTGT = 230, LCAP = 600;
constexpr int DOWN = 30, AFROM = 3, EFROM = 12, SLACK = 10;
constexpr int PROBE_MIN = 10;
constexpr int AUX_SC1 = 16, AUX_NT = 2;
constexpr unsigned SPIN_CAP = 1u << 17;
__device__ __forceinline__ unsigned nz_bits(float v) { const unsigned u = __float_as_uint(v); return u == 0u ? 0x80000000u : u; }
__device__ __forceinline__ void lds_put(int* p, int v) { __hip_atomic_store(p, v, __ATOMIC_RELAXED, __HIP_MEMORY_SCOPE_WORKGROUP); }
__device__ __forceinline__ int lds_get(int* p) { return __hip_atomic_load(p, __ATOMIC_RELAXED, __HIP_MEMORY_SCOPE_WORKGROUP); }
using bf16x8 = __attribute__((ext_vector_type(8))) short;
__device__ __forceinline__ unsigned pk_lo(unsigned a, unsigned b) { return __builtin_amdgcn_perm(b, a, 0x05040100u); }
__device__ __forceinline__ unsigned pk_hi(unsigned a, unsigned b) { return __builtin_amdgcn_perm(b, a, 0x07060302u); }
__device__ __forceinline__ void split3(float v, unsigned& t0, unsigned& t1, unsigned& t2) {
  t0 = __float_as_uint(v) & 0xffff0000u;
  const float r1 = v - __uint_as_float(t0);
  t1 = __float_as_uint(r1) & 0xffff0000u;
  t2 = __float_as_uint(r1 - __uint_as_float(t1));
}
__device__ __forceinline__ f32x4 mfma16(u32x4 a, u32x4 b, f32x4 c) {
  return __builtin_amdgcn_mfma_f32_16x16x32_bf16(__builtin_bit_cast(bf16x8, a), __builtin_bit_cast(bf16x8, b), c, 0, 0, 0);
}
__device__ __forceinline__ void put_terms4(char* img, int rowbytes, f32x4 v) {
  unsigned t[3][4];
#pragma unroll
  for (int e = 0; e < 4; ++e) split3(v[e], t[0][e], t[1][e], t[2][e]);
#pragma unroll
  for (int k = 0; k < 3; ++k) *reinterpret_cast<u32x2*>(img + k * rowbytes) = u32x2{pk_hi(t[k][0], t[k][1]), pk_hi(t[k][2], t[k][3])};
}
__device__ __forceinline__ bool all_nz(u32x4 v) { return v[0] != 0u && v[1] != 0u && v[2] != 0u && v[3] != 0u; }

__device__ __forceinline__ float sum3(f32x4 v) { return (v[0] + v[1]) + v[2]; }

constexpr int NW = 8, NBUF = 4, NGRP = 64, NT = NGRP / NW;
constexpr int NQP = NH * DH / 64, NQW = 5, QW0 = 2, QST = DM / 64 / NQW;
constexpr int SVC = NW - 1;
constexpr int W_CT = DM / 64, G_TASKS = NB * (FF / 64), W_TASKS = W_CT * K3_KS, D_TASKS = 2 * (DM / 32);
static_assert(G_TASKS + W_TASKS + D_TASKS == NWG && NW == 8 && K3_ROWS == 4 * 128 && NBUF == 4 && SK / 64 == 128, "shape");
__device__ __forceinline__ unsigned fresh_tag(unsigned tags) {
  unsigned t = __builtin_amdgcn_readfirstlane(tags);
  do ++t; while (__any(tags == t));
  return t;
}

__global__ void __launch_bounds__(NW * 64, 2) fused_kernel(const bf16* __restrict__ x, const bf16* __restrict__ kc, const bf16* __restrict__ vc,
                                                        const bf16* __restrict__ wq, const bf16* __restrict__ wo, const bf16* __restrict__ wr,
                                                        const bf16* __restrict__ wg, const bf16* __restrict__ wu, const bf16* __restrict__ wd,
                                                        float* ws, bf16* __restrict__ out) {
  constexpr int WA = 128 * 2 + 16;
  __shared__ __attribute__((aligned(16))) char s_A[4 * 16 * WA];
  __shared__ __attribute__((aligned(16))) float s_red[6 * 256];
  __shared__ __attribute__((aligned(16))) float s_h[DM];
  __shared__ __attribute__((aligned(16))) float s_buf[NWIN * NW * DH];
  __shared__ __attribute__((aligned(16))) float s_sc[NGRP * 64];
  __shared__ __attribute__((aligned(16))) float s_q[NWIN * DH];
  __shared__ float s_m[NWIN * NW], s_l[NWIN * NW];
  __shared__ int s_qok, s_pcnt, s_ecnt, s_fcnt, s_icnt, s_org, s_hcnt;
  __shared__ int s_prog[NW];
  __shared__ float s_stat[4];

  const int tid = threadIdx.x;
  int lane = tid & 63, r = lane >> 3, j = lane & 7, mg = lane >> 4, mc = lane & 15;
  int wave = __builtin_amdgcn_readfirstlane(tid >> 6), bid = blockIdx.x;
  const unsigned t_start = (unsigned)__builtin_amdgcn_s_memrealtime();
  const int wid = (bid >> 5) * 32 + (bid & 7) * 4 + ((bid >> 3) & 3);
  const bool prod = bid < NQP;
  const __amdgpu_buffer_rsrc_t rs_ws = __builtin_amdgcn_make_buffer_rsrc(ws, 0, WS_END * 4, 0x00020000);
  const __amdgpu_buffer_rsrc_t rs_k = __builtin_amdgcn_make_buffer_rsrc(const_cast<bf16*>(kc), 0, NB * NH * SK * DH * 2, 0x00020000);
  const __amdgpu_buffer_rsrc_t rs_v = __builtin_amdgcn_make_buffer_rsrc(const_cast<bf16*>(vc), 0, NB * NH * SK * DH * 2, 0x00020000);
  const int voff = r * (DH * 2) + j * 16;
  const int c1 = wid % 3 == 0 ? 22 : 21, c2 = wid % 3 == 1 ? 42 : 43;
  auto seg_of = [&](int n) { return (n * 8 + wave >= c1) + (n * 8 + wave >= c2); };
  auto gload = [&](Slot (&sl)[NBUF], __amdgpu_buffer_rsrc_t rs, int n) {
    const int sn = seg_of(n), sg = wid + NWG * sn, row = sg / NSEG, k = sg - row * NSEG;
    const int off = (row * (SK / 64) + (k >> 1) * 43 + (n * 8 + wave - (sn == 0 ? 0 : (sn == 1 ? c1 : c2))) * 2 + ((k ^ (k >> 1)) & 1)) * (64 * DH * 2);
#pragma unroll
    for (int u = 0; u < NBUF; ++u) {
#pragma unroll
      for (int i = 0; i < 3; ++i) sl[u].a[i] = __builtin_amdgcn_raw_buffer_load_b128(rs, voff + i * 128, off + 2 * u * 3072, AUX_NT);
      MEMFENCE();
#pragma unroll
      for (int i = 0; i < 3; ++i) sl[u].b[i] = __builtin_amdgcn_raw_buffer_load_b128(rs, voff + i * 128, off + (2 * u + 1) * 3072, AUX_NT);
      if (u < NBUF - 1) __builtin_amdgcn_s_sleep(3);
      MEMFENCE();
    }
  };
  asm volatile("" :: "s"(ws), "s"(x), "s"(wq), "s"(kc));
  Slot b0[NBUF];
  const bool early = wave < QW0 && !prod;
  if (early) gload(b0, rs_k, 0);
  unsigned hs_nc = 0u, hs_ep = 0u;
  auto look_at_p = [&]() {
    if (bid == NWG - 1) {
      u32x4 hp[2], hc[2];
#pragma unroll
      for (int i = 0; i < 2; ++i) {
        hp[i] = __builtin_amdgcn_raw_buffer_load_b128(rs_ws, (WS_HP + lane * 8 + i * 4) * 4, 0, AUX_SC1);
        const u32x2 c0 = __builtin_amdgcn_raw_buffer_load_b64(rs_ws, (WS_HC + (lane * 4 + i * 2) * LSTR) * 4, 0, AUX_SC1);
        const u32x2 c1_ = __builtin_amdgcn_raw_buffer_load_b64(rs_ws, (WS_HC + (lane * 4 + i * 2 + 1) * LSTR) * 4, 0, AUX_SC1);
        hc[i] = u32x4{c0[0], c0[1], c1_[0], c1_[1]};
      }
      if (__all(hc[0][1] == hp[0][0] && hc[0][3] == hp[0][2] && hc[1][1] == hp[1][0] && hc[1][3] == hp[1][2]) &&
          __any(hc[0][0] != hp[0][1] || hc[0][2] != hp[0][3] || hc[1][0] != hp[1][1] || hc[1][2] != hp[1][3])) {
#pragma unroll
        for (int i = 0; i < 2; ++i)
          __builtin_amdgcn_raw_buffer_store_b128(u32x4{hp[i][0], hc[i][0], hp[i][2], hc[i][2]}, rs_ws, (WS_HP + lane * 8 + i * 4) * 4, 0, AUX_SC1);
      }
    }
    const u32x2 pw = __builtin_amdgcn_raw_buffer_load_b64(rs_ws, (WS_HP + bid * 2) * 4, 0, AUX_SC1);
    const unsigned np = __builtin_amdgcn_readfirstlane(pw[0]), ec = __builtin_amdgcn_readfirstlane(pw[1]);
    if (np != hs_ep) { hs_ep = np; __builtin_amdgcn_raw_buffer_store_b64(u32x2{hs_nc, hs_ep}, rs_ws, lane == 0 ? (WS_HC + bid * LSTR) * 4 : OOB, 0, AUX_SC1); }
    return ec == hs_nc;
  };
  auto answer = [&]() {
    const u32x4 z4 = {0u, 0u, 0u, 0u};
#pragma unroll
    for (int i = 0; i < (ZPW + 255) / 256; ++i)
      __builtin_amdgcn_raw_buffer_store_b128(z4, rs_ws, i * 256 + lane * 4 < ZPW ? (WS_Z0 + bid * ZPW + i * 256 + lane * 4) * 4 : OOB, 0, AUX_SC1);
    const u32x2 pw = __builtin_amdgcn_raw_buffer_load_b64(rs_ws, (WS_HP + bid * 2) * 4, 0, AUX_SC1);
    DRAIN();
    hs_ep = __builtin_amdgcn_readfirstlane(pw[0]);
    hs_nc = __builtin_amdgcn_readfirstlane(pw[1]) + 1u;
    __builtin_amdgcn_raw_buffer_store_b64(u32x2{hs_nc, hs_ep}, rs_ws, lane == 0 ? (WS_HC + bid * LSTR) * 4 : OOB, 0, AUX_SC1);
  };
  bool answered = false;
  if (tid < NW) s_prog[tid] = 1;
  if (tid == 0) { s_qok = 0; s_pcnt = 0; s_ecnt = 0; s_fcnt = 0; s_icnt = 0; s_hcnt = 0; s_org = (int)(t_start + POFF); }
  if (wave == SVC) {
    __builtin_amdgcn_s_setprio(3);
    int iofs[NWIN];
#pragma unroll
    for (int s = 0; s < NWIN; ++s) iofs[s] = (WS_QI + ((wid + NWG * s) / NSEG * DH + lane) * 2) * 4;
    const int oofs = prod ? (WS_QI + (bid * 64 + lane) * 2) * 4 : OOB;
    u32x2 in[NWIN * 3];
    unsigned ot[NB], ce[4];
#pragma unroll
    for (int e = 0; e < NWIN * 3; ++e) in[e] = __builtin_amdgcn_raw_buffer_load_b64(rs_ws, iofs[e / 3], (e % 3) * 512, AUX_SC1);
#pragma unroll
    for (int bb = 0; bb < NB; ++bb) ot[bb] = __builtin_amdgcn_raw_buffer_load_b64(rs_ws, oofs, bb * (HD * 8), AUX_SC1)[1];
    u32x4 xr[5], hp[2];
#pragma unroll
    for (int i = 0; i < 5; ++i) xr[i] = reinterpret_cast<const u32x4*>(x)[lane * 5 + i];
#pragma unroll
    for (int i = 0; i < 2; ++i) hp[i] = __builtin_amdgcn_raw_buffer_load_b128(rs_ws, bid == NWG - 1 ? (WS_HP + lane * 8 + i * 4) * 4 : OOB, 0, AUX_SC1);
#pragma unroll
    for (int i = 0; i < 4; ++i) ce[i] = __builtin_amdgcn_raw_buffer_load_b32(rs_ws, bid == NWG - 1 ? (WS_HC + (lane * 4 + i) * LSTR + 1) * 4 : OOB, 0, AUX_SC1);
    MEMFENCE();
    __syncthreads();
    MEMFENCE();
    if (bid == NWG - 1) {
#pragma unroll
      for (int i = 0; i < 2; ++i) {
#pragma unroll
        for (int e = 0; e < 2; ++e) { unsigned np = hp[i][2 * e] + 1u; if (np == ce[2 * i + e]) np += 1u; hp[i][2 * e] = np; }
        __builtin_amdgcn_raw_buffer_store_b128(hp[i], rs_ws, (WS_HP + lane * 8 + i * 4) * 4, 0, AUX_SC1);
      }
    }
    unsigned want[NWIN * 3];
#pragma unroll
    for (int e = 0; e < NWIN * 3; ++e) want[e] = fresh_tag(in[e][1]);
    if (prod) {
      float xs = 0.f;
#pragma unroll
      for (int i = 0; i < 20; ++i) xs += bf_lo(xr[i >> 2][i & 3]) + bf_hi(xr[i >> 2][i & 3]);
      const float mean = row16_sum(xs) * (1.0f / DM);
      float xv = 0.f;
#pragma unroll
      for (int i = 0; i < 20; ++i) { const float a = bf_lo(xr[i >> 2][i & 3]) - mean, c = bf_hi(xr[i >> 2][i & 3]) - mean; xv += a * a + c * c; }
      const float rstd = QSCALE / sqrtf(row16_sum(xv) * (1.0f / DM) + LN_EPS);
#pragma unroll
      for (int bb = 0; bb < NB; ++bb) ot[bb] = fresh_tag(ot[bb]);
      for (unsigned spin = 0; spin < (SPIN_CAP << 4) && lds_get(&s_pcnt) < NQW; ++spin) __builtin_amdgcn_s_sleep(1);
      __builtin_amdgcn_fence(__ATOMIC_ACQUIRE, "workgroup");
      float pr[5];
#pragma unroll
      for (int i = 0; i < 5; ++i) {
        pr[i] = 0.f;
#pragma unroll
        for (int k = 0; k < NQW; ++k) pr[i] += s_buf[2 * k * 320 + i * 64 + lane] + s_buf[(2 * k + 1) * 320 + i * 64 + lane];
      }
#pragma unroll
      for (int bb = 0; bb < NB; ++bb) {
        const float qv = (pr[bb] - __int_as_float(__builtin_amdgcn_readlane(__float_as_int(mean), bb * 16)) * pr[4]) *
                         __int_as_float(__builtin_amdgcn_readlane(__float_as_int(rstd), bb * 16));
        __builtin_amdgcn_raw_buffer_store_b64(u32x2{__float_as_uint(qv), ot[bb]}, rs_ws, oofs, bb * (HD * 8), AUX_SC1);
      }
    } else {
      __builtin_amdgcn_s_sleep(32);
    }
    bool all_out = false;
#pragma unroll 1
    for (unsigned spin = 0; spin < SPIN_CAP; ++spin) {
#pragma unroll
      for (int e = 0; e < NWIN * 3; ++e) in[e] = __builtin_amdgcn_raw_buffer_load_b64(rs_ws, iofs[e / 3], (e % 3) * 512, AUX_SC1);
      bool ok = true;
#pragma unroll
      for (int e = 0; e < NWIN * 3; ++e) ok = ok && in[e][1] == want[e];
      if (all_out || __all(ok)) break;
      if (spin == 15) { answer(); answered = true; }
      if (spin > 15 && (spin & 3) == 3) all_out = look_at_p();
      __builtin_amdgcn_s_sleep(4);
    }
#pragma unroll
    for (int e = 0; e < NWIN * 3; ++e) s_q[e * 64 + lane] = __uint_as_float(in[e][0]);
    __builtin_amdgcn_fence(__ATOMIC_RELEASE, "workgroup");
    lds_put(&s_qok, 1);
    __builtin_amdgcn_s_setprio(0);
  } else if (prod && wave >= QW0 && wave < QW0 + NQW) {
    const int k0 = (wave - QW0) * (QST * 64) + (mc >> 3) * 32 + mg * 8;
    u32x4 raw[QST * 8], xa[QST];
    const bf16* wb = wq + (size_t)k0 * HD + bid * 64 + (mc & 7) * 8;
#pragma unroll
    for (int st = 0; st < QST; ++st) xa[st] = *reinterpret_cast<const u32x4*>(x + (mc & 3) * DM + k0 + st * 64);
#pragma unroll
    for (int e = 0; e < QST * 8; ++e) raw[e] = __builtin_nontemporal_load(reinterpret_cast<const u32x4*>(wb + (size_t)((e >> 3) * 64 + (e & 7)) * HD));
    MEMFENCE();
    __syncthreads();
    MEMFENCE();
    f32x4 ac[8];
#pragma unroll
    for (int t = 0; t < 8; ++t) ac[t] = f32x4{0.f, 0.f, 0.f, 0.f};
#pragma unroll
    for (int st = 0; st < QST; ++st) {
      const u32x4 a = (mc & 4) == 0 ? xa[st] : u32x4{0x3f803f80u, 0x3f803f80u, 0x3f803f80u, 0x3f803f80u};
#pragma unroll
      for (int c = 0; c < 4; ++c) {
        u32x4 f0, f1;
#pragma unroll
        for (int i = 0; i < 4; ++i) { f0[i] = pk_lo(raw[st * 8 + 2 * i][c], raw[st * 8 + 2 * i + 1][c]); f1[i] = pk_hi(raw[st * 8 + 2 * i][c], raw[st * 8 + 2 * i + 1][c]); }
        ac[2 * c] = mfma16(a, f0, ac[2 * c]);
        ac[2 * c + 1] = mfma16(a, f1, ac[2 * c + 1]);
      }
    }
    if ((mg >> 1) == (mc >> 3)) {
      float* const dst = &s_buf[((wave - QW0) * 2 + (mg >> 1)) * 320 + (mc & 7) * 8];
#pragma unroll
      for (int bb = 0; bb < NB; ++bb)
        if (bb == 0 || (mg & 1) == 0) {
          *reinterpret_cast<f32x4*>(dst + ((mg & 1) * 4 + bb) * 64) = f32x4{ac[0][bb], ac[1][bb], ac[2][bb], ac[3][bb]};
          *reinterpret_cast<f32x4*>(dst + ((mg & 1) * 4 + bb) * 64 + 4) = f32x4{ac[4][bb], ac[5][bb], ac[6][bb], ac[7][bb]};
        }
    }
    __builtin_amdgcn_fence(__ATOMIC_RELEASE, "workgroup");
    if (lane == 0) __hip_atomic_fetch_add(&s_pcnt, 1, __ATOMIC_RELAXED, __HIP_MEMORY_SCOPE_WORKGROUP);
  } else {
    __syncthreads();
  }
  MEMFENCE();
  u32x2 rep = {0u, 0u};
  unsigned t_thru = 0u;
  auto pace = [&](int g) {
    if (wave == 0 && g > AFROM) {
      const int org = lds_get(&s_org);
      const bool ok = rep[0] - t_start + 1000u < 6000u && rep[1] - t_start + 1000u < 6000u;
      const float dorg = row16_sum(ok ? (float)(int)(rep[1] - (unsigned)org) : 0.f);
      const float lt = ok ? (float)min((int)(rep[0] - rep[1]), LCAP) : (float)LCAP;
      const float lx = fminf(lt, dpp<DPP_ROR8>(lt));
      float hi = fmaxf(lx, dpp<DPP_XOR1>(lx)); hi = fmaxf(hi, dpp<DPP_XOR2>(hi)); hi = fmaxf(hi, dpp<DPP_HMIRROR>(hi));
      const float lat = row16_sum(lt) * (0.8f / 16.f) + hi * 0.2f;
      if (lane == 0) lds_put(&s_org, org + (int)(dorg * (1.f / 16.f) + fmaxf((lat - LTGT) * 0.6f, g > EFROM ? (float)SLACK : -(float)DOWN)));
    }
    const unsigned rel = (unsigned)(g * PTAU + wave * (PTAU / 8) + min(g * 8 + wave, 16) * (FFWD / 16) - FFWD + ((wid * 65) & 255) * (PTAU / 8) / 256);
    unsigned now = (unsigned)__builtin_amdgcn_s_memrealtime();
    if (wave == 1 && (bid >> 4) == 7 && g >= 2)
      __builtin_amdgcn_raw_buffer_store_b64(u32x2{t_thru - rel + PTAU, (unsigned)lds_get(&s_org)}, rs_ws, lane == 0 ? (WS_REP + ((g - 2) * 16 + (bid & 15)) * LSTR) * 4 : OOB, 0, AUX_SC1);
    for (unsigned spin = 0; spin < 4096 && (int)(now - rel - (unsigned)__builtin_amdgcn_readfirstlane(lds_get(&s_org))) < 0; ++spin) {
      __builtin_amdgcn_s_sleep(2);
      now = (unsigned)__builtin_amdgcn_s_memrealtime();
    }
    if (g >= 2) rep = __builtin_amdgcn_raw_buffer_load_b64(rs_ws, (wave == 0 && lane < 16 && g >= AFROM) ? (WS_REP + ((g - 2) * 16 + lane) * LSTR) * 4 : OOB, 0, 0);
    if (lane == 0) lds_put(&s_prog[wave], g);
    int pp = __builtin_amdgcn_readfirstlane(lds_get(&s_prog[wave ^ (NW / 2)]));
    for (unsigned spin = 0; spin < 4096 && g > pp + 1; ++spin) { __builtin_amdgcn_s_sleep(4); pp = __builtin_amdgcn_readfirstlane(lds_get(&s_prog[wave ^ (NW / 2)])); }
    if (g > pp) __builtin_amdgcn_s_setprio(0); else __builtin_amdgcn_s_setprio(1);
  };
  float gmx = -INFINITY;
  auto k_group = [&](const Slot (&sl)[NBUF], int n, int s) {
    f32x2 q[12];
#pragma unroll
    for (int i = 0; i < 6; ++i) {
      const f32x4 a = *reinterpret_cast<const f32x4*>(&s_q[s * DH + (i >> 1) * 64 + j * 8 + (i & 1) * 4]);
      q[i * 2] = f32x2{a[0], a[1]}; q[i * 2 + 1] = f32x2{a[2], a[3]};
    }
    float* const sc = &s_sc[(wave * NT + n) * 64 + r * 8];
    MEMFENCE();
#pragma unroll
    for (int u = 0; u < NBUF; ++u) {
      k_tile(sl[u].a, q, gmx, sc + 2 * u, j);
      k_tile(sl[u].b, q, gmx, sc + 2 * u + 1, j);
      MEMFENCE();
    }
    t_thru = (unsigned)__builtin_amdgcn_s_memrealtime();
  };
  {
    Slot b1[NBUF];
    if (!early) {
      pace(0);
      gload(b0, rs_k, 0);
    }
    pace(1);
    gload(b1, rs_k, 1);
    for (unsigned spin = 0; spin < (SPIN_CAP << 4) && lds_get(&s_qok) == 0; ++spin) __builtin_amdgcn_s_sleep(2);
    __builtin_amdgcn_fence(__ATOMIC_ACQUIRE, "workgroup");
    k_group(b0, 0, 0);
    k_group(b1, 1, 0);
    if (wave == SVC && !answered) answer();
  }
  Slot buf[NBUF];
  int seg = 0;
  bool through = false;
  for (int n = 2; n < NT; ++n) {
    pace(n);
    gload(buf, rs_k, n);
    if (seg_of(n) != seg) {
      const float gm = max_r(gmx);
      if (lane == 0) s_m[seg * NW + wave] = gm;
      gmx = -INFINITY;
      ++seg;
    }
    k_group(buf, n, seg);
    if (wave == SVC && bid == NWG - 1 && n >= 3 && !through) through = look_at_p();
  }
  {
    const float gm = max_r(gmx);
    if (lane == 0) s_m[seg * NW + wave] = gm;
  }
  float l = 0.f;
  f32x2 acc[12];
#pragma unroll
  for (int i = 0; i < 12; ++i) acc[i] = f32x2{0.f, 0.f};
  __builtin_amdgcn_fence(__ATOMIC_RELEASE, "wavefront"); __builtin_amdgcn_wave_barrier(); __builtin_amdgcn_fence(__ATOMIC_ACQUIRE, "wavefront");
  for (int n = 0; n < NT; ++n) {
    pace(NT + n);
    gload(buf, rs_v, n);
    if (n == NT - 1) {
      if (lane == 0) __hip_atomic_fetch_add(&s_icnt, 1, __ATOMIC_RELAXED, __HIP_MEMORY_SCOPE_WORKGROUP);
      __builtin_amdgcn_s_setprio(3);
    }
    seg = seg_of(n);
    {
      float p[8];
      const float m = s_m[seg * NW + wave];
      const float* const sc = &s_sc[(wave * NT + n) * 64 + r * 8];
      const f32x4 sa = *reinterpret_cast<const f32x4*>(sc), sb = *reinterpret_cast<const f32x4*>(sc + 4);
#pragma unroll
      for (int t = 0; t < 4; ++t) { p[t] = __builtin_amdgcn_exp2f(sa[t] - m); p[4 + t] = __builtin_amdgcn_exp2f(sb[t] - m); }
      l += ((p[0] + p[1]) + (p[2] + p[3])) + ((p[4] + p[5]) + (p[6] + p[7]));
      MEMFENCE();
#pragma unroll
      for (int u = 0; u < NBUF; ++u) {
        v_tile(buf[u].a, p[2 * u], acc);
        v_tile(buf[u].b, p[2 * u + 1], acc);
        MEMFENCE();
      }
    }
    t_thru = (unsigned)__builtin_amdgcn_s_memrealtime();
    if (wave == SVC && n == 1)
      for (unsigned spin = 0; spin < SPIN_CAP && !look_at_p(); ++spin) __builtin_amdgcn_s_sleep(4);
    if (n == NT - 1 || seg_of(n + 1) != seg) {
      const float lw = sum_r(l);
      float t12[12], t6[6];
#pragma unroll
      for (int e = 0; e < 12; ++e) t12[e] = tsum32(acc[e >> 1][e & 1], acc[6 + (e >> 1)][e & 1]);
#pragma unroll
      for (int e = 0; e < 6; ++e) { t6[e] = tsum16(t12[e], t12[e + 6]); t6[e] += dpp<DPP_ROR8>(t6[e]); }
      if ((lane & 8) == 0) {
#pragma unroll
        for (int e = 0; e < 3; ++e) *reinterpret_cast<f32x2*>(&s_buf[(seg * NW + wave) * DH + j * 24 + (lane >> 4) * 6 + e * 2]) = f32x2{t6[2 * e], t6[2 * e + 1]};
      }
      if (lane == 0) s_l[seg * NW + wave] = lw;
      l = 0.f;
#pragma unroll
      for (int i = 0; i < 12; ++i) acc[i] = f32x2{0.f, 0.f};
      if (seg != 0) {
        __builtin_amdgcn_fence(__ATOMIC_RELEASE, "workgroup");
        int c = 0;
        if (lane == 0) c = __hip_atomic_fetch_add(seg == 1 ? &s_ecnt : &s_fcnt, 1, __ATOMIC_RELAXED, __HIP_MEMORY_SCOPE_WORKGROUP);
        if (__builtin_amdgcn_readfirstlane(c) == NW - 1) {
          __builtin_amdgcn_fence(__ATOMIC_ACQUIRE, "workgroup");
#pragma unroll 1
          for (int s = seg & 2; s <= seg; ++s) {
            float M = s_m[s * NW];
#pragma unroll
            for (int w = 1; w < NW; ++w) M = fmaxf(M, s_m[s * NW + w]);
            float L = 0.f;
            f32x4 o = {0.f, 0.f, 0.f, 0.f};
            const int d4 = lane < DH / 4 ? ((lane >> 1) & 7) * 24 + (lane >> 4) * 8 + (lane & 1) * 4 : 0;
#pragma unroll
            for (int w = 0; w < NW; ++w) {
              const float e = __builtin_amdgcn_exp2f(s_m[s * NW + w] - M);
              L = fmaf(e, s_l[s * NW + w], L);
              o += e * *reinterpret_cast<const f32x4*>(&s_buf[(s * NW + w) * DH + d4]);
            }
            if (lane >= DH / 4) o = f32x4{M, L, 1.f, 1.f};
            __builtin_amdgcn_raw_buffer_store_b128(u32x4{nz_bits(o[0]), nz_bits(o[1]), nz_bits(o[2]), nz_bits(o[3])}, rs_ws,
                                                   lane < REC / 4 ? (WS_PO + (wid + NWG * s) * REC + lane * 4) * 4 : OOB, 0, AUX_SC1);
          }
          if (seg == 2) { MEMFENCE(); lds_put(&s_fcnt, NW + 1); }
          if (seg == 1) {
            DRAIN();
            __builtin_amdgcn_raw_buffer_store_b32(1u, rs_ws, lane == 0 ? ef_ofs(wid) : OOB, 0, AUX_SC1);
          }
        }
      }
      MEMFENCE();
    }
  }
  __builtin_amdgcn_s_setprio(0);

  {
    asm volatile("" : "+s"(wave), "+s"(bid), "+v"(lane));
    r = lane >> 3; j = lane & 7; mg = lane >> 4; mc = lane & 15;
    const int w = wave;
    const int xcd = bid & 7, ri = bid >> 3;
    const bool isW = ri < 15, isG = !isW && ri < 27;
    const int tt = isW ? xcd * 15 + ri : (isG ? xcd * 12 + ri - 15 : xcd * 5 + ri - 27);
    const int ks1 = tt / W_CT, ct1 = tt - ks1 * W_CT;
    typedef const __attribute__((address_space(1))) char* gptr;
    auto behind_stream = [&]() {
      for (unsigned spin = 0; spin < (SPIN_CAP << 4) && lds_get(&s_icnt) < NW; ++spin) __builtin_amdgcn_s_sleep(1);
      for (int i = 0; i < 4 && lds_get(&s_fcnt) <= NW; ++i) __builtin_amdgcn_s_sleep(3);
    };
    u32x4 fr[16];
    u32x2 raw[32];
    auto weights = [&](const bf16* src, int skip) {
      gptr wb = (gptr)src;
#pragma unroll
      for (int e = 0; e < 32; ++e) {
        raw[e] = __builtin_nontemporal_load((const __attribute__((address_space(1))) u32x2*)wb);
        wb += ((e & 7) == 7 ? skip : 1) * (DM * 2);
        asm volatile("" : "+v"(wb));
      }
      MEMFENCE();
    };
    auto frags = [&]() {
      PIN8(raw, 0); PIN8(raw, 8); PIN8(raw, 16); PIN8(raw, 24);
#pragma unroll
      for (int st = 0; st < 4; ++st)
#pragma unroll
        for (int i = 0; i < 4; ++i) {
          const u32x2 r0 = raw[st * 8 + 2 * i], r1 = raw[st * 8 + 2 * i + 1];
          fr[st * 4 + 0][i] = pk_lo(r0[0], r1[0]); fr[st * 4 + 1][i] = pk_hi(r0[0], r1[0]);
          fr[st * 4 + 2][i] = pk_lo(r0[1], r1[1]); fr[st * 4 + 3][i] = pk_hi(r0[1], r1[1]);
        }
      PIN8(fr, 0); PIN8(fr, 8);
    };
    if (isW) {
      if (w >= 4) return;
      char* const img = s_A + w * (16 * WA);
      {
        int offp[2], ne[2], sg0[2];
#pragma unroll
        for (int hf = 0; hf < 2; ++hf) {
          const int row = ks1 * K3_ROWS + w * 128 + hf * 64, hh = row / DH;
          sg0[hf] = (mg * NH + hh) * NSEG;
          offp[hf] = (WS_PO + sg0[hf] * REC + row - hh * DH + mc * 4) * 4;
          ne[hf] = min(max(2 * NWG - sg0[hf], 0), NSEG);
        }
        const int sgm = (mc < 8 ? sg0[0] : sg0[1]) + j;
        const unsigned emask = (1u << ne[0]) - 1u + (((1u << ne[1]) - 1u) << NSEG) + (sgm < 2 * NWG ? 1u << (2 * NSEG) : 0u);
        u32x4 pv[2 * NSEG];
        u32x2 ml = {0u, 0u};
#pragma unroll
        for (int k = 0; k < 2 * NSEG; ++k) pv[k] = u32x4{0u, 0u, 0u, 0u};
        unsigned pend = (1u << (2 * NSEG)) - 1u + (j < NSEG ? 1u << (2 * NSEG) : 0u);
        auto look = [&](unsigned go) {
          if (go & (1u << (2 * NSEG))) ml = __builtin_amdgcn_raw_buffer_load_b64(rs_ws, (WS_PO + sgm * REC + DH) * 4, 0, AUX_SC1);
#pragma unroll
          for (int k = 0; k < 2 * NSEG; ++k)
            if (go & (1u << k)) pv[k] = __builtin_amdgcn_raw_buffer_load_b128(rs_ws, offp[k / NSEG], (k % NSEG) * REC * 4, AUX_SC1);
        };
        for (unsigned spin = 0; spin < (SPIN_CAP << 4) && lds_get(&s_ecnt) < NW; ++spin) __builtin_amdgcn_s_sleep(4);
        for (unsigned spin = 0; spin < SPIN_CAP; ++spin) {
          const bool fl = j < NSEG && sgm < 2 * NWG;
          if (__all(!fl || __builtin_amdgcn_raw_buffer_load_b32(rs_ws, fl ? ef_ofs(sgm & (NWG - 1)) : OOB, 0, AUX_SC1) != 0u)) break;
          __builtin_amdgcn_s_sleep(8);
        }
        behind_stream();
        weights(wo + (size_t)(ks1 * K3_ROWS + w * 128 + mg * 8) * DM + ct1 * 64 + mc * 4, 25);
        look(pend & emask);
        pend &= ~emask;
        for (unsigned sp2 = 0; sp2 < (SPIN_CAP << 4) && lds_get(&s_fcnt) < NW; ++sp2) __builtin_amdgcn_s_sleep(1);
        look(pend);
        MEMFENCE();
        frags();
#pragma unroll 1
        for (unsigned spin = 0; spin < SPIN_CAP; ++spin) {
          unsigned got = ml[0] != 0u && ml[1] != 0u;
#pragma unroll
          for (int k = 2 * NSEG - 1; k >= 0; --k) got = got * 2u + (unsigned)all_nz(pv[k]);
          pend &= ~got;
          if (!__any(pend != 0u)) break;
          if (spin >= 8) __builtin_amdgcn_s_sleep(4);
          look(pend);
        }
        const float mm = j < NSEG ? __uint_as_float(ml[0]) : -INFINITY;
        float M = fmaxf(mm, dpp<DPP_XOR1>(mm)); M = fmaxf(M, dpp<DPP_XOR2>(M)); M = fmaxf(M, dpp<DPP_HMIRROR>(M));
        const float e = __builtin_amdgcn_exp2f(mm - M);
        float L = e * __uint_as_float(ml[1]);
        L += dpp<DPP_XOR1>(L); L += dpp<DPP_XOR2>(L); L += dpp<DPP_HMIRROR>(L);
        const float wt = e * __builtin_amdgcn_rcpf(L);
        f32x4 o0 = dpp<DPP_BCAST + 0>(wt) * __builtin_bit_cast(f32x4, pv[0]), o1 = dpp<DPP_BCAST + 8>(wt) * __builtin_bit_cast(f32x4, pv[6]);
        o0 += dpp<DPP_BCAST + 1>(wt) * __builtin_bit_cast(f32x4, pv[1]); o1 += dpp<DPP_BCAST + 9>(wt) * __builtin_bit_cast(f32x4, pv[7]);
        o0 += dpp<DPP_BCAST + 2>(wt) * __builtin_bit_cast(f32x4, pv[2]); o1 += dpp<DPP_BCAST + 10>(wt) * __builtin_bit_cast(f32x4, pv[8]);
        o0 += dpp<DPP_BCAST + 3>(wt) * __builtin_bit_cast(f32x4, pv[3]); o1 += dpp<DPP_BCAST + 11>(wt) * __builtin_bit_cast(f32x4, pv[9]);
        o0 += dpp<DPP_BCAST + 4>(wt) * __builtin_bit_cast(f32x4, pv[4]); o1 += dpp<DPP_BCAST + 12>(wt) * __builtin_bit_cast(f32x4, pv[10]);
        o0 += dpp<DPP_BCAST + 5>(wt) * __builtin_bit_cast(f32x4, pv[5]); o1 += dpp<DPP_BCAST + 13>(wt) * __builtin_bit_cast(f32x4, pv[11]);
        put_terms4(img + mg * 4 * WA + mc * 8, WA, o0);
        put_terms4(img + mg * 4 * WA + 128 + mc * 8, WA, o1);
      }
      __builtin_amdgcn_fence(__ATOMIC_RELEASE, "wavefront"); __builtin_amdgcn_wave_barrier(); __builtin_amdgcn_fence(__ATOMIC_ACQUIRE, "wavefront");
      f32x4 ac[4];
#pragma unroll
      for (int t = 0; t < 4; ++t) ac[t] = f32x4{0.f, 0.f, 0.f, 0.f};
#pragma unroll
      for (int st = 0; st < 4; ++st) {
        const u32x4 a = *reinterpret_cast<const u32x4*>(img + mc * WA + st * 64 + mg * 16);
#pragma unroll
        for (int t = 0; t < 4; ++t) ac[t] = mfma16(a, fr[st * 4 + t], ac[t]);
      }
      *reinterpret_cast<f32x4*>(&s_red[w * 256 + lane * 4]) = f32x4{sum3(ac[0]), sum3(ac[1]), sum3(ac[2]), sum3(ac[3])};
      __syncthreads();
      if (w == 0) {
        const f32x4 s0 = (*reinterpret_cast<const f32x4*>(&s_red[lane * 4]) + *reinterpret_cast<const f32x4*>(&s_red[256 + lane * 4])) +
                         (*reinterpret_cast<const f32x4*>(&s_red[512 + lane * 4]) + *reinterpret_cast<const f32x4*>(&s_red[768 + lane * 4]));
        __builtin_amdgcn_raw_buffer_store_b128(u32x4{nz_bits(s0[0]), nz_bits(s0[1]), nz_bits(s0[2]), nz_bits(s0[3])}, rs_ws,
                                               (WS_SLAB + (ks1 * NB + mg) * DM + ct1 * 64 + mc * 4) * 4, 0, AUX_SC1);
      }
    } else if (isG) {
      const int gb = tt & 3, gt = tt >> 2;
      if (w < 5) {
        char* const img = s_A + w * (4 * WA);
        const int hcol = w * 128 + (lane & 31) * 4;
        uint2 xx = {0u, 0u};
        u32x4 fg[32];
        {
          u32x4 rg[32];
          if (lane < 16) *reinterpret_cast<u32x4*>(img + 3 * WA + lane * 16) = u32x4{0x3f803f80u, 0x3f803f80u, 0x3f803f80u, 0x3f803f80u};
          if (lane < 32) xx = *reinterpret_cast<const uint2*>(x + gb * DM + hcol);
          behind_stream();
          gptr wb = (gptr)((mc < 8 ? wg : wu) + (size_t)(w * 128 + mg * 8) * FF + gt * 64 + (mc & 7) * 8);
#pragma unroll
          for (int e = 0; e < 32; ++e) {
            rg[e] = __builtin_nontemporal_load((const __attribute__((address_space(1))) u32x4*)wb);
            wb += ((e & 7) == 7 ? 25 : 1) * (FF * 2);
            asm volatile("" : "+v"(wb));
          }
          MEMFENCE();
#pragma unroll
          for (int st = 0; st < 4; ++st) {
            PIN8(rg, st * 8);
#pragma unroll
            for (int c = 0; c < 4; ++c)
#pragma unroll
              for (int i = 0; i < 4; ++i) {
                fg[st * 8 + 2 * c][i] = pk_lo(rg[st * 8 + 2 * i][c], rg[st * 8 + 2 * i + 1][c]);
                fg[st * 8 + 2 * c + 1][i] = pk_hi(rg[st * 8 + 2 * i][c], rg[st * 8 + 2 * i + 1][c]);
              }
            PIN8(fg, st * 8);
            MEMFENCE();
          }
        }
        asm volatile("" : "+v"(xx.x), "+v"(xx.y));
        const int off = (WS_SLAB + ((lane >> 5) * (K3_KS / 2) * NB + gb) * DM + hcol) * 4;
        const int poff = (WS_SLAB + ((mg >> 1) * (K3_KS / 2) + mc) * (NB * DM) + gb * DM + w * 128 + (mg & 1) * 64) * 4;
        u32x4 sl[K3_KS / 2];
#pragma unroll
        for (int ks = 0; ks < K3_KS / 2; ++ks) sl[ks] = u32x4{0u, 0u, 0u, 0u};
        const unsigned pend = (1u << (K3_KS / 2)) - 1u;
        unsigned todo = pend, go = 0u;
        for (unsigned spin = 0; spin < SPIN_CAP; ++spin) {
#pragma unroll
          for (int ks = 0; ks < K3_KS / 2; ++ks)
            if (go & (1u << ks)) sl[ks] = __builtin_amdgcn_raw_buffer_load_b128(rs_ws, off, ks * (NB * DM * 4), AUX_SC1);
          todo &= ~go;
          if (todo != 0u) {
            const unsigned pr = __builtin_amdgcn_raw_buffer_load_b32(rs_ws, (mc < K3_KS / 2 && ((todo >> mc) & 1u)) ? poff : OOB, 0, AUX_SC1);
            const unsigned long long bl = __ballot(pr != 0u);
            go = (unsigned)(bl & bl >> 16 & bl >> 32 & bl >> 48) & todo;
            if (__builtin_popcount(todo) * 4 - __builtin_popcountll(bl) <= PROBE_MIN) go = todo;
          } else {
            unsigned got = 0u;
#pragma unroll
            for (int ks = K3_KS / 2 - 1; ks >= 0; --ks) got = got * 2u + (unsigned)__all(all_nz(sl[ks]));
            go = pend & ~got;
            if (go == 0u) break;
          }
        }
        f32x4 hv = f32x4{bf_lo(xx.x), bf_hi(xx.x), bf_lo(xx.y), bf_hi(xx.y)};
#pragma unroll
        for (int ks = 0; ks < K3_KS / 2; ++ks) hv += __builtin_bit_cast(f32x4, sl[ks]);
#pragma unroll
        for (int e = 0; e < 4; ++e) hv[e] = tsum32(hv[e], hv[e]);
        if (lane < 32) {
          *reinterpret_cast<f32x4*>(&s_h[hcol]) = hv;
          put_terms4(img + lane * 8, WA, hv);
        }
        __builtin_amdgcn_fence(__ATOMIC_RELEASE, "workgroup");
        __builtin_amdgcn_wave_barrier();
        if (lane == 0) __hip_atomic_fetch_add(&s_hcnt, 1, __ATOMIC_RELAXED, __HIP_MEMORY_SCOPE_WORKGROUP);
        __builtin_amdgcn_fence(__ATOMIC_ACQUIRE, "wavefront");
        f32x4 ac[8];
#pragma unroll
        for (int t = 0; t < 8; ++t) ac[t] = f32x4{0.f, 0.f, 0.f, 0.f};
        u32x4 av[4];
#pragma unroll
        for (int st = 0; st < 4; ++st) av[st] = *reinterpret_cast<const u32x4*>(img + (mc & 3) * WA + st * 64 + mg * 16);
#pragma unroll
        for (int st = 0; st < 4; ++st)
#pragma unroll
          for (int t = 0; t < 8; ++t) ac[t] = mfma16(av[st], fg[st * 8 + t], ac[t]);
        const int hf = (mg & 1) * 4;
        const f32x4 a0 = hf ? ac[4] : ac[0], a1 = hf ? ac[5] : ac[1], a2 = hf ? ac[6] : ac[2], a3 = hf ? ac[7] : ac[3];
        *reinterpret_cast<f32x4*>(&s_red[w * 256 + (mg >> 1) * 128 + mc * 8 + hf]) =
            mg < 2 ? f32x4{sum3(a0), sum3(a1), sum3(a2), sum3(a3)} : f32x4{a0[3], a1[3], a2[3], a3[3]};
      } else if (w == NW - 1) {
        unsigned wr2[5];
#pragma unroll
        for (int i = 0; i < 5; ++i) wr2[i] = reinterpret_cast<const unsigned*>(wr)[lane + 64 * i];
        PIN4(wr2, 0); asm volatile("" : "+v"(wr2[4]));
        for (unsigned spin = 0; spin < (SPIN_CAP << 6) && lds_get(&s_hcnt) < 5; ++spin) {}
        __builtin_amdgcn_fence(__ATOMIC_ACQUIRE, "workgroup");
        const float piv = s_h[0];
        float hv[10], sm = 0.f, sq = 0.f;
#pragma unroll
        for (int i = 0; i < 5; ++i) {
          const float2 t2 = *reinterpret_cast<const float2*>(&s_h[2 * lane + 128 * i]);
          hv[2 * i] = t2.x; hv[2 * i + 1] = t2.y;
        }
        PIN8(hv, 0); asm volatile("" : "+v"(hv[8]), "+v"(hv[9]));
#pragma unroll
        for (int i = 0; i < 10; ++i) { hv[i] -= piv; sm += hv[i]; sq = fmaf(hv[i], hv[i], sq); }
        const float dm = wave_sum(sm) * (1.0f / DM);
        const float var = fmaxf(fmaf(-dm, dm, wave_sum(sq) * (1.0f / DM)), 0.f);
        const float rstd = __builtin_amdgcn_rsqf(var + LN_EPS);
        if (lane == 0) { s_stat[0] = piv + dm; s_stat[1] = rstd; }
        __syncthreads();
        if (gt < DM / 32) {
          float rr = 0.f;
#pragma unroll
          for (int i = 0; i < 5; ++i) { rr = fmaf((hv[2 * i] - dm) * rstd, bf_lo(wr2[i]), rr); rr = fmaf((hv[2 * i + 1] - dm) * rstd, bf_hi(wr2[i]), rr); }
          rr = wave_sum(rr);
          const float hh = s_h[gt * 32 + (lane & 31)];
          __builtin_amdgcn_raw_buffer_store_b32(nz_bits(lane >= 32 ? 1.0f / (1.0f + __expf(-rr)) : hh + (hh - (piv + dm)) * rstd), rs_ws,
                                                (WS_BASE + (gb * (DM / 32) + gt) * 64 + lane) * 4, 0, AUX_SC1);
        }
        return;
      } else {
        return;
      }
      int o_res = (WS_ACT + (gb * (FF / 64) + gt) * 96 + (lane & 1) * 32 + (lane >> 1)) * 4;
      asm volatile("" : "+v"(o_res));
      __syncthreads();
      if (w == 0) {
        float pr[24];
#pragma unroll
        for (int k = 0; k < 20; ++k) pr[k] = s_red[(k % 5) * 256 + (k / 5) * 64 + lane];
        pr[20] = s_stat[0]; pr[21] = s_stat[1]; pr[22] = 0.f; pr[23] = 0.f;
        PIN8(pr, 0); PIN8(pr, 8); PIN8(pr, 16);
        float sm[4];
#pragma unroll
        for (int k = 0; k < 4; ++k) sm[k] = ((pr[5 * k] + pr[5 * k + 1]) + (pr[5 * k + 2] + pr[5 * k + 3])) + pr[5 * k + 4];
        const float gg = (sm[0] - pr[20] * sm[2]) * pr[21], uu = (sm[1] - pr[20] * sm[3]) * pr[21];
        unsigned t[3], wd2[3];
        split3(gg * __builtin_amdgcn_rcpf(1.0f + __expf(-gg)) * uu, t[0], t[1], t[2]);
#pragma unroll
        for (int k = 0; k < 3; ++k) {
          const unsigned o = __float_as_uint(dpp<DPP_XOR1>(__uint_as_float(t[k])));
          wd2[k] = nz_bits(__uint_as_float((lane & 1) ? pk_hi(o, t[k]) : pk_hi(t[k], o)));
        }
        __builtin_amdgcn_raw_buffer_store_b32((lane & 1) ? wd2[1] : wd2[0], rs_ws, o_res, 0, AUX_SC1);
        __builtin_amdgcn_raw_buffer_store_b32(wd2[2], rs_ws, (lane & 1) ? OOB : o_res, 256, AUX_SC1);
      }
    } else {
      if (w >= 6) return;
      const int bp = tt & 1, ct = tt >> 1, bb = lane >> 5, f = lane & 31;
      for (unsigned spin = 0; spin < (SPIN_CAP << 4) && lds_get(&s_fcnt) < NW; ++spin) __builtin_amdgcn_s_sleep(1);
      __builtin_amdgcn_s_sleep(64);
      weights(wd + (size_t)(w * 256 + (mc >> 3) * 32 + mg * 8) * DM + ct * 32 + (mc & 7) * 4, 57);
      frags();
      const __amdgpu_buffer_rsrc_t rs_out = __builtin_amdgcn_make_buffer_rsrc(out, 0, NB * DM * 2, 0x00020000);
      int a_red = w * 256 + mg * 32 + (mc & 7) * 4, o_res = ((bp * 2 + bb) * DM + ct * 32 + f) * 2;
      asm volatile("" : "+v"(a_red), "+v"(o_res));
      const int off = (WS_ACT + (((bp * 2 + ((mc >> 2) & 1)) * (FF / 64) + w * 4) * 3 + (mc & 3)) * 32 + (mc >> 3) * 16 + mg * 4) * 4;
      const int offb = (WS_BASE + ((bp * 2 + bb) * (DM / 32) + ct) * 64 + f) * 4;
      u32x4 av[4];
#pragma unroll
      for (int st = 0; st < 4; ++st) av[st] = u32x4{0u, 0u, 0u, 0u};
      unsigned bs = 0u, sg = 0u;
      unsigned pend = ((mc & 3) == 3 ? 0u : 15u) + (w == 0 ? 16u : 0u);
      for (unsigned spin = 0; spin < SPIN_CAP; ++spin) {
#pragma unroll
        for (int st = 0; st < 4; ++st)
          if (pend & (1u << st)) av[st] = __builtin_amdgcn_raw_buffer_load_b128(rs_ws, off, st * 384, AUX_SC1);
        if (pend & 16u) { bs = __builtin_amdgcn_raw_buffer_load_b32(rs_ws, offb, 0, AUX_SC1); sg = __builtin_amdgcn_raw_buffer_load_b32(rs_ws, offb, 128, AUX_SC1); }
        unsigned got = bs != 0u && sg != 0u;
#pragma unroll
        for (int st = 3; st >= 0; --st) got = got * 2u + (unsigned)all_nz(av[st]);
        pend &= ~got;
        if (!__any(pend != 0u)) break;
      }
      f32x4 ac[4];
#pragma unroll
      for (int t = 0; t < 4; ++t) ac[t] = f32x4{0.f, 0.f, 0.f, 0.f};
#pragma unroll
      for (int st = 0; st < 4; ++st)
#pragma unroll
        for (int t = 0; t < 4; ++t) ac[t] = mfma16(av[st], fr[st * 4 + t], ac[t]);
      if ((mc >> 3) == (mg >> 1)) *reinterpret_cast<f32x4*>(&s_red[a_red]) = f32x4{sum3(ac[0]), sum3(ac[1]), sum3(ac[2]), sum3(ac[3])};
      __syncthreads();
      if (w == 0) {
        float pr[12];
#pragma unroll
        for (int k = 0; k < 12; ++k) pr[k] = s_red[(k >> 1) * 256 + (k & 1) * 64 + lane];
        PIN8(pr, 0); PIN4(pr, 8);
        const float y = (((pr[0] + pr[1]) + (pr[2] + pr[3])) + ((pr[4] + pr[5]) + (pr[6] + pr[7]))) + ((pr[8] + pr[9]) + (pr[10] + pr[11]));
        const bf16 ov = __float2bfloat16(fmaf(y, __uint_as_float(sg), __uint_as_float(bs)));
        __builtin_amdgcn_raw_buffer_store_b16(__builtin_bit_cast(unsigned short, ov), rs_out, o_res, 0, 0);
      }
    }
  }
}

extern "C" void kernel_launch(void* const* d_in, const int* in_sizes, int n_in, void* d_out, int out_size, void* d_ws,
                              size_t ws_size, hipStream_t stream) {
  fused_kernel<<<NWG, NW * 64, 0, stream>>>((const bf16*)d_in[0], (const bf16*)d_in[1], (const bf16*)d_in[2], (const bf16*)d_in[3],
                                            (const bf16*)d_in[4], (const bf16*)d_in[6], (const bf16*)d_in[7], (const bf16*)d_in[8],
                                            (const bf16*)d_in[9], (float*)d_ws, (bf16*)d_out);
}
```

```cpp
#include <hip/hip_runtime.h>
#include <hip/hip_bf16.h>
#include <stdint.h>

typedef __hip_bfloat16 bf16;
using u32x4 = __attribute__((ext_vector_type(4))) unsigned;
using u32x2 = __attribute__((ext_vector_type(2))) unsigned;
using f32x4 = __attribute__((ext_vector_type(4))) float;
using f32x2 = __attribute__((ext_vector_type(2))) float;

constexpr int NB = 4, DM = 640, NH = 32, SK = 8192, DH = 192, HD = NH * DH, FF = 1536;
constexpr int NWG = 256, NSEG = 6, NWIN = NB * NH * NSEG / NWG;
constexpr float LN_EPS = 1e-5f;
constexpr float QSCALE = 0.07216878364870322f * 1.4426950408889634f;

constexpr int WS_HP = 0;
constexpr int REC = DH + 32;
constexpr int WS_PO = WS_HP + 2 * NWG;
constexpr int WS_Z0 = WS_PO + 2 * NWG * REC;
constexpr int K3_KS = 12, K3_ROWS = HD / K3_KS;
constexpr int WS_SLAB = WS_PO + NB * NH * NSEG * REC;
constexpr int SLS = NB * DM;
constexpr int WS_ACT = WS_SLAB + K3_KS * SLS;
constexpr int WS_BASE = WS_ACT + NB * FF * 3 / 2;
constexpr int ZPW = 416;
constexpr int WS_QI = WS_Z0 + NWG * ZPW;
constexpr int LSTR = 64;
constexpr int WS_REP = WS_QI + NB * NH * DH * 2;
constexpr int WS_HC = WS_REP + 256 * LSTR;
constexpr int WS_END = WS_HC + NWG * LSTR;
static_assert(WS_BASE + NB * DM * 2 <= WS_QI && WS_Z0 % 64 == 0 && WS_PO % 32 == 0 && ZPW % 32 == 0 && NWIN == 3, "layout");
__device__ __forceinline__ int ef_ofs(int i) { return (WS_PO + (2 * NWG + i) * REC + DH + 2) * 4; }
constexpr int OOB = 0x7ffffff0;

__device__ __forceinline__ float bf_lo(unsigned u) { return __uint_as_float(u << 16); }
__device__ __forceinline__ float bf_hi(unsigned u) { return __uint_as_float(u & 0xffff0000u); }
__device__ __forceinline__ f32x2 bf_pair(unsigned u) { return f32x2{bf_lo(u), bf_hi(u)}; }
__device__ __forceinline__ f32x2 fma2(float a, f32x2 b, f32x2 c) { return __builtin_elementwise_fma(f32x2{a, a}, b, c); }
__device__ __forceinline__ f32x2 fma2(f32x2 a, f32x2 b, f32x2 c) { return __builtin_elementwise_fma(a, b, c); }

template <int CTRL> __device__ __forceinline__ float dpp(float x) {
  return __builtin_bit_cast(float, __builtin_amdgcn_mov_dpp(__builtin_bit_cast(int, x), CTRL, 0xf, 0xf, true));
}
constexpr int DPP_XOR1 = 0xB1, DPP_XOR2 = 0x4E, DPP_HMIRROR = 0x141;

__device__ __forceinline__ float sum_x16_32(float x) {
  auto s = __builtin_amdgcn_permlane16_swap(__float_as_uint(x), __float_as_uint(x), false, false);
  x = __uint_as_float(s[0]) + __uint_as_float(s[1]);
  auto t = __builtin_amdgcn_permlane32_swap(__float_as_uint(x), __float_as_uint(x), false, false);
  return __uint_as_float(t[0]) + __uint_as_float(t[1]);
}
__device__ __forceinline__ float max_x16_32(float x) {
  auto s = __builtin_amdgcn_permlane16_swap(__float_as_uint(x), __float_as_uint(x), false, false);
  x = fmaxf(__uint_as_float(s[0]), __uint_as_float(s[1]));
  auto t = __builtin_amdgcn_permlane32_swap(__float_as_uint(x), __float_as_uint(x), false, false);
  return fmaxf(__uint_as_float(t[0]), __uint_as_float(t[1]));
}
__device__ __forceinline__ float tsum32(float a, float b) {
  auto t = __builtin_amdgcn_permlane32_swap(__float_as_uint(a), __float_as_uint(b), false, false);
  return __uint_as_float(t[0]) + __uint_as_float(t[1]);
}
__device__ __forceinline__ float tsum16(float a, float b) {
  auto t = __builtin_amdgcn_permlane16_swap(__float_as_uint(a), __float_as_uint(b), false, false);
  return __uint_as_float(t[0]) + __uint_as_float(t[1]);
}
constexpr int DPP_ROR8 = 0x128, DPP_BCAST = 0x150;
__device__ __forceinline__ float sum_r(float v) { v += dpp<DPP_ROR8>(v); return sum_x16_32(v); }
__device__ __forceinline__ float max_r(float v) { v = fmaxf(v, dpp<DPP_ROR8>(v)); return max_x16_32(v); }
__device__ __forceinline__ float wave_sum(float v) {
  v += dpp<DPP_XOR1>(v); v += dpp<DPP_XOR2>(v); v += dpp<DPP_HMIRROR>(v);
  return sum_r(v);
}

__device__ __forceinline__ float row16_sum(float v) {
  v += dpp<DPP_XOR1>(v); v += dpp<DPP_XOR2>(v); v += dpp<DPP_HMIRROR>(v);
  return v + dpp<DPP_ROR8>(v);
}

struct Slot { u32x4 a[3]; u32x4 b[3]; };

#define PIN8(a, o) asm volatile("" : "+v"((a)[(o) + 0]), "+v"((a)[(o) + 1]), "+v"((a)[(o) + 2]), "+v"((a)[(o) + 3]), \
                                       "+v"((a)[(o) + 4]), "+v"((a)[(o) + 5]), "+v"((a)[(o) + 6]), "+v"((a)[(o) + 7]))
#define PIN4(a, o) asm volatile("" : "+v"((a)[(o) + 0]), "+v"((a)[(o) + 1]), "+v"((a)[(o) + 2]), "+v"((a)[(o) + 3]))
#define MEMFENCE() do { asm volatile("" ::: "memory"); __builtin_amdgcn_sched_barrier(0); } while (0)
#define AGAIN() asm volatile("" ::: "memory")
#define DRAIN() asm volatile("s_waitcnt vmcnt(0)" ::: "memory")

__device__ __forceinline__ void k_tile(const u32x4 (&k)[3], const f32x2 (&q)[12], float& gmx, float* dst, int j) {
  f32x2 s0 = {0.f, 0.f}, s1 = s0, s2 = s0;
#pragma unroll
  for (int d = 0; d < 4; ++d) {
    s0 = fma2(bf_pair(k[0][d]), q[d], s0);
    s1 = fma2(bf_pair(k[1][d]), q[4 + d], s1);
    s2 = fma2(bf_pair(k[2][d]), q[8 + d], s2);
  }
  s0 = (s0 + s1) + s2;
  float s = s0[0] + s0[1];
  s += dpp<DPP_XOR1>(s);
  s += dpp<DPP_XOR2>(s);
  s += dpp<DPP_HMIRROR>(s);
  gmx = fmaxf(gmx, s);
  asm volatile("" : "+v"(gmx));
  if (j == 0) *dst = s;
}
__device__ __forceinline__ void v_tile(u32x4 (&v)[3], float p, f32x2 (&acc)[12]) {
#pragma unroll
  for (int i = 0; i < 3; ++i) {
    asm volatile("" : "+v"(v[i]), "+v"(p));
#pragma unroll
    for (int d = 0; d < 4; ++d) acc[i * 4 + d] = fma2(p, bf_pair(v[i][d]), acc[i * 4 + d]);
    PIN4(acc, i * 4);
  }
}

constexpr int PTAU = 660;
constexpr int POFF = 120, FFWD = 112;
constexpr int LTGT = 230, LCAP = 600;
constexpr int DOWN = 30, AFROM = 3, EFROM = 12, SLACK = 10;
constexpr int PROBE_MIN = 10;
constexpr int AUX_SC1 = 16, AUX_NT = 2;
constexpr unsigned SPIN_CAP = 1u << 17;
__device__ __forceinline__ unsigned nz_bits(float v) { const unsigned u = __float_as_uint(v); return u == 0u ? 0x80000000u : u; }
__device__ __forceinline__ void lds_put(int* p, int v) { __hip_atomic_store(p, v, __ATOMIC_RELAXED, __HIP_MEMORY_SCOPE_WORKGROUP); }
__device__ __forceinline__ int lds_get(int* p) { return __hip_atomic_load(p, __ATOMIC_RELAXED, __HIP_MEMORY_SCOPE_WORKGROUP); }
__device__ __forceinline__ void count_in(int* p, int lane) {
  if (lane == 0) asm volatile("ds_add_u32 %0, %1" :: "v"((unsigned)(size_t)(__attribute__((address_space(3))) int*)p), "v"(1) : "memory");
}
using bf16x8 = __attribute__((ext_vector_type(8))) short;
__device__ __forceinline__ unsigned pk_lo(unsigned a, unsigned b) { return __builtin_amdgcn_perm(b, a, 0x05040100u); }
__device__ __forceinline__ unsigned pk_hi(unsigned a, unsigned b) { return __builtin_amdgcn_perm(b, a, 0x07060302u); }
__device__ __forceinline__ void split3(float v, unsigned& t0, unsigned& t1, unsigned& t2) {
  t0 = __float_as_uint(v) & 0xffff0000u;
  const float r1 = v - __uint_as_float(t0);
  t1 = __float_as_uint(r1) & 0xffff0000u;
  t2 = __float_as_uint(r1 - __uint_as_float(t1));
}
__device__ __forceinline__ f32x4 mfma16(u32x4 a, u32x4 b, f32x4 c) {
  return __builtin_amdgcn_mfma_f32_16x16x32_bf16(__builtin_bit_cast(bf16x8, a), __builtin_bit_cast(bf16x8, b), c, 0, 0, 0);
}
__device__ __forceinline__ void put_terms4(char* img, int rowbytes, f32x4 v) {
  unsigned t[3][4];
#pragma unroll
  for (int e = 0; e < 4; ++e) split3(v[e], t[0][e], t[1][e], t[2][e]);
#pragma unroll
  for (int k = 0; k < 3; ++k) *reinterpret_cast<u32x2*>(img + k * rowbytes) = u32x2{pk_hi(t[k][0], t[k][1]), pk_hi(t[k][2], t[k][3])};
}
__device__ __forceinline__ unsigned min4(u32x4 v) { return min(min(v[0], v[1]), min(v[2], v[3])); }
__device__ __forceinline__ bool all_nz(u32x4 v) { return min4(v) != 0u; }

__device__ __forceinline__ float sum3(f32x4 v) { return (v[0] + v[1]) + v[2]; }

constexpr int NW = 8, NBUF = 4, NGRP = 64, NT = NGRP / NW;
constexpr int NQP = NH * DH / 64, NQW = 5, QW0 = 2, QST = DM / 64 / NQW;
constexpr int SVC = NW - 1;
constexpr int W_CT = DM / 64, G_TASKS = NB * (FF / 64), W_TASKS = W_CT * K3_KS, D_TASKS = 2 * (DM / 32);
static_assert(G_TASKS + W_TASKS + D_TASKS == NWG && NW == 8 && K3_ROWS == 4 * 128 && NBUF == 4 && SK / 64 == 128, "shape");
__device__ __forceinline__ unsigned fresh_tag(unsigned tags) {
  unsigned t = __builtin_amdgcn_readfirstlane(tags);
  do ++t; while (__any(tags == t));
  return t;
}

__global__ void __launch_bounds__(NW * 64, 2) fused_kernel(const bf16* __restrict__ x, const bf16* __restrict__ kc, const bf16* __restrict__ vc,
                                                        const bf16* __restrict__ wq, const bf16* __restrict__ wo, const bf16* __restrict__ wr,
                                                        const bf16* __restrict__ wg, const bf16* __restrict__ wu, const bf16* __restrict__ wd,
                                                        float* ws, bf16* __restrict__ out) {
  constexpr int PST = DH + 4;
  constexpr int WA = 128 * 2 + 16;
  __shared__ __attribute__((aligned(16))) char s_A[4 * 16 * WA];
  __shared__ __attribute__((aligned(16))) float s_red[6 * 256];
  __shared__ __attribute__((aligned(16))) float s_h[DM];
  __shared__ __attribute__((aligned(16))) float s_buf[NWIN * NW * PST];
  __shared__ __attribute__((aligned(16))) float s_sc[NGRP * 64];
  __shared__ __attribute__((aligned(16))) float s_q[NWIN * DH];
  __shared__ float s_m[NWIN * NW];
  __shared__ int s_mcnt[NWIN];
  __shared__ int s_qok, s_pcnt, s_ecnt, s_fcnt, s_icnt, s_org, s_hcnt, s_ccnt;
  __shared__ int s_prog[NW];

  const int tid = threadIdx.x;
  int lane = tid & 63, r = lane >> 3, j = lane & 7, mg = lane >> 4, mc = lane & 15;
  int wave = __builtin_amdgcn_readfirstlane(tid >> 6), bid = blockIdx.x;
  const unsigned t_start = (unsigned)__builtin_amdgcn_s_memrealtime();
  const int wid = (bid >> 5) * 32 + (bid & 7) * 4 + ((bid >> 3) & 3);
  const bool prod = bid < NQP;
  const __amdgpu_buffer_rsrc_t rs_ws = __builtin_amdgcn_make_buffer_rsrc(ws, 0, WS_END * 4, 0x00020000);
  const __amdgpu_buffer_rsrc_t rs_k = __builtin_amdgcn_make_buffer_rsrc(const_cast<bf16*>(kc), 0, NB * NH * SK * DH * 2, 0x00020000);
  const __amdgpu_buffer_rsrc_t rs_v = __builtin_amdgcn_make_buffer_rsrc(const_cast<bf16*>(vc), 0, NB * NH * SK * DH * 2, 0x00020000);
  const int voff = r * (DH * 2) + j * 16;
  const int c1 = wid % 3 == 0 ? 22 : 21, c2 = wid % 3 == 1 ? 42 : 43;
  auto seg_of = [&](int n) { return (n * 8 + wave >= c1) + (n * 8 + wave >= c2); };
  auto gload = [&](Slot (&sl)[NBUF], __amdgpu_buffer_rsrc_t rs, int n) {
    const int sn = seg_of(n), sg = wid + NWG * sn, row = sg / NSEG, k = sg - row * NSEG;
    const int off = (row * (SK / 64) + (k >> 1) * 43 + (n * 8 + wave - (sn == 0 ? 0 : (sn == 1 ? c1 : c2))) * 2 + ((k ^ (k >> 1)) & 1)) * (64 * DH * 2);
#pragma unroll
    for (int u = 0; u < NBUF; ++u) {
#pragma unroll
      for (int i = 0; i < 3; ++i) sl[u].a[i] = __builtin_amdgcn_raw_buffer_load_b128(rs, voff + i * 128, off + 2 * u * 3072, AUX_NT);
      MEMFENCE();
#pragma unroll
      for (int i = 0; i < 3; ++i) sl[u].b[i] = __builtin_amdgcn_raw_buffer_load_b128(rs, voff + i * 128, off + (2 * u + 1) * 3072, AUX_NT);
      if (u < NBUF - 1) __builtin_amdgcn_s_sleep(3);
      MEMFENCE();
    }
  };
  asm volatile("" :: "s"(ws), "s"(x), "s"(wq), "s"(kc));
  Slot b0[NBUF];
  const bool early = wave < QW0 && !prod;
  if (early) gload(b0, rs_k, 0);
  unsigned hs_nc = 0u, hs_ep = 0u;
  auto look_at_p = [&]() {
    AGAIN();
    if (bid == NWG - 1) {
      u32x4 hp[2], hc[2];
#pragma unroll
      for (int i = 0; i < 2; ++i) {
        hp[i] = __builtin_amdgcn_raw_buffer_load_b128(rs_ws, (WS_HP + lane * 8 + i * 4) * 4, 0, AUX_SC1);
        const u32x2 c0 = __builtin_amdgcn_raw_buffer_load_b64(rs_ws, (WS_HC + (lane * 4 + i * 2) * LSTR) * 4, 0, AUX_SC1);
        const u32x2 c1_ = __builtin_amdgcn_raw_buffer_load_b64(rs_ws, (WS_HC + (lane * 4 + i * 2 + 1) * LSTR) * 4, 0, AUX_SC1);
        hc[i] = u32x4{c0[0], c0[1], c1_[0], c1_[1]};
      }
      if (__all(hc[0][1] == hp[0][0] && hc[0][3] == hp[0][2] && hc[1][1] == hp[1][0] && hc[1][3] == hp[1][2]) &&
          __any(hc[0][0] != hp[0][1] || hc[0][2] != hp[0][3] || hc[1][0] != hp[1][1] || hc[1][2] != hp[1][3])) {
#pragma unroll
        for (int i = 0; i < 2; ++i)
          __builtin_amdgcn_raw_buffer_store_b128(u32x4{hp[i][0], hc[i][0], hp[i][2], hc[i][2]}, rs_ws, (WS_HP + lane * 8 + i * 4) * 4, 0, AUX_SC1);
      }
    }
    const u32x2 pw = __builtin_amdgcn_raw_buffer_load_b64(rs_ws, (WS_HP + bid * 2) * 4, 0, AUX_SC1);
    const unsigned np = __builtin_amdgcn_readfirstlane(pw[0]), ec = __builtin_amdgcn_readfirstlane(pw[1]);
    if (np != hs_ep) { hs_ep = np; __builtin_amdgcn_raw_buffer_store_b64(u32x2{hs_nc, hs_ep}, rs_ws, lane == 0 ? (WS_HC + bid * LSTR) * 4 : OOB, 0, AUX_SC1); }
    return ec == hs_nc;
  };
  auto answer = [&]() {
    const u32x4 z4 = {0u, 0u, 0u, 0u};
#pragma unroll
    for (int i = 0; i < (ZPW + 255) / 256; ++i)
      __builtin_amdgcn_raw_buffer_store_b128(z4, rs_ws, i * 256 + lane * 4 < ZPW ? (WS_Z0 + bid * ZPW + i * 256 + lane * 4) * 4 : OOB, 0, AUX_SC1);
    const u32x2 pw = __builtin_amdgcn_raw_buffer_load_b64(rs_ws, (WS_HP + bid * 2) * 4, 0, AUX_SC1);
    DRAIN();
    hs_ep = __builtin_amdgcn_readfirstlane(pw[0]);
    hs_nc = __builtin_amdgcn_readfirstlane(pw[1]) + 1u;
    __builtin_amdgcn_raw_buffer_store_b64(u32x2{hs_nc, hs_ep}, rs_ws, lane == 0 ? (WS_HC + bid * LSTR) * 4 : OOB, 0, AUX_SC1);
  };
  bool answered = false;
  if (tid < NW) s_prog[tid] = 1;
  if (tid < NWIN) s_mcnt[tid] = 0;
  if (tid == 0) { s_qok = 0; s_pcnt = 0; s_ecnt = 0; s_fcnt = 0; s_icnt = 0; s_hcnt = 0; s_ccnt = 0; s_org = (int)(t_start + POFF); }
  if (wave == SVC) {
    __builtin_amdgcn_s_setprio(3);
    int iofs[NWIN];
#pragma unroll
    for (int s = 0; s < NWIN; ++s) iofs[s] = (WS_QI + ((wid + NWG * s) / NSEG * DH + lane) * 2) * 4;
    const int oofs = prod ? (WS_QI + (bid * 64 + lane) * 2) * 4 : OOB;
    u32x2 in[NWIN * 3];
    unsigned ot[NB], ce[4];
#pragma unroll
    for (int e = 0; e < NWIN * 3; ++e) in[e] = __builtin_amdgcn_raw_buffer_load_b64(rs_ws, iofs[e / 3], (e % 3) * 512, AUX_SC1);
#pragma unroll
    for (int bb = 0; bb < NB; ++bb) ot[bb] = __builtin_amdgcn_raw_buffer_load_b64(rs_ws, oofs, bb * (HD * 8), AUX_SC1)[1];
    u32x4 xr[5], hp[2];
#pragma unroll
    for (int i = 0; i < 5; ++i) xr[i] = reinterpret_cast<const u32x4*>(x)[lane * 5 + i];
#pragma unroll
    for (int i = 0; i < 2; ++i) hp[i] = __builtin_amdgcn_raw_buffer_load_b128(rs_ws, bid == NWG - 1 ? (WS_HP + lane * 8 + i * 4) * 4 : OOB, 0, AUX_SC1);
#pragma unroll
    for (int i = 0; i < 4; ++i) ce[i] = __builtin_amdgcn_raw_buffer_load_b32(rs_ws, bid == NWG - 1 ? (WS_HC + (lane * 4 + i) * LSTR + 1) * 4 : OOB, 0, AUX_SC1);
    MEMFENCE();
    __syncthreads();
    MEMFENCE();
    if (bid == NWG - 1) {
#pragma unroll
      for (int i = 0; i < 2; ++i) {
#pragma unroll
        for (int e = 0; e < 2; ++e) { unsigned np = hp[i][2 * e] + 1u; if (np == ce[2 * i + e]) np += 1u; hp[i][2 * e] = np; }
        __builtin_amdgcn_raw_buffer_store_b128(hp[i], rs_ws, (WS_HP + lane * 8 + i * 4) * 4, 0, AUX_SC1);
      }
    }
    unsigned want[NWIN * 3];
#pragma unroll
    for (int e = 0; e < NWIN * 3; ++e) want[e] = fresh_tag(in[e][1]);
    if (prod) {
      float xs = 0.f;
#pragma unroll
      for (int i = 0; i < 20; ++i) xs += bf_lo(xr[i >> 2][i & 3]) + bf_hi(xr[i >> 2][i & 3]);
      const float mean = row16_sum(xs) * (1.0f / DM);
      float xv = 0.f;
#pragma unroll
      for (int i = 0; i < 20; ++i) { const float a = bf_lo(xr[i >> 2][i & 3]) - mean, c = bf_hi(xr[i >> 2][i & 3]) - mean; xv += a * a + c * c; }
      const float rstd = QSCALE / sqrtf(row16_sum(xv) * (1.0f / DM) + LN_EPS);
#pragma unroll
      for (int bb = 0; bb < NB; ++bb) ot[bb] = fresh_tag(ot[bb]);
      for (unsigned spin = 0; spin < (SPIN_CAP << 4) && lds_get(&s_pcnt) < NQW; ++spin) __builtin_amdgcn_s_sleep(1);
      __builtin_amdgcn_fence(__ATOMIC_ACQUIRE, "workgroup");
      float pr[5];
#pragma unroll
      for (int i = 0; i < 5; ++i) {
        pr[i] = 0.f;
#pragma unroll
        for (int k = 0; k < NQW; ++k) pr[i] += s_buf[2 * k * 320 + i * 64 + lane] + s_buf[(2 * k + 1) * 320 + i * 64 + lane];
      }
#pragma unroll
      for (int bb = 0; bb < NB; ++bb) {
        const float qv = (pr[bb] - __int_as_float(__builtin_amdgcn_readlane(__float_as_int(mean), bb * 16)) * pr[4]) *
                         __int_as_float(__builtin_amdgcn_readlane(__float_as_int(rstd), bb * 16));
        __builtin_amdgcn_raw_buffer_store_b64(u32x2{__float_as_uint(qv), ot[bb]}, rs_ws, oofs, bb * (HD * 8), AUX_SC1);
      }
    } else {
      __builtin_amdgcn_s_sleep(32);
    }
    bool all_out = false;
#pragma unroll 1
    for (unsigned spin = 0; spin < SPIN_CAP; ++spin) {
      AGAIN();
#pragma unroll
      for (int e = 0; e < NWIN * 3; ++e) in[e] = __builtin_amdgcn_raw_buffer_load_b64(rs_ws, iofs[e / 3], (e % 3) * 512, AUX_SC1);
      bool ok = true;
#pragma unroll
      for (int e = 0; e < NWIN * 3; ++e) ok = ok && in[e][1] == want[e];
      if (all_out || __all(ok)) break;
      if (spin == 15) { answer(); answered = true; }
      if (spin > 15 && (spin & 3) == 3) all_out = look_at_p();
      __builtin_amdgcn_s_sleep(4);
    }
#pragma unroll
    for (int e = 0; e < NWIN * 3; ++e) s_q[e * 64 + lane] = __uint_as_float(in[e][0]);
    __builtin_amdgcn_fence(__ATOMIC_RELEASE, "workgroup");
    lds_put(&s_qok, 1);
    __builtin_amdgcn_s_setprio(0);
  } else if (prod && wave >= QW0 && wave < QW0 + NQW) {
    const int k0 = (wave - QW0) * (QST * 64) + (mc >> 3) * 32 + mg * 8;
    u32x4 raw[QST * 8], xa[QST];
    const bf16* wb = wq + (size_t)k0 * HD + bid * 64 + (mc & 7) * 8;
#pragma unroll
    for (int st = 0; st < QST; ++st) xa[st] = *reinterpret_cast<const u32x4*>(x + (mc & 3) * DM + k0 + st * 64);
#pragma unroll
    for (int e = 0; e < QST * 8; ++e) raw[e] = __builtin_nontemporal_load(reinterpret_cast<const u32x4*>(wb + (size_t)((e >> 3) * 64 + (e & 7)) * HD));
    MEMFENCE();
    __syncthreads();
    MEMFENCE();
    f32x4 ac[8];
#pragma unroll
    for (int t = 0; t < 8; ++t) ac[t] = f32x4{0.f, 0.f, 0.f, 0.f};
#pragma unroll
    for (int st = 0; st < QST; ++st) {
      const u32x4 a = (mc & 4) == 0 ? xa[st] : u32x4{0x3f803f80u, 0x3f803f80u, 0x3f803f80u, 0x3f803f80u};
#pragma unroll
      for (int c = 0; c < 4; ++c) {
        u32x4 f0, f1;
#pragma unroll
        for (int i = 0; i < 4; ++i) { f0[i] = pk_lo(raw[st * 8 + 2 * i][c], raw[st * 8 + 2 * i + 1][c]); f1[i] = pk_hi(raw[st * 8 + 2 * i][c], raw[st * 8 + 2 * i + 1][c]); }
        ac[2 * c] = mfma16(a, f0, ac[2 * c]);
        ac[2 * c + 1] = mfma16(a, f1, ac[2 * c + 1]);
      }
    }
    if ((mg >> 1) == (mc >> 3)) {
      float* const dst = &s_buf[((wave - QW0) * 2 + (mg >> 1)) * 320 + (mc & 7) * 8];
#pragma unroll
      for (int bb = 0; bb < NB; ++bb)
        if (bb == 0 || (mg & 1) == 0) {
          *reinterpret_cast<f32x4*>(dst + ((mg & 1) * 4 + bb) * 64) = f32x4{ac[0][bb], ac[1][bb], ac[2][bb], ac[3][bb]};
          *reinterpret_cast<f32x4*>(dst + ((mg & 1) * 4 + bb) * 64 + 4) = f32x4{ac[4][bb], ac[5][bb], ac[6][bb], ac[7][bb]};
        }
    }
    __builtin_amdgcn_fence(__ATOMIC_RELEASE, "workgroup");
    if (lane == 0) __hip_atomic_fetch_add(&s_pcnt, 1, __ATOMIC_RELAXED, __HIP_MEMORY_SCOPE_WORKGROUP);
  } else {
    __syncthreads();
  }
  MEMFENCE();
  const int gtile = ((bid & 7) * 12 + (bid >> 3) - 15) >> 2;
  const int tau = PTAU - (((bid >> 3) >= 15 && (bid >> 3) < 27 && (((((unsigned)(size_t)wg >> 7) + gtile) & 7) == 3 || ((((unsigned)(size_t)wu >> 7) + gtile) & 7) == 3)) ? 10 : 0);
  u32x2 rep = {0u, 0u};
  unsigned t_thru = 0u;
  auto pace = [&](int g) {
    if (wave == 0 && g > AFROM) {
      const int org = lds_get(&s_org);
      const bool ok = rep[0] - t_start + 1000u < 6000u && rep[1] - t_start + 1000u < 6000u;
      const float dorg = row16_sum(ok ? (float)(int)(rep[1] - (unsigned)org) : 0.f);
      const float lt = ok ? (float)min((int)(rep[0] - rep[1]), LCAP) : (float)LCAP;
      const float lx = fminf(lt, dpp<DPP_ROR8>(lt));
      float hi = fmaxf(lx, dpp<DPP_XOR1>(lx)); hi = fmaxf(hi, dpp<DPP_XOR2>(hi)); hi = fmaxf(hi, dpp<DPP_HMIRROR>(hi));
      const float lat = row16_sum(lt) * (0.8f / 16.f) + hi * 0.2f;
      if (lane == 0) lds_put(&s_org, org + (int)(dorg * (1.f / 16.f) + fmaxf((lat - LTGT) * 0.6f, g > EFROM ? (float)SLACK : -(float)DOWN)));
    }
    const unsigned rel = (unsigned)(g * tau + wave * (PTAU / 8) + min(g * 8 + wave, 16) * (FFWD / 16) - FFWD + ((wid * 65) & 255) * (PTAU / 8) / 256);
    unsigned now = (unsigned)__builtin_amdgcn_s_memrealtime();
    if (wave == 1 && (bid >> 4) == 7 && g >= 2)
      __builtin_amdgcn_raw_buffer_store_b64(u32x2{t_thru - rel + tau, (unsigned)lds_get(&s_org)}, rs_ws, lane == 0 ? (WS_REP + ((g - 2) * 16 + (bid & 15)) * LSTR) * 4 : OOB, 0, AUX_SC1);
    for (unsigned spin = 0; spin < 4096 && (int)(now - rel - (unsigned)__builtin_amdgcn_readfirstlane(lds_get(&s_org))) < 0; ++spin) {
      __builtin_amdgcn_s_sleep(2);
      now = (unsigned)__builtin_amdgcn_s_memrealtime();
    }
    if (g >= 2) rep = __builtin_amdgcn_raw_buffer_load_b64(rs_ws, (wave == 0 && lane < 16 && g >= AFROM) ? (WS_REP + ((g - 2) * 16 + lane) * LSTR) * 4 : OOB, 0, 0);
    if (lane == 0) lds_put(&s_prog[wave], g);
    int pp = __builtin_amdgcn_readfirstlane(lds_get(&s_prog[wave ^ (NW / 2)]));
    for (unsigned spin = 0; spin < 4096 && g > pp + 1; ++spin) { __builtin_amdgcn_s_sleep(4); pp = __builtin_amdgcn_readfirstlane(lds_get(&s_prog[wave ^ (NW / 2)])); }
    if (g > pp) __builtin_amdgcn_s_setprio(0); else __builtin_amdgcn_s_setprio(1);
  };
  float gmx = -INFINITY;
  auto k_group = [&](const Slot (&sl)[NBUF], int n, int s) {
    f32x2 q[12];
#pragma unroll
    for (int i = 0; i < 6; ++i) {
      const f32x4 a = *reinterpret_cast<const f32x4*>(&s_q[s * DH + (i >> 1) * 64 + j * 8 + (i & 1) * 4]);
      q[i * 2] = f32x2{a[0], a[1]}; q[i * 2 + 1] = f32x2{a[2], a[3]};
    }
    float* const sc = &s_sc[(wave * NT + n) * 64 + r * 8];
    MEMFENCE();
#pragma unroll
    for (int u = 0; u < NBUF; ++u) {
      k_tile(sl[u].a, q, gmx, sc + 2 * u, j);
      k_tile(sl[u].b, q, gmx, sc + 2 * u + 1, j);
      MEMFENCE();
    }
    t_thru = (unsigned)__builtin_amdgcn_s_memrealtime();
  };
  {
    Slot b1[NBUF];
    if (!early) {
      pace(0);
      gload(b0, rs_k, 0);
    }
    pace(1);
    gload(b1, rs_k, 1);
    for (unsigned spin = 0; spin < (SPIN_CAP << 4) && lds_get(&s_qok) == 0; ++spin) __builtin_amdgcn_s_sleep(2);
    __builtin_amdgcn_fence(__ATOMIC_ACQUIRE, "workgroup");
    k_group(b0, 0, 0);
    k_group(b1, 1, 0);
    if (wave == SVC && !answered) answer();
  }
  auto k_done = [&](int s) {
    const float gm = max_r(gmx);
    if (lane == 0) s_m[s * NW + wave] = gm;
    __builtin_amdgcn_fence(__ATOMIC_RELEASE, "workgroup");
    if (lane == 0) __hip_atomic_fetch_add(&s_mcnt[s], 1, __ATOMIC_RELAXED, __HIP_MEMORY_SCOPE_WORKGROUP);
    gmx = -INFINITY;
  };
  Slot buf[NBUF];
  int seg = 0;
  bool through = false;
  for (int n = 2; n < NT; ++n) {
    pace(n);
    gload(buf, rs_k, n);
    if (seg_of(n) != seg) {
      k_done(seg);
      ++seg;
    }
    k_group(buf, n, seg);
    if (wave == SVC && bid == NWG - 1 && n >= 3 && !through) through = look_at_p();
  }
  k_done(seg);
  const int d4 = lane < DH / 4 ? ((lane >> 1) & 7) * 6 + (lane >> 4) * 2 + (lane & 1) : DH / 4;
  const int rofs = lane < REC / 4 ? (WS_PO + wid * REC + lane * 4) * 4 : OOB;
  float l = 0.f;
  f32x2 acc[12];
#pragma unroll
  for (int i = 0; i < 12; ++i) acc[i] = f32x2{0.f, 0.f};
  __builtin_amdgcn_fence(__ATOMIC_RELEASE, "wavefront"); __builtin_amdgcn_wave_barrier(); __builtin_amdgcn_fence(__ATOMIC_ACQUIRE, "wavefront");
  for (int n = 0; n < NT; ++n) {
    pace(NT + n);
    gload(buf, rs_v, n);
    if (n == NT - 1) {
      if (lane == 0) __hip_atomic_fetch_add(&s_icnt, 1, __ATOMIC_RELAXED, __HIP_MEMORY_SCOPE_WORKGROUP);
      __builtin_amdgcn_s_setprio(3);
    }
    seg = seg_of(n);
    for (unsigned spin = 0; spin < (SPIN_CAP << 4) && lds_get(&s_mcnt[seg]) < NW; ++spin) __builtin_amdgcn_s_sleep(2);
    __builtin_amdgcn_fence(__ATOMIC_ACQUIRE, "workgroup");
    float m = s_m[seg * NW + j], lw;
    m = fmaxf(m, dpp<DPP_XOR1>(m)); m = fmaxf(m, dpp<DPP_XOR2>(m)); m = fmaxf(m, dpp<DPP_HMIRROR>(m));
    {
      float p[8];
      const float* const sc = &s_sc[(wave * NT + n) * 64 + r * 8];
      const f32x4 sa = *reinterpret_cast<const f32x4*>(sc), sb = *reinterpret_cast<const f32x4*>(sc + 4);
#pragma unroll
      for (int t = 0; t < 4; ++t) { p[t] = __builtin_amdgcn_exp2f(sa[t] - m); p[4 + t] = __builtin_amdgcn_exp2f(sb[t] - m); }
      l += ((p[0] + p[1]) + (p[2] + p[3])) + ((p[4] + p[5]) + (p[6] + p[7]));
      lw = sum_r(l);
      asm volatile("" : "+v"(lw));
      MEMFENCE();
#pragma unroll
      for (int u = 0; u < NBUF; ++u) {
        v_tile(buf[u].a, p[2 * u], acc);
        v_tile(buf[u].b, p[2 * u + 1], acc);
        MEMFENCE();
      }
    }
    t_thru = (unsigned)__builtin_amdgcn_s_memrealtime();
    if (wave == SVC && n == 1)
      for (unsigned spin = 0; spin < SPIN_CAP && !look_at_p(); ++spin) __builtin_amdgcn_s_sleep(4);
    if (n == NT - 1 || seg_of(n + 1) != seg) {
      float t12[12], t6[6];
#pragma unroll
      for (int e = 0; e < 12; ++e) t12[e] = tsum32(acc[e >> 1][e & 1], acc[6 + (e >> 1)][e & 1]);
#pragma unroll
      for (int e = 0; e < 6; ++e) { t6[e] = tsum16(t12[e], t12[e + 6]); t6[e] += dpp<DPP_ROR8>(t6[e]); }
      if ((lane & 8) == 0) {
#pragma unroll
        for (int e = 0; e < 3; ++e) *reinterpret_cast<f32x2*>(&s_buf[(seg * NW + wave) * PST + j * 24 + (lane >> 4) * 6 + e * 2]) = f32x2{t6[2 * e], t6[2 * e + 1]};
      }
      if (lane == 0) *reinterpret_cast<f32x2*>(&s_buf[(seg * NW + wave) * PST + DH]) = f32x2{m, lw};
      l = 0.f;
#pragma unroll
      for (int i = 0; i < 12; ++i) acc[i] = f32x2{0.f, 0.f};
      if (seg != 0) {
        int c = 0;
        if (lane == 0) asm volatile("ds_add_rtn_u32 %0, %1, %2" : "=v"(c) : "v"((unsigned)(size_t)(__attribute__((address_space(3))) int*)(seg == 1 ? &s_ecnt : &s_fcnt)), "v"(1) : "memory");
        MEMFENCE();
        auto merged = [&](int s) {
          f32x4 pw[NW];
#pragma unroll
          for (int w = 0; w < NW; ++w) pw[w] = reinterpret_cast<const f32x4*>(s_buf)[(s * NW + w) * (PST / 4) + d4];
          f32x4 o = ((pw[0] + pw[1]) + (pw[2] + pw[3])) + ((pw[4] + pw[5]) + (pw[6] + pw[7]));
          if (lane >= DH / 4) o = f32x4{pw[0][0], o[1], 1.f, 1.f};
          return u32x4{nz_bits(o[0]), nz_bits(o[1]), nz_bits(o[2]), nz_bits(o[3])};
        };
        u32x4 rec = merged(seg);
        asm volatile("s_waitcnt lgkmcnt(0)" : "+v"(rec), "+v"(c) :: "memory");
        if (__builtin_amdgcn_readfirstlane(c) == NW - 1) {
          __builtin_amdgcn_raw_buffer_store_b128(rec, rs_ws, rofs, seg * (NWG * REC * 4), AUX_SC1);
          if (seg == 1) __builtin_amdgcn_raw_buffer_store_b128(merged(0), rs_ws, rofs, 0, AUX_SC1);
          if (seg == 2) { MEMFENCE(); lds_put(&s_fcnt, NW + 1); }
          if (seg == 1) {
            DRAIN();
            __builtin_amdgcn_raw_buffer_store_b32(1u, rs_ws, lane == 0 ? ef_ofs(wid) : OOB, 0, AUX_SC1);
          }
        }
      }
      MEMFENCE();
    }
  }
  __builtin_amdgcn_s_setprio(0);

  {
    asm volatile("" : "+s"(wave), "+s"(bid), "+v"(lane));
    r = lane >> 3; j = lane & 7; mg = lane >> 4; mc = lane & 15;
    const int w = wave;
    const int xcd = bid & 7, ri = bid >> 3;
    const bool isW = ri < 15, isG = !isW && ri < 27;
    const int tt = isW ? xcd * 15 + ri : (isG ? xcd * 12 + ri - 15 : xcd * 5 + ri - 27);
    const int ks1 = tt / W_CT, ct1 = tt - ks1 * W_CT;
    typedef const __attribute__((address_space(1))) char* gptr;
    auto behind_stream = [&](int nsl, int cnt) {
      for (unsigned spin = 0; spin < (SPIN_CAP << 4) && lds_get(&s_icnt) < cnt; ++spin) __builtin_amdgcn_s_sleep(1);
      for (int i = 0; i < nsl && lds_get(&s_fcnt) <= NW; ++i) __builtin_amdgcn_s_sleep(3);
    };
    u32x4 fr[16];
    u32x2 raw[32];
    auto weights = [&](const bf16* src, int skip) {
      gptr wb = (gptr)src;
#pragma unroll
      for (int e = 0; e < 32; ++e) {
        raw[e] = __builtin_nontemporal_load((const __attribute__((address_space(1))) u32x2*)wb);
        wb += ((e & 7) == 7 ? skip : 1) * (DM * 2);
        asm volatile("" : "+v"(wb));
      }
      MEMFENCE();
    };
    auto frags = [&]() {
#pragma unroll
      for (int st = 0; st < 4; ++st) {
        PIN8(raw, st * 8);
#pragma unroll
        for (int i = 0; i < 4; ++i) {
          const u32x2 r0 = raw[st * 8 + 2 * i], r1 = raw[st * 8 + 2 * i + 1];
          fr[st * 4 + 0][i] = pk_lo(r0[0], r1[0]); fr[st * 4 + 1][i] = pk_hi(r0[0], r1[0]);
          fr[st * 4 + 2][i] = pk_lo(r0[1], r1[1]); fr[st * 4 + 3][i] = pk_hi(r0[1], r1[1]);
        }
        PIN4(fr, st * 4);
        MEMFENCE();
      }
    };
    if (isW) {
      if (w >= 4) return;
      int a_put = w * (16 * WA) + mg * 4 * WA + mc * 8, a_get = w * (16 * WA / 16) + mc * (WA / 16) + mg, a_red = w * 64 + lane;
      int o_res = (WS_SLAB + ks1 * SLS + mg * DM + ct1 * 64 + mc * 4) * 4;
      {
        int offp[2], sg0[2];
        bool er[2];
#pragma unroll
        for (int hf = 0; hf < 2; ++hf) {
          const int row = ks1 * K3_ROWS + w * 128 + hf * 64, hh = row / DH;
          sg0[hf] = (mg * NH + hh) * NSEG;
          offp[hf] = (WS_PO + sg0[hf] * REC + row - hh * DH + mc * 4) * 4;
          er[hf] = sg0[hf] + NSEG <= 2 * NWG;
        }
        const int sgm = (mc < 8 ? sg0[0] : sg0[1]) + j, offm = j < NSEG ? (WS_PO + sgm * REC + DH) * 4 : OOB;
        for (unsigned spin = 0; spin < (SPIN_CAP << 4) && lds_get(&s_ecnt) < NW; ++spin) __builtin_amdgcn_s_sleep(4);
        for (unsigned spin = 0; spin < SPIN_CAP; ++spin) {
          AGAIN();
          const bool fl = j < NSEG && sgm < 2 * NWG;
          if (__all(!fl || __builtin_amdgcn_raw_buffer_load_b32(rs_ws, fl ? ef_ofs(sgm & (NWG - 1)) : OOB, 0, AUX_SC1) != 0u)) break;
          __builtin_amdgcn_s_sleep(8);
        }
        auto look = [&](u32x4 (&d)[2 * NSEG], bool h0, bool h1) {
          const int o0_ = h0 ? offp[0] : OOB, o1_ = h1 ? offp[1] : OOB;
#pragma unroll
          for (int k = 0; k < 2 * NSEG; ++k) d[k] = __builtin_amdgcn_raw_buffer_load_b128(rs_ws, k < NSEG ? o0_ : o1_, (k % NSEG) * REC * 4, AUX_SC1);
          MEMFENCE();
        };
        auto wsum = [&](float wt, const u32x4 (&d)[2 * NSEG], f32x4& a0, f32x4& a1) {
          a0 += dpp<DPP_BCAST + 0>(wt) * __builtin_bit_cast(f32x4, d[0]); a1 += dpp<DPP_BCAST + 8>(wt) * __builtin_bit_cast(f32x4, d[6]);
          a0 += dpp<DPP_BCAST + 1>(wt) * __builtin_bit_cast(f32x4, d[1]); a1 += dpp<DPP_BCAST + 9>(wt) * __builtin_bit_cast(f32x4, d[7]);
          a0 += dpp<DPP_BCAST + 2>(wt) * __builtin_bit_cast(f32x4, d[2]); a1 += dpp<DPP_BCAST + 10>(wt) * __builtin_bit_cast(f32x4, d[8]);
          a0 += dpp<DPP_BCAST + 3>(wt) * __builtin_bit_cast(f32x4, d[3]); a1 += dpp<DPP_BCAST + 11>(wt) * __builtin_bit_cast(f32x4, d[9]);
          a0 += dpp<DPP_BCAST + 4>(wt) * __builtin_bit_cast(f32x4, d[4]); a1 += dpp<DPP_BCAST + 12>(wt) * __builtin_bit_cast(f32x4, d[10]);
          a0 += dpp<DPP_BCAST + 5>(wt) * __builtin_bit_cast(f32x4, d[5]); a1 += dpp<DPP_BCAST + 13>(wt) * __builtin_bit_cast(f32x4, d[11]);
        };
        u32x4 pe[2 * NSEG], pl[2 * NSEG];
        behind_stream(6, NW - 1);
        weights(wo + (size_t)(ks1 * K3_ROWS + w * 128 + mg * 8) * DM + ct1 * 64 + mc * 4, 25);
        look(pe, er[0], er[1]);
        for (unsigned sp2 = 0; sp2 < (SPIN_CAP << 4) && lds_get(&s_fcnt) < NW; ++sp2) __builtin_amdgcn_s_sleep(1);
        f32x4 o0, o1;
        asm volatile("" : "+v"(a_put), "+v"(a_get), "+v"(a_red), "+v"(o_res));
        u32x2 ml = __builtin_amdgcn_raw_buffer_load_b64(rs_ws, offm, 0, AUX_SC1);
        look(pl, !er[0], !er[1]);
        frags();
        MEMFENCE();
#pragma unroll 1
        for (unsigned spin = 0; spin < SPIN_CAP; ++spin) {
          AGAIN();
          const float mm = j < NSEG ? __uint_as_float(ml[0]) : -INFINITY;
          float M = fmaxf(mm, dpp<DPP_XOR1>(mm)); M = fmaxf(M, dpp<DPP_XOR2>(M)); M = fmaxf(M, dpp<DPP_HMIRROR>(M));
          const float e = __builtin_amdgcn_exp2f(mm - M);
          float L = e * __uint_as_float(ml[1]);
          L += dpp<DPP_XOR1>(L); L += dpp<DPP_XOR2>(L); L += dpp<DPP_HMIRROR>(L);
          const float wt = e * __builtin_amdgcn_rcpf(L);
          o0 = f32x4{0.f, 0.f, 0.f, 0.f}; o1 = o0;
          wsum(wt, pe, o0, o1);
          unsigned m0 = er[0], m1 = er[1], a0 = ~0u, a1 = ~0u;
          const bool okm = j >= NSEG || min(ml[0], ml[1]) != 0u;
          asm volatile("" : "+v"(o0), "+v"(o1));
          MEMFENCE();
          wsum(wt, pl, o0, o1);
          asm volatile("" : "+v"(o0), "+v"(o1));
#pragma unroll
          for (int k = 0; k < NSEG; ++k) { a0 = min(a0, min4(pl[k])); a1 = min(a1, min4(pl[NSEG + k])); }
          if (__all(okm && (a0 | m0) != 0u && (a1 | m1) != 0u)) break;
          if (spin >= 8) __builtin_amdgcn_s_sleep(4);
          ml = __builtin_amdgcn_raw_buffer_load_b64(rs_ws, offm, 0, AUX_SC1);
          look(pl, !er[0], !er[1]);
        }
        put_terms4(s_A + a_put, WA, o0);
        put_terms4(s_A + a_put + 128, WA, o1);
      }
      __builtin_amdgcn_fence(__ATOMIC_RELEASE, "wavefront"); __builtin_amdgcn_wave_barrier(); __builtin_amdgcn_fence(__ATOMIC_ACQUIRE, "wavefront");
      f32x4 ac[4];
#pragma unroll
      for (int t = 0; t < 4; ++t) ac[t] = f32x4{0.f, 0.f, 0.f, 0.f};
      u32x4 av[4];
#pragma unroll
      for (int st = 0; st < 4; ++st) av[st] = reinterpret_cast<const u32x4*>(s_A)[a_get + st * 4];
      MEMFENCE();
#pragma unroll
      for (int st = 0; st < 4; ++st)
#pragma unroll
        for (int t = 0; t < 4; ++t) ac[t] = mfma16(av[st], fr[st * 4 + t], ac[t]);
      reinterpret_cast<f32x4*>(s_red)[a_red] = f32x4{sum3(ac[0]), sum3(ac[1]), sum3(ac[2]), sum3(ac[3])};
      __syncthreads();
      if (w == 0) {
        const f32x4* const red = reinterpret_cast<const f32x4*>(s_red) + a_red;
        const f32x4 s0 = (red[0] + red[64]) + (red[128] + red[192]);
        __builtin_amdgcn_raw_buffer_store_b128(u32x4{nz_bits(s0[0]), nz_bits(s0[1]), nz_bits(s0[2]), nz_bits(s0[3])}, rs_ws, o_res, 0, AUX_SC1);
      }
    } else if (isG) {
      const int gb = tt & 3, gt = tt >> 2;
      int o_res = (WS_ACT + (gb * (FF / 64) + gt) * 96 + (lane & 1) * 32 + (lane >> 1)) * 4;
      asm volatile("" : "+v"(o_res));
      if (w < 5) {
        const int hcol = w * 128 + (lane & 31) * 4;
        uint2 xx = {0u, 0u};
        u32x4 fg[32];
        {
          u32x4 rg[32];
          if (lane < 32) xx = *reinterpret_cast<const uint2*>(x + gb * DM + hcol);
          behind_stream(5, NW);
          gptr wb = (gptr)((mc < 8 ? wg : wu) + (size_t)(w * 128 + mg * 8) * FF + gt * 64 + (mc & 7) * 8);
#pragma unroll
          for (int e = 0; e < 32; ++e) {
            rg[e] = __builtin_nontemporal_load((const __attribute__((address_space(1))) u32x4*)wb);
            wb += ((e & 7) == 7 ? 25 : 1) * (FF * 2);
            asm volatile("" : "+v"(wb));
          }
          MEMFENCE();
#pragma unroll
          for (int st = 0; st < 4; ++st) {
            PIN8(rg, st * 8);
#pragma unroll
            for (int c = 0; c < 4; ++c)
#pragma unroll
              for (int i = 0; i < 4; ++i) {
                fg[st * 8 + 2 * c][i] = pk_lo(rg[st * 8 + 2 * i][c], rg[st * 8 + 2 * i + 1][c]);
                fg[st * 8 + 2 * c + 1][i] = pk_hi(rg[st * 8 + 2 * i][c], rg[st * 8 + 2 * i + 1][c]);
              }
            PIN8(fg, st * 8);
            MEMFENCE();
          }
        }
        {
          f32x4 cs[8];
#pragma unroll
          for (int t = 0; t < 8; ++t) cs[t] = f32x4{0.f, 0.f, 0.f, 0.f};
#pragma unroll
          for (int e = 0; e < 32; ++e) cs[e & 7] = mfma16(u32x4{0x3f803f80u, 0x3f803f80u, 0x3f803f80u, 0x3f803f80u}, fg[e], cs[e & 7]);
          if (lane < 16) {
            *reinterpret_cast<f32x4*>(&s_red[w * 256 + 128 + lane * 8]) = f32x4{cs[0][0], cs[1][0], cs[2][0], cs[3][0]};
            *reinterpret_cast<f32x4*>(&s_red[w * 256 + 128 + lane * 8 + 4]) = f32x4{cs[4][0], cs[5][0], cs[6][0], cs[7][0]};
          }
          count_in(&s_ccnt, lane);
        }
        f32x4 hv = f32x4{bf_lo(xx.x), bf_hi(xx.x), bf_lo(xx.y), bf_hi(xx.y)};
        int a_h = hcol + (lane >> 5) * 2, a_put = w * (4 * WA) + (lane & 31) * 8 + (lane >> 5) * 4, a_get = w * (4 * WA / 16) + min(mc, 2) * (WA / 16) + mg, a_red = w * 64 + mc * 2;
        asm volatile("" : "+v"(hv), "+v"(a_h), "+v"(a_put), "+v"(a_get), "+v"(a_red));
        const int off = (WS_SLAB + (lane >> 5) * (K3_KS / 2) * SLS + gb * DM + hcol) * 4;
        const int poff = (WS_SLAB + ((mg >> 1) * (K3_KS / 2) + mc) * SLS + gb * DM + w * 128 + (mg & 1) * 64) * 4;
        u32x4 sl[K3_KS / 2];
#pragma unroll
        for (int ks = 0; ks < K3_KS / 2; ++ks) sl[ks] = u32x4{0u, 0u, 0u, 0u};
        const unsigned pend = (1u << (K3_KS / 2)) - 1u;
        unsigned todo = pend, go = 0u;
        for (unsigned spin = 0; spin < SPIN_CAP; ++spin) {
          AGAIN();
#pragma unroll
          for (int ks = 0; ks < K3_KS / 2; ++ks)
            if (go & (1u << ks)) sl[ks] = __builtin_amdgcn_raw_buffer_load_b128(rs_ws, off, ks * (SLS * 4), AUX_SC1);
          todo &= ~go;
          if (todo != 0u) {
            const unsigned pr = __builtin_amdgcn_raw_buffer_load_b32(rs_ws, (mc < K3_KS / 2 && ((todo >> mc) & 1u)) ? poff : OOB, 0, AUX_SC1);
            const unsigned long long bl = __ballot(pr != 0u);
            go = (unsigned)(bl & bl >> 16 & bl >> 32 & bl >> 48) & todo;
            if (__builtin_popcount(todo) * 4 - __builtin_popcountll(bl) <= PROBE_MIN) go = todo;
          } else {
            unsigned mn[K3_KS / 2], all = 1u;
#pragma unroll
            for (int ks = 0; ks < K3_KS / 2; ++ks) { mn[ks] = min4(sl[ks]); all = min(all, mn[ks]); }
            if (__all(all != 0u)) break;
            go = 0u;
#pragma unroll
            for (int ks = K3_KS / 2 - 1; ks >= 0; --ks) go = go * 2u + (unsigned)__any(mn[ks] == 0u);
          }
        }
        hv = ((hv + __builtin_bit_cast(f32x4, sl[0])) + (__builtin_bit_cast(f32x4, sl[1]) + __builtin_bit_cast(f32x4, sl[2]))) +
             ((__builtin_bit_cast(f32x4, sl[3]) + __builtin_bit_cast(f32x4, sl[4])) + __builtin_bit_cast(f32x4, sl[5]));
        const float h0 = tsum32(hv[0], hv[2]), h1 = tsum32(hv[1], hv[3]);
        *reinterpret_cast<f32x2*>(&s_h[a_h]) = f32x2{h0, h1};
        count_in(&s_hcnt, lane);
        {
          unsigned t0[3], t1[3];
          split3(h0, t0[0], t0[1], t0[2]); split3(h1, t1[0], t1[1], t1[2]);
#pragma unroll
          for (int k = 0; k < 3; ++k) *reinterpret_cast<unsigned*>(s_A + a_put + k * WA) = pk_hi(t0[k], t1[k]);
        }
        __builtin_amdgcn_fence(__ATOMIC_RELEASE, "wavefront"); __builtin_amdgcn_wave_barrier(); __builtin_amdgcn_fence(__ATOMIC_ACQUIRE, "wavefront");
        f32x4 ac[8];
#pragma unroll
        for (int t = 0; t < 8; ++t) ac[t] = f32x4{0.f, 0.f, 0.f, 0.f};
        u32x4 av[4];
#pragma unroll
        for (int st = 0; st < 4; ++st) av[st] = reinterpret_cast<const u32x4*>(s_A)[a_get + st * 4];
        MEMFENCE();
#pragma unroll
        for (int st = 0; st < 4; ++st)
#pragma unroll
          for (int t = 0; t < 8; ++t) ac[t] = mfma16(av[st], fg[st * 8 + t], ac[t]);
        if (lane < 16) {
          reinterpret_cast<f32x4*>(s_red)[a_red] = f32x4{sum3(ac[0]), sum3(ac[1]), sum3(ac[2]), sum3(ac[3])};
          reinterpret_cast<f32x4*>(s_red)[a_red + 1] = f32x4{sum3(ac[4]), sum3(ac[5]), sum3(ac[6]), sum3(ac[7])};
        }
      } else if (w == NW - 1) {
        unsigned wr2[5];
#pragma unroll
        for (int i = 0; i < 5; ++i) wr2[i] = reinterpret_cast<const unsigned*>(wr)[lane + 64 * i];
        PIN4(wr2, 0); asm volatile("" : "+v"(wr2[4]));
        for (unsigned spin = 0; spin < (SPIN_CAP << 4) && lds_get(&s_ccnt) < 5; ++spin) __builtin_amdgcn_s_sleep(1);
        __builtin_amdgcn_fence(__ATOMIC_ACQUIRE, "workgroup");
        float cg = 0.f, cu = 0.f;
#pragma unroll
        for (int k = 0; k < 5; ++k) { cg += s_red[k * 256 + 128 + lane]; cu += s_red[k * 256 + 192 + lane]; }
        asm volatile("" : "+v"(cg), "+v"(cu));
        for (unsigned spin = 0; spin < (SPIN_CAP << 6) && lds_get(&s_hcnt) < 5; ++spin) {}
        __builtin_amdgcn_fence(__ATOMIC_ACQUIRE, "workgroup");
        const float piv = s_h[0];
        float hv[10], sm = 0.f, sq = 0.f;
#pragma unroll
        for (int i = 0; i < 5; ++i) {
          const float2 t2 = *reinterpret_cast<const float2*>(&s_h[2 * lane + 128 * i]);
          hv[2 * i] = t2.x; hv[2 * i + 1] = t2.y;
        }
        PIN8(hv, 0); asm volatile("" : "+v"(hv[8]), "+v"(hv[9]));
#pragma unroll
        for (int i = 0; i < 10; ++i) { hv[i] -= piv; sm += hv[i]; sq = fmaf(hv[i], hv[i], sq); }
        float t2 = row16_sum(tsum32(sm, sq));
        t2 = tsum16(t2, t2);
        const float dm = __int_as_float(__builtin_amdgcn_readlane(__float_as_int(t2), 0)) * (1.0f / DM);
        const float var = fmaxf(fmaf(-dm, dm, __int_as_float(__builtin_amdgcn_readlane(__float_as_int(t2), 32)) * (1.0f / DM)), 0.f);
        const float rstd = __builtin_amdgcn_rsqf(var + LN_EPS);
        s_red[5 * 256 + lane] = -(piv + dm) * cg; s_red[5 * 256 + 64 + lane] = -(piv + dm) * cu; s_red[5 * 256 + 128 + lane] = rstd;
        __syncthreads();
        if (gt < DM / 32) {
          float rr = 0.f;
#pragma unroll
          for (int i = 0; i < 5; ++i) { rr = fmaf((hv[2 * i] - dm) * rstd, bf_lo(wr2[i]), rr); rr = fmaf((hv[2 * i + 1] - dm) * rstd, bf_hi(wr2[i]), rr); }
          rr = wave_sum(rr);
          const float hh = s_h[gt * 32 + (lane & 31)];
          __builtin_amdgcn_raw_buffer_store_b32(nz_bits(lane >= 32 ? 1.0f / (1.0f + __expf(-rr)) : hh + (hh - (piv + dm)) * rstd), rs_ws,
                                                (WS_BASE + (gb * (DM / 32) + gt) * 64 + lane) * 4, 0, AUX_SC1);
        }
        return;
      } else {
        return;
      }
      __syncthreads();
      if (w == 0) {
        f32x2 pr[6];
#pragma unroll
        for (int k = 0; k < 6; ++k) pr[k] = f32x2{s_red[k * 256 + lane], s_red[k * 256 + 64 + lane]};
        float rstd = s_red[5 * 256 + 128 + lane];
        PIN4(pr, 0); asm volatile("" : "+v"(pr[4]), "+v"(pr[5]), "+v"(rstd));
        const f32x2 gu = (((pr[0] + pr[1]) + (pr[2] + pr[3])) + (pr[4] + pr[5])) * rstd;
        const float gg = gu[0], uu = gu[1];
        unsigned t[3], wd2[3];
        split3(gg * __builtin_amdgcn_rcpf(1.0f + __expf(-gg)) * uu, t[0], t[1], t[2]);
#pragma unroll
        for (int k = 0; k < 3; ++k) {
          const unsigned o = __float_as_uint(dpp<DPP_XOR1>(__uint_as_float(t[k])));
          wd2[k] = nz_bits(__uint_as_float((lane & 1) ? pk_hi(o, t[k]) : pk_hi(t[k], o)));
        }
        __builtin_amdgcn_raw_buffer_store_b32((lane & 1) ? wd2[1] : wd2[0], rs_ws, o_res, 0, AUX_SC1);
        __builtin_amdgcn_raw_buffer_store_b32(wd2[2], rs_ws, (lane & 1) ? OOB : o_res, 256, AUX_SC1);
      }
    } else {
      if (w == 7) return;
      const int bp = tt & 1, ct = tt >> 1;
      for (unsigned spin = 0; spin < (SPIN_CAP << 4) && lds_get(&s_fcnt) < NW; ++spin) __builtin_amdgcn_s_sleep(1);
      if (w == 6) {
        const int bb = lane >> 5, f = lane & 31, offb = (WS_BASE + ((bp * 2 + bb) * (DM / 32) + ct) * 64 + f) * 4;
        const __amdgpu_buffer_rsrc_t rs_out = __builtin_amdgcn_make_buffer_rsrc(out, 0, NB * DM * 2, 0x00020000);
        int o_res = ((bp * 2 + bb) * DM + ct * 32 + f) * 2;
        unsigned bs = 0u, sg = 0u;
        __builtin_amdgcn_s_sleep(100);
        for (unsigned spin = 0; spin < SPIN_CAP; ++spin) {
          AGAIN();
          bs = __builtin_amdgcn_raw_buffer_load_b32(rs_ws, offb, 0, AUX_SC1); sg = __builtin_amdgcn_raw_buffer_load_b32(rs_ws, offb, 128, AUX_SC1);
          if (__all(min(bs, sg) != 0u)) break;
          __builtin_amdgcn_s_sleep(2);
        }
        asm volatile("" : "+v"(o_res));
        __syncthreads();
        float pr[12];
#pragma unroll
        for (int k = 0; k < 12; ++k) pr[k] = s_red[(k >> 1) * 256 + (k & 1) * 64 + lane];
        PIN8(pr, 0); PIN4(pr, 8);
        const float y = (((pr[0] + pr[1]) + (pr[2] + pr[3])) + ((pr[4] + pr[5]) + (pr[6] + pr[7]))) + ((pr[8] + pr[9]) + (pr[10] + pr[11]));
        const bf16 ov = __float2bfloat16(fmaf(y, __uint_as_float(sg), __uint_as_float(bs)));
        __builtin_amdgcn_raw_buffer_store_b16(__builtin_bit_cast(unsigned short, ov), rs_out, o_res, 0, 0);
        return;
      }
      __builtin_amdgcn_s_sleep(8);
      weights(wd + (size_t)(w * 256 + (mc >> 3) * 32 + mg * 8) * DM + ct * 32 + (mc & 7) * 4, 57);
      frags();
      int a_red = w * 64 + mg * 8 + (mc & 7);
      asm volatile("" : "+v"(a_red));
      const int off = (WS_ACT + (((bp * 2 + ((mc >> 2) & 1)) * (FF / 64) + w * 4) * 3 + min(mc & 3, 2)) * 32 + (mc >> 3) * 16 + mg * 4) * 4;
      u32x4 av[4];
      for (unsigned spin = 0; spin < SPIN_CAP; ++spin) {
        AGAIN();
#pragma unroll
        for (int st = 0; st < 4; ++st) av[st] = __builtin_amdgcn_raw_buffer_load_b128(rs_ws, off, st * 384, AUX_SC1);
        if (__all(min(min(min4(av[0]), min4(av[1])), min(min4(av[2]), min4(av[3]))) != 0u)) break;
      }
      f32x4 ac[4];
#pragma unroll
      for (int t = 0; t < 4; ++t) ac[t] = f32x4{0.f, 0.f, 0.f, 0.f};
#pragma unroll
      for (int st = 0; st < 4; ++st)
#pragma unroll
        for (int t = 0; t < 4; ++t) ac[t] = mfma16(av[st], fr[st * 4 + t], ac[t]);
      if ((mc >> 3) == (mg >> 1)) reinterpret_cast<f32x4*>(s_red)[a_red] = f32x4{sum3(ac[0]), sum3(ac[1]), sum3(ac[2]), sum3(ac[3])};
      __syncthreads();
    }
  }
}

extern "C" void kernel_launch(void* const* d_in, const int* in_sizes, int n_in, void* d_out, int out_size, void* d_ws,
                              size_t ws_size, hipStream_t stream) {
  fused_kernel<<<NWG, NW * 64, 0, stream>>>((const bf16*)d_in[0], (const bf16*)d_in[1], (const bf16*)d_in[2], (const bf16*)d_in[3],
                                            (const bf16*)d_in[4], (const bf16*)d_in[6], (const bf16*)d_in[7], (const bf16*)d_in[8],
                                            (const bf16*)d_in[9], (float*)d_ws, (bf16*)d_out);
}
```

```cpp
#include <hip/hip_runtime.h>
#include <hip/hip_bf16.h>
#include <stdint.h>

typedef __hip_bfloat16 bf16;
using u32x4 = __attribute__((ext_vector_type(4))) unsigned;
using u32x2 = __attribute__((ext_vector_type(2))) unsigned;
using f32x4 = __attribute__((ext_vector_type(4))) float;
using f32x2 = __attribute__((ext_vector_type(2))) float;

constexpr int NB = 4, DM = 640, NH = 32, SK = 8192, DH = 192, HD = NH * DH, FF = 1536;
constexpr int NWG = 256, NSEG = 6, NWIN = NB * NH * NSEG / NWG;
constexpr float LN_EPS = 1e-5f;
constexpr float QSCALE = 0.07216878364870322f * 1.4426950408889634f;

constexpr int WS_HP = 0;
constexpr int REC = DH + 32;
constexpr int WS_PO = WS_HP + 2 * NWG;
constexpr int WS_Z0 = WS_PO + 2 * NWG * REC;
constexpr int K3_KS = 12, K3_ROWS = HD / K3_KS;
constexpr int WS_SLAB = WS_PO + NB * NH * NSEG * REC;
constexpr int SLS = NB * DM;
constexpr int WS_ACT = WS_SLAB + K3_KS * SLS;
constexpr int WS_BASE = WS_ACT + NB * FF * 3 / 2;
constexpr int ZPW = 416;
constexpr int WS_QI = WS_Z0 + NWG * ZPW;
constexpr int LSTR = 64;
constexpr int WS_REP = WS_QI + NB * NH * DH * 2;
constexpr int WS_HC = WS_REP + 256 * LSTR;
constexpr int WS_END = WS_HC + NWG * LSTR;
static_assert(WS_BASE + NB * DM * 2 <= WS_QI && WS_Z0 % 64 == 0 && WS_PO % 32 == 0 && ZPW % 32 == 0 && NWIN == 3, "layout");
__device__ __forceinline__ int ef_ofs(int i) { return (WS_PO + (2 * NWG + i) * REC + DH + 2) * 4; }
constexpr int OOB = 0x7ffffff0;

__device__ __forceinline__ float bf_lo(unsigned u) { return __uint_as_float(u << 16); }
__device__ __forceinline__ float bf_hi(unsigned u) { return __uint_as_float(u & 0xffff0000u); }
__device__ __forceinline__ f32x2 bf_pair(unsigned u) { return f32x2{bf_lo(u), bf_hi(u)}; }
__device__ __forceinline__ f32x2 fma2(float a, f32x2 b, f32x2 c) { return __builtin_elementwise_fma(f32x2{a, a}, b, c); }
__device__ __forceinline__ f32x2 fma2(f32x2 a, f32x2 b, f32x2 c) { return __builtin_elementwise_fma(a, b, c); }

template <int CTRL> __device__ __forceinline__ float dpp(float x) {
  return __builtin_bit_cast(float, __builtin_amdgcn_mov_dpp(__builtin_bit_cast(int, x), CTRL, 0xf, 0xf, true));
}
constexpr int DPP_XOR1 = 0xB1, DPP_XOR2 = 0x4E, DPP_HMIRROR = 0x141;

__device__ __forceinline__ float sum_x16_32(float x) {
  auto s = __builtin_amdgcn_permlane16_swap(__float_as_uint(x), __float_as_uint(x), false, false);
  x = __uint_as_float(s[0]) + __uint_as_float(s[1]);
  auto t = __builtin_amdgcn_permlane32_swap(__float_as_uint(x), __float_as_uint(x), false, false);
  return __uint_as_float(t[0]) + __uint_as_float(t[1]);
}
__device__ __forceinline__ float max_x16_32(float x) {
  auto s = __builtin_amdgcn_permlane16_swap(__float_as_uint(x), __float_as_uint(x), false, false);
  x = fmaxf(__uint_as_float(s[0]), __uint_as_float(s[1]));
  auto t = __builtin_amdgcn_permlane32_swap(__float_as_uint(x), __float_as_uint(x), false, false);
  return fmaxf(__uint_as_float(t[0]), __uint_as_float(t[1]));
}
__device__ __forceinline__ float tsum32(float a, float b) {
  auto t = __builtin_amdgcn_permlane32_swap(__float_as_uint(a), __float_as_uint(b), false, false);
  return __uint_as_float(t[0]) + __uint_as_float(t[1]);
}
__device__ __forceinline__ float tsum16(float a, float b) {
  auto t = __builtin_amdgcn_permlane16_swap(__float_as_uint(a), __float_as_uint(b), false, false);
  return __uint_as_float(t[0]) + __uint_as_float(t[1]);
}
constexpr int DPP_ROR8 = 0x128, DPP_BCAST = 0x150;
__device__ __forceinline__ float sum_r(float v) { v += dpp<DPP_ROR8>(v); return sum_x16_32(v); }
__device__ __forceinline__ float max_r(float v) { v = fmaxf(v, dpp<DPP_ROR8>(v)); return max_x16_32(v); }
__device__ __forceinline__ float wave_sum(float v) {
  v += dpp<DPP_XOR1>(v); v += dpp<DPP_XOR2>(v); v += dpp<DPP_HMIRROR>(v);
  return sum_r(v);
}

__device__ __forceinline__ float row16_sum(float v) {
  v += dpp<DPP_XOR1>(v); v += dpp<DPP_XOR2>(v); v += dpp<DPP_HMIRROR>(v);
  return v + dpp<DPP_ROR8>(v);
}

struct Slot { u32x4 a[3]; u32x4 b[3]; };

#define PIN8(a, o) asm volatile("" : "+v"((a)[(o) + 0]), "+v"((a)[(o) + 1]), "+v"((a)[(o) + 2]), "+v"((a)[(o) + 3]), \
                                       "+v"((a)[(o) + 4]), "+v"((a)[(o) + 5]), "+v"((a)[(o) + 6]), "+v"((a)[(o) + 7]))
#define PIN4(a, o) asm volatile("" : "+v"((a)[(o) + 0]), "+v"((a)[(o) + 1]), "+v"((a)[(o) + 2]), "+v"((a)[(o) + 3]))
#define MEMFENCE() do { asm volatile("" ::: "memory"); __builtin_amdgcn_sched_barrier(0); } while (0)
#define AGAIN() asm volatile("" ::: "memory")
#define DRAIN() asm volatile("s_waitcnt vmcnt(0)" ::: "memory")

__device__ __forceinline__ void k_tile(const u32x4 (&k)[3], const f32x2 (&q)[12], float& gmx, float* dst, int j) {
  f32x2 s0 = {0.f, 0.f}, s1 = s0, s2 = s0;
#pragma unroll
  for (int d = 0; d < 4; ++d) {
    s0 = fma2(bf_pair(k[0][d]), q[d], s0);
    s1 = fma2(bf_pair(k[1][d]), q[4 + d], s1);
    s2 = fma2(bf_pair(k[2][d]), q[8 + d], s2);
  }
  s0 = (s0 + s1) + s2;
  float s = s0[0] + s0[1];
  s += dpp<DPP_XOR1>(s);
  s += dpp<DPP_XOR2>(s);
  s += dpp<DPP_HMIRROR>(s);
  gmx = fmaxf(gmx, s);
  asm volatile("" : "+v"(gmx));
  if (j == 0) *dst = s;
}
__device__ __forceinline__ void v_tile(u32x4 (&v)[3], float p, f32x2 (&acc)[12]) {
#pragma unroll
  for (int i = 0; i < 3; ++i) {
    asm volatile("" : "+v"(v[i]), "+v"(p));
#pragma unroll
    for (int d = 0; d < 4; ++d) acc[i * 4 + d] = fma2(p, bf_pair(v[i][d]), acc[i * 4 + d]);
    PIN4(acc, i * 4);
  }
}

constexpr int PTAU = 660;
constexpr int POFF = 140, FFWD = 112;
constexpr int LTGT = 230, LCAP = 600;
constexpr int DOWN = 30, AFROM = 3, EFROM = 12, SLACK = 10;
constexpr int PROBE_MIN = 10;
constexpr int AUX_SC1 = 16, AUX_NT = 2;
constexpr unsigned SPIN_CAP = 1u << 17;
__device__ __forceinline__ unsigned nz_bits(float v) { const unsigned u = __float_as_uint(v); return u == 0u ? 0x80000000u : u; }
__device__ __forceinline__ void lds_put(int* p, int v) { __hip_atomic_store(p, v, __ATOMIC_RELAXED, __HIP_MEMORY_SCOPE_WORKGROUP); }
__device__ __forceinline__ int lds_get(int* p) { return __hip_atomic_load(p, __ATOMIC_RELAXED, __HIP_MEMORY_SCOPE_WORKGROUP); }
__device__ __forceinline__ void count_in(int* p, int lane) {
  if (lane == 0) asm volatile("ds_add_u32 %0, %1" :: "v"((unsigned)(size_t)(__attribute__((address_space(3))) int*)p), "v"(1) : "memory");
}
using bf16x8 = __attribute__((ext_vector_type(8))) short;
__device__ __forceinline__ unsigned pk_lo(unsigned a, unsigned b) { return __builtin_amdgcn_perm(b, a, 0x05040100u); }
__device__ __forceinline__ unsigned pk_hi(unsigned a, unsigned b) { return __builtin_amdgcn_perm(b, a, 0x07060302u); }
__device__ __forceinline__ void split3(float v, unsigned& t0, unsigned& t1, unsigned& t2) {
  t0 = __float_as_uint(v) & 0xffff0000u;
  const float r1 = v - __uint_as_float(t0);
  t1 = __float_as_uint(r1) & 0xffff0000u;
  t2 = __float_as_uint(r1 - __uint_as_float(t1));
}
__device__ __forceinline__ f32x4 mfma16(u32x4 a, u32x4 b, f32x4 c) {
  return __builtin_amdgcn_mfma_f32_16x16x32_bf16(__builtin_bit_cast(bf16x8, a), __builtin_bit_cast(bf16x8, b), c, 0, 0, 0);
}
__device__ __forceinline__ void put_terms4(char* img, int rowbytes, f32x4 v) {
  unsigned t[3][4];
#pragma unroll
  for (int e = 0; e < 4; ++e) split3(v[e], t[0][e], t[1][e], t[2][e]);
#pragma unroll
  for (int k = 0; k < 3; ++k) *reinterpret_cast<u32x2*>(img + k * rowbytes) = u32x2{pk_hi(t[k][0], t[k][1]), pk_hi(t[k][2], t[k][3])};
}
__device__ __forceinline__ unsigned min4(u32x4 v) { return min(min(v[0], v[1]), min(v[2], v[3])); }
__device__ __forceinline__ bool all_nz(u32x4 v) { return min4(v) != 0u; }

__device__ __forceinline__ float sum3(f32x4 v) { return (v[0] + v[1]) + v[2]; }

constexpr int NW = 8, NBUF = 4, NGRP = 64, NT = NGRP / NW;
constexpr int NQP = NH * DH / 64, NQW = 5, QW0 = 2, QST = DM / 64 / NQW;
constexpr int SVC = NW - 1;
constexpr int W_CT = DM / 64, G_TASKS = NB * (FF / 64), W_TASKS = W_CT * K3_KS, D_TASKS = 2 * (DM / 32);
static_assert(G_TASKS + W_TASKS + D_TASKS == NWG && NW == 8 && K3_ROWS == 4 * 128 && NBUF == 4 && SK / 64 == 128, "shape");
__device__ __forceinline__ unsigned fresh_tag(unsigned tags) {
  unsigned t = __builtin_amdgcn_readfirstlane(tags);
  do ++t; while (__any(tags == t));
  return t;
}

__global__ void __launch_bounds__(NW * 64, 2) fused_kernel(const bf16* __restrict__ x, const bf16* __restrict__ kc, const bf16* __restrict__ vc,
                                                        const bf16* __restrict__ wq, const bf16* __restrict__ wo, const bf16* __restrict__ wr,
                                                        const bf16* __restrict__ wg, const bf16* __restrict__ wu, const bf16* __restrict__ wd,
                                                        float* ws, bf16* __restrict__ out) {
  constexpr int PST = DH + 4;
  constexpr int WA = 128 * 2 + 16;
  __shared__ __attribute__((aligned(16))) char s_A[4 * 16 * WA];
  __shared__ __attribute__((aligned(16))) float s_red[6 * 256];
  __shared__ __attribute__((aligned(16))) float s_h[DM];
  __shared__ __attribute__((aligned(16))) float s_buf[NWIN * NW * PST];
  __shared__ __attribute__((aligned(16))) float s_sc[NGRP * 64];
  __shared__ __attribute__((aligned(16))) float s_q[NWIN * DH];
  __shared__ float s_m[NWIN * NW];
  __shared__ int s_mcnt[NWIN];
  __shared__ int s_qok, s_pcnt, s_ecnt, s_fcnt, s_icnt, s_org, s_hcnt, s_ccnt;
  __shared__ int s_prog[NW];

  const int tid = threadIdx.x;
  int lane = tid & 63, r = lane >> 3, j = lane & 7, mg = lane >> 4, mc = lane & 15;
  int wave = __builtin_amdgcn_readfirstlane(tid >> 6), bid = blockIdx.x;
  const unsigned t_start = (unsigned)__builtin_amdgcn_s_memrealtime();
  const int wid = (bid >> 5) * 32 + (bid & 7) * 4 + ((bid >> 3) & 3);
  const bool prod = bid < NQP;
  const __amdgpu_buffer_rsrc_t rs_ws = __builtin_amdgcn_make_buffer_rsrc(ws, 0, WS_END * 4, 0x00020000);
  const __amdgpu_buffer_rsrc_t rs_k = __builtin_amdgcn_make_buffer_rsrc(const_cast<bf16*>(kc), 0, NB * NH * SK * DH * 2, 0x00020000);
  const __amdgpu_buffer_rsrc_t rs_v = __builtin_amdgcn_make_buffer_rsrc(const_cast<bf16*>(vc), 0, NB * NH * SK * DH * 2, 0x00020000);
  const int voff = r * (DH * 2) + j * 16;
  const int c1 = wid % 3 == 0 ? 22 : 21, c2 = wid % 3 == 1 ? 42 : 43;
  auto seg_of = [&](int n) { return (n * 8 + wave >= c1) + (n * 8 + wave >= c2); };
  auto gload = [&](Slot (&sl)[NBUF], __amdgpu_buffer_rsrc_t rs, int n) {
    const int sn = seg_of(n), sg = wid + NWG * sn, row = sg / NSEG, k = sg - row * NSEG;
    const int off = (row * (SK / 64) + (k >> 1) * 43 + (n * 8 + wave - (sn == 0 ? 0 : (sn == 1 ? c1 : c2))) * 2 + ((k ^ (k >> 1)) & 1)) * (64 * DH * 2);
#pragma unroll
    for (int u = 0; u < NBUF; ++u) {
#pragma unroll
      for (int i = 0; i < 3; ++i) sl[u].a[i] = __builtin_amdgcn_raw_buffer_load_b128(rs, voff + i * 128, off + 2 * u * 3072, AUX_NT);
      MEMFENCE();
#pragma unroll
      for (int i = 0; i < 3; ++i) sl[u].b[i] = __builtin_amdgcn_raw_buffer_load_b128(rs, voff + i * 128, off + (2 * u + 1) * 3072, AUX_NT);
      if (u < NBUF - 1) __builtin_amdgcn_s_sleep(3);
      MEMFENCE();
    }
  };
  asm volatile("" :: "s"(ws), "s"(x), "s"(wq), "s"(kc));
  Slot b0[NBUF];
  const bool early = wave == 0 && !prod;
  if (early) gload(b0, rs_k, 0);
  unsigned hs_nc = 0u, hs_ep = 0u;
  auto look_at_p = [&]() {
    AGAIN();
    if (bid == NWG - 1) {
      u32x4 hp[2], hc[2];
#pragma unroll
      for (int i = 0; i < 2; ++i) {
        hp[i] = __builtin_amdgcn_raw_buffer_load_b128(rs_ws, (WS_HP + lane * 8 + i * 4) * 4, 0, AUX_SC1);
        const u32x2 c0 = __builtin_amdgcn_raw_buffer_load_b64(rs_ws, (WS_HC + (lane * 4 + i * 2) * LSTR) * 4, 0, AUX_SC1);
        const u32x2 c1_ = __builtin_amdgcn_raw_buffer_load_b64(rs_ws, (WS_HC + (lane * 4 + i * 2 + 1) * LSTR) * 4, 0, AUX_SC1);
        hc[i] = u32x4{c0[0], c0[1], c1_[0], c1_[1]};
      }
      if (__all(hc[0][1] == hp[0][0] && hc[0][3] == hp[0][2] && hc[1][1] == hp[1][0] && hc[1][3] == hp[1][2]) &&
          __any(hc[0][0] != hp[0][1] || hc[0][2] != hp[0][3] || hc[1][0] != hp[1][1] || hc[1][2] != hp[1][3])) {
#pragma unroll
        for (int i = 0; i < 2; ++i)
          __builtin_amdgcn_raw_buffer_store_b128(u32x4{hp[i][0], hc[i][0], hp[i][2], hc[i][2]}, rs_ws, (WS_HP + lane * 8 + i * 4) * 4, 0, AUX_SC1);
      }
    }
    const u32x2 pw = __builtin_amdgcn_raw_buffer_load_b64(rs_ws, (WS_HP + bid * 2) * 4, 0, AUX_SC1);
    const unsigned np = __builtin_amdgcn_readfirstlane(pw[0]), ec = __builtin_amdgcn_readfirstlane(pw[1]);
    if (np != hs_ep) { hs_ep = np; __builtin_amdgcn_raw_buffer_store_b64(u32x2{hs_nc, hs_ep}, rs_ws, lane == 0 ? (WS_HC + bid * LSTR) * 4 : OOB, 0, AUX_SC1); }
    return ec == hs_nc;
  };
  auto answer = [&]() {
    const u32x4 z4 = {0u, 0u, 0u, 0u};
#pragma unroll
    for (int i = 0; i < (ZPW + 255) / 256; ++i)
      __builtin_amdgcn_raw_buffer_store_b128(z4, rs_ws, i * 256 + lane * 4 < ZPW ? (WS_Z0 + bid * ZPW + i * 256 + lane * 4) * 4 : OOB, 0, AUX_SC1);
    const u32x2 pw = __builtin_amdgcn_raw_buffer_load_b64(rs_ws, (WS_HP + bid * 2) * 4, 0, AUX_SC1);
    DRAIN();
    hs_ep = __builtin_amdgcn_readfirstlane(pw[0]);
    hs_nc = __builtin_amdgcn_readfirstlane(pw[1]) + 1u;
    __builtin_amdgcn_raw_buffer_store_b64(u32x2{hs_nc, hs_ep}, rs_ws, lane == 0 ? (WS_HC + bid * LSTR) * 4 : OOB, 0, AUX_SC1);
  };
  bool answered = false;
  if (tid < NW) s_prog[tid] = 1;
  if (tid < NWIN) s_mcnt[tid] = 0;
  if (tid == 0) { s_qok = 0; s_pcnt = 0; s_ecnt = 0; s_fcnt = 0; s_icnt = 0; s_hcnt = 0; s_ccnt = 0; s_org = (int)(t_start + POFF); }
  if (wave == SVC) {
    __builtin_amdgcn_s_setprio(3);
    int iofs[NWIN];
#pragma unroll
    for (int s = 0; s < NWIN; ++s) iofs[s] = (WS_QI + ((wid + NWG * s) / NSEG * DH + lane) * 2) * 4;
    const int oofs = prod ? (WS_QI + (bid * 64 + lane) * 2) * 4 : OOB;
    u32x2 in[NWIN * 3];
    unsigned ot[NB], ce[4];
#pragma unroll
    for (int e = 0; e < NWIN * 3; ++e) in[e] = __builtin_amdgcn_raw_buffer_load_b64(rs_ws, iofs[e / 3], (e % 3) * 512, AUX_SC1);
#pragma unroll
    for (int bb = 0; bb < NB; ++bb) ot[bb] = __builtin_amdgcn_raw_buffer_load_b64(rs_ws, oofs, bb * (HD * 8), AUX_SC1)[1];
    u32x4 xr[5], hp[2];
#pragma unroll
    for (int i = 0; i < 5; ++i) xr[i] = reinterpret_cast<const u32x4*>(x)[lane * 5 + i];
#pragma unroll
    for (int i = 0; i < 2; ++i) hp[i] = __builtin_amdgcn_raw_buffer_load_b128(rs_ws, bid == NWG - 1 ? (WS_HP + lane * 8 + i * 4) * 4 : OOB, 0, AUX_SC1);
#pragma unroll
    for (int i = 0; i < 4; ++i) ce[i] = __builtin_amdgcn_raw_buffer_load_b32(rs_ws, bid == NWG - 1 ? (WS_HC + (lane * 4 + i) * LSTR + 1) * 4 : OOB, 0, AUX_SC1);
    MEMFENCE();
    __syncthreads();
    MEMFENCE();
    gload(b0, rs_k, 0);
    if (bid == NWG - 1) {
#pragma unroll
      for (int i = 0; i < 2; ++i) {
#pragma unroll
        for (int e = 0; e < 2; ++e) { unsigned np = hp[i][2 * e] + 1u; if (np == ce[2 * i + e]) np += 1u; hp[i][2 * e] = np; }
        __builtin_amdgcn_raw_buffer_store_b128(hp[i], rs_ws, (WS_HP + lane * 8 + i * 4) * 4, 0, AUX_SC1);
      }
    }
    unsigned want[NWIN * 3];
#pragma unroll
    for (int e = 0; e < NWIN * 3; ++e) want[e] = fresh_tag(in[e][1]);
    if (prod) {
      float xs = 0.f;
#pragma unroll
      for (int i = 0; i < 20; ++i) xs += bf_lo(xr[i >> 2][i & 3]) + bf_hi(xr[i >> 2][i & 3]);
      const float mean = row16_sum(xs) * (1.0f / DM);
      float xv = 0.f;
#pragma unroll
      for (int i = 0; i < 20; ++i) { const float a = bf_lo(xr[i >> 2][i & 3]) - mean, c = bf_hi(xr[i >> 2][i & 3]) - mean; xv += a * a + c * c; }
      const float rstd = QSCALE / sqrtf(row16_sum(xv) * (1.0f / DM) + LN_EPS);
#pragma unroll
      for (int bb = 0; bb < NB; ++bb) ot[bb] = fresh_tag(ot[bb]);
      for (unsigned spin = 0; spin < (SPIN_CAP << 4) && lds_get(&s_pcnt) < NQW; ++spin) __builtin_amdgcn_s_sleep(1);
      __builtin_amdgcn_fence(__ATOMIC_ACQUIRE, "workgroup");
      float pr[5];
#pragma unroll
      for (int i = 0; i < 5; ++i) {
        pr[i] = 0.f;
#pragma unroll
        for (int k = 0; k < NQW; ++k) pr[i] += s_buf[2 * k * 320 + i * 64 + lane] + s_buf[(2 * k + 1) * 320 + i * 64 + lane];
      }
#pragma unroll
      for (int bb = 0; bb < NB; ++bb) {
        const float qv = (pr[bb] - __int_as_float(__builtin_amdgcn_readlane(__float_as_int(mean), bb * 16)) * pr[4]) *
                         __int_as_float(__builtin_amdgcn_readlane(__float_as_int(rstd), bb * 16));
        __builtin_amdgcn_raw_buffer_store_b64(u32x2{__float_as_uint(qv), ot[bb]}, rs_ws, oofs, bb * (HD * 8), AUX_SC1);
      }
    } else {
      __builtin_amdgcn_s_sleep(32);
    }
    bool all_out = false;
#pragma unroll 1
    for (unsigned spin = 0; spin < SPIN_CAP; ++spin) {
      AGAIN();
#pragma unroll
      for (int e = 0; e < NWIN * 3; ++e) in[e] = __builtin_amdgcn_raw_buffer_load_b64(rs_ws, iofs[e / 3], (e % 3) * 512, AUX_SC1);
      bool ok = true;
#pragma unroll
      for (int e = 0; e < NWIN * 3; ++e) ok = ok && in[e][1] == want[e];
      if (all_out || __all(ok)) break;
      if (spin == 15) { answer(); answered = true; }
      if (spin > 15 && (spin & 3) == 3) all_out = look_at_p();
      __builtin_amdgcn_s_sleep(4);
    }
#pragma unroll
    for (int e = 0; e < NWIN * 3; ++e) s_q[e * 64 + lane] = __uint_as_float(in[e][0]);
    __builtin_amdgcn_fence(__ATOMIC_RELEASE, "workgroup");
    lds_put(&s_qok, 1);
    __builtin_amdgcn_s_setprio(0);
  } else if (prod && wave >= QW0 && wave < QW0 + NQW) {
    const int k0 = (wave - QW0) * (QST * 64) + (mc >> 3) * 32 + mg * 8;
    u32x4 raw[QST * 8], xa[QST];
    const bf16* wb = wq + (size_t)k0 * HD + bid * 64 + (mc & 7) * 8;
#pragma unroll
    for (int st = 0; st < QST; ++st) xa[st] = *reinterpret_cast<const u32x4*>(x + (mc & 3) * DM + k0 + st * 64);
#pragma unroll
    for (int e = 0; e < QST * 8; ++e) raw[e] = __builtin_nontemporal_load(reinterpret_cast<const u32x4*>(wb + (size_t)((e >> 3) * 64 + (e & 7)) * HD));
    MEMFENCE();
    __syncthreads();
    MEMFENCE();
    f32x4 ac[8];
#pragma unroll
    for (int t = 0; t < 8; ++t) ac[t] = f32x4{0.f, 0.f, 0.f, 0.f};
#pragma unroll
    for (int st = 0; st < QST; ++st) {
      const u32x4 a = (mc & 4) == 0 ? xa[st] : u32x4{0x3f803f80u, 0x3f803f80u, 0x3f803f80u, 0x3f803f80u};
#pragma unroll
      for (int c = 0; c < 4; ++c) {
        u32x4 f0, f1;
#pragma unroll
        for (int i = 0; i < 4; ++i) { f0[i] = pk_lo(raw[st * 8 + 2 * i][c], raw[st * 8 + 2 * i + 1][c]); f1[i] = pk_hi(raw[st * 8 + 2 * i][c], raw[st * 8 + 2 * i + 1][c]); }
        ac[2 * c] = mfma16(a, f0, ac[2 * c]);
        ac[2 * c + 1] = mfma16(a, f1, ac[2 * c + 1]);
      }
    }
    if ((mg >> 1) == (mc >> 3)) {
      float* const dst = &s_buf[((wave - QW0) * 2 + (mg >> 1)) * 320 + (mc & 7) * 8];
#pragma unroll
      for (int bb = 0; bb < NB; ++bb)
        if (bb == 0 || (mg & 1) == 0) {
          *reinterpret_cast<f32x4*>(dst + ((mg & 1) * 4 + bb) * 64) = f32x4{ac[0][bb], ac[1][bb], ac[2][bb], ac[3][bb]};
          *reinterpret_cast<f32x4*>(dst + ((mg & 1) * 4 + bb) * 64 + 4) = f32x4{ac[4][bb], ac[5][bb], ac[6][bb], ac[7][bb]};
        }
    }
    __builtin_amdgcn_fence(__ATOMIC_RELEASE, "workgroup");
    if (lane == 0) __hip_atomic_fetch_add(&s_pcnt, 1, __ATOMIC_RELAXED, __HIP_MEMORY_SCOPE_WORKGROUP);
  } else {
    __syncthreads();
  }
  MEMFENCE();
  const int gtile = ((bid & 7) * 12 + (bid >> 3) - 15) >> 2;
  const int tau = PTAU - (((bid >> 3) >= 15 && (bid >> 3) < 27 && (((((unsigned)(size_t)wg >> 7) + gtile) & 7) == 3 || ((((unsigned)(size_t)wu >> 7) + gtile) & 7) == 3)) ? 10 : 0);
  u32x2 rep = {0u, 0u};
  unsigned t_thru = 0u;
  auto pace = [&](int g) {
    if (wave == 0 && g > AFROM) {
      const int org = lds_get(&s_org);
      const bool ok = rep[0] - t_start + 1000u < 6000u && rep[1] - t_start + 1000u < 6000u;
      const float dorg = row16_sum(ok ? (float)(int)(rep[1] - (unsigned)org) : 0.f);
      const float lt = ok ? (float)min((int)(rep[0] - rep[1]), LCAP) : (float)LCAP;
      const float lx = fminf(lt, dpp<DPP_ROR8>(lt));
      float hi = fmaxf(lx, dpp<DPP_XOR1>(lx)); hi = fmaxf(hi, dpp<DPP_XOR2>(hi)); hi = fmaxf(hi, dpp<DPP_HMIRROR>(hi));
      const float lat = row16_sum(lt) * (0.8f / 16.f) + hi * 0.2f;
      if (lane == 0) lds_put(&s_org, org + (int)(dorg * (1.f / 16.f) + fmaxf((lat - LTGT) * 0.6f, g > EFROM ? (float)SLACK : -(float)DOWN)));
    }
    const unsigned rel = (unsigned)(g * tau + (wave + (g == 0 && wave > 0)) * (PTAU / 8) + min(g * 8 + wave, 16) * (FFWD / 16) - FFWD + ((wid * 65) & 255) * (PTAU / 8) / 256);
    unsigned now = (unsigned)__builtin_amdgcn_s_memrealtime();
    if (wave == 1 && (bid >> 4) == 7 && g >= 2)
      __builtin_amdgcn_raw_buffer_store_b64(u32x2{t_thru - rel + tau, (unsigned)lds_get(&s_org)}, rs_ws, lane == 0 ? (WS_REP + ((g - 2) * 16 + (bid & 15)) * LSTR) * 4 : OOB, 0, AUX_SC1);
    for (unsigned spin = 0; spin < 4096 && (int)(now - rel - (unsigned)__builtin_amdgcn_readfirstlane(lds_get(&s_org))) < 0; ++spin) {
      __builtin_amdgcn_s_sleep(2);
      now = (unsigned)__builtin_amdgcn_s_memrealtime();
    }
    if (g >= 2) rep = __builtin_amdgcn_raw_buffer_load_b64(rs_ws, (wave == 0 && lane < 16 && g >= AFROM) ? (WS_REP + ((g - 2) * 16 + lane) * LSTR) * 4 : OOB, 0, 0);
    if (lane == 0) lds_put(&s_prog[wave], g);
    int pp = __builtin_amdgcn_readfirstlane(lds_get(&s_prog[wave ^ (NW / 2)]));
    for (unsigned spin = 0; spin < 4096 && g > pp + 1; ++spin) { __builtin_amdgcn_s_sleep(4); pp = __builtin_amdgcn_readfirstlane(lds_get(&s_prog[wave ^ (NW / 2)])); }
    if (g > pp) __builtin_amdgcn_s_setprio(0); else __builtin_amdgcn_s_setprio(1);
  };
  float gmx = -INFINITY;
  auto k_group = [&](const Slot (&sl)[NBUF], int n, int s) {
    f32x2 q[12];
#pragma unroll
    for (int i = 0; i < 6; ++i) {
      const f32x4 a = *reinterpret_cast<const f32x4*>(&s_q[s * DH + (i >> 1) * 64 + j * 8 + (i & 1) * 4]);
      q[i * 2] = f32x2{a[0], a[1]}; q[i * 2 + 1] = f32x2{a[2], a[3]};
    }
    float* const sc = &s_sc[(wave * NT + n) * 64 + r * 8];
    MEMFENCE();
#pragma unroll
    for (int u = 0; u < NBUF; ++u) {
      k_tile(sl[u].a, q, gmx, sc + 2 * u, j);
      k_tile(sl[u].b, q, gmx, sc + 2 * u + 1, j);
      MEMFENCE();
    }
    t_thru = (unsigned)__builtin_amdgcn_s_memrealtime();
  };
  {
    Slot b1[NBUF];
    if (!early && wave != SVC) {
      pace(0);
      gload(b0, rs_k, 0);
    }
    pace(1);
    gload(b1, rs_k, 1);
    for (unsigned spin = 0; spin < (SPIN_CAP << 4) && lds_get(&s_qok) == 0; ++spin) __builtin_amdgcn_s_sleep(2);
    __builtin_amdgcn_fence(__ATOMIC_ACQUIRE, "workgroup");
    k_group(b0, 0, 0);
    k_group(b1, 1, 0);
    if (wave == SVC && !answered) answer();
  }
  auto k_done = [&](int s) {
    const float gm = max_r(gmx);
    if (lane == 0) s_m[s * NW + wave] = gm;
    __builtin_amdgcn_fence(__ATOMIC_RELEASE, "workgroup");
    if (lane == 0) __hip_atomic_fetch_add(&s_mcnt[s], 1, __ATOMIC_RELAXED, __HIP_MEMORY_SCOPE_WORKGROUP);
    gmx = -INFINITY;
  };
  Slot buf[NBUF];
  int seg = 0;
  bool through = false;
  for (int n = 2; n < NT; ++n) {
    pace(n);
    gload(buf, rs_k, n);
    if (seg_of(n) != seg) {
      k_done(seg);
      ++seg;
    }
    k_group(buf, n, seg);
    if (wave == SVC && bid == NWG - 1 && n >= 3 && !through) through = look_at_p();
  }
  k_done(seg);
  const int d4 = lane < DH / 4 ? ((lane >> 1) & 7) * 6 + (lane >> 4) * 2 + (lane & 1) : DH / 4;
  const int rofs = lane < REC / 4 ? (WS_PO + wid * REC + lane * 4) * 4 : OOB;
  float l = 0.f;
  f32x2 acc[12];
#pragma unroll
  for (int i = 0; i < 12; ++i) acc[i] = f32x2{0.f, 0.f};
  __builtin_amdgcn_fence(__ATOMIC_RELEASE, "wavefront"); __builtin_amdgcn_wave_barrier(); __builtin_amdgcn_fence(__ATOMIC_ACQUIRE, "wavefront");
  for (int n = 0; n < NT; ++n) {
    pace(NT + n);
    gload(buf, rs_v, n);
    if (n == NT - 1) {
      if (lane == 0) __hip_atomic_fetch_add(&s_icnt, 1, __ATOMIC_RELAXED, __HIP_MEMORY_SCOPE_WORKGROUP);
      __builtin_amdgcn_s_setprio(3);
    }
    seg = seg_of(n);
    for (unsigned spin = 0; spin < (SPIN_CAP << 4) && lds_get(&s_mcnt[seg]) < NW; ++spin) __builtin_amdgcn_s_sleep(2);
    __builtin_amdgcn_fence(__ATOMIC_ACQUIRE, "workgroup");
    float m = s_m[seg * NW + j], lw;
    m = fmaxf(m, dpp<DPP_XOR1>(m)); m = fmaxf(m, dpp<DPP_XOR2>(m)); m = fmaxf(m, dpp<DPP_HMIRROR>(m));
    {
      float p[8];
      const float* const sc = &s_sc[(wave * NT + n) * 64 + r * 8];
      const f32x4 sa = *reinterpret_cast<const f32x4*>(sc), sb = *reinterpret_cast<const f32x4*>(sc + 4);
#pragma unroll
      for (int t = 0; t < 4; ++t) { p[t] = __builtin_amdgcn_exp2f(sa[t] - m); p[4 + t] = __builtin_amdgcn_exp2f(sb[t] - m); }
      l += ((p[0] + p[1]) + (p[2] + p[3])) + ((p[4] + p[5]) + (p[6] + p[7]));
      lw = sum_r(l);
      asm volatile("" : "+v"(lw));
      MEMFENCE();
#pragma unroll
      for (int u = 0; u < NBUF; ++u) {
        v_tile(buf[u].a, p[2 * u], acc);
        v_tile(buf[u].b, p[2 * u + 1], acc);
        MEMFENCE();
      }
    }
    t_thru = (unsigned)__builtin_amdgcn_s_memrealtime();
    if (wave == SVC && n == 1)
      for (unsigned spin = 0; spin < SPIN_CAP && !look_at_p(); ++spin) __builtin_amdgcn_s_sleep(4);
    if (n == NT - 1 || seg_of(n + 1) != seg) {
      float t12[12], t6[6];
#pragma unroll
      for (int e = 0; e < 12; ++e) t12[e] = tsum32(acc[e >> 1][e & 1], acc[6 + (e >> 1)][e & 1]);
#pragma unroll
      for (int e = 0; e < 6; ++e) { t6[e] = tsum16(t12[e], t12[e + 6]); t6[e] += dpp<DPP_ROR8>(t6[e]); }
      if ((lane & 8) == 0) {
#pragma unroll
        for (int e = 0; e < 3; ++e) *reinterpret_cast<f32x2*>(&s_buf[(seg * NW + wave) * PST + j * 24 + (lane >> 4) * 6 + e * 2]) = f32x2{t6[2 * e], t6[2 * e + 1]};
      }
      if (lane == 0) *reinterpret_cast<f32x2*>(&s_buf[(seg * NW + wave) * PST + DH]) = f32x2{m, lw};
      l = 0.f;
#pragma unroll
      for (int i = 0; i < 12; ++i) acc[i] = f32x2{0.f, 0.f};
      if (seg != 0) {
        int c = 0;
        if (lane == 0) asm volatile("ds_add_rtn_u32 %0, %1, %2" : "=v"(c) : "v"((unsigned)(size_t)(__attribute__((address_space(3))) int*)(seg == 1 ? &s_ecnt : &s_fcnt)), "v"(1) : "memory");
        MEMFENCE();
        auto merged = [&](int s) {
          f32x4 pw[NW];
#pragma unroll
          for (int w = 0; w < NW; ++w) pw[w] = reinterpret_cast<const f32x4*>(s_buf)[(s * NW + w) * (PST / 4) + d4];
          f32x4 o = ((pw[0] + pw[1]) + (pw[2] + pw[3])) + ((pw[4] + pw[5]) + (pw[6] + pw[7]));
          if (lane >= DH / 4) o = f32x4{pw[0][0], o[1], 1.f, 1.f};
          return u32x4{nz_bits(o[0]), nz_bits(o[1]), nz_bits(o[2]), nz_bits(o[3])};
        };
        u32x4 rec = merged(seg);
        asm volatile("s_waitcnt lgkmcnt(0)" : "+v"(rec), "+v"(c) :: "memory");
        if (__builtin_amdgcn_readfirstlane(c) == NW - 1) {
          __builtin_amdgcn_raw_buffer_store_b128(rec, rs_ws, rofs, seg * (NWG * REC * 4), AUX_SC1);
          if (seg == 1) __builtin_amdgcn_raw_buffer_store_b128(merged(0), rs_ws, rofs, 0, AUX_SC1);
          if (seg == 2) { MEMFENCE(); lds_put(&s_fcnt, NW + 1); }
          if (seg == 1) {
            DRAIN();
            __builtin_amdgcn_raw_buffer_store_b32(1u, rs_ws, lane == 0 ? ef_ofs(wid) : OOB, 0, AUX_SC1);
          }
        }
      }
      MEMFENCE();
    }
  }
  __builtin_amdgcn_s_setprio(0);

  {
    asm volatile("" : "+s"(wave), "+s"(bid), "+v"(lane));
    r = lane >> 3; j = lane & 7; mg = lane >> 4; mc = lane & 15;
    const int w = wave;
    const int xcd = bid & 7, ri = bid >> 3;
    const bool isW = ri < 15, isG = !isW && ri < 27;
    const int tt = isW ? xcd * 15 + ri : (isG ? xcd * 12 + ri - 15 : xcd * 5 + ri - 27);
    const int ks1 = tt / W_CT, ct1 = tt - ks1 * W_CT;
    typedef const __attribute__((address_space(1))) char* gptr;
    auto behind_stream = [&](int nsl, int cnt) {
      for (unsigned spin = 0; spin < (SPIN_CAP << 4) && lds_get(&s_icnt) < cnt; ++spin) __builtin_amdgcn_s_sleep(1);
      for (int i = 0; i < nsl && lds_get(&s_fcnt) <= NW; ++i) __builtin_amdgcn_s_sleep(3);
    };
    u32x4 fr[16];
    u32x2 raw[32];
    auto weights = [&](const bf16* src, int skip) {
      gptr wb = (gptr)src;
#pragma unroll
      for (int e = 0; e < 32; ++e) {
        raw[e] = __builtin_nontemporal_load((const __attribute__((address_space(1))) u32x2*)wb);
        wb += ((e & 7) == 7 ? skip : 1) * (DM * 2);
        asm volatile("" : "+v"(wb));
      }
      MEMFENCE();
    };
    auto frags = [&]() {
#pragma unroll
      for (int st = 0; st < 4; ++st) {
        PIN8(raw, st * 8);
#pragma unroll
        for (int i = 0; i < 4; ++i) {
          const u32x2 r0 = raw[st * 8 + 2 * i], r1 = raw[st * 8 + 2 * i + 1];
          fr[st * 4 + 0][i] = pk_lo(r0[0], r1[0]); fr[st * 4 + 1][i] = pk_hi(r0[0], r1[0]);
          fr[st * 4 + 2][i] = pk_lo(r0[1], r1[1]); fr[st * 4 + 3][i] = pk_hi(r0[1], r1[1]);
        }
        PIN4(fr, st * 4);
        MEMFENCE();
      }
    };
    if (isW) {
      if (w >= 4) return;
      int a_put = w * (16 * WA) + mg * 4 * WA + mc * 8, a_get = w * (16 * WA / 16) + mc * (WA / 16) + mg, a_red = w * 64 + lane;
      int o_res = (WS_SLAB + ks1 * SLS + mg * DM + ct1 * 64 + mc * 4) * 4;
      {
        int offp[2], sg0[2];
        bool er[2];
#pragma unroll
        for (int hf = 0; hf < 2; ++hf) {
          const int row = ks1 * K3_ROWS + w * 128 + hf * 64, hh = row / DH;
          sg0[hf] = (mg * NH + hh) * NSEG;
          offp[hf] = (WS_PO + sg0[hf] * REC + row - hh * DH + mc * 4) * 4;
          er[hf] = sg0[hf] + NSEG <= 2 * NWG;
        }
        const int sgm = (mc < 8 ? sg0[0] : sg0[1]) + j, offm = j < NSEG ? (WS_PO + sgm * REC + DH) * 4 : OOB;
        for (unsigned spin = 0; spin < (SPIN_CAP << 4) && lds_get(&s_ecnt) < NW; ++spin) __builtin_amdgcn_s_sleep(4);
        for (unsigned spin = 0; spin < SPIN_CAP; ++spin) {
          AGAIN();
          const bool fl = j < NSEG && sgm < 2 * NWG;
          if (__all(!fl || __builtin_amdgcn_raw_buffer_load_b32(rs_ws, fl ? ef_ofs(sgm & (NWG - 1)) : OOB, 0, AUX_SC1) != 0u)) break;
          __builtin_amdgcn_s_sleep(8);
        }
        auto look = [&](u32x4 (&d)[2 * NSEG], bool h0, bool h1) {
          const int o0_ = h0 ? offp[0] : OOB, o1_ = h1 ? offp[1] : OOB;
#pragma unroll
          for (int k = 0; k < 2 * NSEG; ++k) d[k] = __builtin_amdgcn_raw_buffer_load_b128(rs_ws, k < NSEG ? o0_ : o1_, (k % NSEG) * REC * 4, AUX_SC1);
          MEMFENCE();
        };
        auto wsum = [&](float wt, const u32x4 (&d)[2 * NSEG], f32x4& a0, f32x4& a1) {
          a0 += dpp<DPP_BCAST + 0>(wt) * __builtin_bit_cast(f32x4, d[0]); a1 += dpp<DPP_BCAST + 8>(wt) * __builtin_bit_cast(f32x4, d[6]);
          a0 += dpp<DPP_BCAST + 1>(wt) * __builtin_bit_cast(f32x4, d[1]); a1 += dpp<DPP_BCAST + 9>(wt) * __builtin_bit_cast(f32x4, d[7]);
          a0 += dpp<DPP_BCAST + 2>(wt) * __builtin_bit_cast(f32x4, d[2]); a1 += dpp<DPP_BCAST + 10>(wt) * __builtin_bit_cast(f32x4, d[8]);
          a0 += dpp<DPP_BCAST + 3>(wt) * __builtin_bit_cast(f32x4, d[3]); a1 += dpp<DPP_BCAST + 11>(wt) * __builtin_bit_cast(f32x4, d[9]);
          a0 += dpp<DPP_BCAST + 4>(wt) * __builtin_bit_cast(f32x4, d[4]); a1 += dpp<DPP_BCAST + 12>(wt) * __builtin_bit_cast(f32x4, d[10]);
          a0 += dpp<DPP_BCAST + 5>(wt) * __builtin_bit_cast(f32x4, d[5]); a1 += dpp<DPP_BCAST + 13>(wt) * __builtin_bit_cast(f32x4, d[11]);
        };
        u32x4 pe[2 * NSEG], pl[2 * NSEG];
        behind_stream(6, NW - 1);
        weights(wo + (size_t)(ks1 * K3_ROWS + w * 128 + mg * 8) * DM + ct1 * 64 + mc * 4, 25);
        look(pe, er[0], er[1]);
        for (unsigned sp2 = 0; sp2 < (SPIN_CAP << 4) && lds_get(&s_fcnt) < NW; ++sp2) __builtin_amdgcn_s_sleep(1);
        f32x4 o0, o1;
        asm volatile("" : "+v"(a_put), "+v"(a_get), "+v"(a_red), "+v"(o_res));
        u32x2 ml = __builtin_amdgcn_raw_buffer_load_b64(rs_ws, offm, 0, AUX_SC1);
        look(pl, !er[0], !er[1]);
        frags();
        MEMFENCE();
#pragma unroll 1
        for (unsigned spin = 0; spin < SPIN_CAP; ++spin) {
          AGAIN();
          const float mm = j < NSEG ? __uint_as_float(ml[0]) : -INFINITY;
          float M = fmaxf(mm, dpp<DPP_XOR1>(mm)); M = fmaxf(M, dpp<DPP_XOR2>(M)); M = fmaxf(M, dpp<DPP_HMIRROR>(M));
          const float e = __builtin_amdgcn_exp2f(mm - M);
          float L = e * __uint_as_float(ml[1]);
          L += dpp<DPP_XOR1>(L); L += dpp<DPP_XOR2>(L); L += dpp<DPP_HMIRROR>(L);
          const float wt = e * __builtin_amdgcn_rcpf(L);
          o0 = f32x4{0.f, 0.f, 0.f, 0.f}; o1 = o0;
          wsum(wt, pe, o0, o1);
          unsigned m0 = er[0], m1 = er[1], a0 = ~0u, a1 = ~0u;
          const bool okm = j >= NSEG || min(ml[0], ml[1]) != 0u;
          asm volatile("" : "+v"(o0), "+v"(o1));
          MEMFENCE();
          wsum(wt, pl, o0, o1);
          asm volatile("" : "+v"(o0), "+v"(o1));
#pragma unroll
          for (int k = 0; k < NSEG; ++k) { a0 = min(a0, min4(pl[k])); a1 = min(a1, min4(pl[NSEG + k])); }
          if (__all(okm && (a0 | m0) != 0u && (a1 | m1) != 0u)) break;
          if (spin >= 8) __builtin_amdgcn_s_sleep(4);
          ml = __builtin_amdgcn_raw_buffer_load_b64(rs_ws, offm, 0, AUX_SC1);
          look(pl, !er[0], !er[1]);
        }
        put_terms4(s_A + a_put, WA, o0);
        put_terms4(s_A + a_put + 128, WA, o1);
      }
      __builtin_amdgcn_fence(__ATOMIC_RELEASE, "wavefront"); __builtin_amdgcn_wave_barrier(); __builtin_amdgcn_fence(__ATOMIC_ACQUIRE, "wavefront");
      f32x4 ac[4];
#pragma unroll
      for (int t = 0; t < 4; ++t) ac[t] = f32x4{0.f, 0.f, 0.f, 0.f};
      u32x4 av[4];
#pragma unroll
      for (int st = 0; st < 4; ++st) av[st] = reinterpret_cast<const u32x4*>(s_A)[a_get + st * 4];
      MEMFENCE();
#pragma unroll
      for (int st = 0; st < 4; ++st)
#pragma unroll
        for (int t = 0; t < 4; ++t) ac[t] = mfma16(av[st], fr[st * 4 + t], ac[t]);
      reinterpret_cast<f32x4*>(s_red)[a_red] = f32x4{sum3(ac[0]), sum3(ac[1]), sum3(ac[2]), sum3(ac[3])};
      __syncthreads();
      if (w == 0) {
        const f32x4* const red = reinterpret_cast<const f32x4*>(s_red) + a_red;
        const f32x4 s0 = (red[0] + red[64]) + (red[128] + red[192]);
        __builtin_amdgcn_raw_buffer_store_b128(u32x4{nz_bits(s0[0]), nz_bits(s0[1]), nz_bits(s0[2]), nz_bits(s0[3])}, rs_ws, o_res, 0, AUX_SC1);
      }
    } else if (isG) {
      const int gb = tt & 3, gt = tt >> 2;
      int o_res = (WS_ACT + (gb * (FF / 64) + gt) * 96 + (lane & 1) * 32 + (lane >> 1)) * 4;
      asm volatile("" : "+v"(o_res));
      if (w < 5) {
        const int hcol = w * 128 + (lane & 31) * 4;
        uint2 xx = {0u, 0u};
        u32x4 fg[32];
        {
          u32x4 rg[32];
          if (lane < 32) xx = *reinterpret_cast<const uint2*>(x + gb * DM + hcol);
          behind_stream(5, NW);
          gptr wb = (gptr)((mc < 8 ? wg : wu) + (size_t)(w * 128 + mg * 8) * FF + gt * 64 + (mc & 7) * 8);
#pragma unroll
          for (int e = 0; e < 32; ++e) {
            rg[e] = __builtin_nontemporal_load((const __attribute__((address_space(1))) u32x4*)wb);
            wb += ((e & 7) == 7 ? 25 : 1) * (FF * 2);
            asm volatile("" : "+v"(wb));
          }
          MEMFENCE();
#pragma unroll
          for (int st = 0; st < 4; ++st) {
            PIN8(rg, st * 8);
#pragma unroll
            for (int c = 0; c < 4; ++c)
#pragma unroll
              for (int i = 0; i < 4; ++i) {
                fg[st * 8 + 2 * c][i] = pk_lo(rg[st * 8 + 2 * i][c], rg[st * 8 + 2 * i + 1][c]);
                fg[st * 8 + 2 * c + 1][i] = pk_hi(rg[st * 8 + 2 * i][c], rg[st * 8 + 2 * i + 1][c]);
              }
            PIN8(fg, st * 8);
            MEMFENCE();
          }
        }
        {
          f32x4 cs[8];
#pragma unroll
          for (int t = 0; t < 8; ++t) cs[t] = f32x4{0.f, 0.f, 0.f, 0.f};
#pragma unroll
          for (int e = 0; e < 32; ++e) cs[e & 7] = mfma16(u32x4{0x3f803f80u, 0x3f803f80u, 0x3f803f80u, 0x3f803f80u}, fg[e], cs[e & 7]);
          if (lane < 16) {
            *reinterpret_cast<f32x4*>(&s_red[w * 256 + 128 + lane * 8]) = f32x4{cs[0][0], cs[1][0], cs[2][0], cs[3][0]};
            *reinterpret_cast<f32x4*>(&s_red[w * 256 + 128 + lane * 8 + 4]) = f32x4{cs[4][0], cs[5][0], cs[6][0], cs[7][0]};
          }
          count_in(&s_ccnt, lane);
        }
        f32x4 hv = f32x4{bf_lo(xx.x), bf_hi(xx.x), bf_lo(xx.y), bf_hi(xx.y)};
        int a_h = hcol + (lane >> 5) * 2, a_put = w * (4 * WA) + (lane & 31) * 8 + (lane >> 5) * 4, a_get = w * (4 * WA / 16) + min(mc, 2) * (WA / 16) + mg, a_red = w * 64 + mc * 2;
        asm volatile("" : "+v"(hv), "+v"(a_h), "+v"(a_put), "+v"(a_get), "+v"(a_red));
        const int off = (WS_SLAB + (lane >> 5) * (K3_KS / 2) * SLS + gb * DM + hcol) * 4;
        const int poff = (WS_SLAB + ((mg >> 1) * (K3_KS / 2) + mc) * SLS + gb * DM + w * 128 + (mg & 1) * 64) * 4;
        u32x4 sl[K3_KS / 2];
#pragma unroll
        for (int ks = 0; ks < K3_KS / 2; ++ks) sl[ks] = u32x4{0u, 0u, 0u, 0u};
        const unsigned pend = (1u << (K3_KS / 2)) - 1u;
        unsigned todo = pend, go = 0u;
        for (unsigned spin = 0; spin < SPIN_CAP; ++spin) {
          AGAIN();
#pragma unroll
          for (int ks = 0; ks < K3_KS / 2; ++ks)
            if (go & (1u << ks)) sl[ks] = __builtin_amdgcn_raw_buffer_load_b128(rs_ws, off, ks * (SLS * 4), AUX_SC1);
          todo &= ~go;
          if (todo != 0u) {
            const unsigned pr = __builtin_amdgcn_raw_buffer_load_b32(rs_ws, (mc < K3_KS / 2 && ((todo >> mc) & 1u)) ? poff : OOB, 0, AUX_SC1);
            const unsigned long long bl = __ballot(pr != 0u);
            go = (unsigned)(bl & bl >> 16 & bl >> 32 & bl >> 48) & todo;
            if (__builtin_popcount(todo) * 4 - __builtin_popcountll(bl) <= PROBE_MIN) go = todo;
          } else {
            unsigned mn[K3_KS / 2], all = 1u;
#pragma unroll
            for (int ks = 0; ks < K3_KS / 2; ++ks) { mn[ks] = min4(sl[ks]); all = min(all, mn[ks]); }
            if (__all(all != 0u)) break;
            go = 0u;
#pragma unroll
            for (int ks = K3_KS / 2 - 1; ks >= 0; --ks) go = go * 2u + (unsigned)__any(mn[ks] == 0u);
          }
        }
        hv = ((hv + __builtin_bit_cast(f32x4, sl[0])) + (__builtin_bit_cast(f32x4, sl[1]) + __builtin_bit_cast(f32x4, sl[2]))) +
             ((__builtin_bit_cast(f32x4, sl[3]) + __builtin_bit_cast(f32x4, sl[4])) + __builtin_bit_cast(f32x4, sl[5]));
        const float h0 = tsum32(hv[0], hv[2]), h1 = tsum32(hv[1], hv[3]);
        *reinterpret_cast<f32x2*>(&s_h[a_h]) = f32x2{h0, h1};
        count_in(&s_hcnt, lane);
        {
          unsigned t0[3], t1[3];
          split3(h0, t0[0], t0[1], t0[2]); split3(h1, t1[0], t1[1], t1[2]);
#pragma unroll
          for (int k = 0; k < 3; ++k) *reinterpret_cast<unsigned*>(s_A + a_put + k * WA) = pk_hi(t0[k], t1[k]);
        }
        __builtin_amdgcn_fence(__ATOMIC_RELEASE, "wavefront"); __builtin_amdgcn_wave_barrier(); __builtin_amdgcn_fence(__ATOMIC_ACQUIRE, "wavefront");
        f32x4 ac[8];
#pragma unroll
        for (int t = 0; t < 8; ++t) ac[t] = f32x4{0.f, 0.f, 0.f, 0.f};
        u32x4 av[4];
#pragma unroll
        for (int st = 0; st < 4; ++st) av[st] = reinterpret_cast<const u32x4*>(s_A)[a_get + st * 4];
        MEMFENCE();
#pragma unroll
        for (int st = 0; st < 4; ++st)
#pragma unroll
          for (int t = 0; t < 8; ++t) ac[t] = mfma16(av[st], fg[st * 8 + t], ac[t]);
        if (lane < 16) {
          reinterpret_cast<f32x4*>(s_red)[a_red] = f32x4{sum3(ac[0]), sum3(ac[1]), sum3(ac[2]), sum3(ac[3])};
          reinterpret_cast<f32x4*>(s_red)[a_red + 1] = f32x4{sum3(ac[4]), sum3(ac[5]), sum3(ac[6]), sum3(ac[7])};
        }
      } else if (w == NW - 1) {
        unsigned wr2[5];
#pragma unroll
        for (int i = 0; i < 5; ++i) wr2[i] = reinterpret_cast<const unsigned*>(wr)[lane + 64 * i];
        PIN4(wr2, 0); asm volatile("" : "+v"(wr2[4]));
        for (unsigned spin = 0; spin < (SPIN_CAP << 4) && lds_get(&s_ccnt) < 5; ++spin) __builtin_amdgcn_s_sleep(1);
        __builtin_amdgcn_fence(__ATOMIC_ACQUIRE, "workgroup");
        float cg = 0.f, cu = 0.f;
#pragma unroll
        for (int k = 0; k < 5; ++k) { cg += s_red[k * 256 + 128 + lane]; cu += s_red[k * 256 + 192 + lane]; }
        asm volatile("" : "+v"(cg), "+v"(cu));
        for (unsigned spin = 0; spin < (SPIN_CAP << 6) && lds_get(&s_hcnt) < 5; ++spin) {}
        __builtin_amdgcn_fence(__ATOMIC_ACQUIRE, "workgroup");
        const float piv = s_h[0];
        float hv[10], sm = 0.f, sq = 0.f;
#pragma unroll
        for (int i = 0; i < 5; ++i) {
          const float2 t2 = *reinterpret_cast<const float2*>(&s_h[2 * lane + 128 * i]);
          hv[2 * i] = t2.x; hv[2 * i + 1] = t2.y;
        }
        PIN8(hv, 0); asm volatile("" : "+v"(hv[8]), "+v"(hv[9]));
#pragma unroll
        for (int i = 0; i < 10; ++i) { hv[i] -= piv; sm += hv[i]; sq = fmaf(hv[i], hv[i], sq); }
        float t2 = row16_sum(tsum32(sm, sq));
        t2 = tsum16(t2, t2);
        const float dm = __int_as_float(__builtin_amdgcn_readlane(__float_as_int(t2), 0)) * (1.0f / DM);
        const float var = fmaxf(fmaf(-dm, dm, __int_as_float(__builtin_amdgcn_readlane(__float_as_int(t2), 32)) * (1.0f / DM)), 0.f);
        const float rstd = __builtin_amdgcn_rsqf(var + LN_EPS);
        s_red[5 * 256 + lane] = -(piv + dm) * cg; s_red[5 * 256 + 64 + lane] = -(piv + dm) * cu; s_red[5 * 256 + 128 + lane] = rstd;
        __syncthreads();
        if (gt < DM / 32) {
          float rr = 0.f;
#pragma unroll
          for (int i = 0; i < 5; ++i) { rr = fmaf((hv[2 * i] - dm) * rstd, bf_lo(wr2[i]), rr); rr = fmaf((hv[2 * i + 1] - dm) * rstd, bf_hi(wr2[i]), rr); }
          rr = wave_sum(rr);
          const float hh = s_h[gt * 32 + (lane & 31)];
          __builtin_amdgcn_raw_buffer_store_b32(nz_bits(lane >= 32 ? 1.0f / (1.0f + __expf(-rr)) : hh + (hh - (piv + dm)) * rstd), rs_ws,
                                                (WS_BASE + (gb * (DM / 32) + gt) * 64 + lane) * 4, 0, AUX_SC1);
        }
        return;
      } else {
        return;
      }
      __syncthreads();
      if (w == 0) {
        f32x2 pr[6];
#pragma unroll
        for (int k = 0; k < 6; ++k) pr[k] = f32x2{s_red[k * 256 + lane], s_red[k * 256 + 64 + lane]};
        float rstd = s_red[5 * 256 + 128 + lane];
        PIN4(pr, 0); asm volatile("" : "+v"(pr[4]), "+v"(pr[5]), "+v"(rstd));
        const f32x2 gu = (((pr[0] + pr[1]) + (pr[2] + pr[3])) + (pr[4] + pr[5])) * rstd;
        const float gg = gu[0], uu = gu[1];
        unsigned t[3], wd2[3];
        split3(gg * __builtin_amdgcn_rcpf(1.0f + __expf(-gg)) * uu, t[0], t[1], t[2]);
#pragma unroll
        for (int k = 0; k < 3; ++k) {
          const unsigned o = __float_as_uint(dpp<DPP_XOR1>(__uint_as_float(t[k])));
          wd2[k] = nz_bits(__uint_as_float((lane & 1) ? pk_hi(o, t[k]) : pk_hi(t[k], o)));
        }
        __builtin_amdgcn_raw_buffer_store_b32((lane & 1) ? wd2[1] : wd2[0], rs_ws, o_res, 0, AUX_SC1);
        __builtin_amdgcn_raw_buffer_store_b32(wd2[2], rs_ws, (lane & 1) ? OOB : o_res, 256, AUX_SC1);
      }
    } else {
      if (w == 7) return;
      const int bp = tt & 1, ct = tt >> 1;
      for (unsigned spin = 0; spin < (SPIN_CAP << 4) && lds_get(&s_fcnt) < NW; ++spin) __builtin_amdgcn_s_sleep(1);
      if (w == 6) {
        const int bb = lane >> 5, f = lane & 31, offb = (WS_BASE + ((bp * 2 + bb) * (DM / 32) + ct) * 64 + f) * 4;
        const __amdgpu_buffer_rsrc_t rs_out = __builtin_amdgcn_make_buffer_rsrc(out, 0, NB * DM * 2, 0x00020000);
        int o_res = ((bp * 2 + bb) * DM + ct * 32 + f) * 2;
        unsigned bs = 0u, sg = 0u;
        __builtin_amdgcn_s_sleep(100);
        for (unsigned spin = 0; spin < SPIN_CAP; ++spin) {
          AGAIN();
          bs = __builtin_amdgcn_raw_buffer_load_b32(rs_ws, offb, 0, AUX_SC1); sg = __builtin_amdgcn_raw_buffer_load_b32(rs_ws, offb, 128, AUX_SC1);
          if (__all(min(bs, sg) != 0u)) break;
          __builtin_amdgcn_s_sleep(2);
        }
        asm volatile("" : "+v"(o_res));
        __syncthreads();
        float pr[12];
#pragma unroll
        for (int k = 0; k < 12; ++k) pr[k] = s_red[(k >> 1) * 256 + (k & 1) * 64 + lane];
        PIN8(pr, 0); PIN4(pr, 8);
        const float y = (((pr[0] + pr[1]) + (pr[2] + pr[3])) + ((pr[4] + pr[5]) + (pr[6] + pr[7]))) + ((pr[8] + pr[9]) + (pr[10] + pr[11]));
        const bf16 ov = __float2bfloat16(fmaf(y, __uint_as_float(sg), __uint_as_float(bs)));
        __builtin_amdgcn_raw_buffer_store_b16(__builtin_bit_cast(unsigned short, ov), rs_out, o_res, 0, 0);
        return;
      }
      __builtin_amdgcn_s_sleep(8);
      weights(wd + (size_t)(w * 256 + (mc >> 3) * 32 + mg * 8) * DM + ct * 32 + (mc & 7) * 4, 57);
      frags();
      int a_red = w * 64 + mg * 8 + (mc & 7);
      asm volatile("" : "+v"(a_red));
      const int off = (WS_ACT + (((bp * 2 + ((mc >> 2) & 1)) * (FF / 64) + w * 4) * 3 + min(mc & 3, 2)) * 32 + (mc >> 3) * 16 + mg * 4) * 4;
      u32x4 av[4];
      for (unsigned spin = 0; spin < SPIN_CAP; ++spin) {
        AGAIN();
#pragma unroll
        for (int st = 0; st < 4; ++st) av[st] = __builtin_amdgcn_raw_buffer_load_b128(rs_ws, off, st * 384, AUX_SC1);
        if (__all(min(min(min4(av[0]), min4(av[1])), min(min4(av[2]), min4(av[3]))) != 0u)) break;
      }
      f32x4 ac[4];
#pragma unroll
      for (int t = 0; t < 4; ++t) ac[t] = f32x4{0.f, 0.f, 0.f, 0.f};
#pragma unroll
      for (int st = 0; st < 4; ++st)
#pragma unroll
        for (int t = 0; t < 4; ++t) ac[t] = mfma16(av[st], fr[st * 4 + t], ac[t]);
      if ((mc >> 3) == (mg >> 1)) reinterpret_cast<f32x4*>(s_red)[a_red] = f32x4{sum3(ac[0]), sum3(ac[1]), sum3(ac[2]), sum3(ac[3])};
      __syncthreads();
    }
  }
}

extern "C" void kernel_launch(void* const* d_in, const int* in_sizes, int n_in, void* d_out, int out_size, void* d_ws,
                              size_t ws_size, hipStream_t stream) {
  fused_kernel<<<NWG, NW * 64, 0, stream>>>((const bf16*)d_in[0], (const bf16*)d_in[1], (const bf16*)d_in[2], (const bf16*)d_in[3],
                                            (const bf16*)d_in[4], (const bf16*)d_in[6], (const bf16*)d_in[7], (const bf16*)d_in[8],
                                            (const bf16*)d_in[9], (float*)d_ws, (bf16*)d_out);
}
```

```cpp
#include <hip/hip_runtime.h>
#include <hip/hip_bf16.h>
#include <stdint.h>

typedef __hip_bfloat16 bf16;
using u32x4 = __attribute__((ext_vector_type(4))) unsigned;
using u32x2 = __attribute__((ext_vector_type(2))) unsigned;
using f32x4 = __attribute__((ext_vector_type(4))) float;
using f32x2 = __attribute__((ext_vector_type(2))) float;

constexpr int NB = 4, DM = 640, NH = 32, SK = 8192, DH = 192, HD = NH * DH, FF = 1536;
constexpr int NWG = 256, NSEG = 6, NWIN = NB * NH * NSEG / NWG;
constexpr float LN_EPS = 1e-5f;
constexpr float QSCALE = 0.07216878364870322f * 1.4426950408889634f;

constexpr int WS_HP = 0;
constexpr int REC = DH + 32;
constexpr int WS_PO = WS_HP + 2 * NWG;
constexpr int WS_Z0 = WS_PO + 2 * NWG * REC;
constexpr int K3_KS = 12, K3_ROWS = HD / K3_KS;
constexpr int WS_SLAB = WS_PO + NB * NH * NSEG * REC;
constexpr int SLS = NB * DM;
constexpr int WS_ACT = WS_SLAB + K3_KS * SLS;
constexpr int WS_BASE = WS_ACT + NB * FF * 3 / 2;
constexpr int ZPW = 416;
constexpr int WS_QI = WS_Z0 + NWG * ZPW;
constexpr int LSTR = 64;
constexpr int WS_REP = WS_QI + NB * NH * DH * 2;
constexpr int WS_HC = WS_REP + 256 * LSTR;
constexpr int WS_END = WS_HC + NWG * LSTR;
static_assert(WS_BASE + NB * DM * 2 <= WS_QI && WS_Z0 % 64 == 0 && WS_PO % 32 == 0 && ZPW % 32 == 0 && NWIN == 3, "layout");
__device__ __forceinline__ int ef_ofs(int i) { return (WS_PO + (2 * NWG + i) * REC + DH + 2) * 4; }
constexpr int OOB = 0x7ffffff0;

__device__ __forceinline__ float bf_lo(unsigned u) { return __uint_as_float(u << 16); }
__device__ __forceinline__ float bf_hi(unsigned u) { return __uint_as_float(u & 0xffff0000u); }
__device__ __forceinline__ f32x2 bf_pair(unsigned u) { return f32x2{bf_lo(u), bf_hi(u)}; }
__device__ __forceinline__ f32x2 fma2(float a, f32x2 b, f32x2 c) { return __builtin_elementwise_fma(f32x2{a, a}, b, c); }
__device__ __forceinline__ f32x2 fma2(f32x2 a, f32x2 b, f32x2 c) { return __builtin_elementwise_fma(a, b, c); }

template <int CTRL> __device__ __forceinline__ float dpp(float x) {
  return __builtin_bit_cast(float, __builtin_amdgcn_mov_dpp(__builtin_bit_cast(int, x), CTRL, 0xf, 0xf, true));
}
constexpr int DPP_XOR1 = 0xB1, DPP_XOR2 = 0x4E, DPP_HMIRROR = 0x141;

__device__ __forceinline__ float sum_x16_32(float x) {
  auto s = __builtin_amdgcn_permlane16_swap(__float_as_uint(x), __float_as_uint(x), false, false);
  x = __uint_as_float(s[0]) + __uint_as_float(s[1]);
  auto t = __builtin_amdgcn_permlane32_swap(__float_as_uint(x), __float_as_uint(x), false, false);
  return __uint_as_float(t[0]) + __uint_as_float(t[1]);
}
__device__ __forceinline__ float max_x16_32(float x) {
  auto s = __builtin_amdgcn_permlane16_swap(__float_as_uint(x), __float_as_uint(x), false, false);
  x = fmaxf(__uint_as_float(s[0]), __uint_as_float(s[1]));
  auto t = __builtin_amdgcn_permlane32_swap(__float_as_uint(x), __float_as_uint(x), false, false);
  return fmaxf(__uint_as_float(t[0]), __uint_as_float(t[1]));
}
__device__ __forceinline__ float tsum32(float a, float b) {
  auto t = __builtin_amdgcn_permlane32_swap(__float_as_uint(a), __float_as_uint(b), false, false);
  return __uint_as_float(t[0]) + __uint_as_float(t[1]);
}
__device__ __forceinline__ float tsum16(float a, float b) {
  auto t = __builtin_amdgcn_permlane16_swap(__float_as_uint(a), __float_as_uint(b), false, false);
  return __uint_as_float(t[0]) + __uint_as_float(t[1]);
}
constexpr int DPP_ROR8 = 0x128, DPP_BCAST = 0x150;
__device__ __forceinline__ float sum_r(float v) { v += dpp<DPP_ROR8>(v); return sum_x16_32(v); }
__device__ __forceinline__ float max_r(float v) { v = fmaxf(v, dpp<DPP_ROR8>(v)); return max_x16_32(v); }
__device__ __forceinline__ float wave_sum(float v) {
  v += dpp<DPP_XOR1>(v); v += dpp<DPP_XOR2>(v); v += dpp<DPP_HMIRROR>(v);
  return sum_r(v);
}

__device__ __forceinline__ float row16_sum(float v) {
  v += dpp<DPP_XOR1>(v); v += dpp<DPP_XOR2>(v); v += dpp<DPP_HMIRROR>(v);
  return v + dpp<DPP_ROR8>(v);
}

struct Slot { u32x4 a[3]; u32x4 b[3]; };

#define PIN8(a, o) asm volatile("" : "+v"((a)[(o) + 0]), "+v"((a)[(o) + 1]), "+v"((a)[(o) + 2]), "+v"((a)[(o) + 3]), \
                                       "+v"((a)[(o) + 4]), "+v"((a)[(o) + 5]), "+v"((a)[(o) + 6]), "+v"((a)[(o) + 7]))
#define PIN4(a, o) asm volatile("" : "+v"((a)[(o) + 0]), "+v"((a)[(o) + 1]), "+v"((a)[(o) + 2]), "+v"((a)[(o) + 3]))
#define MEMFENCE() do { asm volatile("" ::: "memory"); __builtin_amdgcn_sched_barrier(0); } while (0)
#define AGAIN() asm volatile("" ::: "memory")
#define DRAIN() asm volatile("s_waitcnt vmcnt(0)" ::: "memory")

__device__ __forceinline__ void k_tile(const u32x4 (&k)[3], const f32x2 (&q)[12], float& gmx, float* dst, int j) {
  f32x2 s0 = {0.f, 0.f}, s1 = s0, s2 = s0;
#pragma unroll
  for (int d = 0; d < 4; ++d) {
    s0 = fma2(bf_pair(k[0][d]), q[d], s0);
    s1 = fma2(bf_pair(k[1][d]), q[4 + d], s1);
    s2 = fma2(bf_pair(k[2][d]), q[8 + d], s2);
  }
  s0 = (s0 + s1) + s2;
  float s = s0[0] + s0[1];
  s += dpp<DPP_XOR1>(s);
  s += dpp<DPP_XOR2>(s);
  s += dpp<DPP_HMIRROR>(s);
  gmx = fmaxf(gmx, s);
  asm volatile("" : "+v"(gmx));
  if (j == 0) *dst = s;
}
__device__ __forceinline__ void v_tile(u32x4 (&v)[3], float p, f32x2 (&acc)[12]) {
#pragma unroll
  for (int i = 0; i < 3; ++i) {
    asm volatile("" : "+v"(v[i]), "+v"(p));
#pragma unroll
    for (int d = 0; d < 4; ++d) acc[i * 4 + d] = fma2(p, bf_pair(v[i][d]), acc[i * 4 + d]);
    PIN4(acc, i * 4);
  }
}

constexpr int PTAU = 660;
constexpr int POFF = 140, FFWD = 112;
constexpr int LTGT = 230, LCAP = 600;
constexpr int DOWN = 30, AFROM = 3, EFROM = 12, SLACK = 10;
constexpr int QLOOK = 780;
constexpr int AUX_SC1 = 16, AUX_NT = 2;
constexpr unsigned SPIN_CAP = 1u << 17;
__device__ __forceinline__ unsigned nz_bits(float v) { const unsigned u = __float_as_uint(v); return u == 0u ? 0x80000000u : u; }
__device__ __forceinline__ void lds_put(int* p, int v) { __hip_atomic_store(p, v, __ATOMIC_RELAXED, __HIP_MEMORY_SCOPE_WORKGROUP); }
__device__ __forceinline__ int lds_get(int* p) { return __hip_atomic_load(p, __ATOMIC_RELAXED, __HIP_MEMORY_SCOPE_WORKGROUP); }
__device__ __forceinline__ void count_in(int* p) {
  unsigned long long keep;
  asm volatile("s_mov_b64 %0, exec\n\ts_mov_b64 exec, 1\n\tds_add_u32 %1, %2\n\ts_mov_b64 exec, %0"
               : "=&s"(keep) : "v"((unsigned)(size_t)(__attribute__((address_space(3))) int*)p), "v"(1) : "memory");
}
__device__ __forceinline__ int count_in_rtn(int* p) {
  unsigned long long keep;
  int c;
  asm volatile("s_mov_b64 %0, exec\n\ts_mov_b64 exec, 1\n\tds_add_rtn_u32 %1, %2, %3\n\ts_mov_b64 exec, %0"
               : "=&s"(keep), "=&v"(c) : "v"((unsigned)(size_t)(__attribute__((address_space(3))) int*)p), "v"(1) : "memory");
  return c;
}
using bf16x8 = __attribute__((ext_vector_type(8))) short;
__device__ __forceinline__ unsigned pk_lo(unsigned a, unsigned b) { return __builtin_amdgcn_perm(b, a, 0x05040100u); }
__device__ __forceinline__ unsigned pk_hi(unsigned a, unsigned b) { return __builtin_amdgcn_perm(b, a, 0x07060302u); }
__device__ __forceinline__ void split3(float v, unsigned& t0, unsigned& t1, unsigned& t2) {
  t0 = __float_as_uint(v) & 0xffff0000u;
  const float r1 = v - __uint_as_float(t0);
  t1 = __float_as_uint(r1) & 0xffff0000u;
  t2 = __float_as_uint(r1 - __uint_as_float(t1));
}
__device__ __forceinline__ f32x4 mfma16(u32x4 a, u32x4 b, f32x4 c) {
  return __builtin_amdgcn_mfma_f32_16x16x32_bf16(__builtin_bit_cast(bf16x8, a), __builtin_bit_cast(bf16x8, b), c, 0, 0, 0);
}
__device__ __forceinline__ void put_terms4(char* img, int rowbytes, f32x4 v) {
  unsigned t[3][4];
#pragma unroll
  for (int e = 0; e < 4; ++e) split3(v[e], t[0][e], t[1][e], t[2][e]);
#pragma unroll
  for (int k = 0; k < 3; ++k) *reinterpret_cast<u32x2*>(img + k * rowbytes) = u32x2{pk_hi(t[k][0], t[k][1]), pk_hi(t[k][2], t[k][3])};
}
__device__ __forceinline__ unsigned min4(u32x4 v) { return min(min(v[0], v[1]), min(v[2], v[3])); }

__device__ __forceinline__ float sum3(f32x4 v) { return (v[0] + v[1]) + v[2]; }

constexpr int NW = 8, NBUF = 4, NGRP = 64, NT = NGRP / NW;
constexpr int NQP = NH * DH / 64, NQW = 5, QW0 = 2, QST = DM / 64 / NQW;
constexpr int SVC = NW - 1;
constexpr int W_CT = DM / 64, G_TASKS = NB * (FF / 64), W_TASKS = W_CT * K3_KS, D_TASKS = 2 * (DM / 32);
static_assert(G_TASKS + W_TASKS + D_TASKS == NWG && NW == 8 && K3_ROWS == 4 * 128 && NBUF == 4 && SK / 64 == 128, "shape");
__device__ __forceinline__ unsigned fresh_tag(unsigned tags) {
  unsigned t = __builtin_amdgcn_readfirstlane(tags);
  do ++t; while (__any(tags == t));
  return t;
}

__global__ void __launch_bounds__(NW * 64, 2) fused_kernel(const bf16* __restrict__ x, const bf16* __restrict__ kc, const bf16* __restrict__ vc,
                                                        const bf16* __restrict__ wq, const bf16* __restrict__ wo, const bf16* __restrict__ wr,
                                                        const bf16* __restrict__ wg, const bf16* __restrict__ wu, const bf16* __restrict__ wd,
                                                        float* ws, bf16* __restrict__ out) {
  constexpr int PST = DH + 4;
  constexpr int WA = 128 * 2 + 16;
  __shared__ __attribute__((aligned(16))) char s_A[4 * 16 * WA];
  __shared__ __attribute__((aligned(16))) float s_red[6 * 256];
  __shared__ __attribute__((aligned(16))) float s_h[DM];
  __shared__ __attribute__((aligned(16))) float s_buf[NWIN * NW * PST];
  __shared__ __attribute__((aligned(16))) float s_sc[NGRP * 64];
  __shared__ __attribute__((aligned(16))) float s_q[NWIN * DH];
  __shared__ float s_m[NWIN * NW];
  __shared__ int s_mcnt[NWIN];
  __shared__ int s_qok, s_pcnt, s_ecnt, s_fcnt, s_icnt, s_org, s_hcnt, s_ccnt;
  __shared__ int s_prog[NW];

  const int tid = threadIdx.x;
  int lane = tid & 63, r = lane >> 3, j = lane & 7, mg = lane >> 4, mc = lane & 15;
  int wave = __builtin_amdgcn_readfirstlane(tid >> 6), bid = blockIdx.x;
  const unsigned t_start = (unsigned)__builtin_amdgcn_s_memrealtime();
  const int wid = (bid >> 5) * 32 + (bid & 7) * 4 + ((bid >> 3) & 3);
  const bool prod = bid < NQP;
  const __amdgpu_buffer_rsrc_t rs_ws = __builtin_amdgcn_make_buffer_rsrc(ws, 0, WS_END * 4, 0x00020000);
  const __amdgpu_buffer_rsrc_t rs_k = __builtin_amdgcn_make_buffer_rsrc(const_cast<bf16*>(kc), 0, NB * NH * SK * DH * 2, 0x00020000);
  const __amdgpu_buffer_rsrc_t rs_v = __builtin_amdgcn_make_buffer_rsrc(const_cast<bf16*>(vc), 0, NB * NH * SK * DH * 2, 0x00020000);
  const int voff = r * (DH * 2) + j * 16;
  const int c1 = wid % 3 == 0 ? 22 : 21, c2 = wid % 3 == 1 ? 42 : 43;
  auto seg_of = [&](int n) { return (n * 8 + wave >= c1) + (n * 8 + wave >= c2); };
  auto gload = [&](Slot (&sl)[NBUF], __amdgpu_buffer_rsrc_t rs, int n) {
    const int sn = seg_of(n), sg = wid + NWG * sn, row = sg / NSEG, k = sg - row * NSEG;
    const int off = (row * (SK / 64) + (k >> 1) * 43 + (n * 8 + wave - (sn == 0 ? 0 : (sn == 1 ? c1 : c2))) * 2 + ((k ^ (k >> 1)) & 1)) * (64 * DH * 2);
#pragma unroll
    for (int u = 0; u < NBUF; ++u) {
#pragma unroll
      for (int i = 0; i < 3; ++i) sl[u].a[i] = __builtin_amdgcn_raw_buffer_load_b128(rs, voff + i * 128, off + 2 * u * 3072, AUX_NT);
      MEMFENCE();
#pragma unroll
      for (int i = 0; i < 3; ++i) sl[u].b[i] = __builtin_amdgcn_raw_buffer_load_b128(rs, voff + i * 128, off + (2 * u + 1) * 3072, AUX_NT);
      if (u < NBUF - 1) __builtin_amdgcn_s_sleep(3);
      MEMFENCE();
    }
  };
  asm volatile("" :: "s"(ws), "s"(x), "s"(wq), "s"(kc));
  Slot b0[NBUF];
  const bool early = wave == 0 && !prod;
  if (early) gload(b0, rs_k, 0);
  unsigned hs_nc = 0u, hs_ep = 0u;
  auto look_at_p = [&]() {
    AGAIN();
    if (bid == NWG - 1) {
      u32x4 hp[2], hc[2];
#pragma unroll
      for (int i = 0; i < 2; ++i) {
        hp[i] = __builtin_amdgcn_raw_buffer_load_b128(rs_ws, (WS_HP + lane * 8 + i * 4) * 4, 0, AUX_SC1);
        const u32x2 c0 = __builtin_amdgcn_raw_buffer_load_b64(rs_ws, (WS_HC + (lane * 4 + i * 2) * LSTR) * 4, 0, AUX_SC1);
        const u32x2 c1_ = __builtin_amdgcn_raw_buffer_load_b64(rs_ws, (WS_HC + (lane * 4 + i * 2 + 1) * LSTR) * 4, 0, AUX_SC1);
        hc[i] = u32x4{c0[0], c0[1], c1_[0], c1_[1]};
      }
      if (__all(hc[0][1] == hp[0][0] && hc[0][3] == hp[0][2] && hc[1][1] == hp[1][0] && hc[1][3] == hp[1][2]) &&
          __any(hc[0][0] != hp[0][1] || hc[0][2] != hp[0][3] || hc[1][0] != hp[1][1] || hc[1][2] != hp[1][3])) {
#pragma unroll
        for (int i = 0; i < 2; ++i)
          __builtin_amdgcn_raw_buffer_store_b128(u32x4{hp[i][0], hc[i][0], hp[i][2], hc[i][2]}, rs_ws, (WS_HP + lane * 8 + i * 4) * 4, 0, AUX_SC1);
      }
    }
    const u32x2 pw = __builtin_amdgcn_raw_buffer_load_b64(rs_ws, (WS_HP + bid * 2) * 4, 0, AUX_SC1);
    const unsigned np = __builtin_amdgcn_readfirstlane(pw[0]), ec = __builtin_amdgcn_readfirstlane(pw[1]);
    if (np != hs_ep) { hs_ep = np; __builtin_amdgcn_raw_buffer_store_b64(u32x2{hs_nc, hs_ep}, rs_ws, lane == 0 ? (WS_HC + bid * LSTR) * 4 : OOB, 0, AUX_SC1); }
    return ec == hs_nc;
  };
  auto answer = [&]() {
    const u32x4 z4 = {0u, 0u, 0u, 0u};
#pragma unroll
    for (int i = 0; i < (ZPW + 255) / 256; ++i)
      __builtin_amdgcn_raw_buffer_store_b128(z4, rs_ws, i * 256 + lane * 4 < ZPW ? (WS_Z0 + bid * ZPW + i * 256 + lane * 4) * 4 : OOB, 0, AUX_SC1);
    const u32x2 pw = __builtin_amdgcn_raw_buffer_load_b64(rs_ws, (WS_HP + bid * 2) * 4, 0, AUX_SC1);
    DRAIN();
    hs_ep = __builtin_amdgcn_readfirstlane(pw[0]);
    hs_nc = __builtin_amdgcn_readfirstlane(pw[1]) + 1u;
    __builtin_amdgcn_raw_buffer_store_b64(u32x2{hs_nc, hs_ep}, rs_ws, lane == 0 ? (WS_HC + bid * LSTR) * 4 : OOB, 0, AUX_SC1);
  };
  bool answered = false;
  if (tid < NW) s_prog[tid] = 1;
  if (tid < NWIN) s_mcnt[tid] = 0;
  if (tid == 0) { s_qok = 0; s_pcnt = 0; s_ecnt = 0; s_fcnt = 0; s_icnt = 0; s_hcnt = 0; s_ccnt = 0; s_org = (int)(t_start + POFF); }
  if (wave == SVC) {
    __builtin_amdgcn_s_setprio(3);
    int iofs[NWIN];
#pragma unroll
    for (int s = 0; s < NWIN; ++s) iofs[s] = (WS_QI + ((wid + NWG * s) / NSEG * DH + lane) * 2) * 4;
    const int oofs = prod ? (WS_QI + (bid * 64 + lane) * 2) * 4 : OOB;
    u32x2 in[NWIN * 3];
    unsigned ot[NB], ce[4];
#pragma unroll
    for (int e = 0; e < NWIN * 3; ++e) in[e] = __builtin_amdgcn_raw_buffer_load_b64(rs_ws, iofs[e / 3], (e % 3) * 512, AUX_SC1);
#pragma unroll
    for (int bb = 0; bb < NB; ++bb) ot[bb] = __builtin_amdgcn_raw_buffer_load_b64(rs_ws, oofs, bb * (HD * 8), AUX_SC1)[1];
    u32x4 xr[5], hp[2];
#pragma unroll
    for (int i = 0; i < 5; ++i) xr[i] = reinterpret_cast<const u32x4*>(x)[lane * 5 + i];
#pragma unroll
    for (int i = 0; i < 2; ++i) hp[i] = __builtin_amdgcn_raw_buffer_load_b128(rs_ws, bid == NWG - 1 ? (WS_HP + lane * 8 + i * 4) * 4 : OOB, 0, AUX_SC1);
#pragma unroll
    for (int i = 0; i < 4; ++i) ce[i] = __builtin_amdgcn_raw_buffer_load_b32(rs_ws, bid == NWG - 1 ? (WS_HC + (lane * 4 + i) * LSTR + 1) * 4 : OOB, 0, AUX_SC1);
    MEMFENCE();
    __syncthreads();
    MEMFENCE();
    gload(b0, rs_k, 0);
    if (bid == NWG - 1) {
#pragma unroll
      for (int i = 0; i < 2; ++i) {
#pragma unroll
        for (int e = 0; e < 2; ++e) { unsigned np = hp[i][2 * e] + 1u; if (np == ce[2 * i + e]) np += 1u; hp[i][2 * e] = np; }
        __builtin_amdgcn_raw_buffer_store_b128(hp[i], rs_ws, (WS_HP + lane * 8 + i * 4) * 4, 0, AUX_SC1);
      }
    }
    unsigned want[NWIN * 3];
#pragma unroll
    for (int e = 0; e < NWIN * 3; ++e) want[e] = fresh_tag(in[e][1]);
    if (prod) {
      float xs = 0.f;
#pragma unroll
      for (int i = 0; i < 20; ++i) xs += bf_lo(xr[i >> 2][i & 3]) + bf_hi(xr[i >> 2][i & 3]);
      const float mean = row16_sum(xs) * (1.0f / DM);
      float xv = 0.f;
#pragma unroll
      for (int i = 0; i < 20; ++i) { const float a = bf_lo(xr[i >> 2][i & 3]) - mean, c = bf_hi(xr[i >> 2][i & 3]) - mean; xv += a * a + c * c; }
      const float rstd = QSCALE / sqrtf(row16_sum(xv) * (1.0f / DM) + LN_EPS);
#pragma unroll
      for (int bb = 0; bb < NB; ++bb) ot[bb] = fresh_tag(ot[bb]);
      for (unsigned spin = 0; spin < (SPIN_CAP << 4) && lds_get(&s_pcnt) < NQW; ++spin) __builtin_amdgcn_s_sleep(1);
      __builtin_amdgcn_fence(__ATOMIC_ACQUIRE, "workgroup");
      float pr[5];
#pragma unroll
      for (int i = 0; i < 5; ++i) {
        pr[i] = 0.f;
#pragma unroll
        for (int k = 0; k < NQW; ++k) pr[i] += s_buf[2 * k * 320 + i * 64 + lane] + s_buf[(2 * k + 1) * 320 + i * 64 + lane];
      }
#pragma unroll
      for (int bb = 0; bb < NB; ++bb) {
        const float qv = (pr[bb] - __int_as_float(__builtin_amdgcn_readlane(__float_as_int(mean), bb * 16)) * pr[4]) *
                         __int_as_float(__builtin_amdgcn_readlane(__float_as_int(rstd), bb * 16));
        __builtin_amdgcn_raw_buffer_store_b64(u32x2{__float_as_uint(qv), ot[bb]}, rs_ws, oofs, bb * (HD * 8), AUX_SC1);
      }
    }
    for (unsigned spin = 0; spin < 1024 && (int)((unsigned)__builtin_amdgcn_s_memrealtime() - t_start) < QLOOK; ++spin) __builtin_amdgcn_s_sleep(4);
    bool all_out = false;
#pragma unroll 1
    for (unsigned spin = 0; spin < SPIN_CAP; ++spin) {
      AGAIN();
#pragma unroll
      for (int e = 0; e < NWIN * 3; ++e) in[e] = __builtin_amdgcn_raw_buffer_load_b64(rs_ws, iofs[e / 3], (e % 3) * 512, AUX_SC1);
      bool ok = true;
#pragma unroll
      for (int e = 0; e < NWIN * 3; ++e) ok = ok && in[e][1] == want[e];
      if (all_out || __all(ok)) break;
      if (spin == 15) { answer(); answered = true; }
      if (spin > 15 && (spin & 3) == 3) all_out = look_at_p();
      __builtin_amdgcn_s_sleep(4);
    }
#pragma unroll
    for (int e = 0; e < NWIN * 3; ++e) s_q[e * 64 + lane] = __uint_as_float(in[e][0]);
    __builtin_amdgcn_fence(__ATOMIC_RELEASE, "workgroup");
    lds_put(&s_qok, 1);
    __builtin_amdgcn_s_setprio(0);
  } else if (prod && wave >= QW0 && wave < QW0 + NQW) {
    const int k0 = (wave - QW0) * (QST * 64) + (mc >> 3) * 32 + mg * 8;
    u32x4 raw[QST * 8], xa[QST];
    const bf16* wb = wq + (size_t)k0 * HD + bid * 64 + (mc & 7) * 8;
#pragma unroll
    for (int st = 0; st < QST; ++st) xa[st] = *reinterpret_cast<const u32x4*>(x + (mc & 3) * DM + k0 + st * 64);
#pragma unroll
    for (int e = 0; e < QST * 8; ++e) raw[e] = __builtin_nontemporal_load(reinterpret_cast<const u32x4*>(wb + (size_t)((e >> 3) * 64 + (e & 7)) * HD));
    MEMFENCE();
    __syncthreads();
    MEMFENCE();
    f32x4 ac[8];
#pragma unroll
    for (int t = 0; t < 8; ++t) ac[t] = f32x4{0.f, 0.f, 0.f, 0.f};
#pragma unroll
    for (int st = 0; st < QST; ++st) {
      const u32x4 a = (mc & 4) == 0 ? xa[st] : u32x4{0x3f803f80u, 0x3f803f80u, 0x3f803f80u, 0x3f803f80u};
#pragma unroll
      for (int c = 0; c < 4; ++c) {
        u32x4 f0, f1;
#pragma unroll
        for (int i = 0; i < 4; ++i) { f0[i] = pk_lo(raw[st * 8 + 2 * i][c], raw[st * 8 + 2 * i + 1][c]); f1[i] = pk_hi(raw[st * 8 + 2 * i][c], raw[st * 8 + 2 * i + 1][c]); }
        ac[2 * c] = mfma16(a, f0, ac[2 * c]);
        ac[2 * c + 1] = mfma16(a, f1, ac[2 * c + 1]);
      }
    }
    if ((mg >> 1) == (mc >> 3)) {
      float* const dst = &s_buf[((wave - QW0) * 2 + (mg >> 1)) * 320 + (mc & 7) * 8];
#pragma unroll
      for (int bb = 0; bb < NB; ++bb)
        if (bb == 0 || (mg & 1) == 0) {
          *reinterpret_cast<f32x4*>(dst + ((mg & 1) * 4 + bb) * 64) = f32x4{ac[0][bb], ac[1][bb], ac[2][bb], ac[3][bb]};
          *reinterpret_cast<f32x4*>(dst + ((mg & 1) * 4 + bb) * 64 + 4) = f32x4{ac[4][bb], ac[5][bb], ac[6][bb], ac[7][bb]};
        }
    }
    __builtin_amdgcn_fence(__ATOMIC_RELEASE, "workgroup");
    if (lane == 0) __hip_atomic_fetch_add(&s_pcnt, 1, __ATOMIC_RELAXED, __HIP_MEMORY_SCOPE_WORKGROUP);
  } else {
    __syncthreads();
  }
  MEMFENCE();
  const int gtile = ((bid & 7) * 12 + (bid >> 3) - 15) >> 2;
  const int tau = PTAU - (((bid >> 3) >= 15 && (bid >> 3) < 27 && (((((unsigned)(size_t)wg >> 7) + gtile) & 7) == 3 || ((((unsigned)(size_t)wu >> 7) + gtile) & 7) == 3)) ? 10 : 0);
  u32x2 rep = {0u, 0u};
  unsigned t_thru = 0u;
  auto pace = [&](int g) {
    if (wave == 0 && g > AFROM) {
      const int org = lds_get(&s_org);
      const bool ok = rep[0] - t_start + 1000u < 6000u && rep[1] - t_start + 1000u < 6000u;
      const float dorg = row16_sum(ok ? (float)(int)(rep[1] - (unsigned)org) : 0.f);
      const float lt = ok ? (float)min((int)(rep[0] - rep[1]), LCAP) : (float)LCAP;
      const float lx = fminf(lt, dpp<DPP_ROR8>(lt));
      float hi = fmaxf(lx, dpp<DPP_XOR1>(lx)); hi = fmaxf(hi, dpp<DPP_XOR2>(hi)); hi = fmaxf(hi, dpp<DPP_HMIRROR>(hi));
      const float lat = row16_sum(lt) * (0.8f / 16.f) + hi * 0.2f;
      if (lane == 0) lds_put(&s_org, org + (int)(dorg * (1.f / 16.f) + fmaxf((lat - LTGT) * 0.6f, g > EFROM ? (float)SLACK : -(float)DOWN)));
    }
    const unsigned rel = (unsigned)(g * tau + (wave + (g == 0 && wave > 0)) * (PTAU / 8) + min(g * 8 + wave, 16) * (FFWD / 16) - FFWD + ((wid * 65) & 255) * (PTAU / 8) / 256);
    unsigned now = (unsigned)__builtin_amdgcn_s_memrealtime();
    if (wave == 1 && (bid >> 4) == 7 && g >= 2)
      __builtin_amdgcn_raw_buffer_store_b64(u32x2{t_thru - rel + tau, (unsigned)lds_get(&s_org)}, rs_ws, lane == 0 ? (WS_REP + ((g - 2) * 16 + (bid & 15)) * LSTR) * 4 : OOB, 0, AUX_SC1);
    for (unsigned spin = 0; spin < 4096 && (int)(now - rel - (unsigned)__builtin_amdgcn_readfirstlane(lds_get(&s_org))) < 0; ++spin) {
      __builtin_amdgcn_s_sleep(2);
      now = (unsigned)__builtin_amdgcn_s_memrealtime();
    }
    if (g >= 2) rep = __builtin_amdgcn_raw_buffer_load_b64(rs_ws, (wave == 0 && lane < 16 && g >= AFROM) ? (WS_REP + ((g - 2) * 16 + lane) * LSTR) * 4 : OOB, 0, AUX_SC1);
    if (lane == 0) lds_put(&s_prog[wave], g);
    int pp = __builtin_amdgcn_readfirstlane(lds_get(&s_prog[wave ^ (NW / 2)]));
    for (unsigned spin = 0; spin < 4096 && g > pp + 1; ++spin) { __builtin_amdgcn_s_sleep(4); pp = __builtin_amdgcn_readfirstlane(lds_get(&s_prog[wave ^ (NW / 2)])); }
    if (g > pp) __builtin_amdgcn_s_setprio(0); else __builtin_amdgcn_s_setprio(1);
  };
  float gmx = -INFINITY;
  auto k_group = [&](const Slot (&sl)[NBUF], int n, int s) {
    f32x2 q[12];
#pragma unroll
    for (int i = 0; i < 6; ++i) {
      const f32x4 a = *reinterpret_cast<const f32x4*>(&s_q[s * DH + (i >> 1) * 64 + j * 8 + (i & 1) * 4]);
      q[i * 2] = f32x2{a[0], a[1]}; q[i * 2 + 1] = f32x2{a[2], a[3]};
    }
    float* const sc = &s_sc[(wave * NT + n) * 64 + r * 8];
    MEMFENCE();
#pragma unroll
    for (int u = 0; u < NBUF; ++u) {
      k_tile(sl[u].a, q, gmx, sc + 2 * u, j);
      k_tile(sl[u].b, q, gmx, sc + 2 * u + 1, j);
      MEMFENCE();
    }
    t_thru = (unsigned)__builtin_amdgcn_s_memrealtime();
  };
  {
    Slot b1[NBUF];
    if (!early && wave != SVC) {
      pace(0);
      gload(b0, rs_k, 0);
    }
    pace(1);
    gload(b1, rs_k, 1);
    for (unsigned spin = 0; spin < (SPIN_CAP << 4) && lds_get(&s_qok) == 0; ++spin) __builtin_amdgcn_s_sleep(2);
    __builtin_amdgcn_fence(__ATOMIC_ACQUIRE, "workgroup");
    k_group(b0, 0, 0);
    k_group(b1, 1, 0);
    if (wave == SVC && !answered) answer();
  }
  auto k_done = [&](int s) {
    const float gm = max_r(gmx);
    if (lane == 0) s_m[s * NW + wave] = gm;
    __builtin_amdgcn_fence(__ATOMIC_RELEASE, "workgroup");
    if (lane == 0) __hip_atomic_fetch_add(&s_mcnt[s], 1, __ATOMIC_RELAXED, __HIP_MEMORY_SCOPE_WORKGROUP);
    gmx = -INFINITY;
  };
  Slot buf[NBUF];
  int seg = 0;
  bool through = false;
  for (int n = 2; n < NT; ++n) {
    pace(n);
    gload(buf, rs_k, n);
    if (seg_of(n) != seg) {
      k_done(seg);
      ++seg;
    }
    k_group(buf, n, seg);
    if (wave == SVC && bid == NWG - 1 && n >= 3 && !through) through = look_at_p();
  }
  k_done(seg);
  const int d4 = lane < DH / 4 ? ((lane >> 1) & 7) * 6 + (lane >> 4) * 2 + (lane & 1) : DH / 4;
  const int rofs = lane < REC / 4 ? (WS_PO + wid * REC + lane * 4) * 4 : OOB;
  float l = 0.f;
  f32x2 acc[12];
#pragma unroll
  for (int i = 0; i < 12; ++i) acc[i] = f32x2{0.f, 0.f};
  __builtin_amdgcn_fence(__ATOMIC_RELEASE, "wavefront"); __builtin_amdgcn_wave_barrier(); __builtin_amdgcn_fence(__ATOMIC_ACQUIRE, "wavefront");
  for (int n = 0; n < NT; ++n) {
    pace(NT + n);
    gload(buf, rs_v, n);
    if (n == NT - 1) {
      if (lane == 0) __hip_atomic_fetch_add(&s_icnt, 1, __ATOMIC_RELAXED, __HIP_MEMORY_SCOPE_WORKGROUP);
      __builtin_amdgcn_s_setprio(3);
    }
    seg = seg_of(n);
    for (unsigned spin = 0; spin < (SPIN_CAP << 4) && lds_get(&s_mcnt[seg]) < NW; ++spin) __builtin_amdgcn_s_sleep(2);
    __builtin_amdgcn_fence(__ATOMIC_ACQUIRE, "workgroup");
    float m = s_m[seg * NW + j], lw;
    m = fmaxf(m, dpp<DPP_XOR1>(m)); m = fmaxf(m, dpp<DPP_XOR2>(m)); m = fmaxf(m, dpp<DPP_HMIRROR>(m));
    {
      float p[8];
      const float* const sc = &s_sc[(wave * NT + n) * 64 + r * 8];
      const f32x4 sa = *reinterpret_cast<const f32x4*>(sc), sb = *reinterpret_cast<const f32x4*>(sc + 4);
#pragma unroll
      for (int t = 0; t < 4; ++t) { p[t] = __builtin_amdgcn_exp2f(sa[t] - m); p[4 + t] = __builtin_amdgcn_exp2f(sb[t] - m); }
      l += ((p[0] + p[1]) + (p[2] + p[3])) + ((p[4] + p[5]) + (p[6] + p[7]));
      lw = sum_r(l);
      asm volatile("" : "+v"(lw));
      MEMFENCE();
#pragma unroll
      for (int u = 0; u < NBUF; ++u) {
        v_tile(buf[u].a, p[2 * u], acc);
        v_tile(buf[u].b, p[2 * u + 1], acc);
        MEMFENCE();
      }
    }
    t_thru = (unsigned)__builtin_amdgcn_s_memrealtime();
    if (wave == SVC && n == 1)
      for (unsigned spin = 0; spin < SPIN_CAP && !look_at_p(); ++spin) __builtin_amdgcn_s_sleep(4);
    if (n == NT - 1 || seg_of(n + 1) != seg) {
      float t12[12], t6[6];
#pragma unroll
      for (int e = 0; e < 12; ++e) t12[e] = tsum32(acc[e >> 1][e & 1], acc[6 + (e >> 1)][e & 1]);
#pragma unroll
      for (int e = 0; e < 6; ++e) { t6[e] = tsum16(t12[e], t12[e + 6]); t6[e] += dpp<DPP_ROR8>(t6[e]); }
      if ((lane & 8) == 0) {
#pragma unroll
        for (int e = 0; e < 3; ++e) *reinterpret_cast<f32x2*>(&s_buf[(seg * NW + wave) * PST + j * 24 + (lane >> 4) * 6 + e * 2]) = f32x2{t6[2 * e], t6[2 * e + 1]};
      }
      if (lane == 0) *reinterpret_cast<f32x2*>(&s_buf[(seg * NW + wave) * PST + DH]) = f32x2{m, lw};
      l = 0.f;
#pragma unroll
      for (int i = 0; i < 12; ++i) acc[i] = f32x2{0.f, 0.f};
      if (seg != 0) {
        int c = count_in_rtn(seg == 1 ? &s_ecnt : &s_fcnt);
        MEMFENCE();
        auto merged = [&](int s) {
          f32x4 pw[NW];
#pragma unroll
          for (int w = 0; w < NW; ++w) pw[w] = reinterpret_cast<const f32x4*>(s_buf)[(s * NW + w) * (PST / 4) + d4];
          f32x4 o = ((pw[0] + pw[1]) + (pw[2] + pw[3])) + ((pw[4] + pw[5]) + (pw[6] + pw[7]));
          if (lane >= DH / 4) o = f32x4{pw[0][0], o[1], 1.f, 1.f};
          return u32x4{nz_bits(o[0]), nz_bits(o[1]), nz_bits(o[2]), nz_bits(o[3])};
        };
        u32x4 rec = merged(seg);
        asm volatile("s_waitcnt lgkmcnt(0)" : "+v"(rec), "+v"(c) :: "memory");
        if (__builtin_amdgcn_readfirstlane(c) == NW - 1) {
          __builtin_amdgcn_raw_buffer_store_b128(rec, rs_ws, rofs, seg * (NWG * REC * 4), AUX_SC1);
          if (seg == 1) __builtin_amdgcn_raw_buffer_store_b128(merged(0), rs_ws, rofs, 0, AUX_SC1);
          if (seg == 2) { MEMFENCE(); lds_put(&s_fcnt, NW + 1); }
          if (seg == 1) {
            DRAIN();
            __builtin_amdgcn_raw_buffer_store_b32(1u, rs_ws, lane == 0 ? ef_ofs(wid) : OOB, 0, AUX_SC1);
          }
        }
      }
      MEMFENCE();
    }
  }
  __builtin_amdgcn_s_setprio(0);

  {
    asm volatile("" : "+s"(wave), "+s"(bid), "+v"(lane));
    r = lane >> 3; j = lane & 7; mg = lane >> 4; mc = lane & 15;
    const int w = wave;
    const int xcd = bid & 7, ri = bid >> 3;
    const bool isW = ri < 15, isG = !isW && ri < 27;
    const int tt = isW ? xcd * 15 + ri : (isG ? xcd * 12 + ri - 15 : xcd * 5 + ri - 27);
    const int ks1 = tt / W_CT, ct1 = tt - ks1 * W_CT;
    typedef const __attribute__((address_space(1))) char* gptr;
    auto behind_stream = [&](int nsl, int cnt) {
      for (unsigned spin = 0; spin < (SPIN_CAP << 4) && lds_get(&s_icnt) < cnt; ++spin) __builtin_amdgcn_s_sleep(1);
      for (int i = 0; i < nsl && lds_get(&s_fcnt) <= NW; ++i) __builtin_amdgcn_s_sleep(3);
    };
    u32x4 fr[16];
    u32x2 raw[32];
    auto weights = [&](const bf16* src, int skip) {
      gptr wb = (gptr)src;
#pragma unroll
      for (int e = 0; e < 32; ++e) {
        raw[e] = __builtin_nontemporal_load((const __attribute__((address_space(1))) u32x2*)wb);
        wb += ((e & 7) == 7 ? skip : 1) * (DM * 2);
        asm volatile("" : "+v"(wb));
      }
      MEMFENCE();
    };
    auto frags = [&]() {
#pragma unroll
      for (int st = 0; st < 4; ++st) {
        PIN8(raw, st * 8);
#pragma unroll
        for (int i = 0; i < 4; ++i) {
          const u32x2 r0 = raw[st * 8 + 2 * i], r1 = raw[st * 8 + 2 * i + 1];
          fr[st * 4 + 0][i] = pk_lo(r0[0], r1[0]); fr[st * 4 + 1][i] = pk_hi(r0[0], r1[0]);
          fr[st * 4 + 2][i] = pk_lo(r0[1], r1[1]); fr[st * 4 + 3][i] = pk_hi(r0[1], r1[1]);
        }
        PIN4(fr, st * 4);
        MEMFENCE();
      }
    };
    if (isW) {
      if (w >= 4) return;
      int a_put = w * (16 * WA) + mg * 4 * WA + mc * 8, a_get = w * (16 * WA / 16) + mc * (WA / 16) + mg, a_red = w * 64 + lane;
      int o_res = (WS_SLAB + ks1 * SLS + mg * DM + ct1 * 64 + mc * 4) * 4;
      {
        int offp[2], sg0[2];
        bool er[2];
#pragma unroll
        for (int hf = 0; hf < 2; ++hf) {
          const int row = ks1 * K3_ROWS + w * 128 + hf * 64, hh = row / DH;
          sg0[hf] = (mg * NH + hh) * NSEG;
          offp[hf] = (WS_PO + sg0[hf] * REC + row - hh * DH + mc * 4) * 4;
          er[hf] = sg0[hf] + NSEG <= 2 * NWG;
        }
        const int sgm = (mc < 8 ? sg0[0] : sg0[1]) + j, offm = j < NSEG ? (WS_PO + sgm * REC + DH) * 4 : OOB;
        for (unsigned spin = 0; spin < (SPIN_CAP << 4) && lds_get(&s_ecnt) < NW; ++spin) __builtin_amdgcn_s_sleep(4);
        for (unsigned spin = 0; spin < SPIN_CAP; ++spin) {
          AGAIN();
          const bool fl = j < NSEG && sgm < 2 * NWG;
          if (__all(!fl || __builtin_amdgcn_raw_buffer_load_b32(rs_ws, fl ? ef_ofs(sgm & (NWG - 1)) : OOB, 0, AUX_SC1) != 0u)) break;
          __builtin_amdgcn_s_sleep(8);
        }
        auto look = [&](u32x4 (&d)[2 * NSEG], bool h0, bool h1) {
          const int o0_ = h0 ? offp[0] : OOB, o1_ = h1 ? offp[1] : OOB;
#pragma unroll
          for (int k = 0; k < 2 * NSEG; ++k) d[k] = __builtin_amdgcn_raw_buffer_load_b128(rs_ws, k < NSEG ? o0_ : o1_, (k % NSEG) * REC * 4, AUX_SC1);
          MEMFENCE();
        };
        auto wsum = [&](float wt, const u32x4 (&d)[2 * NSEG], f32x4& a0, f32x4& a1) {
          a0 += dpp<DPP_BCAST + 0>(wt) * __builtin_bit_cast(f32x4, d[0]); a1 += dpp<DPP_BCAST + 8>(wt) * __builtin_bit_cast(f32x4, d[6]);
          a0 += dpp<DPP_BCAST + 1>(wt) * __builtin_bit_cast(f32x4, d[1]); a1 += dpp<DPP_BCAST + 9>(wt) * __builtin_bit_cast(f32x4, d[7]);
          a0 += dpp<DPP_BCAST + 2>(wt) * __builtin_bit_cast(f32x4, d[2]); a1 += dpp<DPP_BCAST + 10>(wt) * __builtin_bit_cast(f32x4, d[8]);
          a0 += dpp<DPP_BCAST + 3>(wt) * __builtin_bit_cast(f32x4, d[3]); a1 += dpp<DPP_BCAST + 11>(wt) * __builtin_bit_cast(f32x4, d[9]);
          a0 += dpp<DPP_BCAST + 4>(wt) * __builtin_bit_cast(f32x4, d[4]); a1 += dpp<DPP_BCAST + 12>(wt) * __builtin_bit_cast(f32x4, d[10]);
          a0 += dpp<DPP_BCAST + 5>(wt) * __builtin_bit_cast(f32x4, d[5]); a1 += dpp<DPP_BCAST + 13>(wt) * __builtin_bit_cast(f32x4, d[11]);
        };
        u32x4 pe[2 * NSEG], pl[2 * NSEG];
        behind_stream(6, NW - 1);
        weights(wo + (size_t)(ks1 * K3_ROWS + w * 128 + mg * 8) * DM + ct1 * 64 + mc * 4, 25);
        look(pe, er[0], er[1]);
        for (unsigned sp2 = 0; sp2 < (SPIN_CAP << 4) && lds_get(&s_fcnt) < NW; ++sp2) __builtin_amdgcn_s_sleep(1);
        f32x4 o0, o1;
        asm volatile("" : "+v"(a_put), "+v"(a_get), "+v"(a_red), "+v"(o_res));
        u32x2 ml = __builtin_amdgcn_raw_buffer_load_b64(rs_ws, offm, 0, AUX_SC1);
        look(pl, !er[0], !er[1]);
        frags();
        MEMFENCE();
#pragma unroll 1
        for (unsigned spin = 0; spin < SPIN_CAP; ++spin) {
          AGAIN();
          const float mm = j < NSEG ? __uint_as_float(ml[0]) : -INFINITY;
          float M = fmaxf(mm, dpp<DPP_XOR1>(mm)); M = fmaxf(M, dpp<DPP_XOR2>(M)); M = fmaxf(M, dpp<DPP_HMIRROR>(M));
          const float e = __builtin_amdgcn_exp2f(mm - M);
          float L = e * __uint_as_float(ml[1]);
          L += dpp<DPP_XOR1>(L); L += dpp<DPP_XOR2>(L); L += dpp<DPP_HMIRROR>(L);
          const float wt = e * __builtin_amdgcn_rcpf(L);
          o0 = f32x4{0.f, 0.f, 0.f, 0.f}; o1 = o0;
          wsum(wt, pe, o0, o1);
          unsigned m0 = er[0], m1 = er[1], a0 = ~0u, a1 = ~0u;
          const bool okm = j >= NSEG || min(ml[0], ml[1]) != 0u;
          asm volatile("" : "+v"(o0), "+v"(o1));
          MEMFENCE();
          wsum(wt, pl, o0, o1);
          asm volatile("" : "+v"(o0), "+v"(o1));
#pragma unroll
          for (int k = 0; k < NSEG; ++k) { a0 = min(a0, min4(pl[k])); a1 = min(a1, min4(pl[NSEG + k])); }
          if (__all(okm && (a0 | m0) != 0u && (a1 | m1) != 0u)) break;
          if (spin >= 8) __builtin_amdgcn_s_sleep(4);
          ml = __builtin_amdgcn_raw_buffer_load_b64(rs_ws, offm, 0, AUX_SC1);
          look(pl, !er[0], !er[1]);
        }
        put_terms4(s_A + a_put, WA, o0);
        put_terms4(s_A + a_put + 128, WA, o1);
      }
      __builtin_amdgcn_fence(__ATOMIC_RELEASE, "wavefront"); __builtin_amdgcn_wave_barrier(); __builtin_amdgcn_fence(__ATOMIC_ACQUIRE, "wavefront");
      f32x4 ac[4];
#pragma unroll
      for (int t = 0; t < 4; ++t) ac[t] = f32x4{0.f, 0.f, 0.f, 0.f};
      u32x4 av[4];
#pragma unroll
      for (int st = 0; st < 4; ++st) av[st] = reinterpret_cast<const u32x4*>(s_A)[a_get + st * 4];
      MEMFENCE();
#pragma unroll
      for (int st = 0; st < 4; ++st)
#pragma unroll
        for (int t = 0; t < 4; ++t) ac[t] = mfma16(av[st], fr[st * 4 + t], ac[t]);
      reinterpret_cast<f32x4*>(s_red)[a_red] = f32x4{sum3(ac[0]), sum3(ac[1]), sum3(ac[2]), sum3(ac[3])};
      __syncthreads();
      if (w == 0) {
        const f32x4* const red = reinterpret_cast<const f32x4*>(s_red) + a_red;
        const f32x4 s0 = (red[0] + red[64]) + (red[128] + red[192]);
        __builtin_amdgcn_raw_buffer_store_b128(u32x4{nz_bits(s0[0]), nz_bits(s0[1]), nz_bits(s0[2]), nz_bits(s0[3])}, rs_ws, o_res, 0, AUX_SC1);
      }
    } else if (isG) {
      const int gb = tt & 3, gt = tt >> 2;
      int o_res = (WS_ACT + (gb * (FF / 64) + gt) * 96 + (lane & 1) * 32 + (lane >> 1)) * 4;
      asm volatile("" : "+v"(o_res));
      if (w < 5) {
        const int hcol = w * 128 + (lane & 31) * 4;
        uint2 xx = {0u, 0u};
        u32x4 fg[32];
        {
          u32x4 rg[32];
          if (lane < 32) xx = *reinterpret_cast<const uint2*>(x + gb * DM + hcol);
          behind_stream(5, NW);
          gptr wb = (gptr)((mc < 8 ? wg : wu) + (size_t)(w * 128 + mg * 8) * FF + gt * 64 + (mc & 7) * 8);
#pragma unroll
          for (int e = 0; e < 32; ++e) {
            rg[e] = __builtin_nontemporal_load((const __attribute__((address_space(1))) u32x4*)wb);
            wb += ((e & 7) == 7 ? 25 : 1) * (FF * 2);
            asm volatile("" : "+v"(wb));
          }
          MEMFENCE();
#pragma unroll
          for (int st = 0; st < 4; ++st) {
            PIN8(rg, st * 8);
#pragma unroll
            for (int c = 0; c < 4; ++c)
#pragma unroll
              for (int i = 0; i < 4; ++i) {
                fg[st * 8 + 2 * c][i] = pk_lo(rg[st * 8 + 2 * i][c], rg[st * 8 + 2 * i + 1][c]);
                fg[st * 8 + 2 * c + 1][i] = pk_hi(rg[st * 8 + 2 * i][c], rg[st * 8 + 2 * i + 1][c]);
              }
            PIN8(fg, st * 8);
            MEMFENCE();
          }
        }
        {
          f32x4 cs[8];
#pragma unroll
          for (int t = 0; t < 8; ++t) cs[t] = f32x4{0.f, 0.f, 0.f, 0.f};
#pragma unroll
          for (int e = 0; e < 32; ++e) cs[e & 7] = mfma16(u32x4{0x3f803f80u, 0x3f803f80u, 0x3f803f80u, 0x3f803f80u}, fg[e], cs[e & 7]);
          if (lane < 16) {
            *reinterpret_cast<f32x4*>(&s_red[w * 256 + 128 + lane * 8]) = f32x4{cs[0][0], cs[1][0], cs[2][0], cs[3][0]};
            *reinterpret_cast<f32x4*>(&s_red[w * 256 + 128 + lane * 8 + 4]) = f32x4{cs[4][0], cs[5][0], cs[6][0], cs[7][0]};
          }
          count_in(&s_ccnt);
        }
        f32x4 hv = f32x4{bf_lo(xx.x), bf_hi(xx.x), bf_lo(xx.y), bf_hi(xx.y)};
        int a_h = hcol + (lane >> 5) * 2, a_put = w * (4 * WA) + (lane & 31) * 8 + (lane >> 5) * 4, a_get = w * (4 * WA / 16) + min(mc, 2) * (WA / 16) + mg, a_red = w * 64 + mc * 2;
        asm volatile("" : "+v"(hv), "+v"(a_h), "+v"(a_put), "+v"(a_get), "+v"(a_red));
        const int off = (WS_SLAB + (lane >> 5) * (K3_KS / 2) * SLS + gb * DM + hcol) * 4;
        u32x4 sl[K3_KS / 2];
        for (unsigned spin = 0; spin < SPIN_CAP; ++spin) {
          AGAIN();
#pragma unroll
          for (int ks = 0; ks < K3_KS / 2; ++ks) sl[ks] = __builtin_amdgcn_raw_buffer_load_b128(rs_ws, off, ks * (SLS * 4), AUX_SC1);
          if (__all(min(min(min(min4(sl[0]), min4(sl[1])), min(min4(sl[2]), min4(sl[3]))), min(min4(sl[4]), min4(sl[5]))) != 0u)) break;
          __builtin_amdgcn_s_sleep(2);
        }
        hv = ((hv + __builtin_bit_cast(f32x4, sl[0])) + (__builtin_bit_cast(f32x4, sl[1]) + __builtin_bit_cast(f32x4, sl[2]))) +
             ((__builtin_bit_cast(f32x4, sl[3]) + __builtin_bit_cast(f32x4, sl[4])) + __builtin_bit_cast(f32x4, sl[5]));
        const float h0 = tsum32(hv[0], hv[2]), h1 = tsum32(hv[1], hv[3]);
        *reinterpret_cast<f32x2*>(&s_h[a_h]) = f32x2{h0, h1};
        count_in(&s_hcnt);
        {
          unsigned t0[3], t1[3];
          split3(h0, t0[0], t0[1], t0[2]); split3(h1, t1[0], t1[1], t1[2]);
#pragma unroll
          for (int k = 0; k < 3; ++k) *reinterpret_cast<unsigned*>(s_A + a_put + k * WA) = pk_hi(t0[k], t1[k]);
        }
        __builtin_amdgcn_fence(__ATOMIC_RELEASE, "wavefront"); __builtin_amdgcn_wave_barrier(); __builtin_amdgcn_fence(__ATOMIC_ACQUIRE, "wavefront");
        f32x4 ac[8];
#pragma unroll
        for (int t = 0; t < 8; ++t) ac[t] = f32x4{0.f, 0.f, 0.f, 0.f};
        u32x4 av[4];
#pragma unroll
        for (int st = 0; st < 4; ++st) av[st] = reinterpret_cast<const u32x4*>(s_A)[a_get + st * 4];
        MEMFENCE();
#pragma unroll
        for (int st = 0; st < 4; ++st)
#pragma unroll
          for (int t = 0; t < 8; ++t) ac[t] = mfma16(av[st], fg[st * 8 + t], ac[t]);
        if (lane < 16) {
          reinterpret_cast<f32x4*>(s_red)[a_red] = f32x4{sum3(ac[0]), sum3(ac[1]), sum3(ac[2]), sum3(ac[3])};
          reinterpret_cast<f32x4*>(s_red)[a_red + 1] = f32x4{sum3(ac[4]), sum3(ac[5]), sum3(ac[6]), sum3(ac[7])};
        }
      } else if (w == NW - 1) {
        unsigned wr2[5];
#pragma unroll
        for (int i = 0; i < 5; ++i) wr2[i] = reinterpret_cast<const unsigned*>(wr)[lane + 64 * i];
        PIN4(wr2, 0); asm volatile("" : "+v"(wr2[4]));
        for (unsigned spin = 0; spin < (SPIN_CAP << 4) && lds_get(&s_ccnt) < 5; ++spin) __builtin_amdgcn_s_sleep(1);
        __builtin_amdgcn_fence(__ATOMIC_ACQUIRE, "workgroup");
        float cg = 0.f, cu = 0.f;
#pragma unroll
        for (int k = 0; k < 5; ++k) { cg += s_red[k * 256 + 128 + lane]; cu += s_red[k * 256 + 192 + lane]; }
        asm volatile("" : "+v"(cg), "+v"(cu));
        for (unsigned spin = 0; spin < (SPIN_CAP << 6) && lds_get(&s_hcnt) < 5; ++spin) {}
        __builtin_amdgcn_fence(__ATOMIC_ACQUIRE, "workgroup");
        const float piv = s_h[0];
        float hv[10], sm = 0.f, sq = 0.f;
#pragma unroll
        for (int i = 0; i < 5; ++i) {
          const float2 t2 = *reinterpret_cast<const float2*>(&s_h[2 * lane + 128 * i]);
          hv[2 * i] = t2.x; hv[2 * i + 1] = t2.y;
        }
        PIN8(hv, 0); asm volatile("" : "+v"(hv[8]), "+v"(hv[9]));
#pragma unroll
        for (int i = 0; i < 10; ++i) { hv[i] -= piv; sm += hv[i]; sq = fmaf(hv[i], hv[i], sq); }
        float t2 = row16_sum(tsum32(sm, sq));
        t2 = tsum16(t2, t2);
        const float dm = __int_as_float(__builtin_amdgcn_readlane(__float_as_int(t2), 0)) * (1.0f / DM);
        const float var = fmaxf(fmaf(-dm, dm, __int_as_float(__builtin_amdgcn_readlane(__float_as_int(t2), 32)) * (1.0f / DM)), 0.f);
        const float rstd = __builtin_amdgcn_rsqf(var + LN_EPS);
        s_red[5 * 256 + lane] = -(piv + dm) * cg; s_red[5 * 256 + 64 + lane] = -(piv + dm) * cu; s_red[5 * 256 + 128 + lane] = rstd;
        __syncthreads();
        if (gt < DM / 32) {
          float rr = 0.f;
#pragma unroll
          for (int i = 0; i < 5; ++i) { rr = fmaf((hv[2 * i] - dm) * rstd, bf_lo(wr2[i]), rr); rr = fmaf((hv[2 * i + 1] - dm) * rstd, bf_hi(wr2[i]), rr); }
          rr = wave_sum(rr);
          const float hh = s_h[gt * 32 + (lane & 31)];
          __builtin_amdgcn_raw_buffer_store_b32(nz_bits(lane >= 32 ? 1.0f / (1.0f + __expf(-rr)) : hh + (hh - (piv + dm)) * rstd), rs_ws,
                                                (WS_BASE + (gb * (DM / 32) + gt) * 64 + lane) * 4, 0, AUX_SC1);
        }
        return;
      } else {
        return;
      }
      __syncthreads();
      if (w == 0) {
        f32x2 pr[6];
#pragma unroll
        for (int k = 0; k < 6; ++k) pr[k] = f32x2{s_red[k * 256 + lane], s_red[k * 256 + 64 + lane]};
        float rstd = s_red[5 * 256 + 128 + lane];
        PIN4(pr, 0); asm volatile("" : "+v"(pr[4]), "+v"(pr[5]), "+v"(rstd));
        const f32x2 gu = (((pr[0] + pr[1]) + (pr[2] + pr[3])) + (pr[4] + pr[5])) * rstd;
        const float gg = gu[0], uu = gu[1];
        unsigned t[3], wd2[3];
        split3(gg * __builtin_amdgcn_rcpf(1.0f + __expf(-gg)) * uu, t[0], t[1], t[2]);
#pragma unroll
        for (int k = 0; k < 3; ++k) {
          const unsigned o = __float_as_uint(dpp<DPP_XOR1>(__uint_as_float(t[k])));
          wd2[k] = nz_bits(__uint_as_float((lane & 1) ? pk_hi(o, t[k]) : pk_hi(t[k], o)));
        }
        __builtin_amdgcn_raw_buffer_store_b32((lane & 1) ? wd2[1] : wd2[0], rs_ws, o_res, 0, AUX_SC1);
        __builtin_amdgcn_raw_buffer_store_b32(wd2[2], rs_ws, (lane & 1) ? OOB : o_res, 256, AUX_SC1);
      }
    } else {
      if (w == 7) return;
      const int bp = tt & 1, ct = tt >> 1;
      for (unsigned spin = 0; spin < (SPIN_CAP << 4) && lds_get(&s_fcnt) < NW; ++spin) __builtin_amdgcn_s_sleep(1);
      if (w == 6) {
        const int bb = lane >> 5, f = lane & 31, offb = (WS_BASE + ((bp * 2 + bb) * (DM / 32) + ct) * 64 + f) * 4;
        const __amdgpu_buffer_rsrc_t rs_out = __builtin_amdgcn_make_buffer_rsrc(out, 0, NB * DM * 2, 0x00020000);
        int o_res = ((bp * 2 + bb) * DM + ct * 32 + f) * 2;
        unsigned bs = 0u, sg = 0u;
        __builtin_amdgcn_s_sleep(100);
        for (unsigned spin = 0; spin < SPIN_CAP; ++spin) {
          AGAIN();
          bs = __builtin_amdgcn_raw_buffer_load_b32(rs_ws, offb, 0, AUX_SC1); sg = __builtin_amdgcn_raw_buffer_load_b32(rs_ws, offb, 128, AUX_SC1);
          if (__all(min(bs, sg) != 0u)) break;
          __builtin_amdgcn_s_sleep(2);
        }
        asm volatile("" : "+v"(o_res));
        __syncthreads();
        float pr[12];
#pragma unroll
        for (int k = 0; k < 12; ++k) pr[k] = s_red[(k >> 1) * 256 + (k & 1) * 64 + lane];
        PIN8(pr, 0); PIN4(pr, 8);
        const float y = (((pr[0] + pr[1]) + (pr[2] + pr[3])) + ((pr[4] + pr[5]) + (pr[6] + pr[7]))) + ((pr[8] + pr[9]) + (pr[10] + pr[11]));
        const bf16 ov = __float2bfloat16(fmaf(y, __uint_as_float(sg), __uint_as_float(bs)));
        __builtin_amdgcn_raw_buffer_store_b16(__builtin_bit_cast(unsigned short, ov), rs_out, o_res, 0, 0);
        return;
      }
      __builtin_amdgcn_s_sleep(8);
      weights(wd + (size_t)(w * 256 + (mc >> 3) * 32 + mg * 8) * DM + ct * 32 + (mc & 7) * 4, 57);
      frags();
      int a_red = w * 64 + mg * 8 + (mc & 7);
      asm volatile("" : "+v"(a_red));
      const int off = (WS_ACT + (((bp * 2 + ((mc >> 2) & 1)) * (FF / 64) + w * 4) * 3 + min(mc & 3, 2)) * 32 + (mc >> 3) * 16 + mg * 4) * 4;
      u32x4 av[4];
      for (unsigned spin = 0; spin < SPIN_CAP; ++spin) {
        AGAIN();
#pragma unroll
        for (int st = 0; st < 4; ++st) av[st] = __builtin_amdgcn_raw_buffer_load_b128(rs_ws, off, st * 384, AUX_SC1);
        if (__all(min(min(min4(av[0]), min4(av[1])), min(min4(av[2]), min4(av[3]))) != 0u)) break;
        __builtin_amdgcn_s_sleep(2);
      }
      f32x4 ac[4];
#pragma unroll
      for (int t = 0; t < 4; ++t) ac[t] = f32x4{0.f, 0.f, 0.f, 0.f};
#pragma unroll
      for (int st = 0; st < 4; ++st)
#pragma unroll
        for (int t = 0; t < 4; ++t) ac[t] = mfma16(av[st], fr[st * 4 + t], ac[t]);
      if ((mc >> 3) == (mg >> 1)) reinterpret_cast<f32x4*>(s_red)[a_red] = f32x4{sum3(ac[0]), sum3(ac[1]), sum3(ac[2]), sum3(ac[3])};
      __syncthreads();
    }
  }
}

extern "C" void kernel_launch(void* const* d_in, const int* in_sizes, int n_in, void* d_out, int out_size, void* d_ws,
                              size_t ws_size, hipStream_t stream) {
  fused_kernel<<<NWG, NW * 64, 0, stream>>>((const bf16*)d_in[0], (const bf16*)d_in[1], (const bf16*)d_in[2], (const bf16*)d_in[3],
                                            (const bf16*)d_in[4], (const bf16*)d_in[6], (const bf16*)d_in[7], (const bf16*)d_in[8],
                                            (const bf16*)d_in[9], (float*)d_ws, (bf16*)d_out);
}
```

```cpp
#include <hip/hip_runtime.h>
#include <hip/hip_bf16.h>
#include <stdint.h>

typedef __hip_bfloat16 bf16;
using u32x4 = __attribute__((ext_vector_type(4))) unsigned;
using u32x2 = __attribute__((ext_vector_type(2))) unsigned;
using f32x4 = __attribute__((ext_vector_type(4))) float;
using f32x2 = __attribute__((ext_vector_type(2))) float;

constexpr int NB = 4, DM = 640, NH = 32, SK = 8192, DH = 192, HD = NH * DH, FF = 1536;
constexpr int NWG = 256, NSEG = 6, NWIN = NB * NH * NSEG / NWG;
constexpr float LN_EPS = 1e-5f;
constexpr float QSCALE = 0.07216878364870322f * 1.4426950408889634f;

constexpr int WS_HP = 0;
constexpr int REC = DH + 32;
constexpr int WS_PO = WS_HP + 2 * NWG;
constexpr int WS_Z0 = WS_PO + 2 * NWG * REC;
constexpr int K3_KS = 12, K3_ROWS = HD / K3_KS;
constexpr int WS_SLAB = WS_PO + NB * NH * NSEG * REC;
constexpr int SLS = NB * DM;
constexpr int WS_ACT = WS_SLAB + K3_KS * SLS;
constexpr int WS_BASE = WS_ACT + NB * FF * 3 / 2;
constexpr int ZPW = 416;
constexpr int WS_QI = WS_Z0 + NWG * ZPW;
constexpr int LSTR = 64;
constexpr int WS_REP = WS_QI + NB * NH * DH * 2;
constexpr int WS_HC = WS_REP + 256 * LSTR;
constexpr int WS_END = WS_HC + NWG * LSTR;
static_assert(WS_BASE + NB * DM * 2 <= WS_QI && WS_Z0 % 64 == 0 && WS_PO % 32 == 0 && ZPW % 32 == 0 && NWIN == 3, "layout");
__device__ __forceinline__ int ef_ofs(int i) { return (WS_PO + (2 * NWG + i) * REC + DH + 2) * 4; }
constexpr int OOB = 0x7ffffff0;

__device__ __forceinline__ float bf_lo(unsigned u) { return __uint_as_float(u << 16); }
__device__ __forceinline__ float bf_hi(unsigned u) { return __uint_as_float(u & 0xffff0000u); }
__device__ __forceinline__ f32x2 bf_pair(unsigned u) { return f32x2{bf_lo(u), bf_hi(u)}; }
__device__ __forceinline__ f32x2 fma2(float a, f32x2 b, f32x2 c) { return __builtin_elementwise_fma(f32x2{a, a}, b, c); }
__device__ __forceinline__ f32x2 fma2(f32x2 a, f32x2 b, f32x2 c) { return __builtin_elementwise_fma(a, b, c); }

template <int CTRL> __device__ __forceinline__ float dpp(float x) {
  return __builtin_bit_cast(float, __builtin_amdgcn_mov_dpp(__builtin_bit_cast(int, x), CTRL, 0xf, 0xf, true));
}
constexpr int DPP_XOR1 = 0xB1, DPP_XOR2 = 0x4E, DPP_HMIRROR = 0x141;

__device__ __forceinline__ float sum_x16_32(float x) {
  auto s = __builtin_amdgcn_permlane16_swap(__float_as_uint(x), __float_as_uint(x), false, false);
  x = __uint_as_float(s[0]) + __uint_as_float(s[1]);
  auto t = __builtin_amdgcn_permlane32_swap(__float_as_uint(x), __float_as_uint(x), false, false);
  return __uint_as_float(t[0]) + __uint_as_float(t[1]);
}
__device__ __forceinline__ float max_x16_32(float x) {
  auto s = __builtin_amdgcn_permlane16_swap(__float_as_uint(x), __float_as_uint(x), false, false);
  x = fmaxf(__uint_as_float(s[0]), __uint_as_float(s[1]));
  auto t = __builtin_amdgcn_permlane32_swap(__float_as_uint(x), __float_as_uint(x), false, false);
  return fmaxf(__uint_as_float(t[0]), __uint_as_float(t[1]));
}
__device__ __forceinline__ float tsum32(float a, float b) {
  auto t = __builtin_amdgcn_permlane32_swap(__float_as_uint(a), __float_as_uint(b), false, false);
  return __uint_as_float(t[0]) + __uint_as_float(t[1]);
}
__device__ __forceinline__ float tsum16(float a, float b) {
  auto t = __builtin_amdgcn_permlane16_swap(__float_as_uint(a), __float_as_uint(b), false, false);
  return __uint_as_float(t[0]) + __uint_as_float(t[1]);
}
constexpr int DPP_ROR8 = 0x128, DPP_BCAST = 0x150;
__device__ __forceinline__ float sum_r(float v) { v += dpp<DPP_ROR8>(v); return sum_x16_32(v); }
__device__ __forceinline__ float max_r(float v) { v = fmaxf(v, dpp<DPP_ROR8>(v)); return max_x16_32(v); }
__device__ __forceinline__ float wave_sum(float v) {
  v += dpp<DPP_XOR1>(v); v += dpp<DPP_XOR2>(v); v += dpp<DPP_HMIRROR>(v);
  return sum_r(v);
}

__device__ __forceinline__ float row16_sum(float v) {
  v += dpp<DPP_XOR1>(v); v += dpp<DPP_XOR2>(v); v += dpp<DPP_HMIRROR>(v);
  return v + dpp<DPP_ROR8>(v);
}

struct Slot { u32x4 a[3]; u32x4 b[3]; };

#define PIN8(a, o) asm volatile("" : "+v"((a)[(o) + 0]), "+v"((a)[(o) + 1]), "+v"((a)[(o) + 2]), "+v"((a)[(o) + 3]), \
                                       "+v"((a)[(o) + 4]), "+v"((a)[(o) + 5]), "+v"((a)[(o) + 6]), "+v"((a)[(o) + 7]))
#define PIN4(a, o) asm volatile("" : "+v"((a)[(o) + 0]), "+v"((a)[(o) + 1]), "+v"((a)[(o) + 2]), "+v"((a)[(o) + 3]))
#define MEMFENCE() do { asm volatile("" ::: "memory"); __builtin_amdgcn_sched_barrier(0); } while (0)
#define AGAIN() asm volatile("" ::: "memory")
#define DRAIN() asm volatile("s_waitcnt vmcnt(0)" ::: "memory")

__device__ __forceinline__ void k_tile(const u32x4 (&k)[3], const f32x2 (&q)[12], float& gmx, float* dst, int j) {
  f32x2 s0 = {0.f, 0.f}, s1 = s0, s2 = s0;
#pragma unroll
  for (int d = 0; d < 4; ++d) {
    s0 = fma2(bf_pair(k[0][d]), q[d], s0);
    s1 = fma2(bf_pair(k[1][d]), q[4 + d], s1);
    s2 = fma2(bf_pair(k[2][d]), q[8 + d], s2);
  }
  s0 = (s0 + s1) + s2;
  float s = s0[0] + s0[1];
  s += dpp<DPP_XOR1>(s);
  s += dpp<DPP_XOR2>(s);
  s += dpp<DPP_HMIRROR>(s);
  gmx = fmaxf(gmx, s);
  asm volatile("" : "+v"(gmx));
  if (j == 0) *dst = s;
}
__device__ __forceinline__ void v_tile(u32x4 (&v)[3], float p, f32x2 (&acc)[12]) {
#pragma unroll
  for (int i = 0; i < 3; ++i) {
    asm volatile("" : "+v"(v[i]), "+v"(p));
#pragma unroll
    for (int d = 0; d < 4; ++d) acc[i * 4 + d] = fma2(p, bf_pair(v[i][d]), acc[i * 4 + d]);
    PIN4(acc, i * 4);
  }
}

constexpr int PTAU = 660;
constexpr int POFF = 140, FFWD = 112;
constexpr int LTGT = 230, LCAP = 600;
constexpr int DOWN = 30, AFROM = 3, EFROM = 12, SLACK = 10;
constexpr int QLOOK = 780;
constexpr int CODE_LINES = 300;
constexpr int AUX_SC1 = 16, AUX_NT = 2;
constexpr unsigned SPIN_CAP = 1u << 17;
__device__ __forceinline__ unsigned nz_bits(float v) { const unsigned u = __float_as_uint(v); return u == 0u ? 0x80000000u : u; }
__device__ __forceinline__ void lds_put(int* p, int v) { __hip_atomic_store(p, v, __ATOMIC_RELAXED, __HIP_MEMORY_SCOPE_WORKGROUP); }
__device__ __forceinline__ int lds_get(int* p) { return __hip_atomic_load(p, __ATOMIC_RELAXED, __HIP_MEMORY_SCOPE_WORKGROUP); }
__device__ __forceinline__ void count_in(int* p) {
  unsigned long long keep;
  asm volatile("s_mov_b64 %0, exec\n\ts_mov_b64 exec, 1\n\tds_add_u32 %1, %2\n\ts_mov_b64 exec, %0"
               : "=&s"(keep) : "v"((unsigned)(size_t)(__attribute__((address_space(3))) int*)p), "v"(1) : "memory");
}
__device__ __forceinline__ int count_in_rtn(int* p) {
  unsigned long long keep;
  int c;
  asm volatile("s_mov_b64 %0, exec\n\ts_mov_b64 exec, 1\n\tds_add_rtn_u32 %1, %2, %3\n\ts_mov_b64 exec, %0"
               : "=&s"(keep), "=&v"(c) : "v"((unsigned)(size_t)(__attribute__((address_space(3))) int*)p), "v"(1) : "memory");
  return c;
}
using bf16x8 = __attribute__((ext_vector_type(8))) short;
__device__ __forceinline__ unsigned pk_lo(unsigned a, unsigned b) { return __builtin_amdgcn_perm(b, a, 0x05040100u); }
__device__ __forceinline__ unsigned pk_hi(unsigned a, unsigned b) { return __builtin_amdgcn_perm(b, a, 0x07060302u); }
__device__ __forceinline__ void split3(float v, unsigned& t0, unsigned& t1, unsigned& t2) {
  t0 = __float_as_uint(v) & 0xffff0000u;
  const float r1 = v - __uint_as_float(t0);
  t1 = __float_as_uint(r1) & 0xffff0000u;
  t2 = __float_as_uint(r1 - __uint_as_float(t1));
}
__device__ __forceinline__ f32x4 mfma16(u32x4 a, u32x4 b, f32x4 c) {
  return __builtin_amdgcn_mfma_f32_16x16x32_bf16(__builtin_bit_cast(bf16x8, a), __builtin_bit_cast(bf16x8, b), c, 0, 0, 0);
}
__device__ __forceinline__ void put_terms4(char* img, int rowbytes, f32x4 v) {
  unsigned t[3][4];
#pragma unroll
  for (int e = 0; e < 4; ++e) split3(v[e], t[0][e], t[1][e], t[2][e]);
#pragma unroll
  for (int k = 0; k < 3; ++k) *reinterpret_cast<u32x2*>(img + k * rowbytes) = u32x2{pk_hi(t[k][0], t[k][1]), pk_hi(t[k][2], t[k][3])};
}
__device__ __forceinline__ unsigned min4(u32x4 v) { return min(min(v[0], v[1]), min(v[2], v[3])); }

__device__ __forceinline__ float sum3(f32x4 v) { return (v[0] + v[1]) + v[2]; }

constexpr int NW = 8, NBUF = 4, NGRP = 64, NT = NGRP / NW;
constexpr int NQP = NH * DH / 64, NQW = 5, QW0 = 2, QST = DM / 64 / NQW;
constexpr int SVC = NW - 1;
constexpr int W_CT = DM / 64, G_TASKS = NB * (FF / 64), W_TASKS = W_CT * K3_KS, D_TASKS = 2 * (DM / 32);
static_assert(G_TASKS + W_TASKS + D_TASKS == NWG && NW == 8 && K3_ROWS == 4 * 128 && NBUF == 4 && SK / 64 == 128, "shape");
__device__ __forceinline__ unsigned fresh_tag(unsigned tags) {
  unsigned t = __builtin_amdgcn_readfirstlane(tags);
  do ++t; while (__any(tags == t));
  return t;
}

__global__ void __launch_bounds__(NW * 64, 2) fused_kernel(const bf16* __restrict__ x, const bf16* __restrict__ kc, const bf16* __restrict__ vc,
                                                        const bf16* __restrict__ wq, const bf16* __restrict__ wo, const bf16* __restrict__ wr,
                                                        const bf16* __restrict__ wg, const bf16* __restrict__ wu, const bf16* __restrict__ wd,
                                                        float* ws, bf16* __restrict__ out) {
  constexpr int PST = DH + 4;
  constexpr int WA = 128 * 2 + 16;
  __shared__ __attribute__((aligned(16))) char s_A[4 * 16 * WA];
  __shared__ __attribute__((aligned(16))) float s_red[6 * 256];
  __shared__ __attribute__((aligned(16))) float s_h[DM];
  __shared__ __attribute__((aligned(16))) float s_buf[NWIN * NW * PST];
  __shared__ __attribute__((aligned(16))) float s_sc[NGRP * 64];
  __shared__ __attribute__((aligned(16))) float s_q[NWIN * DH];
  __shared__ float s_m[NWIN * NW];
  __shared__ int s_mcnt[NWIN];
  __shared__ int s_qok, s_pcnt, s_ecnt, s_fcnt, s_icnt, s_org, s_hcnt, s_ccnt;
  __shared__ int s_prog[NW];

  const int tid = threadIdx.x;
  unsigned pf;
  {
    unsigned long long pc;
    asm volatile("s_getpc_b64 %0" : "=s"(pc));
    pf = *(const __attribute__((address_space(1))) unsigned*)((pc & ~127ull) + min(tid, CODE_LINES - 1) * 128);
    asm volatile("" ::: "memory");
  }
  int lane = tid & 63, r = lane >> 3, j = lane & 7, mg = lane >> 4, mc = lane & 15;
  int wave = __builtin_amdgcn_readfirstlane(tid >> 6), bid = blockIdx.x;
  const unsigned long long t64 = __builtin_amdgcn_s_memrealtime();
  const unsigned t_start = (unsigned)t64;
  const int wid = (bid >> 5) * 32 + (bid & 7) * 4 + ((bid >> 3) & 3);
  const bool prod = bid < NQP;
  const int voff = r * (DH * 2) + j * 16;
  asm volatile("" :: "s"(ws), "s"(x), "s"(wq), "s"(kc), "s"(t64), "s"(wid / NSEG), "s"(wave), "v"(voff));
  __builtin_amdgcn_sched_barrier(0);
  const __amdgpu_buffer_rsrc_t rs_ws = __builtin_amdgcn_make_buffer_rsrc(ws, 0, WS_END * 4, 0x00020000);
  const __amdgpu_buffer_rsrc_t rs_k = __builtin_amdgcn_make_buffer_rsrc(const_cast<bf16*>(kc), 0, NB * NH * SK * DH * 2, 0x00020000);
  const __amdgpu_buffer_rsrc_t rs_v = __builtin_amdgcn_make_buffer_rsrc(const_cast<bf16*>(vc), 0, NB * NH * SK * DH * 2, 0x00020000);
  const int c1 = wid % 3 == 0 ? 22 : 21, c2 = wid % 3 == 1 ? 42 : 43;
  auto seg_of = [&](int n) { return (n * 8 + wave >= c1) + (n * 8 + wave >= c2); };
  auto gload = [&](Slot (&sl)[NBUF], __amdgpu_buffer_rsrc_t rs, int n) {
    const int sn = seg_of(n), sg = wid + NWG * sn, row = sg / NSEG, k = sg - row * NSEG;
    const int off = (row * (SK / 64) + (k >> 1) * 43 + (n * 8 + wave - (sn == 0 ? 0 : (sn == 1 ? c1 : c2))) * 2 + ((k ^ (k >> 1)) & 1)) * (64 * DH * 2);
#pragma unroll
    for (int u = 0; u < NBUF; ++u) {
#pragma unroll
      for (int i = 0; i < 3; ++i) sl[u].a[i] = __builtin_amdgcn_raw_buffer_load_b128(rs, voff + i * 128, off + 2 * u * 3072, AUX_NT);
      MEMFENCE();
#pragma unroll
      for (int i = 0; i < 3; ++i) sl[u].b[i] = __builtin_amdgcn_raw_buffer_load_b128(rs, voff + i * 128, off + (2 * u + 1) * 3072, AUX_NT);
      if (u < NBUF - 1) __builtin_amdgcn_s_sleep(3);
      MEMFENCE();
    }
  };
  Slot b0[NBUF];
  const bool early = wave == 0 && !prod;
  if (early) gload(b0, rs_k, 0);
  unsigned hs_nc = 0u, hs_ep = 0u;
  auto look_at_p = [&]() {
    AGAIN();
    if (bid == NWG - 1) {
      u32x4 hp[2], hc[2];
#pragma unroll
      for (int i = 0; i < 2; ++i) {
        hp[i] = __builtin_amdgcn_raw_buffer_load_b128(rs_ws, (WS_HP + lane * 8 + i * 4) * 4, 0, AUX_SC1);
        const u32x2 c0 = __builtin_amdgcn_raw_buffer_load_b64(rs_ws, (WS_HC + (lane * 4 + i * 2) * LSTR) * 4, 0, AUX_SC1);
        const u32x2 c1_ = __builtin_amdgcn_raw_buffer_load_b64(rs_ws, (WS_HC + (lane * 4 + i * 2 + 1) * LSTR) * 4, 0, AUX_SC1);
        hc[i] = u32x4{c0[0], c0[1], c1_[0], c1_[1]};
      }
      if (__all(hc[0][1] == hp[0][0] && hc[0][3] == hp[0][2] && hc[1][1] == hp[1][0] && hc[1][3] == hp[1][2]) &&
          __any(hc[0][0] != hp[0][1] || hc[0][2] != hp[0][3] || hc[1][0] != hp[1][1] || hc[1][2] != hp[1][3])) {
#pragma unroll
        for (int i = 0; i < 2; ++i)
          __builtin_amdgcn_raw_buffer_store_b128(u32x4{hp[i][0], hc[i][0], hp[i][2], hc[i][2]}, rs_ws, (WS_HP + lane * 8 + i * 4) * 4, 0, AUX_SC1);
      }
    }
    const u32x2 pw = __builtin_amdgcn_raw_buffer_load_b64(rs_ws, (WS_HP + bid * 2) * 4, 0, AUX_SC1);
    const unsigned np = __builtin_amdgcn_readfirstlane(pw[0]), ec = __builtin_amdgcn_readfirstlane(pw[1]);
    if (np != hs_ep) { hs_ep = np; __builtin_amdgcn_raw_buffer_store_b64(u32x2{hs_nc, hs_ep}, rs_ws, lane == 0 ? (WS_HC + bid * LSTR) * 4 : OOB, 0, AUX_SC1); }
    return ec == hs_nc;
  };
  auto answer = [&]() {
    const u32x4 z4 = {0u, 0u, 0u, 0u};
#pragma unroll
    for (int i = 0; i < (ZPW + 255) / 256; ++i)
      __builtin_amdgcn_raw_buffer_store_b128(z4, rs_ws, i * 256 + lane * 4 < ZPW ? (WS_Z0 + bid * ZPW + i * 256 + lane * 4) * 4 : OOB, 0, AUX_SC1);
    const u32x2 pw = __builtin_amdgcn_raw_buffer_load_b64(rs_ws, (WS_HP + bid * 2) * 4, 0, AUX_SC1);
    DRAIN();
    hs_ep = __builtin_amdgcn_readfirstlane(pw[0]);
    hs_nc = __builtin_amdgcn_readfirstlane(pw[1]) + 1u;
    __builtin_amdgcn_raw_buffer_store_b64(u32x2{hs_nc, hs_ep}, rs_ws, lane == 0 ? (WS_HC + bid * LSTR) * 4 : OOB, 0, AUX_SC1);
  };
  bool answered = false;
  if (tid < NW) s_prog[tid] = 1;
  if (tid < NWIN) s_mcnt[tid] = 0;
  if (tid == 0) { s_qok = 0; s_pcnt = 0; s_ecnt = 0; s_fcnt = 0; s_icnt = 0; s_hcnt = 0; s_ccnt = 0; s_org = (int)(t_start + POFF); }
  if (wave == SVC) {
    __builtin_amdgcn_s_setprio(3);
    int iofs[NWIN];
#pragma unroll
    for (int s = 0; s < NWIN; ++s) iofs[s] = (WS_QI + ((wid + NWG * s) / NSEG * DH + lane) * 2) * 4;
    const int oofs = prod ? (WS_QI + (bid * 64 + lane) * 2) * 4 : OOB;
    u32x2 in[NWIN * 3];
    unsigned ot[NB], ce[4];
#pragma unroll
    for (int e = 0; e < NWIN * 3; ++e) in[e] = __builtin_amdgcn_raw_buffer_load_b64(rs_ws, iofs[e / 3], (e % 3) * 512, AUX_SC1);
#pragma unroll
    for (int bb = 0; bb < NB; ++bb) ot[bb] = __builtin_amdgcn_raw_buffer_load_b64(rs_ws, oofs, bb * (HD * 8), AUX_SC1)[1];
    u32x4 xr[5], hp[2];
#pragma unroll
    for (int i = 0; i < 5; ++i) xr[i] = reinterpret_cast<const u32x4*>(x)[lane * 5 + i];
#pragma unroll
    for (int i = 0; i < 2; ++i) hp[i] = __builtin_amdgcn_raw_buffer_load_b128(rs_ws, bid == NWG - 1 ? (WS_HP + lane * 8 + i * 4) * 4 : OOB, 0, AUX_SC1);
#pragma unroll
    for (int i = 0; i < 4; ++i) ce[i] = __builtin_amdgcn_raw_buffer_load_b32(rs_ws, bid == NWG - 1 ? (WS_HC + (lane * 4 + i) * LSTR + 1) * 4 : OOB, 0, AUX_SC1);
    MEMFENCE();
    __syncthreads();
    MEMFENCE();
    gload(b0, rs_k, 0);
    if (bid == NWG - 1) {
#pragma unroll
      for (int i = 0; i < 2; ++i) {
#pragma unroll
        for (int e = 0; e < 2; ++e) { unsigned np = hp[i][2 * e] + 1u; if (np == ce[2 * i + e]) np += 1u; hp[i][2 * e] = np; }
        __builtin_amdgcn_raw_buffer_store_b128(hp[i], rs_ws, (WS_HP + lane * 8 + i * 4) * 4, 0, AUX_SC1);
      }
    }
    unsigned want[NWIN * 3];
#pragma unroll
    for (int e = 0; e < NWIN * 3; ++e) want[e] = fresh_tag(in[e][1]);
    if (prod) {
      float xs = 0.f;
#pragma unroll
      for (int i = 0; i < 20; ++i) xs += bf_lo(xr[i >> 2][i & 3]) + bf_hi(xr[i >> 2][i & 3]);
      const float mean = row16_sum(xs) * (1.0f / DM);
      float xv = 0.f;
#pragma unroll
      for (int i = 0; i < 20; ++i) { const float a = bf_lo(xr[i >> 2][i & 3]) - mean, c = bf_hi(xr[i >> 2][i & 3]) - mean; xv += a * a + c * c; }
      const float rstd = QSCALE / sqrtf(row16_sum(xv) * (1.0f / DM) + LN_EPS);
#pragma unroll
      for (int bb = 0; bb < NB; ++bb) ot[bb] = fresh_tag(ot[bb]);
      for (unsigned spin = 0; spin < (SPIN_CAP << 4) && lds_get(&s_pcnt) < NQW; ++spin) __builtin_amdgcn_s_sleep(1);
      __builtin_amdgcn_fence(__ATOMIC_ACQUIRE, "workgroup");
      float pr[5];
#pragma unroll
      for (int i = 0; i < 5; ++i) {
        pr[i] = 0.f;
#pragma unroll
        for (int k = 0; k < NQW; ++k) pr[i] += s_buf[2 * k * 320 + i * 64 + lane] + s_buf[(2 * k + 1) * 320 + i * 64 + lane];
      }
#pragma unroll
      for (int bb = 0; bb < NB; ++bb) {
        const float qv = (pr[bb] - __int_as_float(__builtin_amdgcn_readlane(__float_as_int(mean), bb * 16)) * pr[4]) *
                         __int_as_float(__builtin_amdgcn_readlane(__float_as_int(rstd), bb * 16));
        __builtin_amdgcn_raw_buffer_store_b64(u32x2{__float_as_uint(qv), ot[bb]}, rs_ws, oofs, bb * (HD * 8), AUX_SC1);
      }
    }
    for (unsigned spin = 0; spin < 1024 && (int)((unsigned)__builtin_amdgcn_s_memrealtime() - t_start) < QLOOK; ++spin) __builtin_amdgcn_s_sleep(4);
    bool all_out = false;
#pragma unroll 1
    for (unsigned spin = 0; spin < SPIN_CAP; ++spin) {
      AGAIN();
#pragma unroll
      for (int e = 0; e < NWIN * 3; ++e) in[e] = __builtin_amdgcn_raw_buffer_load_b64(rs_ws, iofs[e / 3], (e % 3) * 512, AUX_SC1);
      bool ok = true;
#pragma unroll
      for (int e = 0; e < NWIN * 3; ++e) ok = ok && in[e][1] == want[e];
      if (all_out || __all(ok)) break;
      if (spin == 15) { answer(); answered = true; }
      if (spin > 15 && (spin & 3) == 3) all_out = look_at_p();
      __builtin_amdgcn_s_sleep(4);
    }
#pragma unroll
    for (int e = 0; e < NWIN * 3; ++e) s_q[e * 64 + lane] = __uint_as_float(in[e][0]);
    __builtin_amdgcn_fence(__ATOMIC_RELEASE, "workgroup");
    lds_put(&s_qok, 1);
    __builtin_amdgcn_s_setprio(0);
  } else if (prod && wave >= QW0 && wave < QW0 + NQW) {
    const int k0 = (wave - QW0) * (QST * 64) + (mc >> 3) * 32 + mg * 8;
    u32x4 raw[QST * 8], xa[QST];
    const bf16* wb = wq + (size_t)k0 * HD + bid * 64 + (mc & 7) * 8;
#pragma unroll
    for (int st = 0; st < QST; ++st) xa[st] = *reinterpret_cast<const u32x4*>(x + (mc & 3) * DM + k0 + st * 64);
#pragma unroll
    for (int e = 0; e < QST * 8; ++e) raw[e] = __builtin_nontemporal_load(reinterpret_cast<const u32x4*>(wb + (size_t)((e >> 3) * 64 + (e & 7)) * HD));
    MEMFENCE();
    __syncthreads();
    MEMFENCE();
    f32x4 ac[8];
#pragma unroll
    for (int t = 0; t < 8; ++t) ac[t] = f32x4{0.f, 0.f, 0.f, 0.f};
#pragma unroll
    for (int st = 0; st < QST; ++st) {
      const u32x4 a = (mc & 4) == 0 ? xa[st] : u32x4{0x3f803f80u, 0x3f803f80u, 0x3f803f80u, 0x3f803f80u};
#pragma unroll
      for (int c = 0; c < 4; ++c) {
        u32x4 f0, f1;
#pragma unroll
        for (int i = 0; i < 4; ++i) { f0[i] = pk_lo(raw[st * 8 + 2 * i][c], raw[st * 8 + 2 * i + 1][c]); f1[i] = pk_hi(raw[st * 8 + 2 * i][c], raw[st * 8 + 2 * i + 1][c]); }
        ac[2 * c] = mfma16(a, f0, ac[2 * c]);
        ac[2 * c + 1] = mfma16(a, f1, ac[2 * c + 1]);
      }
    }
    if ((mg >> 1) == (mc >> 3)) {
      float* const dst = &s_buf[((wave - QW0) * 2 + (mg >> 1)) * 320 + (mc & 7) * 8];
#pragma unroll
      for (int bb = 0; bb < NB; ++bb)
        if (bb == 0 || (mg & 1) == 0) {
          *reinterpret_cast<f32x4*>(dst + ((mg & 1) * 4 + bb) * 64) = f32x4{ac[0][bb], ac[1][bb], ac[2][bb], ac[3][bb]};
          *reinterpret_cast<f32x4*>(dst + ((mg & 1) * 4 + bb) * 64 + 4) = f32x4{ac[4][bb], ac[5][bb], ac[6][bb], ac[7][bb]};
        }
    }
    __builtin_amdgcn_fence(__ATOMIC_RELEASE, "workgroup");
    if (lane == 0) __hip_atomic_fetch_add(&s_pcnt, 1, __ATOMIC_RELAXED, __HIP_MEMORY_SCOPE_WORKGROUP);
  } else {
    __syncthreads();
  }
  MEMFENCE();
  const int gtile = ((bid & 7) * 12 + (bid >> 3) - 15) >> 2;
  const int tau = PTAU - (((bid >> 3) >= 15 && (bid >> 3) < 27 && (((((unsigned)(size_t)wg >> 7) + gtile) & 7) == 3 || ((((unsigned)(size_t)wu >> 7) + gtile) & 7) == 3)) ? 10 : 0);
  u32x2 rep = {0u, 0u};
  unsigned t_thru = 0u;
  auto pace = [&](int g) {
    if (wave == 0 && g > AFROM) {
      const int org = lds_get(&s_org);
      const bool ok = rep[0] - t_start + 1000u < 6000u && rep[1] - t_start + 1000u < 6000u;
      const float dorg = row16_sum(ok ? (float)(int)(rep[1] - (unsigned)org) : 0.f);
      const float lt = ok ? (float)min((int)(rep[0] - rep[1]), LCAP) : (float)LCAP;
      const float lx = fminf(lt, dpp<DPP_ROR8>(lt));
      float hi = fmaxf(lx, dpp<DPP_XOR1>(lx)); hi = fmaxf(hi, dpp<DPP_XOR2>(hi)); hi = fmaxf(hi, dpp<DPP_HMIRROR>(hi));
      const float lat = row16_sum(lt) * (0.8f / 16.f) + hi * 0.2f;
      if (lane == 0) lds_put(&s_org, org + (int)(dorg * (1.f / 16.f) + fmaxf((lat - LTGT) * 0.6f, g > EFROM ? (float)SLACK : -(float)DOWN)));
    }
    const unsigned rel = (unsigned)(g * tau + (wave + (g == 0 && wave > 0)) * (PTAU / 8) + min(g * 8 + wave, 16) * (FFWD / 16) - FFWD + ((wid * 65) & 255) * (PTAU / 8) / 256);
    unsigned now = (unsigned)__builtin_amdgcn_s_memrealtime();
    if (wave == 1 && (bid >> 4) == 7 && g >= 2)
      __builtin_amdgcn_raw_buffer_store_b64(u32x2{t_thru - rel + tau, (unsigned)lds_get(&s_org)}, rs_ws, lane == 0 ? (WS_REP + ((g - 2) * 16 + (bid & 15)) * LSTR) * 4 : OOB, 0, AUX_SC1);
    for (unsigned spin = 0; spin < 4096 && (int)(now - rel - (unsigned)__builtin_amdgcn_readfirstlane(lds_get(&s_org))) < 0; ++spin) {
      __builtin_amdgcn_s_sleep(2);
      now = (unsigned)__builtin_amdgcn_s_memrealtime();
    }
    if (g >= 2) rep = __builtin_amdgcn_raw_buffer_load_b64(rs_ws, (wave == 0 && lane < 16 && g >= AFROM) ? (WS_REP + ((g - 2) * 16 + lane) * LSTR) * 4 : OOB, 0, AUX_SC1);
    if (lane == 0) lds_put(&s_prog[wave], g);
    int pp = __builtin_amdgcn_readfirstlane(lds_get(&s_prog[wave ^ (NW / 2)]));
    for (unsigned spin = 0; spin < 4096 && g > pp + 1; ++spin) { __builtin_amdgcn_s_sleep(4); pp = __builtin_amdgcn_readfirstlane(lds_get(&s_prog[wave ^ (NW / 2)])); }
    if (g > pp) __builtin_amdgcn_s_setprio(0); else __builtin_amdgcn_s_setprio(1);
  };
  float gmx = -INFINITY;
  auto k_group = [&](const Slot (&sl)[NBUF], int n, int s) {
    f32x2 q[12];
#pragma unroll
    for (int i = 0; i < 6; ++i) {
      const f32x4 a = *reinterpret_cast<const f32x4*>(&s_q[s * DH + (i >> 1) * 64 + j * 8 + (i & 1) * 4]);
      q[i * 2] = f32x2{a[0], a[1]}; q[i * 2 + 1] = f32x2{a[2], a[3]};
    }
    float* const sc = &s_sc[(wave * NT + n) * 64 + r * 8];
    MEMFENCE();
#pragma unroll
    for (int u = 0; u < NBUF; ++u) {
      k_tile(sl[u].a, q, gmx, sc + 2 * u, j);
      k_tile(sl[u].b, q, gmx, sc + 2 * u + 1, j);
      MEMFENCE();
    }
    t_thru = (unsigned)__builtin_amdgcn_s_memrealtime();
  };
  {
    Slot b1[NBUF];
    asm volatile("" :: "v"(pf));
    if (!early && wave != SVC) {
      pace(0);
      gload(b0, rs_k, 0);
    }
    pace(1);
    gload(b1, rs_k, 1);
    for (unsigned spin = 0; spin < (SPIN_CAP << 4) && lds_get(&s_qok) == 0; ++spin) __builtin_amdgcn_s_sleep(2);
    __builtin_amdgcn_fence(__ATOMIC_ACQUIRE, "workgroup");
    k_group(b0, 0, 0);
    k_group(b1, 1, 0);
    if (wave == SVC && !answered) answer();
  }
  auto k_done = [&](int s) {
    const float gm = max_r(gmx);
    if (lane == 0) s_m[s * NW + wave] = gm;
    __builtin_amdgcn_fence(__ATOMIC_RELEASE, "workgroup");
    if (lane == 0) __hip_atomic_fetch_add(&s_mcnt[s], 1, __ATOMIC_RELAXED, __HIP_MEMORY_SCOPE_WORKGROUP);
    gmx = -INFINITY;
  };
  Slot buf[NBUF];
  int seg = 0;
  bool through = false;
  for (int n = 2; n < NT; ++n) {
    pace(n);
    gload(buf, rs_k, n);
    if (seg_of(n) != seg) {
      k_done(seg);
      ++seg;
    }
    k_group(buf, n, seg);
    if (wave == SVC && bid == NWG - 1 && n >= 3 && !through) through = look_at_p();
  }
  k_done(seg);
  const int d4 = lane < DH / 4 ? ((lane >> 1) & 7) * 6 + (lane >> 4) * 2 + (lane & 1) : DH / 4;
  const int rofs = lane < REC / 4 ? (WS_PO + wid * REC + lane * 4) * 4 : OOB;
  float l = 0.f;
  f32x2 acc[12];
#pragma unroll
  for (int i = 0; i < 12; ++i) acc[i] = f32x2{0.f, 0.f};
  __builtin_amdgcn_fence(__ATOMIC_RELEASE, "wavefront"); __builtin_amdgcn_wave_barrier(); __builtin_amdgcn_fence(__ATOMIC_ACQUIRE, "wavefront");
  for (int n = 0; n < NT; ++n) {
    pace(NT + n);
    gload(buf, rs_v, n);
    if (n == NT - 1) {
      if (lane == 0) __hip_atomic_fetch_add(&s_icnt, 1, __ATOMIC_RELAXED, __HIP_MEMORY_SCOPE_WORKGROUP);
      __builtin_amdgcn_s_setprio(3);
    }
    seg = seg_of(n);
    for (unsigned spin = 0; spin < (SPIN_CAP << 4) && lds_get(&s_mcnt[seg]) < NW; ++spin) __builtin_amdgcn_s_sleep(2);
    __builtin_amdgcn_fence(__ATOMIC_ACQUIRE, "workgroup");
    float m = s_m[seg * NW + j], lw;
    m = fmaxf(m, dpp<DPP_XOR1>(m)); m = fmaxf(m, dpp<DPP_XOR2>(m)); m = fmaxf(m, dpp<DPP_HMIRROR>(m));
    {
      float p[8];
      const float* const sc = &s_sc[(wave * NT + n) * 64 + r * 8];
      const f32x4 sa = *reinterpret_cast<const f32x4*>(sc), sb = *reinterpret_cast<const f32x4*>(sc + 4);
#pragma unroll
      for (int t = 0; t < 4; ++t) { p[t] = __builtin_amdgcn_exp2f(sa[t] - m); p[4 + t] = __builtin_amdgcn_exp2f(sb[t] - m); }
      l += ((p[0] + p[1]) + (p[2] + p[3])) + ((p[4] + p[5]) + (p[6] + p[7]));
      lw = sum_r(l);
      asm volatile("" : "+v"(lw));
      MEMFENCE();
#pragma unroll
      for (int u = 0; u < NBUF; ++u) {
        v_tile(buf[u].a, p[2 * u], acc);
        v_tile(buf[u].b, p[2 * u + 1], acc);
        MEMFENCE();
      }
    }
    t_thru = (unsigned)__builtin_amdgcn_s_memrealtime();
    if (wave == SVC && n == 1)
      for (unsigned spin = 0; spin < SPIN_CAP && !look_at_p(); ++spin) __builtin_amdgcn_s_sleep(4);
    if (n == NT - 1 || seg_of(n + 1) != seg) {
      float t12[12], t6[6];
#pragma unroll
      for (int e = 0; e < 12; ++e) t12[e] = tsum32(acc[e >> 1][e & 1], acc[6 + (e >> 1)][e & 1]);
#pragma unroll
      for (int e = 0; e < 6; ++e) { t6[e] = tsum16(t12[e], t12[e + 6]); t6[e] += dpp<DPP_ROR8>(t6[e]); }
      if ((lane & 8) == 0) {
#pragma unroll
        for (int e = 0; e < 3; ++e) *reinterpret_cast<f32x2*>(&s_buf[(seg * NW + wave) * PST + j * 24 + (lane >> 4) * 6 + e * 2]) = f32x2{t6[2 * e], t6[2 * e + 1]};
      }
      if (lane == 0) *reinterpret_cast<f32x2*>(&s_buf[(seg * NW + wave) * PST + DH]) = f32x2{m, lw};
      l = 0.f;
#pragma unroll
      for (int i = 0; i < 12; ++i) acc[i] = f32x2{0.f, 0.f};
      if (seg != 0) {
        int c = count_in_rtn(seg == 1 ? &s_ecnt : &s_fcnt);
        MEMFENCE();
        auto merged = [&](int s) {
          f32x4 pw[NW];
#pragma unroll
          for (int w = 0; w < NW; ++w) pw[w] = reinterpret_cast<const f32x4*>(s_buf)[(s * NW + w) * (PST / 4) + d4];
          f32x4 o = ((pw[0] + pw[1]) + (pw[2] + pw[3])) + ((pw[4] + pw[5]) + (pw[6] + pw[7]));
          if (lane >= DH / 4) o = f32x4{pw[0][0], o[1], 1.f, 1.f};
          return u32x4{nz_bits(o[0]), nz_bits(o[1]), nz_bits(o[2]), nz_bits(o[3])};
        };
        u32x4 rec = merged(seg);
        asm volatile("s_waitcnt lgkmcnt(0)" : "+v"(rec), "+v"(c) :: "memory");
        if (__builtin_amdgcn_readfirstlane(c) == NW - 1) {
          __builtin_amdgcn_raw_buffer_store_b128(rec, rs_ws, rofs, seg * (NWG * REC * 4), AUX_SC1);
          if (seg == 1) __builtin_amdgcn_raw_buffer_store_b128(merged(0), rs_ws, rofs, 0, AUX_SC1);
          if (seg == 2) { MEMFENCE(); lds_put(&s_fcnt, NW + 1); }
          if (seg == 1) {
            DRAIN();
            __builtin_amdgcn_raw_buffer_store_b32(1u, rs_ws, lane == 0 ? ef_ofs(wid) : OOB, 0, AUX_SC1);
          }
        }
      }
      MEMFENCE();
    }
  }
  __builtin_amdgcn_s_setprio(0);

  {
    asm volatile("" : "+s"(wave), "+s"(bid), "+v"(lane));
    r = lane >> 3; j = lane & 7; mg = lane >> 4; mc = lane & 15;
    const int w = wave;
    const int xcd = bid & 7, ri = bid >> 3;
    const bool isW = ri < 15, isG = !isW && ri < 27;
    const int tt = isW ? xcd * 15 + ri : (isG ? xcd * 12 + ri - 15 : xcd * 5 + ri - 27);
    const int ks1 = tt / W_CT, ct1 = tt - ks1 * W_CT;
    typedef const __attribute__((address_space(1))) char* gptr;
    auto behind_stream = [&](int nsl, int cnt) {
      for (unsigned spin = 0; spin < (SPIN_CAP << 4) && lds_get(&s_icnt) < cnt; ++spin) __builtin_amdgcn_s_sleep(1);
      for (int i = 0; i < nsl && lds_get(&s_fcnt) <= NW; ++i) __builtin_amdgcn_s_sleep(3);
    };
    u32x4 fr[16];
    u32x2 raw[32];
    auto weights = [&](const bf16* src, int skip) {
      gptr wb = (gptr)src;
#pragma unroll
      for (int e = 0; e < 32; ++e) {
        raw[e] = __builtin_nontemporal_load((const __attribute__((address_space(1))) u32x2*)wb);
        wb += ((e & 7) == 7 ? skip : 1) * (DM * 2);
        asm volatile("" : "+v"(wb));
      }
      MEMFENCE();
    };
    auto frags = [&]() {
#pragma unroll
      for (int st = 0; st < 4; ++st) {
        PIN8(raw, st * 8);
#pragma unroll
        for (int i = 0; i < 4; ++i) {
          const u32x2 r0 = raw[st * 8 + 2 * i], r1 = raw[st * 8 + 2 * i + 1];
          fr[st * 4 + 0][i] = pk_lo(r0[0], r1[0]); fr[st * 4 + 1][i] = pk_hi(r0[0], r1[0]);
          fr[st * 4 + 2][i] = pk_lo(r0[1], r1[1]); fr[st * 4 + 3][i] = pk_hi(r0[1], r1[1]);
        }
        PIN4(fr, st * 4);
        MEMFENCE();
      }
    };
    if (isW) {
      if (w >= 4) return;
      int a_put = w * (16 * WA) + mg * 4 * WA + mc * 8, a_get = w * (16 * WA / 16) + mc * (WA / 16) + mg, a_red = w * 64 + lane;
      int o_res = (WS_SLAB + ks1 * SLS + mg * DM + ct1 * 64 + mc * 4) * 4;
      {
        int offp[2], sg0[2];
        bool er[2];
#pragma unroll
        for (int hf = 0; hf < 2; ++hf) {
          const int row = ks1 * K3_ROWS + w * 128 + hf * 64, hh = row / DH;
          sg0[hf] = (mg * NH + hh) * NSEG;
          offp[hf] = (WS_PO + sg0[hf] * REC + row - hh * DH + mc * 4) * 4;
          er[hf] = sg0[hf] + NSEG <= 2 * NWG;
        }
        const int sgm = (mc < 8 ? sg0[0] : sg0[1]) + j, offm = j < NSEG ? (WS_PO + sgm * REC + DH) * 4 : OOB;
        for (unsigned spin = 0; spin < (SPIN_CAP << 4) && lds_get(&s_ecnt) < NW; ++spin) __builtin_amdgcn_s_sleep(4);
        for (unsigned spin = 0; spin < SPIN_CAP; ++spin) {
          AGAIN();
          const bool fl = j < NSEG && sgm < 2 * NWG;
          if (__all(!fl || __builtin_amdgcn_raw_buffer_load_b32(rs_ws, fl ? ef_ofs(sgm & (NWG - 1)) : OOB, 0, AUX_SC1) != 0u)) break;
          __builtin_amdgcn_s_sleep(8);
        }
        auto look = [&](u32x4 (&d)[2 * NSEG], bool h0, bool h1) {
          const int o0_ = h0 ? offp[0] : OOB, o1_ = h1 ? offp[1] : OOB;
#pragma unroll
          for (int k = 0; k < 2 * NSEG; ++k) d[k] = __builtin_amdgcn_raw_buffer_load_b128(rs_ws, k < NSEG ? o0_ : o1_, (k % NSEG) * REC * 4, AUX_SC1);
          MEMFENCE();
        };
        auto wsum = [&](float wt, const u32x4 (&d)[2 * NSEG], f32x4& a0, f32x4& a1) {
          a0 += dpp<DPP_BCAST + 0>(wt) * __builtin_bit_cast(f32x4, d[0]); a1 += dpp<DPP_BCAST + 8>(wt) * __builtin_bit_cast(f32x4, d[6]);
          a0 += dpp<DPP_BCAST + 1>(wt) * __builtin_bit_cast(f32x4, d[1]); a1 += dpp<DPP_BCAST + 9>(wt) * __builtin_bit_cast(f32x4, d[7]);
          a0 += dpp<DPP_BCAST + 2>(wt) * __builtin_bit_cast(f32x4, d[2]); a1 += dpp<DPP_BCAST + 10>(wt) * __builtin_bit_cast(f32x4, d[8]);
          a0 += dpp<DPP_BCAST + 3>(wt) * __builtin_bit_cast(f32x4, d[3]); a1 += dpp<DPP_BCAST + 11>(wt) * __builtin_bit_cast(f32x4, d[9]);
          a0 += dpp<DPP_BCAST + 4>(wt) * __builtin_bit_cast(f32x4, d[4]); a1 += dpp<DPP_BCAST + 12>(wt) * __builtin_bit_cast(f32x4, d[10]);
          a0 += dpp<DPP_BCAST + 5>(wt) * __builtin_bit_cast(f32x4, d[5]); a1 += dpp<DPP_BCAST + 13>(wt) * __builtin_bit_cast(f32x4, d[11]);
        };
        u32x4 pe[2 * NSEG], pl[2 * NSEG];
        behind_stream(6, NW - 1);
        weights(wo + (size_t)(ks1 * K3_ROWS + w * 128 + mg * 8) * DM + ct1 * 64 + mc * 4, 25);
        look(pe, er[0], er[1]);
        for (unsigned sp2 = 0; sp2 < (SPIN_CAP << 4) && lds_get(&s_fcnt) < NW; ++sp2) __builtin_amdgcn_s_sleep(1);
        f32x4 o0, o1;
        asm volatile("" : "+v"(a_put), "+v"(a_get), "+v"(a_red), "+v"(o_res));
        u32x2 ml = __builtin_amdgcn_raw_buffer_load_b64(rs_ws, offm, 0, AUX_SC1);
        look(pl, !er[0], !er[1]);
        frags();
        MEMFENCE();
#pragma unroll 1
        for (unsigned spin = 0; spin < SPIN_CAP; ++spin) {
          AGAIN();
          const float mm = j < NSEG ? __uint_as_float(ml[0]) : -INFINITY;
          float M = fmaxf(mm, dpp<DPP_XOR1>(mm)); M = fmaxf(M, dpp<DPP_XOR2>(M)); M = fmaxf(M, dpp<DPP_HMIRROR>(M));
          const float e = __builtin_amdgcn_exp2f(mm - M);
          float L = e * __uint_as_float(ml[1]);
          L += dpp<DPP_XOR1>(L); L += dpp<DPP_XOR2>(L); L += dpp<DPP_HMIRROR>(L);
          const float wt = e * __builtin_amdgcn_rcpf(L);
          o0 = f32x4{0.f, 0.f, 0.f, 0.f}; o1 = o0;
          wsum(wt, pe, o0, o1);
          unsigned m0 = er[0], m1 = er[1], a0 = ~0u, a1 = ~0u;
          const bool okm = j >= NSEG || min(ml[0], ml[1]) != 0u;
          asm volatile("" : "+v"(o0), "+v"(o1));
          MEMFENCE();
          wsum(wt, pl, o0, o1);
          asm volatile("" : "+v"(o0), "+v"(o1));
#pragma unroll
          for (int k = 0; k < NSEG; ++k) { a0 = min(a0, min4(pl[k])); a1 = min(a1, min4(pl[NSEG + k])); }
          if (__all(okm && (a0 | m0) != 0u && (a1 | m1) != 0u)) break;
          if (spin >= 8) __builtin_amdgcn_s_sleep(4);
          ml = __builtin_amdgcn_raw_buffer_load_b64(rs_ws, offm, 0, AUX_SC1);
          look(pl, !er[0], !er[1]);
        }
        put_terms4(s_A + a_put, WA, o0);
        put_terms4(s_A + a_put + 128, WA, o1);
      }
      __builtin_amdgcn_fence(__ATOMIC_RELEASE, "wavefront"); __builtin_amdgcn_wave_barrier(); __builtin_amdgcn_fence(__ATOMIC_ACQUIRE, "wavefront");
      f32x4 ac[4];
#pragma unroll
      for (int t = 0; t < 4; ++t) ac[t] = f32x4{0.f, 0.f, 0.f, 0.f};
      u32x4 av[4];
#pragma unroll
      for (int st = 0; st < 4; ++st) av[st] = reinterpret_cast<const u32x4*>(s_A)[a_get + st * 4];
      MEMFENCE();
#pragma unroll
      for (int st = 0; st < 4; ++st)
#pragma unroll
        for (int t = 0; t < 4; ++t) ac[t] = mfma16(av[st], fr[st * 4 + t], ac[t]);
      reinterpret_cast<f32x4*>(s_red)[a_red] = f32x4{sum3(ac[0]), sum3(ac[1]), sum3(ac[2]), sum3(ac[3])};
      __syncthreads();
      if (w == 0) {
        const f32x4* const red = reinterpret_cast<const f32x4*>(s_red) + a_red;
        const f32x4 s0 = (red[0] + red[64]) + (red[128] + red[192]);
        __builtin_amdgcn_raw_buffer_store_b128(u32x4{nz_bits(s0[0]), nz_bits(s0[1]), nz_bits(s0[2]), nz_bits(s0[3])}, rs_ws, o_res, 0, AUX_SC1);
      }
    } else if (isG) {
      const int gb = tt & 3, gt = tt >> 2;
      int o_res = (WS_ACT + (gb * (FF / 64) + gt) * 96 + (lane & 1) * 32 + (lane >> 1)) * 4;
      asm volatile("" : "+v"(o_res));
      if (w < 5) {
        const int hcol = w * 128 + (lane & 31) * 4;
        uint2 xx = {0u, 0u};
        u32x4 fg[32];
        {
          u32x4 rg[32];
          if (lane < 32) xx = *reinterpret_cast<const uint2*>(x + gb * DM + hcol);
          behind_stream(5, NW);
          gptr wb = (gptr)((mc < 8 ? wg : wu) + (size_t)(w * 128 + mg * 8) * FF + gt * 64 + (mc & 7) * 8);
#pragma unroll
          for (int e = 0; e < 32; ++e) {
            rg[e] = __builtin_nontemporal_load((const __attribute__((address_space(1))) u32x4*)wb);
            wb += ((e & 7) == 7 ? 25 : 1) * (FF * 2);
            asm volatile("" : "+v"(wb));
          }
          MEMFENCE();
#pragma unroll
          for (int st = 0; st < 4; ++st) {
            PIN8(rg, st * 8);
#pragma unroll
            for (int c = 0; c < 4; ++c)
#pragma unroll
              for (int i = 0; i < 4; ++i) {
                fg[st * 8 + 2 * c][i] = pk_lo(rg[st * 8 + 2 * i][c], rg[st * 8 + 2 * i + 1][c]);
                fg[st * 8 + 2 * c + 1][i] = pk_hi(rg[st * 8 + 2 * i][c], rg[st * 8 + 2 * i + 1][c]);
              }
            PIN8(fg, st * 8);
            MEMFENCE();
          }
        }
        {
          f32x4 cs[8];
#pragma unroll
          for (int t = 0; t < 8; ++t) cs[t] = f32x4{0.f, 0.f, 0.f, 0.f};
#pragma unroll
          for (int e = 0; e < 32; ++e) cs[e & 7] = mfma16(u32x4{0x3f803f80u, 0x3f803f80u, 0x3f803f80u, 0x3f803f80u}, fg[e], cs[e & 7]);
          if (lane < 16) {
            *reinterpret_cast<f32x4*>(&s_red[w * 256 + 128 + lane * 8]) = f32x4{cs[0][0], cs[1][0], cs[2][0], cs[3][0]};
            *reinterpret_cast<f32x4*>(&s_red[w * 256 + 128 + lane * 8 + 4]) = f32x4{cs[4][0], cs[5][0], cs[6][0], cs[7][0]};
          }
          count_in(&s_ccnt);
        }
        f32x4 hv = f32x4{bf_lo(xx.x), bf_hi(xx.x), bf_lo(xx.y), bf_hi(xx.y)};
        int a_h = hcol + (lane >> 5) * 2, a_put = w * (4 * WA) + (lane & 31) * 8 + (lane >> 5) * 4, a_get = w * (4 * WA / 16) + min(mc, 2) * (WA / 16) + mg, a_red = w * 64 + mc * 2;
        asm volatile("" : "+v"(hv), "+v"(a_h), "+v"(a_put), "+v"(a_get), "+v"(a_red));
        const int off = (WS_SLAB + (lane >> 5) * (K3_KS / 2) * SLS + gb * DM + hcol) * 4;
        u32x4 sl[K3_KS / 2];
        for (unsigned spin = 0; spin < SPIN_CAP; ++spin) {
          AGAIN();
#pragma unroll
          for (int ks = 0; ks < K3_KS / 2; ++ks) sl[ks] = __builtin_amdgcn_raw_buffer_load_b128(rs_ws, off, ks * (SLS * 4), AUX_SC1);
          if (__all(min(min(min(min4(sl[0]), min4(sl[1])), min(min4(sl[2]), min4(sl[3]))), min(min4(sl[4]), min4(sl[5]))) != 0u)) break;
          __builtin_amdgcn_s_sleep(2);
        }
        hv = ((hv + __builtin_bit_cast(f32x4, sl[0])) + (__builtin_bit_cast(f32x4, sl[1]) + __builtin_bit_cast(f32x4, sl[2]))) +
             ((__builtin_bit_cast(f32x4, sl[3]) + __builtin_bit_cast(f32x4, sl[4])) + __builtin_bit_cast(f32x4, sl[5]));
        const float h0 = tsum32(hv[0], hv[2]), h1 = tsum32(hv[1], hv[3]);
        *reinterpret_cast<f32x2*>(&s_h[a_h]) = f32x2{h0, h1};
        count_in(&s_hcnt);
        {
          unsigned t0[3], t1[3];
          split3(h0, t0[0], t0[1], t0[2]); split3(h1, t1[0], t1[1], t1[2]);
#pragma unroll
          for (int k = 0; k < 3; ++k) *reinterpret_cast<unsigned*>(s_A + a_put + k * WA) = pk_hi(t0[k], t1[k]);
        }
        __builtin_amdgcn_fence(__ATOMIC_RELEASE, "wavefront"); __builtin_amdgcn_wave_barrier(); __builtin_amdgcn_fence(__ATOMIC_ACQUIRE, "wavefront");
        f32x4 ac[8];
#pragma unroll
        for (int t = 0; t < 8; ++t) ac[t] = f32x4{0.f, 0.f, 0.f, 0.f};
        u32x4 av[4];
#pragma unroll
        for (int st = 0; st < 4; ++st) av[st] = reinterpret_cast<const u32x4*>(s_A)[a_get + st * 4];
        MEMFENCE();
#pragma unroll
        for (int st = 0; st < 4; ++st)
#pragma unroll
          for (int t = 0; t < 8; ++t) ac[t] = mfma16(av[st], fg[st * 8 + t], ac[t]);
        if (lane < 16) {
          reinterpret_cast<f32x4*>(s_red)[a_red] = f32x4{sum3(ac[0]), sum3(ac[1]), sum3(ac[2]), sum3(ac[3])};
          reinterpret_cast<f32x4*>(s_red)[a_red + 1] = f32x4{sum3(ac[4]), sum3(ac[5]), sum3(ac[6]), sum3(ac[7])};
        }
      } else if (w == NW - 1) {
        unsigned wr2[5];
#pragma unroll
        for (int i = 0; i < 5; ++i) wr2[i] = reinterpret_cast<const unsigned*>(wr)[lane + 64 * i];
        PIN4(wr2, 0); asm volatile("" : "+v"(wr2[4]));
        for (unsigned spin = 0; spin < (SPIN_CAP << 4) && lds_get(&s_ccnt) < 5; ++spin) __builtin_amdgcn_s_sleep(1);
        __builtin_amdgcn_fence(__ATOMIC_ACQUIRE, "workgroup");
        float cg = 0.f, cu = 0.f;
#pragma unroll
        for (int k = 0; k < 5; ++k) { cg += s_red[k * 256 + 128 + lane]; cu += s_red[k * 256 + 192 + lane]; }
        asm volatile("" : "+v"(cg), "+v"(cu));
        for (unsigned spin = 0; spin < (SPIN_CAP << 6) && lds_get(&s_hcnt) < 5; ++spin) {}
        __builtin_amdgcn_fence(__ATOMIC_ACQUIRE, "workgroup");
        const float piv = s_h[0];
        float hv[10], sm = 0.f, sq = 0.f;
#pragma unroll
        for (int i = 0; i < 5; ++i) {
          const float2 t2 = *reinterpret_cast<const float2*>(&s_h[2 * lane + 128 * i]);
          hv[2 * i] = t2.x; hv[2 * i + 1] = t2.y;
        }
        PIN8(hv, 0); asm volatile("" : "+v"(hv[8]), "+v"(hv[9]));
#pragma unroll
        for (int i = 0; i < 10; ++i) { hv[i] -= piv; sm += hv[i]; sq = fmaf(hv[i], hv[i], sq); }
        float t2 = row16_sum(tsum32(sm, sq));
        t2 = tsum16(t2, t2);
        const float dm = __int_as_float(__builtin_amdgcn_readlane(__float_as_int(t2), 0)) * (1.0f / DM);
        const float var = fmaxf(fmaf(-dm, dm, __int_as_float(__builtin_amdgcn_readlane(__float_as_int(t2), 32)) * (1.0f / DM)), 0.f);
        const float rstd = __builtin_amdgcn_rsqf(var + LN_EPS);
        s_red[5 * 256 + lane] = -(piv + dm) * cg; s_red[5 * 256 + 64 + lane] = -(piv + dm) * cu; s_red[5 * 256 + 128 + lane] = rstd;
        __syncthreads();
        if (gt < DM / 32) {
          float rr = 0.f;
#pragma unroll
          for (int i = 0; i < 5; ++i) { rr = fmaf((hv[2 * i] - dm) * rstd, bf_lo(wr2[i]), rr); rr = fmaf((hv[2 * i + 1] - dm) * rstd, bf_hi(wr2[i]), rr); }
          rr = wave_sum(rr);
          const float hh = s_h[gt * 32 + (lane & 31)];
          __builtin_amdgcn_raw_buffer_store_b32(nz_bits(lane >= 32 ? 1.0f / (1.0f + __expf(-rr)) : hh + (hh - (piv + dm)) * rstd), rs_ws,
                                                (WS_BASE + (gb * (DM / 32) + gt) * 64 + lane) * 4, 0, AUX_SC1);
        }
        return;
      } else {
        return;
      }
      __syncthreads();
      if (w == 0) {
        f32x2 pr[6];
#pragma unroll
        for (int k = 0; k < 6; ++k) pr[k] = f32x2{s_red[k * 256 + lane], s_red[k * 256 + 64 + lane]};
        float rstd = s_red[5 * 256 + 128 + lane];
        PIN4(pr, 0); asm volatile("" : "+v"(pr[4]), "+v"(pr[5]), "+v"(rstd));
        const f32x2 gu = (((pr[0] + pr[1]) + (pr[2] + pr[3])) + (pr[4] + pr[5])) * rstd;
        const float gg = gu[0], uu = gu[1];
        unsigned t[3], wd2[3];
        split3(gg * __builtin_amdgcn_rcpf(1.0f + __expf(-gg)) * uu, t[0], t[1], t[2]);
#pragma unroll
        for (int k = 0; k < 3; ++k) {
          const unsigned o = __float_as_uint(dpp<DPP_XOR1>(__uint_as_float(t[k])));
          wd2[k] = nz_bits(__uint_as_float((lane & 1) ? pk_hi(o, t[k]) : pk_hi(t[k], o)));
        }
        __builtin_amdgcn_raw_buffer_store_b32((lane & 1) ? wd2[1] : wd2[0], rs_ws, o_res, 0, AUX_SC1);
        __builtin_amdgcn_raw_buffer_store_b32(wd2[2], rs_ws, (lane & 1) ? OOB : o_res, 256, AUX_SC1);
      }
    } else {
      if (w == 7) return;
      const int bp = tt & 1, ct = tt >> 1;
      for (unsigned spin = 0; spin < (SPIN_CAP << 4) && lds_get(&s_fcnt) < NW; ++spin) __builtin_amdgcn_s_sleep(1);
      if (w == 6) {
        const int bb = lane >> 5, f = lane & 31, offb = (WS_BASE + ((bp * 2 + bb) * (DM / 32) + ct) * 64 + f) * 4;
        const __amdgpu_buffer_rsrc_t rs_out = __builtin_amdgcn_make_buffer_rsrc(out, 0, NB * DM * 2, 0x00020000);
        int o_res = ((bp * 2 + bb) * DM + ct * 32 + f) * 2;
        unsigned bs = 0u, sg = 0u;
        __builtin_amdgcn_s_sleep(100);
        for (unsigned spin = 0; spin < SPIN_CAP; ++spin) {
          AGAIN();
          bs = __builtin_amdgcn_raw_buffer_load_b32(rs_ws, offb, 0, AUX_SC1); sg = __builtin_amdgcn_raw_buffer_load_b32(rs_ws, offb, 128, AUX_SC1);
          if (__all(min(bs, sg) != 0u)) break;
          __builtin_amdgcn_s_sleep(2);
        }
        asm volatile("" : "+v"(o_res));
        __syncthreads();
        float pr[12];
#pragma unroll
        for (int k = 0; k < 12; ++k) pr[k] = s_red[(k >> 1) * 256 + (k & 1) * 64 + lane];
        PIN8(pr, 0); PIN4(pr, 8);
        const float y = (((pr[0] + pr[1]) + (pr[2] + pr[3])) + ((pr[4] + pr[5]) + (pr[6] + pr[7]))) + ((pr[8] + pr[9]) + (pr[10] + pr[11]));
        const bf16 ov = __float2bfloat16(fmaf(y, __uint_as_float(sg), __uint_as_float(bs)));
        __builtin_amdgcn_raw_buffer_store_b16(__builtin_bit_cast(unsigned short, ov), rs_out, o_res, 0, 0);
        return;
      }
      __builtin_amdgcn_s_sleep(8);
      weights(wd + (size_t)(w * 256 + (mc >> 3) * 32 + mg * 8) * DM + ct * 32 + (mc & 7) * 4, 57);
      frags();
      int a_red = w * 64 + mg * 8 + (mc & 7);
      asm volatile("" : "+v"(a_red));
      const int off = (WS_ACT + (((bp * 2 + ((mc >> 2) & 1)) * (FF / 64) + w * 4) * 3 + min(mc & 3, 2)) * 32 + (mc >> 3) * 16 + mg * 4) * 4;
      u32x4 av[4];
      for (unsigned spin = 0; spin < SPIN_CAP; ++spin) {
        AGAIN();
#pragma unroll
        for (int st = 0; st < 4; ++st) av[st] = __builtin_amdgcn_raw_buffer_load_b128(rs_ws, off, st * 384, AUX_SC1);
        if (__all(min(min(min4(av[0]), min4(av[1])), min(min4(av[2]), min4(av[3]))) != 0u)) break;
        __builtin_amdgcn_s_sleep(2);
      }
      f32x4 ac[4];
#pragma unroll
      for (int t = 0; t < 4; ++t) ac[t] = f32x4{0.f, 0.f, 0.f, 0.f};
#pragma unroll
      for (int st = 0; st < 4; ++st)
#pragma unroll
        for (int t = 0; t < 4; ++t) ac[t] = mfma16(av[st], fr[st * 4 + t], ac[t]);
      if ((mc >> 3) == (mg >> 1)) reinterpret_cast<f32x4*>(s_red)[a_red] = f32x4{sum3(ac[0]), sum3(ac[1]), sum3(ac[2]), sum3(ac[3])};
      __syncthreads();
    }
  }
}

extern "C" void kernel_launch(void* const* d_in, const int* in_sizes, int n_in, void* d_out, int out_size, void* d_ws,
                              size_t ws_size, hipStream_t stream) {
  fused_kernel<<<NWG, NW * 64, 0, stream>>>((const bf16*)d_in[0], (const bf16*)d_in[1], (const bf16*)d_in[2], (const bf16*)d_in[3],
                                            (const bf16*)d_in[4], (const bf16*)d_in[6], (const bf16*)d_in[7], (const bf16*)d_in[8],
                                            (const bf16*)d_in[9], (float*)d_ws, (bf16*)d_out);
}
```

```cpp
#include <hip/hip_runtime.h>
#include <hip/hip_bf16.h>
#include <stdint.h>

typedef __hip_bfloat16 bf16;
using u32x4 = __attribute__((ext_vector_type(4))) unsigned;
using u32x2 = __attribute__((ext_vector_type(2))) unsigned;
using f32x4 = __attribute__((ext_vector_type(4))) float;
using f32x2 = __attribute__((ext_vector_type(2))) float;

constexpr int NB = 4, DM = 640, NH = 32, SK = 8192, DH = 192, HD = NH * DH, FF = 1536;
constexpr int NWG = 256, NSEG = 6, NWIN = NB * NH * NSEG / NWG;
constexpr float LN_EPS = 1e-5f;
constexpr float QSCALE = 0.07216878364870322f * 1.4426950408889634f;

constexpr int WS_HP = 0;
constexpr int REC = DH + 32;
constexpr int WS_PO = WS_HP + 2 * NWG;
constexpr int WS_Z0 = WS_PO + 2 * NWG * REC;
constexpr int K3_KS = 12, K3_ROWS = HD / K3_KS;
constexpr int WS_SLAB = WS_PO + NB * NH * NSEG * REC;
constexpr int SLS = NB * DM;
constexpr int WS_ACT = WS_SLAB + K3_KS * SLS;
constexpr int WS_BASE = WS_ACT + NB * FF * 3 / 2;
constexpr int ZPW = 416;
constexpr int WS_QI = WS_Z0 + NWG * ZPW;
constexpr int LSTR = 64;
constexpr int WS_REP = WS_QI + NB * NH * DH * 2;
constexpr int WS_HC = WS_REP + 256 * LSTR;
constexpr int WS_END = WS_HC + NWG * LSTR;
static_assert(WS_BASE + NB * DM * 2 <= WS_QI && WS_Z0 % 64 == 0 && WS_PO % 32 == 0 && ZPW % 32 == 0 && NWIN == 3, "layout");
__device__ __forceinline__ int ef_ofs(int i) { return (WS_PO + (2 * NWG + i) * REC + DH + 2) * 4; }
constexpr int OOB = 0x7ffffff0;

__device__ __forceinline__ float bf_lo(unsigned u) { return __uint_as_float(u << 16); }
__device__ __forceinline__ float bf_hi(unsigned u) { return __uint_as_float(u & 0xffff0000u); }
__device__ __forceinline__ f32x2 bf_pair(unsigned u) { return f32x2{bf_lo(u), bf_hi(u)}; }
__device__ __forceinline__ f32x2 fma2(float a, f32x2 b, f32x2 c) { return __builtin_elementwise_fma(f32x2{a, a}, b, c); }
__device__ __forceinline__ f32x2 fma2(f32x2 a, f32x2 b, f32x2 c) { return __builtin_elementwise_fma(a, b, c); }

template <int CTRL> __device__ __forceinline__ float dpp(float x) {
  return __builtin_bit_cast(float, __builtin_amdgcn_mov_dpp(__builtin_bit_cast(int, x), CTRL, 0xf, 0xf, true));
}
constexpr int DPP_XOR1 = 0xB1, DPP_XOR2 = 0x4E, DPP_HMIRROR = 0x141;

__device__ __forceinline__ float sum_x16_32(float x) {
  auto s = __builtin_amdgcn_permlane16_swap(__float_as_uint(x), __float_as_uint(x), false, false);
  x = __uint_as_float(s[0]) + __uint_as_float(s[1]);
  auto t = __builtin_amdgcn_permlane32_swap(__float_as_uint(x), __float_as_uint(x), false, false);
  return __uint_as_float(t[0]) + __uint_as_float(t[1]);
}
__device__ __forceinline__ float max_x16_32(float x) {
  auto s = __builtin_amdgcn_permlane16_swap(__float_as_uint(x), __float_as_uint(x), false, false);
  x = fmaxf(__uint_as_float(s[0]), __uint_as_float(s[1]));
  auto t = __builtin_amdgcn_permlane32_swap(__float_as_uint(x), __float_as_uint(x), false, false);
  return fmaxf(__uint_as_float(t[0]), __uint_as_float(t[1]));
}
__device__ __forceinline__ float tsum32(float a, float b) {
  auto t = __builtin_amdgcn_permlane32_swap(__float_as_uint(a), __float_as_uint(b), false, false);
  return __uint_as_float(t[0]) + __uint_as_float(t[1]);
}
__device__ __forceinline__ float tsum16(float a, float b) {
  auto t = __builtin_amdgcn_permlane16_swap(__float_as_uint(a), __float_as_uint(b), false, false);
  return __uint_as_float(t[0]) + __uint_as_float(t[1]);
}
constexpr int DPP_ROR8 = 0x128, DPP_BCAST = 0x150;
__device__ __forceinline__ float sum_r(float v) { v += dpp<DPP_ROR8>(v); return sum_x16_32(v); }
__device__ __forceinline__ float max_r(float v) { v = fmaxf(v, dpp<DPP_ROR8>(v)); return max_x16_32(v); }
__device__ __forceinline__ float wave_sum(float v) {
  v += dpp<DPP_XOR1>(v); v += dpp<DPP_XOR2>(v); v += dpp<DPP_HMIRROR>(v);
  return sum_r(v);
}

__device__ __forceinline__ float row16_sum(float v) {
  v += dpp<DPP_XOR1>(v); v += dpp<DPP_XOR2>(v); v += dpp<DPP_HMIRROR>(v);
  return v + dpp<DPP_ROR8>(v);
}

struct Slot { u32x4 a[3]; u32x4 b[3]; };

#define PIN8(a, o) asm volatile("" : "+v"((a)[(o) + 0]), "+v"((a)[(o) + 1]), "+v"((a)[(o) + 2]), "+v"((a)[(o) + 3]), \
                                       "+v"((a)[(o) + 4]), "+v"((a)[(o) + 5]), "+v"((a)[(o) + 6]), "+v"((a)[(o) + 7]))
#define PIN4(a, o) asm volatile("" : "+v"((a)[(o) + 0]), "+v"((a)[(o) + 1]), "+v"((a)[(o) + 2]), "+v"((a)[(o) + 3]))
#define MEMFENCE() do { asm volatile("" ::: "memory"); __builtin_amdgcn_sched_barrier(0); } while (0)
#define AGAIN() asm volatile("" ::: "memory")
#define DRAIN() asm volatile("s_waitcnt vmcnt(0)" ::: "memory")

__device__ __forceinline__ void k_tile(const u32x4 (&k)[3], const f32x2 (&q)[12], float& gmx, float* dst, int j) {
  f32x2 s0 = {0.f, 0.f}, s1 = s0, s2 = s0;
#pragma unroll
  for (int d = 0; d < 4; ++d) {
    s0 = fma2(bf_pair(k[0][d]), q[d], s0);
    s1 = fma2(bf_pair(k[1][d]), q[4 + d], s1);
    s2 = fma2(bf_pair(k[2][d]), q[8 + d], s2);
  }
  s0 = (s0 + s1) + s2;
  float s = s0[0] + s0[1];
  s += dpp<DPP_XOR1>(s);
  s += dpp<DPP_XOR2>(s);
  s += dpp<DPP_HMIRROR>(s);
  gmx = fmaxf(gmx, s);
  asm volatile("" : "+v"(gmx));
  if (j == 0) *dst = s;
}
__device__ __forceinline__ void v_tile(u32x4 (&v)[3], float p, f32x2 (&acc)[12]) {
#pragma unroll
  for (int i = 0; i < 3; ++i) {
    asm volatile("" : "+v"(v[i]), "+v"(p));
#pragma unroll
    for (int d = 0; d < 4; ++d) acc[i * 4 + d] = fma2(p, bf_pair(v[i][d]), acc[i * 4 + d]);
    PIN4(acc, i * 4);
  }
}

constexpr int PTAU = 660;
constexpr int POFF = 140, FFWD = 112;
constexpr int LTGT = 230, LCAP = 600;
constexpr int DOWN = 30, AFROM = 3, EFROM = 12, SLACK = 10;
constexpr int QLOOK = 780;
constexpr int CODE_LINES = 300;
constexpr int AUX_SC1 = 16, AUX_NT = 2;
constexpr unsigned SPIN_CAP = 1u << 17;
__device__ __forceinline__ unsigned nz_bits(float v) { const unsigned u = __float_as_uint(v); return u == 0u ? 0x80000000u : u; }
__device__ __forceinline__ void lds_put(int* p, int v) { __hip_atomic_store(p, v, __ATOMIC_RELAXED, __HIP_MEMORY_SCOPE_WORKGROUP); }
__device__ __forceinline__ int lds_get(int* p) { return __hip_atomic_load(p, __ATOMIC_RELAXED, __HIP_MEMORY_SCOPE_WORKGROUP); }
__device__ __forceinline__ void count_in(int* p) {
  unsigned long long keep;
  asm volatile("s_mov_b64 %0, exec\n\ts_mov_b64 exec, 1\n\tds_add_u32 %1, %2\n\ts_mov_b64 exec, %0"
               : "=&s"(keep) : "v"((unsigned)(size_t)(__attribute__((address_space(3))) int*)p), "v"(1) : "memory");
}
__device__ __forceinline__ int count_in_rtn(int* p) {
  unsigned long long keep;
  int c;
  asm volatile("s_mov_b64 %0, exec\n\ts_mov_b64 exec, 1\n\tds_add_rtn_u32 %1, %2, %3\n\ts_mov_b64 exec, %0"
               : "=&s"(keep), "=&v"(c) : "v"((unsigned)(size_t)(__attribute__((address_space(3))) int*)p), "v"(1) : "memory");
  return c;
}
using bf16x8 = __attribute__((ext_vector_type(8))) short;
__device__ __forceinline__ unsigned pk_lo(unsigned a, unsigned b) { return __builtin_amdgcn_perm(b, a, 0x05040100u); }
__device__ __forceinline__ unsigned pk_hi(unsigned a, unsigned b) { return __builtin_amdgcn_perm(b, a, 0x07060302u); }
__device__ __forceinline__ void split3(float v, unsigned& t0, unsigned& t1, unsigned& t2) {
  t0 = __float_as_uint(v) & 0xffff0000u;
  const float r1 = v - __uint_as_float(t0);
  t1 = __float_as_uint(r1) & 0xffff0000u;
  t2 = __float_as_uint(r1 - __uint_as_float(t1));
}
__device__ __forceinline__ f32x4 mfma16(u32x4 a, u32x4 b, f32x4 c) {
  return __builtin_amdgcn_mfma_f32_16x16x32_bf16(__builtin_bit_cast(bf16x8, a), __builtin_bit_cast(bf16x8, b), c, 0, 0, 0);
}
__device__ __forceinline__ void put_terms4(char* img, int rowbytes, f32x4 v) {
  unsigned t[3][4];
#pragma unroll
  for (int e = 0; e < 4; ++e) split3(v[e], t[0][e], t[1][e], t[2][e]);
#pragma unroll
  for (int k = 0; k < 3; ++k) *reinterpret_cast<u32x2*>(img + k * rowbytes) = u32x2{pk_hi(t[k][0], t[k][1]), pk_hi(t[k][2], t[k][3])};
}
__device__ __forceinline__ unsigned min4(u32x4 v) { return min(min(v[0], v[1]), min(v[2], v[3])); }

__device__ __forceinline__ float sum3(f32x4 v) { return (v[0] + v[1]) + v[2]; }

constexpr int NW = 8, NBUF = 4, NGRP = 64, NT = NGRP / NW;
constexpr int NQP = NH * DH / 64, NQW = 5, QW0 = 2, QST = DM / 64 / NQW;
constexpr int SVC = NW - 1;
constexpr int W_CT = DM / 64, G_TASKS = NB * (FF / 64), W_TASKS = W_CT * K3_KS, D_TASKS = 2 * (DM / 32);
static_assert(G_TASKS + W_TASKS + D_TASKS == NWG && NW == 8 && K3_ROWS == 4 * 128 && NBUF == 4 && SK / 64 == 128, "shape");
__device__ __forceinline__ unsigned fresh_tag(unsigned tags) {
  unsigned t = __builtin_amdgcn_readfirstlane(tags);
  do ++t; while (__any(tags == t));
  return t;
}

__global__ void __launch_bounds__(NW * 64, 2) fused_kernel(const bf16* __restrict__ x, const bf16* __restrict__ kc, const bf16* __restrict__ vc,
                                                        const bf16* __restrict__ wq, const bf16* __restrict__ wo, const bf16* __restrict__ wr,
                                                        const bf16* __restrict__ wg, const bf16* __restrict__ wu, const bf16* __restrict__ wd,
                                                        float* ws, bf16* __restrict__ out) {
  constexpr int PST = DH + 4;
  constexpr int WA = 128 * 2 + 16;
  __shared__ __attribute__((aligned(16))) char s_A[4 * 16 * WA];
  __shared__ __attribute__((aligned(16))) float s_red[6 * 256];
  __shared__ __attribute__((aligned(16))) float s_h[DM];
  __shared__ __attribute__((aligned(16))) float s_buf[NWIN * NW * PST];
  __shared__ __attribute__((aligned(16))) float s_sc[NGRP * 64];
  __shared__ __attribute__((aligned(16))) float s_q[NWIN * DH];
  __shared__ float s_m[NWIN * NW];
  __shared__ int s_mcnt[NWIN];
  __shared__ int s_qok, s_pcnt, s_ecnt, s_fcnt, s_icnt, s_org, s_hcnt, s_ccnt;
  __shared__ int s_prog[NW];

  const int tid = threadIdx.x;
  unsigned pf;
  {
    unsigned long long pc;
    asm volatile("s_getpc_b64 %0" : "=s"(pc));
    pf = *(const __attribute__((address_space(1))) unsigned*)((pc & ~127ull) + min(tid, CODE_LINES - 1) * 128);
    __builtin_amdgcn_sched_barrier(0);
  }
  int lane = tid & 63, r = lane >> 3, j = lane & 7, mg = lane >> 4, mc = lane & 15;
  int wave = __builtin_amdgcn_readfirstlane(tid >> 6), bid = blockIdx.x;
  const unsigned t_start = (unsigned)__builtin_amdgcn_s_memrealtime();
  const int wid = (bid >> 5) * 32 + (bid & 7) * 4 + ((bid >> 3) & 3);
  const bool prod = bid < NQP;
  const __amdgpu_buffer_rsrc_t rs_ws = __builtin_amdgcn_make_buffer_rsrc(ws, 0, WS_END * 4, 0x00020000);
  const __amdgpu_buffer_rsrc_t rs_k = __builtin_amdgcn_make_buffer_rsrc(const_cast<bf16*>(kc), 0, NB * NH * SK * DH * 2, 0x00020000);
  const __amdgpu_buffer_rsrc_t rs_v = __builtin_amdgcn_make_buffer_rsrc(const_cast<bf16*>(vc), 0, NB * NH * SK * DH * 2, 0x00020000);
  const int voff = r * (DH * 2) + j * 16;
  const int c1 = wid % 3 == 0 ? 22 : 21, c2 = wid % 3 == 1 ? 42 : 43;
  auto seg_of = [&](int n) { return (n * 8 + wave >= c1) + (n * 8 + wave >= c2); };
  auto gload = [&](Slot (&sl)[NBUF], __amdgpu_buffer_rsrc_t rs, int n) {
    const int sn = seg_of(n), sg = wid + NWG * sn, row = sg / NSEG, k = sg - row * NSEG;
    const int off = (row * (SK / 64) + (k >> 1) * 43 + (n * 8 + wave - (sn == 0 ? 0 : (sn == 1 ? c1 : c2))) * 2 + ((k ^ (k >> 1)) & 1)) * (64 * DH * 2);
#pragma unroll
    for (int u = 0; u < NBUF; ++u) {
#pragma unroll
      for (int i = 0; i < 3; ++i) sl[u].a[i] = __builtin_amdgcn_raw_buffer_load_b128(rs, voff + i * 128, off + 2 * u * 3072, AUX_NT);
      MEMFENCE();
#pragma unroll
      for (int i = 0; i < 3; ++i) sl[u].b[i] = __builtin_amdgcn_raw_buffer_load_b128(rs, voff + i * 128, off + (2 * u + 1) * 3072, AUX_NT);
      if (u < NBUF - 1) __builtin_amdgcn_s_sleep(3);
      MEMFENCE();
    }
  };
  asm volatile("" :: "s"(ws), "s"(x), "s"(wq), "s"(kc));
  Slot b0[NBUF];
  const bool early = wave == 0 && !prod;
  if (early) gload(b0, rs_k, 0);
  unsigned hs_nc = 0u, hs_ep = 0u;
  auto look_at_p = [&]() {
    AGAIN();
    if (bid == NWG - 1) {
      u32x4 hp[2], hc[2];
#pragma unroll
      for (int i = 0; i < 2; ++i) {
        hp[i] = __builtin_amdgcn_raw_buffer_load_b128(rs_ws, (WS_HP + lane * 8 + i * 4) * 4, 0, AUX_SC1);
        const u32x2 c0 = __builtin_amdgcn_raw_buffer_load_b64(rs_ws, (WS_HC + (lane * 4 + i * 2) * LSTR) * 4, 0, AUX_SC1);
        const u32x2 c1_ = __builtin_amdgcn_raw_buffer_load_b64(rs_ws, (WS_HC + (lane * 4 + i * 2 + 1) * LSTR) * 4, 0, AUX_SC1);
        hc[i] = u32x4{c0[0], c0[1], c1_[0], c1_[1]};
      }
      if (__all(hc[0][1] == hp[0][0] && hc[0][3] == hp[0][2] && hc[1][1] == hp[1][0] && hc[1][3] == hp[1][2]) &&
          __any(hc[0][0] != hp[0][1] || hc[0][2] != hp[0][3] || hc[1][0] != hp[1][1] || hc[1][2] != hp[1][3])) {
#pragma unroll
        for (int i = 0; i < 2; ++i)
          __builtin_amdgcn_raw_buffer_store_b128(u32x4{hp[i][0], hc[i][0], hp[i][2], hc[i][2]}, rs_ws, (WS_HP + lane * 8 + i * 4) * 4, 0, AUX_SC1);
      }
    }
    const u32x2 pw = __builtin_amdgcn_raw_buffer_load_b64(rs_ws, (WS_HP + bid * 2) * 4, 0, AUX_SC1);
    const unsigned np = __builtin_amdgcn_readfirstlane(pw[0]), ec = __builtin_amdgcn_readfirstlane(pw[1]);
    if (np != hs_ep) { hs_ep = np; __builtin_amdgcn_raw_buffer_store_b64(u32x2{hs_nc, hs_ep}, rs_ws, lane == 0 ? (WS_HC + bid * LSTR) * 4 : OOB, 0, AUX_SC1); }
    return ec == hs_nc;
  };
  auto answer = [&]() {
    const u32x4 z4 = {0u, 0u, 0u, 0u};
#pragma unroll
    for (int i = 0; i < (ZPW + 255) / 256; ++i)
      __builtin_amdgcn_raw_buffer_store_b128(z4, rs_ws, i * 256 + lane * 4 < ZPW ? (WS_Z0 + bid * ZPW + i * 256 + lane * 4) * 4 : OOB, 0, AUX_SC1);
    const u32x2 pw = __builtin_amdgcn_raw_buffer_load_b64(rs_ws, (WS_HP + bid * 2) * 4, 0, AUX_SC1);
    DRAIN();
    hs_ep = __builtin_amdgcn_readfirstlane(pw[0]);
    hs_nc = __builtin_amdgcn_readfirstlane(pw[1]) + 1u;
    __builtin_amdgcn_raw_buffer_store_b64(u32x2{hs_nc, hs_ep}, rs_ws, lane == 0 ? (WS_HC + bid * LSTR) * 4 : OOB, 0, AUX_SC1);
  };
  bool answered = false;
  if (tid < NW) s_prog[tid] = 1;
  if (tid < NWIN) s_mcnt[tid] = 0;
  if (tid == 0) { s_qok = 0; s_pcnt = 0; s_ecnt = 0; s_fcnt = 0; s_icnt = 0; s_hcnt = 0; s_ccnt = 0; s_org = (int)(t_start + POFF); }
  if (wave == SVC) {
    __builtin_amdgcn_s_setprio(3);
    int iofs[NWIN];
#pragma unroll
    for (int s = 0; s < NWIN; ++s) iofs[s] = (WS_QI + ((wid + NWG * s) / NSEG * DH + lane) * 2) * 4;
    const int oofs = prod ? (WS_QI + (bid * 64 + lane) * 2) * 4 : OOB;
    u32x2 in[NWIN * 3];
    unsigned ot[NB], ce[4];
#pragma unroll
    for (int e = 0; e < NWIN * 3; ++e) in[e] = __builtin_amdgcn_raw_buffer_load_b64(rs_ws, iofs[e / 3], (e % 3) * 512, AUX_SC1);
#pragma unroll
    for (int bb = 0; bb < NB; ++bb) ot[bb] = __builtin_amdgcn_raw_buffer_load_b64(rs_ws, oofs, bb * (HD * 8), AUX_SC1)[1];
    u32x4 xr[5], hp[2];
#pragma unroll
    for (int i = 0; i < 5; ++i) xr[i] = reinterpret_cast<const u32x4*>(x)[lane * 5 + i];
#pragma unroll
    for (int i = 0; i < 2; ++i) hp[i] = __builtin_amdgcn_raw_buffer_load_b128(rs_ws, bid == NWG - 1 ? (WS_HP + lane * 8 + i * 4) * 4 : OOB, 0, AUX_SC1);
#pragma unroll
    for (int i = 0; i < 4; ++i) ce[i] = __builtin_amdgcn_raw_buffer_load_b32(rs_ws, bid == NWG - 1 ? (WS_HC + (lane * 4 + i) * LSTR + 1) * 4 : OOB, 0, AUX_SC1);
    MEMFENCE();
    __syncthreads();
    MEMFENCE();
    gload(b0, rs_k, 0);
    if (bid == NWG - 1) {
#pragma unroll
      for (int i = 0; i < 2; ++i) {
#pragma unroll
        for (int e = 0; e < 2; ++e) { unsigned np = hp[i][2 * e] + 1u; if (np == ce[2 * i + e]) np += 1u; hp[i][2 * e] = np; }
        __builtin_amdgcn_raw_buffer_store_b128(hp[i], rs_ws, (WS_HP + lane * 8 + i * 4) * 4, 0, AUX_SC1);
      }
    }
    unsigned want[NWIN * 3];
#pragma unroll
    for (int e = 0; e < NWIN * 3; ++e) want[e] = fresh_tag(in[e][1]);
    if (prod) {
      float xs = 0.f;
#pragma unroll
      for (int i = 0; i < 20; ++i) xs += bf_lo(xr[i >> 2][i & 3]) + bf_hi(xr[i >> 2][i & 3]);
      const float mean = row16_sum(xs) * (1.0f / DM);
      float xv = 0.f;
#pragma unroll
      for (int i = 0; i < 20; ++i) { const float a = bf_lo(xr[i >> 2][i & 3]) - mean, c = bf_hi(xr[i >> 2][i & 3]) - mean; xv += a * a + c * c; }
      const float rstd = QSCALE / sqrtf(row16_sum(xv) * (1.0f / DM) + LN_EPS);
#pragma unroll
      for (int bb = 0; bb < NB; ++bb) ot[bb] = fresh_tag(ot[bb]);
      for (unsigned spin = 0; spin < (SPIN_CAP << 4) && lds_get(&s_pcnt) < NQW; ++spin) __builtin_amdgcn_s_sleep(1);
      __builtin_amdgcn_fence(__ATOMIC_ACQUIRE, "workgroup");
      float pr[5];
#pragma unroll
      for (int i = 0; i < 5; ++i) {
        pr[i] = 0.f;
#pragma unroll
        for (int k = 0; k < NQW; ++k) pr[i] += s_buf[2 * k * 320 + i * 64 + lane] + s_buf[(2 * k + 1) * 320 + i * 64 + lane];
      }
#pragma unroll
      for (int bb = 0; bb < NB; ++bb) {
        const float qv = (pr[bb] - __int_as_float(__builtin_amdgcn_readlane(__float_as_int(mean), bb * 16)) * pr[4]) *
                         __int_as_float(__builtin_amdgcn_readlane(__float_as_int(rstd), bb * 16));
        __builtin_amdgcn_raw_buffer_store_b64(u32x2{__float_as_uint(qv), ot[bb]}, rs_ws, oofs, bb * (HD * 8), AUX_SC1);
      }
    }
    for (unsigned spin = 0; spin < 1024 && (int)((unsigned)__builtin_amdgcn_s_memrealtime() - t_start) < QLOOK; ++spin) __builtin_amdgcn_s_sleep(4);
    bool all_out = false;
#pragma unroll 1
    for (unsigned spin = 0; spin < SPIN_CAP; ++spin) {
      AGAIN();
#pragma unroll
      for (int e = 0; e < NWIN * 3; ++e) in[e] = __builtin_amdgcn_raw_buffer_load_b64(rs_ws, iofs[e / 3], (e % 3) * 512, AUX_SC1);
      bool ok = true;
#pragma unroll
      for (int e = 0; e < NWIN * 3; ++e) ok = ok && in[e][1] == want[e];
      if (all_out || __all(ok)) break;
      if (spin == 15) { answer(); answered = true; }
      if (spin > 15 && (spin & 3) == 3) all_out = look_at_p();
      __builtin_amdgcn_s_sleep(4);
    }
#pragma unroll
    for (int e = 0; e < NWIN * 3; ++e) s_q[e * 64 + lane] = __uint_as_float(in[e][0]);
    __builtin_amdgcn_fence(__ATOMIC_RELEASE, "workgroup");
    lds_put(&s_qok, 1);
    __builtin_amdgcn_s_setprio(0);
  } else if (prod && wave >= QW0 && wave < QW0 + NQW) {
    const int k0 = (wave - QW0) * (QST * 64) + (mc >> 3) * 32 + mg * 8;
    u32x4 raw[QST * 8], xa[QST];
    const bf16* wb = wq + (size_t)k0 * HD + bid * 64 + (mc & 7) * 8;
#pragma unroll
    for (int st = 0; st < QST; ++st) xa[st] = *reinterpret_cast<const u32x4*>(x + (mc & 3) * DM + k0 + st * 64);
#pragma unroll
    for (int e = 0; e < QST * 8; ++e) raw[e] = __builtin_nontemporal_load(reinterpret_cast<const u32x4*>(wb + (size_t)((e >> 3) * 64 + (e & 7)) * HD));
    MEMFENCE();
    __syncthreads();
    MEMFENCE();
    f32x4 ac[8];
#pragma unroll
    for (int t = 0; t < 8; ++t) ac[t] = f32x4{0.f, 0.f, 0.f, 0.f};
#pragma unroll
    for (int st = 0; st < QST; ++st) {
      const u32x4 a = (mc & 4) == 0 ? xa[st] : u32x4{0x3f803f80u, 0x3f803f80u, 0x3f803f80u, 0x3f803f80u};
#pragma unroll
      for (int c = 0; c < 4; ++c) {
        u32x4 f0, f1;
#pragma unroll
        for (int i = 0; i < 4; ++i) { f0[i] = pk_lo(raw[st * 8 + 2 * i][c], raw[st * 8 + 2 * i + 1][c]); f1[i] = pk_hi(raw[st * 8 + 2 * i][c], raw[st * 8 + 2 * i + 1][c]); }
        ac[2 * c] = mfma16(a, f0, ac[2 * c]);
        ac[2 * c + 1] = mfma16(a, f1, ac[2 * c + 1]);
      }
    }
    if ((mg >> 1) == (mc >> 3)) {
      float* const dst = &s_buf[((wave - QW0) * 2 + (mg >> 1)) * 320 + (mc & 7) * 8];
#pragma unroll
      for (int bb = 0; bb < NB; ++bb)
        if (bb == 0 || (mg & 1) == 0) {
          *reinterpret_cast<f32x4*>(dst + ((mg & 1) * 4 + bb) * 64) = f32x4{ac[0][bb], ac[1][bb], ac[2][bb], ac[3][bb]};
          *reinterpret_cast<f32x4*>(dst + ((mg & 1) * 4 + bb) * 64 + 4) = f32x4{ac[4][bb], ac[5][bb], ac[6][bb], ac[7][bb]};
        }
    }
    __builtin_amdgcn_fence(__ATOMIC_RELEASE, "workgroup");
    if (lane == 0) __hip_atomic_fetch_add(&s_pcnt, 1, __ATOMIC_RELAXED, __HIP_MEMORY_SCOPE_WORKGROUP);
  } else {
    __syncthreads();
  }
  MEMFENCE();
  const int gtile = ((bid & 7) * 12 + (bid >> 3) - 15) >> 2;
  const int tau = PTAU - (((bid >> 3) >= 15 && (bid >> 3) < 27 && (((((unsigned)(size_t)wg >> 7) + gtile) & 7) == 3 || ((((unsigned)(size_t)wu >> 7) + gtile) & 7) == 3)) ? 10 : 0);
  u32x2 rep = {0u, 0u};
  unsigned t_thru = 0u;
  auto pace = [&](int g) {
    if (wave == 0 && g > AFROM) {
      const int org = lds_get(&s_org);
      const bool ok = rep[0] - t_start + 1000u < 6000u && rep[1] - t_start + 1000u < 6000u;
      const float dorg = row16_sum(ok ? (float)(int)(rep[1] - (unsigned)org) : 0.f);
      const float lt = ok ? (float)min((int)(rep[0] - rep[1]), LCAP) : (float)LCAP;
      const float lx = fminf(lt, dpp<DPP_ROR8>(lt));
      float hi = fmaxf(lx, dpp<DPP_XOR1>(lx)); hi = fmaxf(hi, dpp<DPP_XOR2>(hi)); hi = fmaxf(hi, dpp<DPP_HMIRROR>(hi));
      const float lat = row16_sum(lt) * (0.8f / 16.f) + hi * 0.2f;
      if (lane == 0) lds_put(&s_org, org + (int)(dorg * (1.f / 16.f) + fmaxf((lat - LTGT) * 0.6f, g > EFROM ? (float)SLACK : -(float)DOWN)));
    }
    const unsigned rel = (unsigned)(g * tau + (wave + (g == 0 && wave > 0)) * (PTAU / 8) + min(g * 8 + wave, 16) * (FFWD / 16) - FFWD + ((wid * 65) & 255) * (PTAU / 8) / 256);
    unsigned now = (unsigned)__builtin_amdgcn_s_memrealtime();
    if (wave == 1 && (bid >> 4) == 7 && g >= 2)
      __builtin_amdgcn_raw_buffer_store_b64(u32x2{t_thru - rel + tau, (unsigned)lds_get(&s_org)}, rs_ws, lane == 0 ? (WS_REP + ((g - 2) * 16 + (bid & 15)) * LSTR) * 4 : OOB, 0, AUX_SC1);
    for (unsigned spin = 0; spin < 4096 && (int)(now - rel - (unsigned)__builtin_amdgcn_readfirstlane(lds_get(&s_org))) < 0; ++spin) {
      __builtin_amdgcn_s_sleep(2);
      now = (unsigned)__builtin_amdgcn_s_memrealtime();
    }
    if (g >= 2) rep = __builtin_amdgcn_raw_buffer_load_b64(rs_ws, (wave == 0 && lane < 16 && g >= AFROM) ? (WS_REP + ((g - 2) * 16 + lane) * LSTR) * 4 : OOB, 0, AUX_SC1);
    if (lane == 0) lds_put(&s_prog[wave], g);
    int pp = __builtin_amdgcn_readfirstlane(lds_get(&s_prog[wave ^ (NW / 2)]));
    for (unsigned spin = 0; spin < 4096 && g > pp + 1; ++spin) { __builtin_amdgcn_s_sleep(4); pp = __builtin_amdgcn_readfirstlane(lds_get(&s_prog[wave ^ (NW / 2)])); }
    if (g > pp) __builtin_amdgcn_s_setprio(0); else __builtin_amdgcn_s_setprio(1);
  };
  float gmx = -INFINITY;
  auto k_group = [&](const Slot (&sl)[NBUF], int n, int s) {
    f32x2 q[12];
#pragma unroll
    for (int i = 0; i < 6; ++i) {
      const f32x4 a = *reinterpret_cast<const f32x4*>(&s_q[s * DH + (i >> 1) * 64 + j * 8 + (i & 1) * 4]);
      q[i * 2] = f32x2{a[0], a[1]}; q[i * 2 + 1] = f32x2{a[2], a[3]};
    }
    float* const sc = &s_sc[(wave * NT + n) * 64 + r * 8];
    MEMFENCE();
#pragma unroll
    for (int u = 0; u < NBUF; ++u) {
      k_tile(sl[u].a, q, gmx, sc + 2 * u, j);
      k_tile(sl[u].b, q, gmx, sc + 2 * u + 1, j);
      MEMFENCE();
    }
    t_thru = (unsigned)__builtin_amdgcn_s_memrealtime();
  };
  {
    Slot b1[NBUF];
    asm volatile("" :: "v"(pf));
    if (!early && wave != SVC) {
      pace(0);
      gload(b0, rs_k, 0);
    }
    pace(1);
    gload(b1, rs_k, 1);
    for (unsigned spin = 0; spin < (SPIN_CAP << 4) && lds_get(&s_qok) == 0; ++spin) __builtin_amdgcn_s_sleep(2);
    __builtin_amdgcn_fence(__ATOMIC_ACQUIRE, "workgroup");
    k_group(b0, 0, 0);
    k_group(b1, 1, 0);
    if (wave == SVC && !answered) answer();
  }
  auto k_done = [&](int s) {
    const float gm = max_r(gmx);
    if (lane == 0) s_m[s * NW + wave] = gm;
    __builtin_amdgcn_fence(__ATOMIC_RELEASE, "workgroup");
    if (lane == 0) __hip_atomic_fetch_add(&s_mcnt[s], 1, __ATOMIC_RELAXED, __HIP_MEMORY_SCOPE_WORKGROUP);
    gmx = -INFINITY;
  };
  Slot buf[NBUF];
  int seg = 0;
  bool through = false;
  for (int n = 2; n < NT; ++n) {
    pace(n);
    gload(buf, rs_k, n);
    if (seg_of(n) != seg) {
      k_done(seg);
      ++seg;
    }
    k_group(buf, n, seg);
    if (wave == SVC && bid == NWG - 1 && n >= 3 && !through) through = look_at_p();
  }
  k_done(seg);
  const int d4 = lane < DH / 4 ? ((lane >> 1) & 7) * 6 + (lane >> 4) * 2 + (lane & 1) : DH / 4;
  const int rofs = lane < REC / 4 ? (WS_PO + wid * REC + lane * 4) * 4 : OOB;
  float l = 0.f;
  f32x2 acc[12];
#pragma unroll
  for (int i = 0; i < 12; ++i) acc[i] = f32x2{0.f, 0.f};
  __builtin_amdgcn_fence(__ATOMIC_RELEASE, "wavefront"); __builtin_amdgcn_wave_barrier(); __builtin_amdgcn_fence(__ATOMIC_ACQUIRE, "wavefront");
  for (int n = 0; n < NT; ++n) {
    pace(NT + n);
    gload(buf, rs_v, n);
    if (n == NT - 1) {
      if (lane == 0) __hip_atomic_fetch_add(&s_icnt, 1, __ATOMIC_RELAXED, __HIP_MEMORY_SCOPE_WORKGROUP);
      __builtin_amdgcn_s_setprio(3);
    }
    seg = seg_of(n);
    for (unsigned spin = 0; spin < (SPIN_CAP << 4) && lds_get(&s_mcnt[seg]) < NW; ++spin) __builtin_amdgcn_s_sleep(2);
    __builtin_amdgcn_fence(__ATOMIC_ACQUIRE, "workgroup");
    float m = s_m[seg * NW + j], lw;
    m = fmaxf(m, dpp<DPP_XOR1>(m)); m = fmaxf(m, dpp<DPP_XOR2>(m)); m = fmaxf(m, dpp<DPP_HMIRROR>(m));
    {
      float p[8];
      const float* const sc = &s_sc[(wave * NT + n) * 64 + r * 8];
      const f32x4 sa = *reinterpret_cast<const f32x4*>(sc), sb = *reinterpret_cast<const f32x4*>(sc + 4);
#pragma unroll
      for (int t = 0; t < 4; ++t) { p[t] = __builtin_amdgcn_exp2f(sa[t] - m); p[4 + t] = __builtin_amdgcn_exp2f(sb[t] - m); }
      l += ((p[0] + p[1]) + (p[2] + p[3])) + ((p[4] + p[5]) + (p[6] + p[7]));
      lw = sum_r(l);
      asm volatile("" : "+v"(lw));
      MEMFENCE();
#pragma unroll
      for (int u = 0; u < NBUF; ++u) {
        v_tile(buf[u].a, p[2 * u], acc);
        v_tile(buf[u].b, p[2 * u + 1], acc);
        MEMFENCE();
      }
    }
    t_thru = (unsigned)__builtin_amdgcn_s_memrealtime();
    if (wave == SVC && n == 1)
      for (unsigned spin = 0; spin < SPIN_CAP && !look_at_p(); ++spin) __builtin_amdgcn_s_sleep(4);
    if (n == NT - 1 || seg_of(n + 1) != seg) {
      float t12[12], t6[6];
#pragma unroll
      for (int e = 0; e < 12; ++e) t12[e] = tsum32(acc[e >> 1][e & 1], acc[6 + (e >> 1)][e & 1]);
#pragma unroll
      for (int e = 0; e < 6; ++e) { t6[e] = tsum16(t12[e], t12[e + 6]); t6[e] += dpp<DPP_ROR8>(t6[e]); }
      if ((lane & 8) == 0) {
#pragma unroll
        for (int e = 0; e < 3; ++e) *reinterpret_cast<f32x2*>(&s_buf[(seg * NW + wave) * PST + j * 24 + (lane >> 4) * 6 + e * 2]) = f32x2{t6[2 * e], t6[2 * e + 1]};
      }
      if (lane == 0) *reinterpret_cast<f32x2*>(&s_buf[(seg * NW + wave) * PST + DH]) = f32x2{m, lw};
      l = 0.f;
#pragma unroll
      for (int i = 0; i < 12; ++i) acc[i] = f32x2{0.f, 0.f};
      if (seg != 0) {
        int c = count_in_rtn(seg == 1 ? &s_ecnt : &s_fcnt);
        MEMFENCE();
        auto merged = [&](int s) {
          f32x4 pw[NW];
#pragma unroll
          for (int w = 0; w < NW; ++w) pw[w] = reinterpret_cast<const f32x4*>(s_buf)[(s * NW + w) * (PST / 4) + d4];
          f32x4 o = ((pw[0] + pw[1]) + (pw[2] + pw[3])) + ((pw[4] + pw[5]) + (pw[6] + pw[7]));
          if (lane >= DH / 4) o = f32x4{pw[0][0], o[1], 1.f, 1.f};
          return u32x4{nz_bits(o[0]), nz_bits(o[1]), nz_bits(o[2]), nz_bits(o[3])};
        };
        u32x4 rec = merged(seg);
        asm volatile("s_waitcnt lgkmcnt(0)" : "+v"(rec), "+v"(c) :: "memory");
        if (__builtin_amdgcn_readfirstlane(c) == NW - 1) {
          __builtin_amdgcn_raw_buffer_store_b128(rec, rs_ws, rofs, seg * (NWG * REC * 4), AUX_SC1);
          if (seg == 1) __builtin_amdgcn_raw_buffer_store_b128(merged(0), rs_ws, rofs, 0, AUX_SC1);
          if (seg == 2) { MEMFENCE(); lds_put(&s_fcnt, NW + 1); }
          if (seg == 1) {
            DRAIN();
            __builtin_amdgcn_raw_buffer_store_b32(1u, rs_ws, lane == 0 ? ef_ofs(wid) : OOB, 0, AUX_SC1);
          }
        }
      }
      MEMFENCE();
    }
  }
  __builtin_amdgcn_s_setprio(0);

  {
    asm volatile("" : "+s"(wave), "+s"(bid), "+v"(lane));
    r = lane >> 3; j = lane & 7; mg = lane >> 4; mc = lane & 15;
    const int w = wave;
    const int xcd = bid & 7, ri = bid >> 3;
    const bool isW = ri < 15, isG = !isW && ri < 27;
    const int tt = isW ? xcd * 15 + ri : (isG ? xcd * 12 + ri - 15 : xcd * 5 + ri - 27);
    const int ks1 = tt / W_CT, ct1 = tt - ks1 * W_CT;
    typedef const __attribute__((address_space(1))) char* gptr;
    auto behind_stream = [&](int nsl, int cnt) {
      for (unsigned spin = 0; spin < (SPIN_CAP << 4) && lds_get(&s_icnt) < cnt; ++spin) __builtin_amdgcn_s_sleep(1);
      for (int i = 0; i < nsl && lds_get(&s_fcnt) <= NW; ++i) __builtin_amdgcn_s_sleep(3);
    };
    u32x4 fr[16];
    u32x2 raw[32];
    auto weights = [&](const bf16* src, int skip) {
      gptr wb = (gptr)src;
#pragma unroll
      for (int e = 0; e < 32; ++e) {
        raw[e] = __builtin_nontemporal_load((const __attribute__((address_space(1))) u32x2*)wb);
        wb += ((e & 7) == 7 ? skip : 1) * (DM * 2);
        asm volatile("" : "+v"(wb));
      }
      MEMFENCE();
    };
    auto frags = [&]() {
#pragma unroll
      for (int st = 0; st < 4; ++st) {
        PIN8(raw, st * 8);
#pragma unroll
        for (int i = 0; i < 4; ++i) {
          const u32x2 r0 = raw[st * 8 + 2 * i], r1 = raw[st * 8 + 2 * i + 1];
          fr[st * 4 + 0][i] = pk_lo(r0[0], r1[0]); fr[st * 4 + 1][i] = pk_hi(r0[0], r1[0]);
          fr[st * 4 + 2][i] = pk_lo(r0[1], r1[1]); fr[st * 4 + 3][i] = pk_hi(r0[1], r1[1]);
        }
        PIN4(fr, st * 4);
        MEMFENCE();
      }
    };
    if (isW) {
      if (w >= 4) return;
      int a_put = w * (16 * WA) + mg * 4 * WA + mc * 8, a_get = w * (16 * WA / 16) + mc * (WA / 16) + mg, a_red = w * 64 + lane;
      int o_res = (WS_SLAB + ks1 * SLS + mg * DM + ct1 * 64 + mc * 4) * 4;
      {
        int offp[2], sg0[2];
        bool er[2];
#pragma unroll
        for (int hf = 0; hf < 2; ++hf) {
          const int row = ks1 * K3_ROWS + w * 128 + hf * 64, hh = row / DH;
          sg0[hf] = (mg * NH + hh) * NSEG;
          offp[hf] = (WS_PO + sg0[hf] * REC + row - hh * DH + mc * 4) * 4;
          er[hf] = sg0[hf] + NSEG <= 2 * NWG;
        }
        const int sgm = (mc < 8 ? sg0[0] : sg0[1]) + j, offm = j < NSEG ? (WS_PO + sgm * REC + DH) * 4 : OOB;
        for (unsigned spin = 0; spin < (SPIN_CAP << 4) && lds_get(&s_ecnt) < NW; ++spin) __builtin_amdgcn_s_sleep(4);
        for (unsigned spin = 0; spin < SPIN_CAP; ++spin) {
          AGAIN();
          const bool fl = j < NSEG && sgm < 2 * NWG;
          if (__all(!fl || __builtin_amdgcn_raw_buffer_load_b32(rs_ws, fl ? ef_ofs(sgm & (NWG - 1)) : OOB, 0, AUX_SC1) != 0u)) break;
          __builtin_amdgcn_s_sleep(8);
        }
        auto look = [&](u32x4 (&d)[2 * NSEG], bool h0, bool h1) {
          const int o0_ = h0 ? offp[0] : OOB, o1_ = h1 ? offp[1] : OOB;
#pragma unroll
          for (int k = 0; k < 2 * NSEG; ++k) d[k] = __builtin_amdgcn_raw_buffer_load_b128(rs_ws, k < NSEG ? o0_ : o1_, (k % NSEG) * REC * 4, AUX_SC1);
          MEMFENCE();
        };
        auto wsum = [&](float wt, const u32x4 (&d)[2 * NSEG], f32x4& a0, f32x4& a1) {
          a0 += dpp<DPP_BCAST + 0>(wt) * __builtin_bit_cast(f32x4, d[0]); a1 += dpp<DPP_BCAST + 8>(wt) * __builtin_bit_cast(f32x4, d[6]);
          a0 += dpp<DPP_BCAST + 1>(wt) * __builtin_bit_cast(f32x4, d[1]); a1 += dpp<DPP_BCAST + 9>(wt) * __builtin_bit_cast(f32x4, d[7]);
          a0 += dpp<DPP_BCAST + 2>(wt) * __builtin_bit_cast(f32x4, d[2]); a1 += dpp<DPP_BCAST + 10>(wt) * __builtin_bit_cast(f32x4, d[8]);
          a0 += dpp<DPP_BCAST + 3>(wt) * __builtin_bit_cast(f32x4, d[3]); a1 += dpp<DPP_BCAST + 11>(wt) * __builtin_bit_cast(f32x4, d[9]);
          a0 += dpp<DPP_BCAST + 4>(wt) * __builtin_bit_cast(f32x4, d[4]); a1 += dpp<DPP_BCAST + 12>(wt) * __builtin_bit_cast(f32x4, d[10]);
          a0 += dpp<DPP_BCAST + 5>(wt) * __builtin_bit_cast(f32x4, d[5]); a1 += dpp<DPP_BCAST + 13>(wt) * __builtin_bit_cast(f32x4, d[11]);
        };
        u32x4 pe[2 * NSEG], pl[2 * NSEG];
        behind_stream(6, NW - 1);
        weights(wo + (size_t)(ks1 * K3_ROWS + w * 128 + mg * 8) * DM + ct1 * 64 + mc * 4, 25);
        look(pe, er[0], er[1]);
        for (unsigned sp2 = 0; sp2 < (SPIN_CAP << 4) && lds_get(&s_fcnt) < NW; ++sp2) __builtin_amdgcn_s_sleep(1);
        f32x4 o0, o1;
        asm volatile("" : "+v"(a_put), "+v"(a_get), "+v"(a_red), "+v"(o_res));
        u32x2 ml = __builtin_amdgcn_raw_buffer_load_b64(rs_ws, offm, 0, AUX_SC1);
        look(pl, !er[0], !er[1]);
        frags();
        MEMFENCE();
#pragma unroll 1
        for (unsigned spin = 0; spin < SPIN_CAP; ++spin) {
          AGAIN();
          const float mm = j < NSEG ? __uint_as_float(ml[0]) : -INFINITY;
          float M = fmaxf(mm, dpp<DPP_XOR1>(mm)); M = fmaxf(M, dpp<DPP_XOR2>(M)); M = fmaxf(M, dpp<DPP_HMIRROR>(M));
          const float e = __builtin_amdgcn_exp2f(mm - M);
          float L = e * __uint_as_float(ml[1]);
          L += dpp<DPP_XOR1>(L); L += dpp<DPP_XOR2>(L); L += dpp<DPP_HMIRROR>(L);
          const float wt = e * __builtin_amdgcn_rcpf(L);
          o0 = f32x4{0.f, 0.f, 0.f, 0.f}; o1 = o0;
          wsum(wt, pe, o0, o1);
          unsigned m0 = er[0], m1 = er[1], a0 = ~0u, a1 = ~0u;
          const bool okm = j >= NSEG || min(ml[0], ml[1]) != 0u;
          asm volatile("" : "+v"(o0), "+v"(o1));
          MEMFENCE();
          wsum(wt, pl, o0, o1);
          asm volatile("" : "+v"(o0), "+v"(o1));
#pragma unroll
          for (int k = 0; k < NSEG; ++k) { a0 = min(a0, min4(pl[k])); a1 = min(a1, min4(pl[NSEG + k])); }
          if (__all(okm && (a0 | m0) != 0u && (a1 | m1) != 0u)) break;
          if (spin >= 8) __builtin_amdgcn_s_sleep(4);
          ml = __builtin_amdgcn_raw_buffer_load_b64(rs_ws, offm, 0, AUX_SC1);
          look(pl, !er[0], !er[1]);
        }
        put_terms4(s_A + a_put, WA, o0);
        put_terms4(s_A + a_put + 128, WA, o1);
      }
      __builtin_amdgcn_fence(__ATOMIC_RELEASE, "wavefront"); __builtin_amdgcn_wave_barrier(); __builtin_amdgcn_fence(__ATOMIC_ACQUIRE, "wavefront");
      f32x4 ac[4];
#pragma unroll
      for (int t = 0; t < 4; ++t) ac[t] = f32x4{0.f, 0.f, 0.f, 0.f};
      u32x4 av[4];
#pragma unroll
      for (int st = 0; st < 4; ++st) av[st] = reinterpret_cast<const u32x4*>(s_A)[a_get + st * 4];
      MEMFENCE();
#pragma unroll
      for (int st = 0; st < 4; ++st)
#pragma unroll
        for (int t = 0; t < 4; ++t) ac[t] = mfma16(av[st], fr[st * 4 + t], ac[t]);
      reinterpret_cast<f32x4*>(s_red)[a_red] = f32x4{sum3(ac[0]), sum3(ac[1]), sum3(ac[2]), sum3(ac[3])};
      __syncthreads();
      if (w == 0) {
        const f32x4* const red = reinterpret_cast<const f32x4*>(s_red) + a_red;
        const f32x4 s0 = (red[0] + red[64]) + (red[128] + red[192]);
        __builtin_amdgcn_raw_buffer_store_b128(u32x4{nz_bits(s0[0]), nz_bits(s0[1]), nz_bits(s0[2]), nz_bits(s0[3])}, rs_ws, o_res, 0, AUX_SC1);
      }
    } else if (isG) {
      const int gb = tt & 3, gt = tt >> 2;
      int o_res = (WS_ACT + (gb * (FF / 64) + gt) * 96 + (lane & 1) * 32 + (lane >> 1)) * 4;
      asm volatile("" : "+v"(o_res));
      if (w < 5) {
        const int hcol = w * 128 + (lane & 31) * 4;
        uint2 xx = {0u, 0u};
        u32x4 fg[32];
        {
          u32x4 rg[32];
          if (lane < 32) xx = *reinterpret_cast<const uint2*>(x + gb * DM + hcol);
          behind_stream(5, NW);
          gptr wb = (gptr)((mc < 8 ? wg : wu) + (size_t)(w * 128 + mg * 8) * FF + gt * 64 + (mc & 7) * 8);
#pragma unroll
          for (int e = 0; e < 32; ++e) {
            rg[e] = __builtin_nontemporal_load((const __attribute__((address_space(1))) u32x4*)wb);
            wb += ((e & 7) == 7 ? 25 : 1) * (FF * 2);
            asm volatile("" : "+v"(wb));
          }
          MEMFENCE();
#pragma unroll
          for (int st = 0; st < 4; ++st) {
            PIN8(rg, st * 8);
#pragma unroll
            for (int c = 0; c < 4; ++c)
#pragma unroll
              for (int i = 0; i < 4; ++i) {
                fg[st * 8 + 2 * c][i] = pk_lo(rg[st * 8 + 2 * i][c], rg[st * 8 + 2 * i + 1][c]);
                fg[st * 8 + 2 * c + 1][i] = pk_hi(rg[st * 8 + 2 * i][c], rg[st * 8 + 2 * i + 1][c]);
              }
            PIN8(fg, st * 8);
            MEMFENCE();
          }
        }
        {
          f32x4 cs[8];
#pragma unroll
          for (int t = 0; t < 8; ++t) cs[t] = f32x4{0.f, 0.f, 0.f, 0.f};
#pragma unroll
          for (int e = 0; e < 32; ++e) cs[e & 7] = mfma16(u32x4{0x3f803f80u, 0x3f803f80u, 0x3f803f80u, 0x3f803f80u}, fg[e], cs[e & 7]);
          if (lane < 16) {
            *reinterpret_cast<f32x4*>(&s_red[w * 256 + 128 + lane * 8]) = f32x4{cs[0][0], cs[1][0], cs[2][0], cs[3][0]};
            *reinterpret_cast<f32x4*>(&s_red[w * 256 + 128 + lane * 8 + 4]) = f32x4{cs[4][0], cs[5][0], cs[6][0], cs[7][0]};
          }
          count_in(&s_ccnt);
        }
        f32x4 hv = f32x4{bf_lo(xx.x), bf_hi(xx.x), bf_lo(xx.y), bf_hi(xx.y)};
        int a_h = hcol + (lane >> 5) * 2, a_put = w * (4 * WA) + (lane & 31) * 8 + (lane >> 5) * 4, a_get = w * (4 * WA / 16) + min(mc, 2) * (WA / 16) + mg, a_red = w * 64 + mc * 2;
        asm volatile("" : "+v"(hv), "+v"(a_h), "+v"(a_put), "+v"(a_get), "+v"(a_red));
        const int off = (WS_SLAB + (lane >> 5) * (K3_KS / 2) * SLS + gb * DM + hcol) * 4;
        u32x4 sl[K3_KS / 2];
        for (unsigned spin = 0; spin < SPIN_CAP; ++spin) {
          AGAIN();
#pragma unroll
          for (int ks = 0; ks < K3_KS / 2; ++ks) sl[ks] = __builtin_amdgcn_raw_buffer_load_b128(rs_ws, off, ks * (SLS * 4), AUX_SC1);
          if (__all(min(min(min(min4(sl[0]), min4(sl[1])), min(min4(sl[2]), min4(sl[3]))), min(min4(sl[4]), min4(sl[5]))) != 0u)) break;
          __builtin_amdgcn_s_sleep(2);
        }
        hv = ((hv + __builtin_bit_cast(f32x4, sl[0])) + (__builtin_bit_cast(f32x4, sl[1]) + __builtin_bit_cast(f32x4, sl[2]))) +
             ((__builtin_bit_cast(f32x4, sl[3]) + __builtin_bit_cast(f32x4, sl[4])) + __builtin_bit_cast(f32x4, sl[5]));
        const float h0 = tsum32(hv[0], hv[2]), h1 = tsum32(hv[1], hv[3]);
        *reinterpret_cast<f32x2*>(&s_h[a_h]) = f32x2{h0, h1};
        count_in(&s_hcnt);
        {
          unsigned t0[3], t1[3];
          split3(h0, t0[0], t0[1], t0[2]); split3(h1, t1[0], t1[1], t1[2]);
#pragma unroll
          for (int k = 0; k < 3; ++k) *reinterpret_cast<unsigned*>(s_A + a_put + k * WA) = pk_hi(t0[k], t1[k]);
        }
        __builtin_amdgcn_fence(__ATOMIC_RELEASE, "wavefront"); __builtin_amdgcn_wave_barrier(); __builtin_amdgcn_fence(__ATOMIC_ACQUIRE, "wavefront");
        f32x4 ac[8];
#pragma unroll
        for (int t = 0; t < 8; ++t) ac[t] = f32x4{0.f, 0.f, 0.f, 0.f};
        u32x4 av[4];
#pragma unroll
        for (int st = 0; st < 4; ++st) av[st] = reinterpret_cast<const u32x4*>(s_A)[a_get + st * 4];
        MEMFENCE();
#pragma unroll
        for (int st = 0; st < 4; ++st)
#pragma unroll
          for (int t = 0; t < 8; ++t) ac[t] = mfma16(av[st], fg[st * 8 + t], ac[t]);
        if (lane < 16) {
          reinterpret_cast<f32x4*>(s_red)[a_red] = f32x4{sum3(ac[0]), sum3(ac[1]), sum3(ac[2]), sum3(ac[3])};
          reinterpret_cast<f32x4*>(s_red)[a_red + 1] = f32x4{sum3(ac[4]), sum3(ac[5]), sum3(ac[6]), sum3(ac[7])};
        }
      } else if (w == NW - 1) {
        unsigned wr2[5];
#pragma unroll
        for (int i = 0; i < 5; ++i) wr2[i] = reinterpret_cast<const unsigned*>(wr)[lane + 64 * i];
        PIN4(wr2, 0); asm volatile("" : "+v"(wr2[4]));
        for (unsigned spin = 0; spin < (SPIN_CAP << 4) && lds_get(&s_ccnt) < 5; ++spin) __builtin_amdgcn_s_sleep(1);
        __builtin_amdgcn_fence(__ATOMIC_ACQUIRE, "workgroup");
        float cg = 0.f, cu = 0.f;
#pragma unroll
        for (int k = 0; k < 5; ++k) { cg += s_red[k * 256 + 128 + lane]; cu += s_red[k * 256 + 192 + lane]; }
        asm volatile("" : "+v"(cg), "+v"(cu));
        for (unsigned spin = 0; spin < (SPIN_CAP << 6) && lds_get(&s_hcnt) < 5; ++spin) {}
        __builtin_amdgcn_fence(__ATOMIC_ACQUIRE, "workgroup");
        const float piv = s_h[0];
        float hv[10], sm = 0.f, sq = 0.f;
#pragma unroll
        for (int i = 0; i < 5; ++i) {
          const float2 t2 = *reinterpret_cast<const float2*>(&s_h[2 * lane + 128 * i]);
          hv[2 * i] = t2.x; hv[2 * i + 1] = t2.y;
        }
        PIN8(hv, 0); asm volatile("" : "+v"(hv[8]), "+v"(hv[9]));
#pragma unroll
        for (int i = 0; i < 10; ++i) { hv[i] -= piv; sm += hv[i]; sq = fmaf(hv[i], hv[i], sq); }
        float t2 = row16_sum(tsum32(sm, sq));
        t2 = tsum16(t2, t2);
        const float dm = __int_as_float(__builtin_amdgcn_readlane(__float_as_int(t2), 0)) * (1.0f / DM);
        const float var = fmaxf(fmaf(-dm, dm, __int_as_float(__builtin_amdgcn_readlane(__float_as_int(t2), 32)) * (1.0f / DM)), 0.f);
        const float rstd = __builtin_amdgcn_rsqf(var + LN_EPS);
        s_red[5 * 256 + lane] = -(piv + dm) * cg; s_red[5 * 256 + 64 + lane] = -(piv + dm) * cu; s_red[5 * 256 + 128 + lane] = rstd;
        __syncthreads();
        if (gt < DM / 32) {
          float rr = 0.f;
#pragma unroll
          for (int i = 0; i < 5; ++i) { rr = fmaf((hv[2 * i] - dm) * rstd, bf_lo(wr2[i]), rr); rr = fmaf((hv[2 * i + 1] - dm) * rstd, bf_hi(wr2[i]), rr); }
          rr = wave_sum(rr);
          const float hh = s_h[gt * 32 + (lane & 31)];
          __builtin_amdgcn_raw_buffer_store_b32(nz_bits(lane >= 32 ? 1.0f / (1.0f + __expf(-rr)) : hh + (hh - (piv + dm)) * rstd), rs_ws,
                                                (WS_BASE + (gb * (DM / 32) + gt) * 64 + lane) * 4, 0, AUX_SC1);
        }
        return;
      } else {
        return;
      }
      __syncthreads();
      if (w == 0) {
        f32x2 pr[6];
#pragma unroll
        for (int k = 0; k < 6; ++k) pr[k] = f32x2{s_red[k * 256 + lane], s_red[k * 256 + 64 + lane]};
        float rstd = s_red[5 * 256 + 128 + lane];
        PIN4(pr, 0); asm volatile("" : "+v"(pr[4]), "+v"(pr[5]), "+v"(rstd));
        const f32x2 gu = (((pr[0] + pr[1]) + (pr[2] + pr[3])) + (pr[4] + pr[5])) * rstd;
        const float gg = gu[0], uu = gu[1];
        unsigned t[3], wd2[3];
        split3(gg * __builtin_amdgcn_rcpf(1.0f + __expf(-gg)) * uu, t[0], t[1], t[2]);
#pragma unroll
        for (int k = 0; k < 3; ++k) {
          const unsigned o = __float_as_uint(dpp<DPP_XOR1>(__uint_as_float(t[k])));
          wd2[k] = nz_bits(__uint_as_float((lane & 1) ? pk_hi(o, t[k]) : pk_hi(t[k], o)));
        }
        __builtin_amdgcn_raw_buffer_store_b32((lane & 1) ? wd2[1] : wd2[0], rs_ws, o_res, 0, AUX_SC1);
        __builtin_amdgcn_raw_buffer_store_b32(wd2[2], rs_ws, (lane & 1) ? OOB : o_res, 256, AUX_SC1);
      }
    } else {
      if (w == 7) return;
      const int bp = tt & 1, ct = tt >> 1;
      for (unsigned spin = 0; spin < (SPIN_CAP << 4) && lds_get(&s_fcnt) < NW; ++spin) __builtin_amdgcn_s_sleep(1);
      if (w == 6) {
        const int bb = lane >> 5, f = lane & 31, offb = (WS_BASE + ((bp * 2 + bb) * (DM / 32) + ct) * 64 + f) * 4;
        const __amdgpu_buffer_rsrc_t rs_out = __builtin_amdgcn_make_buffer_rsrc(out, 0, NB * DM * 2, 0x00020000);
        int o_res = ((bp * 2 + bb) * DM + ct * 32 + f) * 2;
        unsigned bs = 0u, sg = 0u;
        __builtin_amdgcn_s_sleep(100);
        for (unsigned spin = 0; spin < SPIN_CAP; ++spin) {
          AGAIN();
          bs = __builtin_amdgcn_raw_buffer_load_b32(rs_ws, offb, 0, AUX_SC1); sg = __builtin_amdgcn_raw_buffer_load_b32(rs_ws, offb, 128, AUX_SC1);
          if (__all(min(bs, sg) != 0u)) break;
          __builtin_amdgcn_s_sleep(2);
        }
        asm volatile("" : "+v"(o_res));
        __syncthreads();
        float pr[12];
#pragma unroll
        for (int k = 0; k < 12; ++k) pr[k] = s_red[(k >> 1) * 256 + (k & 1) * 64 + lane];
        PIN8(pr, 0); PIN4(pr, 8);
        const float y = (((pr[0] + pr[1]) + (pr[2] + pr[3])) + ((pr[4] + pr[5]) + (pr[6] + pr[7]))) + ((pr[8] + pr[9]) + (pr[10] + pr[11]));
        const bf16 ov = __float2bfloat16(fmaf(y, __uint_as_float(sg), __uint_as_float(bs)));
        __builtin_amdgcn_raw_buffer_store_b16(__builtin_bit_cast(unsigned short, ov), rs_out, o_res, 0, 0);
        return;
      }
      __builtin_amdgcn_s_sleep(8);
      weights(wd + (size_t)(w * 256 + (mc >> 3) * 32 + mg * 8) * DM + ct * 32 + (mc & 7) * 4, 57);
      frags();
      int a_red = w * 64 + mg * 8 + (mc & 7);
      asm volatile("" : "+v"(a_red));
      const int off = (WS_ACT + (((bp * 2 + ((mc >> 2) & 1)) * (FF / 64) + w * 4) * 3 + min(mc & 3, 2)) * 32 + (mc >> 3) * 16 + mg * 4) * 4;
      u32x4 av[4];
      for (unsigned spin = 0; spin < SPIN_CAP; ++spin) {
        AGAIN();
#pragma unroll
        for (int st = 0; st < 4; ++st) av[st] = __builtin_amdgcn_raw_buffer_load_b128(rs_ws, off, st * 384, AUX_SC1);
        if (__all(min(min(min4(av[0]), min4(av[1])), min(min4(av[2]), min4(av[3]))) != 0u)) break;
        __builtin_amdgcn_s_sleep(2);
      }
      f32x4 ac[4];
#pragma unroll
      for (int t = 0; t < 4; ++t) ac[t] = f32x4{0.f, 0.f, 0.f, 0.f};
#pragma unroll
      for (int st = 0; st < 4; ++st)
#pragma unroll
        for (int t = 0; t < 4; ++t) ac[t] = mfma16(av[st], fr[st * 4 + t], ac[t]);
      if ((mc >> 3) == (mg >> 1)) reinterpret_cast<f32x4*>(s_red)[a_red] = f32x4{sum3(ac[0]), sum3(ac[1]), sum3(ac[2]), sum3(ac[3])};
      __syncthreads();
    }
  }
}

extern "C" void kernel_launch(void* const* d_in, const int* in_sizes, int n_in, void* d_out, int out_size, void* d_ws,
                              size_t ws_size, hipStream_t stream) {
  fused_kernel<<<NWG, NW * 64, 0, stream>>>((const bf16*)d_in[0], (const bf16*)d_in[1], (const bf16*)d_in[2], (const bf16*)d_in[3],
                                            (const bf16*)d_in[4], (const bf16*)d_in[6], (const bf16*)d_in[7], (const bf16*)d_in[8],
                                            (const bf16*)d_in[9], (float*)d_ws, (bf16*)d_out);
}
```

```cpp
#include <hip/hip_runtime.h>
#include <hip/hip_bf16.h>
#include <stdint.h>

typedef __hip_bfloat16 bf16;
using u32x4 = __attribute__((ext_vector_type(4))) unsigned;
using u32x2 = __attribute__((ext_vector_type(2))) unsigned;
using f32x4 = __attribute__((ext_vector_type(4))) float;
using f32x2 = __attribute__((ext_vector_type(2))) float;

constexpr int NB = 4, DM = 640, NH = 32, SK = 8192, DH = 192, HD = NH * DH, FF = 1536;
constexpr int NWG = 256, NSEG = 6, NWIN = NB * NH * NSEG / NWG;
constexpr float LN_EPS = 1e-5f;
constexpr float QSCALE = 0.07216878364870322f * 1.4426950408889634f;

constexpr int WS_HP = 0;
constexpr int REC = DH + 32;
constexpr int WS_PO = WS_HP + 2 * NWG;
constexpr int WS_Z0 = WS_PO + 2 * NWG * REC;
constexpr int K3_KS = 12, K3_ROWS = HD / K3_KS;
constexpr int WS_SLAB = WS_PO + NB * NH * NSEG * REC;
constexpr int SLS = NB * DM;
constexpr int WS_ACT = WS_SLAB + K3_KS * SLS;
constexpr int WS_BASE = WS_ACT + NB * FF * 3 / 2;
constexpr int ZPW = 416;
constexpr int WS_QI = WS_Z0 + NWG * ZPW;
constexpr int LSTR = 64;
constexpr int WS_REP = WS_QI + NB * NH * DH * 2;
constexpr int WS_HC = WS_REP + 256 * LSTR;
constexpr int WS_END = WS_HC + NWG * LSTR;
static_assert(WS_BASE + NB * DM * 2 <= WS_QI && WS_Z0 % 64 == 0 && WS_PO % 32 == 0 && ZPW % 32 == 0 && NWIN == 3, "layout");
__device__ __forceinline__ int ef_ofs(int i) { return (WS_PO + (2 * NWG + i) * REC + DH + 2) * 4; }
constexpr int OOB = 0x7ffffff0;

__device__ __forceinline__ float bf_lo(unsigned u) { return __uint_as_float(u << 16); }
__device__ __forceinline__ float bf_hi(unsigned u) { return __uint_as_float(u & 0xffff0000u); }
__device__ __forceinline__ f32x2 bf_pair(unsigned u) { return f32x2{bf_lo(u), bf_hi(u)}; }
__device__ __forceinline__ f32x2 fma2(float a, f32x2 b, f32x2 c) { return __builtin_elementwise_fma(f32x2{a, a}, b, c); }
__device__ __forceinline__ f32x2 fma2(f32x2 a, f32x2 b, f32x2 c) { return __builtin_elementwise_fma(a, b, c); }

template <int CTRL> __device__ __forceinline__ float dpp(float x) {
  return __builtin_bit_cast(float, __builtin_amdgcn_mov_dpp(__builtin_bit_cast(int, x), CTRL, 0xf, 0xf, true));
}
constexpr int DPP_XOR1 = 0xB1, DPP_XOR2 = 0x4E, DPP_HMIRROR = 0x141;

__device__ __forceinline__ float sum_x16_32(float x) {
  auto s = __builtin_amdgcn_permlane16_swap(__float_as_uint(x), __float_as_uint(x), false, false);
  x = __uint_as_float(s[0]) + __uint_as_float(s[1]);
  auto t = __builtin_amdgcn_permlane32_swap(__float_as_uint(x), __float_as_uint(x), false, false);
  return __uint_as_float(t[0]) + __uint_as_float(t[1]);
}
__device__ __forceinline__ float max_x16_32(float x) {
  auto s = __builtin_amdgcn_permlane16_swap(__float_as_uint(x), __float_as_uint(x), false, false);
  x = fmaxf(__uint_as_float(s[0]), __uint_as_float(s[1]));
  auto t = __builtin_amdgcn_permlane32_swap(__float_as_uint(x), __float_as_uint(x), false, false);
  return fmaxf(__uint_as_float(t[0]), __uint_as_float(t[1]));
}
__device__ __forceinline__ float tsum32(float a, float b) {
  auto t = __builtin_amdgcn_permlane32_swap(__float_as_uint(a), __float_as_uint(b), false, false);
  return __uint_as_float(t[0]) + __uint_as_float(t[1]);
}
__device__ __forceinline__ float tsum16(float a, float b) {
  auto t = __builtin_amdgcn_permlane16_swap(__float_as_uint(a), __float_as_uint(b), false, false);
  return __uint_as_float(t[0]) + __uint_as_float(t[1]);
}
constexpr int DPP_ROR8 = 0x128, DPP_BCAST = 0x150;
__device__ __forceinline__ float sum_r(float v) { v += dpp<DPP_ROR8>(v); return sum_x16_32(v); }
__device__ __forceinline__ float max_r(float v) { v = fmaxf(v, dpp<DPP_ROR8>(v)); return max_x16_32(v); }
__device__ __forceinline__ float wave_sum(float v) {
  v += dpp<DPP_XOR1>(v); v += dpp<DPP_XOR2>(v); v += dpp<DPP_HMIRROR>(v);
  return sum_r(v);
}

__device__ __forceinline__ float row16_sum(float v) {
  v += dpp<DPP_XOR1>(v); v += dpp<DPP_XOR2>(v); v += dpp<DPP_HMIRROR>(v);
  return v + dpp<DPP_ROR8>(v);
}

struct Slot { u32x4 a[3]; u32x4 b[3]; };

#define PIN8(a, o) asm volatile("" : "+v"((a)[(o) + 0]), "+v"((a)[(o) + 1]), "+v"((a)[(o) + 2]), "+v"((a)[(o) + 3]), \
                                       "+v"((a)[(o) + 4]), "+v"((a)[(o) + 5]), "+v"((a)[(o) + 6]), "+v"((a)[(o) + 7]))
#define PIN4(a, o) asm volatile("" : "+v"((a)[(o) + 0]), "+v"((a)[(o) + 1]), "+v"((a)[(o) + 2]), "+v"((a)[(o) + 3]))
#define MEMFENCE() do { asm volatile("" ::: "memory"); __builtin_amdgcn_sched_barrier(0); } while (0)
#define AGAIN() asm volatile("" ::: "memory")
#define DRAIN() asm volatile("s_waitcnt vmcnt(0)" ::: "memory")

__device__ __forceinline__ void k_tile(const u32x4 (&k)[3], const f32x2 (&q)[12], float& gmx, float* dst, int j) {
  f32x2 s0 = {0.f, 0.f}, s1 = s0, s2 = s0;
#pragma unroll
  for (int d = 0; d < 4; ++d) {
    s0 = fma2(bf_pair(k[0][d]), q[d], s0);
    s1 = fma2(bf_pair(k[1][d]), q[4 + d], s1);
    s2 = fma2(bf_pair(k[2][d]), q[8 + d], s2);
  }
  s0 = (s0 + s1) + s2;
  float s = s0[0] + s0[1];
  s += dpp<DPP_XOR1>(s);
  s += dpp<DPP_XOR2>(s);
  s += dpp<DPP_HMIRROR>(s);
  gmx = fmaxf(gmx, s);
  asm volatile("" : "+v"(gmx));
  if (j == 0) *dst = s;
}
__device__ __forceinline__ void v_tile(u32x4 (&v)[3], float p, f32x2 (&acc)[12]) {
#pragma unroll
  for (int i = 0; i < 3; ++i) {
    asm volatile("" : "+v"(v[i]), "+v"(p));
#pragma unroll
    for (int d = 0; d < 4; ++d) acc[i * 4 + d] = fma2(p, bf_pair(v[i][d]), acc[i * 4 + d]);
    PIN4(acc, i * 4);
  }
}

constexpr int PTAU = 660;
constexpr int POFF = 140, FFWD = 112;
constexpr int LTGT = 230, LCAP = 600;
constexpr int DOWN = 30, AFROM = 3, EFROM = 12, SLACK = 10;
constexpr int QLOOK = 780;
constexpr int AUX_SC1 = 16, AUX_NT = 2;
constexpr unsigned SPIN_CAP = 1u << 17;
__device__ __forceinline__ unsigned nz_bits(float v) { const unsigned u = __float_as_uint(v); return u == 0u ? 0x80000000u : u; }
__device__ __forceinline__ void lds_put(int* p, int v) { __hip_atomic_store(p, v, __ATOMIC_RELAXED, __HIP_MEMORY_SCOPE_WORKGROUP); }
__device__ __forceinline__ int lds_get(int* p) { return __hip_atomic_load(p, __ATOMIC_RELAXED, __HIP_MEMORY_SCOPE_WORKGROUP); }
__device__ __forceinline__ void count_in(int* p) {
  unsigned long long keep;
  asm volatile("s_mov_b64 %0, exec\n\ts_mov_b64 exec, 1\n\tds_add_u32 %1, %2\n\ts_mov_b64 exec, %0"
               : "=&s"(keep) : "v"((unsigned)(size_t)(__attribute__((address_space(3))) int*)p), "v"(1) : "memory");
}
__device__ __forceinline__ int count_in_rtn(int* p) {
  unsigned long long keep;
  int c;
  asm volatile("s_mov_b64 %0, exec\n\ts_mov_b64 exec, 1\n\tds_add_rtn_u32 %1, %2, %3\n\ts_mov_b64 exec, %0"
               : "=&s"(keep), "=&v"(c) : "v"((unsigned)(size_t)(__attribute__((address_space(3))) int*)p), "v"(1) : "memory");
  return c;
}
using bf16x8 = __attribute__((ext_vector_type(8))) short;
__device__ __forceinline__ unsigned pk_lo(unsigned a, unsigned b) { return __builtin_amdgcn_perm(b, a, 0x05040100u); }
__device__ __forceinline__ unsigned pk_hi(unsigned a, unsigned b) { return __builtin_amdgcn_perm(b, a, 0x07060302u); }
__device__ __forceinline__ void split3(float v, unsigned& t0, unsigned& t1, unsigned& t2) {
  t0 = __float_as_uint(v) & 0xffff0000u;
  const float r1 = v - __uint_as_float(t0);
  t1 = __float_as_uint(r1) & 0xffff0000u;
  t2 = __float_as_uint(r1 - __uint_as_float(t1));
}
__device__ __forceinline__ f32x4 mfma16(u32x4 a, u32x4 b, f32x4 c) {
  return __builtin_amdgcn_mfma_f32_16x16x32_bf16(__builtin_bit_cast(bf16x8, a), __builtin_bit_cast(bf16x8, b), c, 0, 0, 0);
}
__device__ __forceinline__ void put_terms4(char* img, int rowbytes, f32x4 v) {
  unsigned t[3][4];
#pragma unroll
  for (int e = 0; e < 4; ++e) split3(v[e], t[0][e], t[1][e], t[2][e]);
#pragma unroll
  for (int k = 0; k < 3; ++k) *reinterpret_cast<u32x2*>(img + k * rowbytes) = u32x2{pk_hi(t[k][0], t[k][1]), pk_hi(t[k][2], t[k][3])};
}
__device__ __forceinline__ unsigned min4(u32x4 v) { return min(min(v[0], v[1]), min(v[2], v[3])); }

__device__ __forceinline__ float sum3(f32x4 v) { return (v[0] + v[1]) + v[2]; }

constexpr int NW = 8, NBUF = 4, NGRP = 64, NT = NGRP / NW;
constexpr int NQP = NH * DH / 64, NQW = 5, QW0 = 2, QST = DM / 64 / NQW;
constexpr int SVC = NW - 1;
constexpr int W_CT = DM / 64, G_TASKS = NB * (FF / 64), W_TASKS = W_CT * K3_KS, D_TASKS = 2 * (DM / 32);
static_assert(G_TASKS + W_TASKS + D_TASKS == NWG && NW == 8 && K3_ROWS == 4 * 128 && NBUF == 4 && SK / 64 == 128, "shape");
__device__ __forceinline__ unsigned fresh_tag(unsigned tags) {
  unsigned t = __builtin_amdgcn_readfirstlane(tags);
  do ++t; while (__any(tags == t));
  return t;
}

__global__ void __launch_bounds__(NW * 64, 2) fused_kernel(const bf16* __restrict__ x, const bf16* __restrict__ kc, const bf16* __restrict__ vc,
                                                        const bf16* __restrict__ wq, const bf16* __restrict__ wo, const bf16* __restrict__ wr,
                                                        const bf16* __restrict__ wg, const bf16* __restrict__ wu, const bf16* __restrict__ wd,
                                                        float* ws, bf16* __restrict__ out) {
  constexpr int PST = DH + 4;
  constexpr int WA = 128 * 2 + 16;
  __shared__ __attribute__((aligned(16))) char s_A[4 * 16 * WA];
  __shared__ __attribute__((aligned(16))) float s_red[6 * 256];
  __shared__ __attribute__((aligned(16))) float s_h[DM];
  __shared__ __attribute__((aligned(16))) float s_buf[NWIN * NW * PST];
  __shared__ __attribute__((aligned(16))) float s_sc[NGRP * 64];
  __shared__ __attribute__((aligned(16))) float s_q[NWIN * DH];
  __shared__ float s_m[NWIN * NW];
  __shared__ int s_mcnt[NWIN];
  __shared__ int s_qok, s_pcnt, s_ecnt, s_fcnt, s_icnt, s_org, s_hcnt, s_ccnt;
  __shared__ int s_prog[NW];

  const int tid = threadIdx.x;
  int lane = tid & 63, r = lane >> 3, j = lane & 7, mg = lane >> 4, mc = lane & 15;
  int wave = __builtin_amdgcn_readfirstlane(tid >> 6), bid = blockIdx.x;
  const int wid = (bid >> 5) * 32 + (bid & 7) * 4 + ((bid >> 3) & 3);
  const bool prod = bid < NQP;
  const __amdgpu_buffer_rsrc_t rs_ws = __builtin_amdgcn_make_buffer_rsrc(ws, 0, WS_END * 4, 0x00020000);
  const __amdgpu_buffer_rsrc_t rs_k = __builtin_amdgcn_make_buffer_rsrc(const_cast<bf16*>(kc), 0, NB * NH * SK * DH * 2, 0x00020000);
  const __amdgpu_buffer_rsrc_t rs_v = __builtin_amdgcn_make_buffer_rsrc(const_cast<bf16*>(vc), 0, NB * NH * SK * DH * 2, 0x00020000);
  const int voff = r * (DH * 2) + j * 16;
  const int c1 = wid % 3 == 0 ? 22 : 21, c2 = wid % 3 == 1 ? 42 : 43;
  auto seg_of = [&](int n) { return (n * 8 + wave >= c1) + (n * 8 + wave >= c2); };
  auto gload = [&](Slot (&sl)[NBUF], __amdgpu_buffer_rsrc_t rs, int n) {
    const int sn = seg_of(n), sg = wid + NWG * sn, row = sg / NSEG, k = sg - row * NSEG;
    const int off = (row * (SK / 64) + (k >> 1) * 43 + (n * 8 + wave - (sn == 0 ? 0 : (sn == 1 ? c1 : c2))) * 2 + ((k ^ (k >> 1)) & 1)) * (64 * DH * 2);
#pragma unroll
    for (int u = 0; u < NBUF; ++u) {
#pragma unroll
      for (int i = 0; i < 3; ++i) sl[u].a[i] = __builtin_amdgcn_raw_buffer_load_b128(rs, voff + i * 128, off + 2 * u * 3072, AUX_NT);
      MEMFENCE();
#pragma unroll
      for (int i = 0; i < 3; ++i) sl[u].b[i] = __builtin_amdgcn_raw_buffer_load_b128(rs, voff + i * 128, off + (2 * u + 1) * 3072, AUX_NT);
      if (u < NBUF - 1) __builtin_amdgcn_s_sleep(3);
      MEMFENCE();
    }
  };
  asm volatile("" :: "s"(ws), "s"(x), "s"(wq), "s"(kc));
  __builtin_amdgcn_sched_barrier(0);
  Slot b0[NBUF];
  const bool early = wave == 0 && !prod;
  if (early) gload(b0, rs_k, 0);
  const unsigned t_start = (unsigned)__builtin_amdgcn_s_memrealtime() - 80u;
  unsigned hs_nc = 0u, hs_ep = 0u;
  auto look_at_p = [&]() {
    AGAIN();
    if (bid == NWG - 1) {
      u32x4 hp[2], hc[2];
#pragma unroll
      for (int i = 0; i < 2; ++i) {
        hp[i] = __builtin_amdgcn_raw_buffer_load_b128(rs_ws, (WS_HP + lane * 8 + i * 4) * 4, 0, AUX_SC1);
        const u32x2 c0 = __builtin_amdgcn_raw_buffer_load_b64(rs_ws, (WS_HC + (lane * 4 + i * 2) * LSTR) * 4, 0, AUX_SC1);
        const u32x2 c1_ = __builtin_amdgcn_raw_buffer_load_b64(rs_ws, (WS_HC + (lane * 4 + i * 2 + 1) * LSTR) * 4, 0, AUX_SC1);
        hc[i] = u32x4{c0[0], c0[1], c1_[0], c1_[1]};
      }
      if (__all(hc[0][1] == hp[0][0] && hc[0][3] == hp[0][2] && hc[1][1] == hp[1][0] && hc[1][3] == hp[1][2]) &&
          __any(hc[0][0] != hp[0][1] || hc[0][2] != hp[0][3] || hc[1][0] != hp[1][1] || hc[1][2] != hp[1][3])) {
#pragma unroll
        for (int i = 0; i < 2; ++i)
          __builtin_amdgcn_raw_buffer_store_b128(u32x4{hp[i][0], hc[i][0], hp[i][2], hc[i][2]}, rs_ws, (WS_HP + lane * 8 + i * 4) * 4, 0, AUX_SC1);
      }
    }
    const u32x2 pw = __builtin_amdgcn_raw_buffer_load_b64(rs_ws, (WS_HP + bid * 2) * 4, 0, AUX_SC1);
    const unsigned np = __builtin_amdgcn_readfirstlane(pw[0]), ec = __builtin_amdgcn_readfirstlane(pw[1]);
    if (np != hs_ep) { hs_ep = np; __builtin_amdgcn_raw_buffer_store_b64(u32x2{hs_nc, hs_ep}, rs_ws, lane == 0 ? (WS_HC + bid * LSTR) * 4 : OOB, 0, AUX_SC1); }
    return ec == hs_nc;
  };
  auto answer = [&]() {
    const u32x4 z4 = {0u, 0u, 0u, 0u};
#pragma unroll
    for (int i = 0; i < (ZPW + 255) / 256; ++i)
      __builtin_amdgcn_raw_buffer_store_b128(z4, rs_ws, i * 256 + lane * 4 < ZPW ? (WS_Z0 + bid * ZPW + i * 256 + lane * 4) * 4 : OOB, 0, AUX_SC1);
    const u32x2 pw = __builtin_amdgcn_raw_buffer_load_b64(rs_ws, (WS_HP + bid * 2) * 4, 0, AUX_SC1);
    DRAIN();
    hs_ep = __builtin_amdgcn_readfirstlane(pw[0]);
    hs_nc = __builtin_amdgcn_readfirstlane(pw[1]) + 1u;
    __builtin_amdgcn_raw_buffer_store_b64(u32x2{hs_nc, hs_ep}, rs_ws, lane == 0 ? (WS_HC + bid * LSTR) * 4 : OOB, 0, AUX_SC1);
  };
  bool answered = false;
  if (tid < NW) s_prog[tid] = 1;
  if (tid < NWIN) s_mcnt[tid] = 0;
  if (tid == 0) { s_qok = 0; s_pcnt = 0; s_ecnt = 0; s_fcnt = 0; s_icnt = 0; s_hcnt = 0; s_ccnt = 0; s_org = (int)(t_start + POFF); }
  if (wave == SVC) {
    __builtin_amdgcn_s_setprio(3);
    int iofs[NWIN];
#pragma unroll
    for (int s = 0; s < NWIN; ++s) iofs[s] = (WS_QI + ((wid + NWG * s) / NSEG * DH + lane) * 2) * 4;
    const int oofs = prod ? (WS_QI + (bid * 64 + lane) * 2) * 4 : OOB;
    u32x2 in[NWIN * 3];
    unsigned ot[NB], ce[4];
#pragma unroll
    for (int e = 0; e < NWIN * 3; ++e) in[e] = __builtin_amdgcn_raw_buffer_load_b64(rs_ws, iofs[e / 3], (e % 3) * 512, AUX_SC1);
#pragma unroll
    for (int bb = 0; bb < NB; ++bb) ot[bb] = __builtin_amdgcn_raw_buffer_load_b64(rs_ws, oofs, bb * (HD * 8), AUX_SC1)[1];
    u32x4 xr[5], hp[2];
#pragma unroll
    for (int i = 0; i < 5; ++i) xr[i] = reinterpret_cast<const u32x4*>(x)[lane * 5 + i];
#pragma unroll
    for (int i = 0; i < 2; ++i) hp[i] = __builtin_amdgcn_raw_buffer_load_b128(rs_ws, bid == NWG - 1 ? (WS_HP + lane * 8 + i * 4) * 4 : OOB, 0, AUX_SC1);
#pragma unroll
    for (int i = 0; i < 4; ++i) ce[i] = __builtin_amdgcn_raw_buffer_load_b32(rs_ws, bid == NWG - 1 ? (WS_HC + (lane * 4 + i) * LSTR + 1) * 4 : OOB, 0, AUX_SC1);
    MEMFENCE();
    __syncthreads();
    MEMFENCE();
    gload(b0, rs_k, 0);
    if (bid == NWG - 1) {
#pragma unroll
      for (int i = 0; i < 2; ++i) {
#pragma unroll
        for (int e = 0; e < 2; ++e) { unsigned np = hp[i][2 * e] + 1u; if (np == ce[2 * i + e]) np += 1u; hp[i][2 * e] = np; }
        __builtin_amdgcn_raw_buffer_store_b128(hp[i], rs_ws, (WS_HP + lane * 8 + i * 4) * 4, 0, AUX_SC1);
      }
    }
    unsigned want[NWIN * 3];
#pragma unroll
    for (int e = 0; e < NWIN * 3; ++e) want[e] = fresh_tag(in[e][1]);
    if (!prod && lane == 0) __hip_atomic_fetch_max(&s_org, (int)((unsigned)__builtin_amdgcn_s_memrealtime() - 60u), __ATOMIC_RELAXED, __HIP_MEMORY_SCOPE_WORKGROUP);
    if (prod) {
      float xs = 0.f;
#pragma unroll
      for (int i = 0; i < 20; ++i) xs += bf_lo(xr[i >> 2][i & 3]) + bf_hi(xr[i >> 2][i & 3]);
      const float mean = row16_sum(xs) * (1.0f / DM);
      float xv = 0.f;
#pragma unroll
      for (int i = 0; i < 20; ++i) { const float a = bf_lo(xr[i >> 2][i & 3]) - mean, c = bf_hi(xr[i >> 2][i & 3]) - mean; xv += a * a + c * c; }
      const float rstd = QSCALE / sqrtf(row16_sum(xv) * (1.0f / DM) + LN_EPS);
#pragma unroll
      for (int bb = 0; bb < NB; ++bb) ot[bb] = fresh_tag(ot[bb]);
      for (unsigned spin = 0; spin < (SPIN_CAP << 4) && lds_get(&s_pcnt) < NQW; ++spin) __builtin_amdgcn_s_sleep(1);
      __builtin_amdgcn_fence(__ATOMIC_ACQUIRE, "workgroup");
      float pr[5];
#pragma unroll
      for (int i = 0; i < 5; ++i) {
        pr[i] = 0.f;
#pragma unroll
        for (int k = 0; k < NQW; ++k) pr[i] += s_buf[2 * k * 320 + i * 64 + lane] + s_buf[(2 * k + 1) * 320 + i * 64 + lane];
      }
#pragma unroll
      for (int bb = 0; bb < NB; ++bb) {
        const float qv = (pr[bb] - __int_as_float(__builtin_amdgcn_readlane(__float_as_int(mean), bb * 16)) * pr[4]) *
                         __int_as_float(__builtin_amdgcn_readlane(__float_as_int(rstd), bb * 16));
        __builtin_amdgcn_raw_buffer_store_b64(u32x2{__float_as_uint(qv), ot[bb]}, rs_ws, oofs, bb * (HD * 8), AUX_SC1);
      }
    }
    for (unsigned spin = 0; spin < 1024 && (int)((unsigned)__builtin_amdgcn_s_memrealtime() - t_start) < QLOOK; ++spin) __builtin_amdgcn_s_sleep(4);
    bool all_out = false;
#pragma unroll 1
    for (unsigned spin = 0; spin < SPIN_CAP; ++spin) {
      AGAIN();
#pragma unroll
      for (int e = 0; e < NWIN * 3; ++e) in[e] = __builtin_amdgcn_raw_buffer_load_b64(rs_ws, iofs[e / 3], (e % 3) * 512, AUX_SC1);
      bool ok = true;
#pragma unroll
      for (int e = 0; e < NWIN * 3; ++e) ok = ok && in[e][1] == want[e];
      if (all_out || __all(ok)) break;
      if (spin == 15) { answer(); answered = true; }
      if (spin > 15 && (spin & 3) == 3) all_out = look_at_p();
      __builtin_amdgcn_s_sleep(4);
    }
#pragma unroll
    for (int e = 0; e < NWIN * 3; ++e) s_q[e * 64 + lane] = __uint_as_float(in[e][0]);
    __builtin_amdgcn_fence(__ATOMIC_RELEASE, "workgroup");
    lds_put(&s_qok, 1);
    __builtin_amdgcn_s_setprio(0);
  } else if (prod && wave >= QW0 && wave < QW0 + NQW) {
    const int k0 = (wave - QW0) * (QST * 64) + (mc >> 3) * 32 + mg * 8;
    u32x4 raw[QST * 8], xa[QST];
    const bf16* wb = wq + (size_t)k0 * HD + bid * 64 + (mc & 7) * 8;
#pragma unroll
    for (int st = 0; st < QST; ++st) xa[st] = *reinterpret_cast<const u32x4*>(x + (mc & 3) * DM + k0 + st * 64);
#pragma unroll
    for (int e = 0; e < QST * 8; ++e) raw[e] = __builtin_nontemporal_load(reinterpret_cast<const u32x4*>(wb + (size_t)((e >> 3) * 64 + (e & 7)) * HD));
    MEMFENCE();
    __syncthreads();
    MEMFENCE();
    f32x4 ac[8];
#pragma unroll
    for (int t = 0; t < 8; ++t) ac[t] = f32x4{0.f, 0.f, 0.f, 0.f};
#pragma unroll
    for (int st = 0; st < QST; ++st) {
      const u32x4 a = (mc & 4) == 0 ? xa[st] : u32x4{0x3f803f80u, 0x3f803f80u, 0x3f803f80u, 0x3f803f80u};
#pragma unroll
      for (int c = 0; c < 4; ++c) {
        u32x4 f0, f1;
#pragma unroll
        for (int i = 0; i < 4; ++i) { f0[i] = pk_lo(raw[st * 8 + 2 * i][c], raw[st * 8 + 2 * i + 1][c]); f1[i] = pk_hi(raw[st * 8 + 2 * i][c], raw[st * 8 + 2 * i + 1][c]); }
        ac[2 * c] = mfma16(a, f0, ac[2 * c]);
        ac[2 * c + 1] = mfma16(a, f1, ac[2 * c + 1]);
      }
    }
    if ((mg >> 1) == (mc >> 3)) {
      float* const dst = &s_buf[((wave - QW0) * 2 + (mg >> 1)) * 320 + (mc & 7) * 8];
#pragma unroll
      for (int bb = 0; bb < NB; ++bb)
        if (bb == 0 || (mg & 1) == 0) {
          *reinterpret_cast<f32x4*>(dst + ((mg & 1) * 4 + bb) * 64) = f32x4{ac[0][bb], ac[1][bb], ac[2][bb], ac[3][bb]};
          *reinterpret_cast<f32x4*>(dst + ((mg & 1) * 4 + bb) * 64 + 4) = f32x4{ac[4][bb], ac[5][bb], ac[6][bb], ac[7][bb]};
        }
    }
    __builtin_amdgcn_fence(__ATOMIC_RELEASE, "workgroup");
    if (lane == 0) __hip_atomic_fetch_add(&s_pcnt, 1, __ATOMIC_RELAXED, __HIP_MEMORY_SCOPE_WORKGROUP);
  } else {
    __syncthreads();
  }
  MEMFENCE();
  const int gtile = ((bid & 7) * 12 + (bid >> 3) - 15) >> 2;
  const int tau = PTAU - (((bid >> 3) >= 15 && (bid >> 3) < 27 && (((((unsigned)(size_t)wg >> 7) + gtile) & 7) == 3 || ((((unsigned)(size_t)wu >> 7) + gtile) & 7) == 3)) ? 10 : 0);
  u32x2 rep = {0u, 0u};
  unsigned t_thru = 0u;
  auto pace = [&](int g) {
    if (wave == 0 && g > AFROM) {
      const int org = lds_get(&s_org);
      const bool ok = rep[0] - t_start + 1000u < 6000u && rep[1] - t_start + 1000u < 6000u;
      const float dorg = row16_sum(ok ? (float)(int)(rep[1] - (unsigned)org) : 0.f);
      const float lt = ok ? (float)min((int)(rep[0] - rep[1]), LCAP) : (float)LCAP;
      const float lx = fminf(lt, dpp<DPP_ROR8>(lt));
      float hi = fmaxf(lx, dpp<DPP_XOR1>(lx)); hi = fmaxf(hi, dpp<DPP_XOR2>(hi)); hi = fmaxf(hi, dpp<DPP_HMIRROR>(hi));
      const float lat = row16_sum(lt) * (0.8f / 16.f) + hi * 0.2f;
      if (lane == 0) lds_put(&s_org, org + (int)(dorg * (1.f / 16.f) + fmaxf((lat - LTGT) * 0.6f, g > EFROM ? (float)SLACK : -(float)DOWN)));
    }
    const unsigned rel = (unsigned)(g * tau + (wave + (g == 0 && wave > 0)) * (PTAU / 8) + min(g * 8 + wave, 16) * (FFWD / 16) - FFWD + ((wid * 65) & 255) * (PTAU / 8) / 256);
    unsigned now = (unsigned)__builtin_amdgcn_s_memrealtime();
    if (wave == 1 && (bid >> 4) == 7 && g >= 2)
      __builtin_amdgcn_raw_buffer_store_b64(u32x2{t_thru - rel + tau, (unsigned)lds_get(&s_org)}, rs_ws, lane == 0 ? (WS_REP + ((g - 2) * 16 + (bid & 15)) * LSTR) * 4 : OOB, 0, AUX_SC1);
    for (unsigned spin = 0; spin < 4096 && (int)(now - rel - (unsigned)__builtin_amdgcn_readfirstlane(lds_get(&s_org))) < 0; ++spin) {
      __builtin_amdgcn_s_sleep(2);
      now = (unsigned)__builtin_amdgcn_s_memrealtime();
    }
    if (g >= 2) rep = __builtin_amdgcn_raw_buffer_load_b64(rs_ws, (wave == 0 && lane < 16 && g >= AFROM) ? (WS_REP + ((g - 2) * 16 + lane) * LSTR) * 4 : OOB, 0, AUX_SC1);
    if (lane == 0) lds_put(&s_prog[wave], g);
    int pp = __builtin_amdgcn_readfirstlane(lds_get(&s_prog[wave ^ (NW / 2)]));
    for (unsigned spin = 0; spin < 4096 && g > pp + 1; ++spin) { __builtin_amdgcn_s_sleep(4); pp = __builtin_amdgcn_readfirstlane(lds_get(&s_prog[wave ^ (NW / 2)])); }
    if (g > pp) __builtin_amdgcn_s_setprio(0); else __builtin_amdgcn_s_setprio(1);
  };
  float gmx = -INFINITY;
  auto k_group = [&](const Slot (&sl)[NBUF], int n, int s) {
    f32x2 q[12];
#pragma unroll
    for (int i = 0; i < 6; ++i) {
      const f32x4 a = *reinterpret_cast<const f32x4*>(&s_q[s * DH + (i >> 1) * 64 + j * 8 + (i & 1) * 4]);
      q[i * 2] = f32x2{a[0], a[1]}; q[i * 2 + 1] = f32x2{a[2], a[3]};
    }
    float* const sc = &s_sc[(wave * NT + n) * 64 + r * 8];
    MEMFENCE();
#pragma unroll
    for (int u = 0; u < NBUF; ++u) {
      k_tile(sl[u].a, q, gmx, sc + 2 * u, j);
      k_tile(sl[u].b, q, gmx, sc + 2 * u + 1, j);
      MEMFENCE();
    }
    t_thru = (unsigned)__builtin_amdgcn_s_memrealtime();
  };
  {
    Slot b1[NBUF];
    if (!early && wave != SVC) {
      pace(0);
      gload(b0, rs_k, 0);
    }
    pace(1);
    gload(b1, rs_k, 1);
    for (unsigned spin = 0; spin < (SPIN_CAP << 4) && lds_get(&s_qok) == 0; ++spin) __builtin_amdgcn_s_sleep(2);
    __builtin_amdgcn_fence(__ATOMIC_ACQUIRE, "workgroup");
    k_group(b0, 0, 0);
    k_group(b1, 1, 0);
    if (wave == SVC && !answered) answer();
  }
  auto k_done = [&](int s) {
    const float gm = max_r(gmx);
    if (lane == 0) s_m[s * NW + wave] = gm;
    __builtin_amdgcn_fence(__ATOMIC_RELEASE, "workgroup");
    if (lane == 0) __hip_atomic_fetch_add(&s_mcnt[s], 1, __ATOMIC_RELAXED, __HIP_MEMORY_SCOPE_WORKGROUP);
    gmx = -INFINITY;
  };
  Slot buf[NBUF];
  int seg = 0;
  bool through = false;
  for (int n = 2; n < NT; ++n) {
    pace(n);
    gload(buf, rs_k, n);
    if (seg_of(n) != seg) {
      k_done(seg);
      ++seg;
    }
    k_group(buf, n, seg);
    if (wave == SVC && bid == NWG - 1 && n >= 3 && !through) through = look_at_p();
  }
  k_done(seg);
  const int d4 = lane < DH / 4 ? ((lane >> 1) & 7) * 6 + (lane >> 4) * 2 + (lane & 1) : DH / 4;
  const int rofs = lane < REC / 4 ? (WS_PO + wid * REC + lane * 4) * 4 : OOB;
  float l = 0.f;
  f32x2 acc[12];
#pragma unroll
  for (int i = 0; i < 12; ++i) acc[i] = f32x2{0.f, 0.f};
  __builtin_amdgcn_fence(__ATOMIC_RELEASE, "wavefront"); __builtin_amdgcn_wave_barrier(); __builtin_amdgcn_fence(__ATOMIC_ACQUIRE, "wavefront");
  for (int n = 0; n < NT; ++n) {
    pace(NT + n);
    gload(buf, rs_v, n);
    if (n == NT - 1) {
      if (lane == 0) __hip_atomic_fetch_add(&s_icnt, 1, __ATOMIC_RELAXED, __HIP_MEMORY_SCOPE_WORKGROUP);
      __builtin_amdgcn_s_setprio(3);
    }
    seg = seg_of(n);
    for (unsigned spin = 0; spin < (SPIN_CAP << 4) && lds_get(&s_mcnt[seg]) < NW; ++spin) __builtin_amdgcn_s_sleep(2);
    __builtin_amdgcn_fence(__ATOMIC_ACQUIRE, "workgroup");
    float m = s_m[seg * NW + j], lw;
    m = fmaxf(m, dpp<DPP_XOR1>(m)); m = fmaxf(m, dpp<DPP_XOR2>(m)); m = fmaxf(m, dpp<DPP_HMIRROR>(m));
    {
      float p[8];
      const float* const sc = &s_sc[(wave * NT + n) * 64 + r * 8];
      const f32x4 sa = *reinterpret_cast<const f32x4*>(sc), sb = *reinterpret_cast<const f32x4*>(sc + 4);
#pragma unroll
      for (int t = 0; t < 4; ++t) { p[t] = __builtin_amdgcn_exp2f(sa[t] - m); p[4 + t] = __builtin_amdgcn_exp2f(sb[t] - m); }
      l += ((p[0] + p[1]) + (p[2] + p[3])) + ((p[4] + p[5]) + (p[6] + p[7]));
      lw = sum_r(l);
      asm volatile("" : "+v"(lw));
      MEMFENCE();
#pragma unroll
      for (int u = 0; u < NBUF; ++u) {
        v_tile(buf[u].a, p[2 * u], acc);
        v_tile(buf[u].b, p[2 * u + 1], acc);
        MEMFENCE();
      }
    }
    t_thru = (unsigned)__builtin_amdgcn_s_memrealtime();
    if (wave == SVC && n == 1)
      for (unsigned spin = 0; spin < SPIN_CAP && !look_at_p(); ++spin) __builtin_amdgcn_s_sleep(4);
    if (n == NT - 1 || seg_of(n + 1) != seg) {
      float t12[12], t6[6];
#pragma unroll
      for (int e = 0; e < 12; ++e) t12[e] = tsum32(acc[e >> 1][e & 1], acc[6 + (e >> 1)][e & 1]);
#pragma unroll
      for (int e = 0; e < 6; ++e) { t6[e] = tsum16(t12[e], t12[e + 6]); t6[e] += dpp<DPP_ROR8>(t6[e]); }
      if ((lane & 8) == 0) {
#pragma unroll
        for (int e = 0; e < 3; ++e) *reinterpret_cast<f32x2*>(&s_buf[(seg * NW + wave) * PST + j * 24 + (lane >> 4) * 6 + e * 2]) = f32x2{t6[2 * e], t6[2 * e + 1]};
      }
      if (lane == 0) *reinterpret_cast<f32x2*>(&s_buf[(seg * NW + wave) * PST + DH]) = f32x2{m, lw};
      l = 0.f;
#pragma unroll
      for (int i = 0; i < 12; ++i) acc[i] = f32x2{0.f, 0.f};
      if (seg != 0) {
        int c = count_in_rtn(seg == 1 ? &s_ecnt : &s_fcnt);
        MEMFENCE();
        auto merged = [&](int s) {
          f32x4 pw[NW];
#pragma unroll
          for (int w = 0; w < NW; ++w) pw[w] = reinterpret_cast<const f32x4*>(s_buf)[(s * NW + w) * (PST / 4) + d4];
          f32x4 o = ((pw[0] + pw[1]) + (pw[2] + pw[3])) + ((pw[4] + pw[5]) + (pw[6] + pw[7]));
          if (lane >= DH / 4) o = f32x4{pw[0][0], o[1], 1.f, 1.f};
          return u32x4{nz_bits(o[0]), nz_bits(o[1]), nz_bits(o[2]), nz_bits(o[3])};
        };
        u32x4 rec = merged(seg);
        asm volatile("s_waitcnt lgkmcnt(0)" : "+v"(rec), "+v"(c) :: "memory");
        if (__builtin_amdgcn_readfirstlane(c) == NW - 1) {
          __builtin_amdgcn_raw_buffer_store_b128(rec, rs_ws, rofs, seg * (NWG * REC * 4), AUX_SC1);
          if (seg == 1) __builtin_amdgcn_raw_buffer_store_b128(merged(0), rs_ws, rofs, 0, AUX_SC1);
          if (seg == 2) { MEMFENCE(); lds_put(&s_fcnt, NW + 1); }
          if (seg == 1) {
            DRAIN();
            __builtin_amdgcn_raw_buffer_store_b32(1u, rs_ws, lane == 0 ? ef_ofs(wid) : OOB, 0, AUX_SC1);
          }
        }
      }
      MEMFENCE();
    }
  }
  __builtin_amdgcn_s_setprio(0);

  {
    asm volatile("" : "+s"(wave), "+s"(bid), "+v"(lane));
    r = lane >> 3; j = lane & 7; mg = lane >> 4; mc = lane & 15;
    const int w = wave;
    const int xcd = bid & 7, ri = bid >> 3;
    const bool isW = ri < 15, isG = !isW && ri < 27;
    const int tt = isW ? xcd * 15 + ri : (isG ? xcd * 12 + ri - 15 : xcd * 5 + ri - 27);
    const int ks1 = tt / W_CT, ct1 = tt - ks1 * W_CT;
    typedef const __attribute__((address_space(1))) char* gptr;
    auto behind_stream = [&](int nsl, int cnt) {
      for (unsigned spin = 0; spin < (SPIN_CAP << 4) && lds_get(&s_icnt) < cnt; ++spin) __builtin_amdgcn_s_sleep(1);
      for (int i = 0; i < nsl && lds_get(&s_fcnt) <= NW; ++i) __builtin_amdgcn_s_sleep(3);
    };
    u32x4 fr[16];
    u32x2 raw[32];
    auto weights = [&](const bf16* src, int skip) {
      gptr wb = (gptr)src;
#pragma unroll
      for (int e = 0; e < 32; ++e) {
        raw[e] = __builtin_nontemporal_load((const __attribute__((address_space(1))) u32x2*)wb);
        wb += ((e & 7) == 7 ? skip : 1) * (DM * 2);
        asm volatile("" : "+v"(wb));
      }
      MEMFENCE();
    };
    auto frags = [&]() {
#pragma unroll
      for (int st = 0; st < 4; ++st) {
        PIN8(raw, st * 8);
#pragma unroll
        for (int i = 0; i < 4; ++i) {
          const u32x2 r0 = raw[st * 8 + 2 * i], r1 = raw[st * 8 + 2 * i + 1];
          fr[st * 4 + 0][i] = pk_lo(r0[0], r1[0]); fr[st * 4 + 1][i] = pk_hi(r0[0], r1[0]);
          fr[st * 4 + 2][i] = pk_lo(r0[1], r1[1]); fr[st * 4 + 3][i] = pk_hi(r0[1], r1[1]);
        }
        PIN4(fr, st * 4);
        MEMFENCE();
      }
    };
    if (isW) {
      if (w >= 4) return;
      int a_put = w * (16 * WA) + mg * 4 * WA + mc * 8, a_get = w * (16 * WA / 16) + mc * (WA / 16) + mg, a_red = w * 64 + lane;
      int o_res = (WS_SLAB + ks1 * SLS + mg * DM + ct1 * 64 + mc * 4) * 4;
      {
        int offp[2], sg0[2];
        bool er[2];
#pragma unroll
        for (int hf = 0; hf < 2; ++hf) {
          const int row = ks1 * K3_ROWS + w * 128 + hf * 64, hh = row / DH;
          sg0[hf] = (mg * NH + hh) * NSEG;
          offp[hf] = (WS_PO + sg0[hf] * REC + row - hh * DH + mc * 4) * 4;
          er[hf] = sg0[hf] + NSEG <= 2 * NWG;
        }
        const int sgm = (mc < 8 ? sg0[0] : sg0[1]) + j, offm = j < NSEG ? (WS_PO + sgm * REC + DH) * 4 : OOB;
        for (unsigned spin = 0; spin < (SPIN_CAP << 4) && lds_get(&s_ecnt) < NW; ++spin) __builtin_amdgcn_s_sleep(4);
        for (unsigned spin = 0; spin < SPIN_CAP; ++spin) {
          AGAIN();
          const bool fl = j < NSEG && sgm < 2 * NWG;
          if (__all(!fl || __builtin_amdgcn_raw_buffer_load_b32(rs_ws, fl ? ef_ofs(sgm & (NWG - 1)) : OOB, 0, AUX_SC1) != 0u)) break;
          __builtin_amdgcn_s_sleep(8);
        }
        auto look = [&](u32x4 (&d)[2 * NSEG], bool h0, bool h1) {
          const int o0_ = h0 ? offp[0] : OOB, o1_ = h1 ? offp[1] : OOB;
#pragma unroll
          for (int k = 0; k < 2 * NSEG; ++k) d[k] = __builtin_amdgcn_raw_buffer_load_b128(rs_ws, k < NSEG ? o0_ : o1_, (k % NSEG) * REC * 4, AUX_SC1);
          MEMFENCE();
        };
        auto wsum = [&](float wt, const u32x4 (&d)[2 * NSEG], f32x4& a0, f32x4& a1) {
          a0 += dpp<DPP_BCAST + 0>(wt) * __builtin_bit_cast(f32x4, d[0]); a1 += dpp<DPP_BCAST + 8>(wt) * __builtin_bit_cast(f32x4, d[6]);
          a0 += dpp<DPP_BCAST + 1>(wt) * __builtin_bit_cast(f32x4, d[1]); a1 += dpp<DPP_BCAST + 9>(wt) * __builtin_bit_cast(f32x4, d[7]);
          a0 += dpp<DPP_BCAST + 2>(wt) * __builtin_bit_cast(f32x4, d[2]); a1 += dpp<DPP_BCAST + 10>(wt) * __builtin_bit_cast(f32x4, d[8]);
          a0 += dpp<DPP_BCAST + 3>(wt) * __builtin_bit_cast(f32x4, d[3]); a1 += dpp<DPP_BCAST + 11>(wt) * __builtin_bit_cast(f32x4, d[9]);
          a0 += dpp<DPP_BCAST + 4>(wt) * __builtin_bit_cast(f32x4, d[4]); a1 += dpp<DPP_BCAST + 12>(wt) * __builtin_bit_cast(f32x4, d[10]);
          a0 += dpp<DPP_BCAST + 5>(wt) * __builtin_bit_cast(f32x4, d[5]); a1 += dpp<DPP_BCAST + 13>(wt) * __builtin_bit_cast(f32x4, d[11]);
        };
        u32x4 pe[2 * NSEG], pl[2 * NSEG];
        behind_stream(6, NW - 1);
        weights(wo + (size_t)(ks1 * K3_ROWS + w * 128 + mg * 8) * DM + ct1 * 64 + mc * 4, 25);
        look(pe, er[0], er[1]);
        for (unsigned sp2 = 0; sp2 < (SPIN_CAP << 4) && lds_get(&s_fcnt) < NW; ++sp2) __builtin_amdgcn_s_sleep(1);
        f32x4 o0, o1;
        asm volatile("" : "+v"(a_put), "+v"(a_get), "+v"(a_red), "+v"(o_res));
        u32x2 ml = __builtin_amdgcn_raw_buffer_load_b64(rs_ws, offm, 0, AUX_SC1);
        look(pl, !er[0], !er[1]);
        frags();
        MEMFENCE();
#pragma unroll 1
        for (unsigned spin = 0; spin < SPIN_CAP; ++spin) {
          AGAIN();
          const float mm = j < NSEG ? __uint_as_float(ml[0]) : -INFINITY;
          float M = fmaxf(mm, dpp<DPP_XOR1>(mm)); M = fmaxf(M, dpp<DPP_XOR2>(M)); M = fmaxf(M, dpp<DPP_HMIRROR>(M));
          const float e = __builtin_amdgcn_exp2f(mm - M);
          float L = e * __uint_as_float(ml[1]);
          L += dpp<DPP_XOR1>(L); L += dpp<DPP_XOR2>(L); L += dpp<DPP_HMIRROR>(L);
          const float wt = e * __builtin_amdgcn_rcpf(L);
          o0 = f32x4{0.f, 0.f, 0.f, 0.f}; o1 = o0;
          wsum(wt, pe, o0, o1);
          unsigned m0 = er[0], m1 = er[1], a0 = ~0u, a1 = ~0u;
          const bool okm = j >= NSEG || min(ml[0], ml[1]) != 0u;
          asm volatile("" : "+v"(o0), "+v"(o1));
          MEMFENCE();
          wsum(wt, pl, o0, o1);
          asm volatile("" : "+v"(o0), "+v"(o1));
#pragma unroll
          for (int k = 0; k < NSEG; ++k) { a0 = min(a0, min4(pl[k])); a1 = min(a1, min4(pl[NSEG + k])); }
          if (__all(okm && (a0 | m0) != 0u && (a1 | m1) != 0u)) break;
          if (spin >= 8) __builtin_amdgcn_s_sleep(4);
          ml = __builtin_amdgcn_raw_buffer_load_b64(rs_ws, offm, 0, AUX_SC1);
          look(pl, !er[0], !er[1]);
        }
        put_terms4(s_A + a_put, WA, o0);
        put_terms4(s_A + a_put + 128, WA, o1);
      }
      __builtin_amdgcn_fence(__ATOMIC_RELEASE, "wavefront"); __builtin_amdgcn_wave_barrier(); __builtin_amdgcn_fence(__ATOMIC_ACQUIRE, "wavefront");
      f32x4 ac[4];
#pragma unroll
      for (int t = 0; t < 4; ++t) ac[t] = f32x4{0.f, 0.f, 0.f, 0.f};
      u32x4 av[4];
#pragma unroll
      for (int st = 0; st < 4; ++st) av[st] = reinterpret_cast<const u32x4*>(s_A)[a_get + st * 4];
      MEMFENCE();
#pragma unroll
      for (int st = 0; st < 4; ++st)
#pragma unroll
        for (int t = 0; t < 4; ++t) ac[t] = mfma16(av[st], fr[st * 4 + t], ac[t]);
      reinterpret_cast<f32x4*>(s_red)[a_red] = f32x4{sum3(ac[0]), sum3(ac[1]), sum3(ac[2]), sum3(ac[3])};
      __syncthreads();
      if (w == 0) {
        const f32x4* const red = reinterpret_cast<const f32x4*>(s_red) + a_red;
        const f32x4 s0 = (red[0] + red[64]) + (red[128] + red[192]);
        __builtin_amdgcn_raw_buffer_store_b128(u32x4{nz_bits(s0[0]), nz_bits(s0[1]), nz_bits(s0[2]), nz_bits(s0[3])}, rs_ws, o_res, 0, AUX_SC1);
      }
    } else if (isG) {
      const int gb = tt & 3, gt = tt >> 2;
      int o_res = (WS_ACT + (gb * (FF / 64) + gt) * 96 + (lane & 1) * 32 + (lane >> 1)) * 4;
      asm volatile("" : "+v"(o_res));
      if (w < 5) {
        const int hcol = w * 128 + (lane & 31) * 4;
        uint2 xx = {0u, 0u};
        u32x4 fg[32];
        {
          u32x4 rg[32];
          if (lane < 32) xx = *reinterpret_cast<const uint2*>(x + gb * DM + hcol);
          behind_stream(5, NW);
          gptr wb = (gptr)((mc < 8 ? wg : wu) + (size_t)(w * 128 + mg * 8) * FF + gt * 64 + (mc & 7) * 8);
#pragma unroll
          for (int e = 0; e < 32; ++e) {
            rg[e] = __builtin_nontemporal_load((const __attribute__((address_space(1))) u32x4*)wb);
            wb += ((e & 7) == 7 ? 25 : 1) * (FF * 2);
            asm volatile("" : "+v"(wb));
          }
          MEMFENCE();
#pragma unroll
          for (int st = 0; st < 4; ++st) {
            PIN8(rg, st * 8);
#pragma unroll
            for (int c = 0; c < 4; ++c)
#pragma unroll
              for (int i = 0; i < 4; ++i) {
                fg[st * 8 + 2 * c][i] = pk_lo(rg[st * 8 + 2 * i][c], rg[st * 8 + 2 * i + 1][c]);
                fg[st * 8 + 2 * c + 1][i] = pk_hi(rg[st * 8 + 2 * i][c], rg[st * 8 + 2 * i + 1][c]);
              }
            PIN8(fg, st * 8);
            MEMFENCE();
          }
        }
        {
          f32x4 cs[8];
#pragma unroll
          for (int t = 0; t < 8; ++t) cs[t] = f32x4{0.f, 0.f, 0.f, 0.f};
#pragma unroll
          for (int e = 0; e < 32; ++e) cs[e & 7] = mfma16(u32x4{0x3f803f80u, 0x3f803f80u, 0x3f803f80u, 0x3f803f80u}, fg[e], cs[e & 7]);
          if (lane < 16) {
            *reinterpret_cast<f32x4*>(&s_red[w * 256 + 128 + lane * 8]) = f32x4{cs[0][0], cs[1][0], cs[2][0], cs[3][0]};
            *reinterpret_cast<f32x4*>(&s_red[w * 256 + 128 + lane * 8 + 4]) = f32x4{cs[4][0], cs[5][0], cs[6][0], cs[7][0]};
          }
          count_in(&s_ccnt);
        }
        f32x4 hv = f32x4{bf_lo(xx.x), bf_hi(xx.x), bf_lo(xx.y), bf_hi(xx.y)};
        int a_h = hcol + (lane >> 5) * 2, a_put = w * (4 * WA) + (lane & 31) * 8 + (lane >> 5) * 4, a_get = w * (4 * WA / 16) + min(mc, 2) * (WA / 16) + mg, a_red = w * 64 + mc * 2;
        asm volatile("" : "+v"(hv), "+v"(a_h), "+v"(a_put), "+v"(a_get), "+v"(a_red));
        const int off = (WS_SLAB + (lane >> 5) * (K3_KS / 2) * SLS + gb * DM + hcol) * 4;
        u32x4 sl[K3_KS / 2];
        for (unsigned spin = 0; spin < SPIN_CAP; ++spin) {
          AGAIN();
#pragma unroll
          for (int ks = 0; ks < K3_KS / 2; ++ks) sl[ks] = __builtin_amdgcn_raw_buffer_load_b128(rs_ws, off, ks * (SLS * 4), AUX_SC1);
          if (__all(min(min(min(min4(sl[0]), min4(sl[1])), min(min4(sl[2]), min4(sl[3]))), min(min4(sl[4]), min4(sl[5]))) != 0u)) break;
          __builtin_amdgcn_s_sleep(2);
        }
        hv = ((hv + __builtin_bit_cast(f32x4, sl[0])) + (__builtin_bit_cast(f32x4, sl[1]) + __builtin_bit_cast(f32x4, sl[2]))) +
             ((__builtin_bit_cast(f32x4, sl[3]) + __builtin_bit_cast(f32x4, sl[4])) + __builtin_bit_cast(f32x4, sl[5]));
        const float h0 = tsum32(hv[0], hv[2]), h1 = tsum32(hv[1], hv[3]);
        *reinterpret_cast<f32x2*>(&s_h[a_h]) = f32x2{h0, h1};
        count_in(&s_hcnt);
        {
          unsigned t0[3], t1[3];
          split3(h0, t0[0], t0[1], t0[2]); split3(h1, t1[0], t1[1], t1[2]);
#pragma unroll
          for (int k = 0; k < 3; ++k) *reinterpret_cast<unsigned*>(s_A + a_put + k * WA) = pk_hi(t0[k], t1[k]);
        }
        __builtin_amdgcn_fence(__ATOMIC_RELEASE, "wavefront"); __builtin_amdgcn_wave_barrier(); __builtin_amdgcn_fence(__ATOMIC_ACQUIRE, "wavefront");
        f32x4 ac[8];
#pragma unroll
        for (int t = 0; t < 8; ++t) ac[t] = f32x4{0.f, 0.f, 0.f, 0.f};
        u32x4 av[4];
#pragma unroll
        for (int st = 0; st < 4; ++st) av[st] = reinterpret_cast<const u32x4*>(s_A)[a_get + st * 4];
        MEMFENCE();
#pragma unroll
        for (int st = 0; st < 4; ++st)
#pragma unroll
          for (int t = 0; t < 8; ++t) ac[t] = mfma16(av[st], fg[st * 8 + t], ac[t]);
        if (lane < 16) {
          reinterpret_cast<f32x4*>(s_red)[a_red] = f32x4{sum3(ac[0]), sum3(ac[1]), sum3(ac[2]), sum3(ac[3])};
          reinterpret_cast<f32x4*>(s_red)[a_red + 1] = f32x4{sum3(ac[4]), sum3(ac[5]), sum3(ac[6]), sum3(ac[7])};
        }
      } else if (w == NW - 1) {
        unsigned wr2[5];
#pragma unroll
        for (int i = 0; i < 5; ++i) wr2[i] = reinterpret_cast<const unsigned*>(wr)[lane + 64 * i];
        PIN4(wr2, 0); asm volatile("" : "+v"(wr2[4]));
        for (unsigned spin = 0; spin < (SPIN_CAP << 4) && lds_get(&s_ccnt) < 5; ++spin) __builtin_amdgcn_s_sleep(1);
        __builtin_amdgcn_fence(__ATOMIC_ACQUIRE, "workgroup");
        float cg = 0.f, cu = 0.f;
#pragma unroll
        for (int k = 0; k < 5; ++k) { cg += s_red[k * 256 + 128 + lane]; cu += s_red[k * 256 + 192 + lane]; }
        asm volatile("" : "+v"(cg), "+v"(cu));
        for (unsigned spin = 0; spin < (SPIN_CAP << 6) && lds_get(&s_hcnt) < 5; ++spin) {}
        __builtin_amdgcn_fence(__ATOMIC_ACQUIRE, "workgroup");
        const float piv = s_h[0];
        float hv[10], sm = 0.f, sq = 0.f;
#pragma unroll
        for (int i = 0; i < 5; ++i) {
          const float2 t2 = *reinterpret_cast<const float2*>(&s_h[2 * lane + 128 * i]);
          hv[2 * i] = t2.x; hv[2 * i + 1] = t2.y;
        }
        PIN8(hv, 0); asm volatile("" : "+v"(hv[8]), "+v"(hv[9]));
#pragma unroll
        for (int i = 0; i < 10; ++i) { hv[i] -= piv; sm += hv[i]; sq = fmaf(hv[i], hv[i], sq); }
        float t2 = row16_sum(tsum32(sm, sq));
        t2 = tsum16(t2, t2);
        const float dm = __int_as_float(__builtin_amdgcn_readlane(__float_as_int(t2), 0)) * (1.0f / DM);
        const float var = fmaxf(fmaf(-dm, dm, __int_as_float(__builtin_amdgcn_readlane(__float_as_int(t2), 32)) * (1.0f / DM)), 0.f);
        const float rstd = __builtin_amdgcn_rsqf(var + LN_EPS);
        s_red[5 * 256 + lane] = -(piv + dm) * cg; s_red[5 * 256 + 64 + lane] = -(piv + dm) * cu; s_red[5 * 256 + 128 + lane] = rstd;
        __syncthreads();
        if (gt < DM / 32) {
          float rr = 0.f;
#pragma unroll
          for (int i = 0; i < 5; ++i) { rr = fmaf((hv[2 * i] - dm) * rstd, bf_lo(wr2[i]), rr); rr = fmaf((hv[2 * i + 1] - dm) * rstd, bf_hi(wr2[i]), rr); }
          rr = wave_sum(rr);
          const float hh = s_h[gt * 32 + (lane & 31)];
          __builtin_amdgcn_raw_buffer_store_b32(nz_bits(lane >= 32 ? 1.0f / (1.0f + __expf(-rr)) : hh + (hh - (piv + dm)) * rstd), rs_ws,
                                                (WS_BASE + (gb * (DM / 32) + gt) * 64 + lane) * 4, 0, AUX_SC1);
        }
        return;
      } else {
        return;
      }
      __syncthreads();
      if (w == 0) {
        f32x2 pr[6];
#pragma unroll
        for (int k = 0; k < 6; ++k) pr[k] = f32x2{s_red[k * 256 + lane], s_red[k * 256 + 64 + lane]};
        float rstd = s_red[5 * 256 + 128 + lane];
        PIN4(pr, 0); asm volatile("" : "+v"(pr[4]), "+v"(pr[5]), "+v"(rstd));
        const f32x2 gu = (((pr[0] + pr[1]) + (pr[2] + pr[3])) + (pr[4] + pr[5])) * rstd;
        const float gg = gu[0], uu = gu[1];
        unsigned t[3], wd2[3];
        split3(gg * __builtin_amdgcn_rcpf(1.0f + __expf(-gg)) * uu, t[0], t[1], t[2]);
#pragma unroll
        for (int k = 0; k < 3; ++k) {
          const unsigned o = __float_as_uint(dpp<DPP_XOR1>(__uint_as_float(t[k])));
          wd2[k] = nz_bits(__uint_as_float((lane & 1) ? pk_hi(o, t[k]) : pk_hi(t[k], o)));
        }
        __builtin_amdgcn_raw_buffer_store_b32((lane & 1) ? wd2[1] : wd2[0], rs_ws, o_res, 0, AUX_SC1);
        __builtin_amdgcn_raw_buffer_store_b32(wd2[2], rs_ws, (lane & 1) ? OOB : o_res, 256, AUX_SC1);
      }
    } else {
      if (w == 7) return;
      const int bp = tt & 1, ct = tt >> 1;
      for (unsigned spin = 0; spin < (SPIN_CAP << 4) && lds_get(&s_fcnt) < NW; ++spin) __builtin_amdgcn_s_sleep(1);
      if (w == 6) {
        const int bb = lane >> 5, f = lane & 31, offb = (WS_BASE + ((bp * 2 + bb) * (DM / 32) + ct) * 64 + f) * 4;
        const __amdgpu_buffer_rsrc_t rs_out = __builtin_amdgcn_make_buffer_rsrc(out, 0, NB * DM * 2, 0x00020000);
        int o_res = ((bp * 2 + bb) * DM + ct * 32 + f) * 2;
        unsigned bs = 0u, sg = 0u;
        __builtin_amdgcn_s_sleep(100);
        for (unsigned spin = 0; spin < SPIN_CAP; ++spin) {
          AGAIN();
          bs = __builtin_amdgcn_raw_buffer_load_b32(rs_ws, offb, 0, AUX_SC1); sg = __builtin_amdgcn_raw_buffer_load_b32(rs_ws, offb, 128, AUX_SC1);
          if (__all(min(bs, sg) != 0u)) break;
          __builtin_amdgcn_s_sleep(2);
        }
        asm volatile("" : "+v"(o_res));
        __syncthreads();
        float pr[12];
#pragma unroll
        for (int k = 0; k < 12; ++k) pr[k] = s_red[(k >> 1) * 256 + (k & 1) * 64 + lane];
        PIN8(pr, 0); PIN4(pr, 8);
        const float y = (((pr[0] + pr[1]) + (pr[2] + pr[3])) + ((pr[4] + pr[5]) + (pr[6] + pr[7]))) + ((pr[8] + pr[9]) + (pr[10] + pr[11]));
        const bf16 ov = __float2bfloat16(fmaf(y, __uint_as_float(sg), __uint_as_float(bs)));
        __builtin_amdgcn_raw_buffer_store_b16(__builtin_bit_cast(unsigned short, ov), rs_out, o_res, 0, 0);
        return;
      }
      __builtin_amdgcn_s_sleep(8);
      weights(wd + (size_t)(w * 256 + (mc >> 3) * 32 + mg * 8) * DM + ct * 32 + (mc & 7) * 4, 57);
      frags();
      int a_red = w * 64 + mg * 8 + (mc & 7);
      asm volatile("" : "+v"(a_red));
      const int off = (WS_ACT + (((bp * 2 + ((mc >> 2) & 1)) * (FF / 64) + w * 4) * 3 + min(mc & 3, 2)) * 32 + (mc >> 3) * 16 + mg * 4) * 4;
      u32x4 av[4];
      for (unsigned spin = 0; spin < SPIN_CAP; ++spin) {
        AGAIN();
#pragma unroll
        for (int st = 0; st < 4; ++st) av[st] = __builtin_amdgcn_raw_buffer_load_b128(rs_ws, off, st * 384, AUX_SC1);
        if (__all(min(min(min4(av[0]), min4(av[1])), min(min4(av[2]), min4(av[3]))) != 0u)) break;
        __builtin_amdgcn_s_sleep(2);
      }
      f32x4 ac[4];
#pragma unroll
      for (int t = 0; t < 4; ++t) ac[t] = f32x4{0.f, 0.f, 0.f, 0.f};
#pragma unroll
      for (int st = 0; st < 4; ++st)
#pragma unroll
        for (int t = 0; t < 4; ++t) ac[t] = mfma16(av[st], fr[st * 4 + t], ac[t]);
      if ((mc >> 3) == (mg >> 1)) reinterpret_cast<f32x4*>(s_red)[a_red] = f32x4{sum3(ac[0]), sum3(ac[1]), sum3(ac[2]), sum3(ac[3])};
      __syncthreads();
    }
  }
}

extern "C" void kernel_launch(void* const* d_in, const int* in_sizes, int n_in, void* d_out, int out_size, void* d_ws,
                              size_t ws_size, hipStream_t stream) {
  fused_kernel<<<NWG, NW * 64, 0, stream>>>((const bf16*)d_in[0], (const bf16*)d_in[1], (const bf16*)d_in[2], (const bf16*)d_in[3],
                                            (const bf16*)d_in[4], (const bf16*)d_in[6], (const bf16*)d_in[7], (const bf16*)d_in[8],
                                            (const bf16*)d_in[9], (float*)d_ws, (bf16*)d_out);
}
```
